```python
import torch

def _softmax(x, axis=-1):
    return torch.softmax(x, dim=axis)
def _gelu(x):
    return torch.nn.functional.gelu(x, approximate="tanh")
def _silu(x):
    return torch.nn.functional.silu(x)
def _sigmoid(x):
    return torch.sigmoid(x)
def _layernorm(x, axis=-1, eps=1e-5):
    m = x.mean(dim=axis, keepdim=True)
    v = x.var(dim=axis, keepdim=True, unbiased=False)
    return (x - m) / torch.sqrt(v + eps)
def _scatter_add(tgt, idx, src, axis=0):
    return tgt.clone().index_add_(axis, idx, src)
def _segment_sum(src, seg, n):
    out = torch.zeros((n,) + src.shape[1:], dtype=src.dtype)
    return out.index_add_(0, seg, src)
def _pad(x, pw):
    flat = tuple(v for pair in reversed(pw) for v in pair)
    return torch.nn.functional.pad(x, flat)

kernel_name = 'concat_heads__gqa_block_cached__mha_block_cached__near_ridge'

def setup_inputs():

    _w = (lambda a: (lambda t: t.to(torch.bfloat16) if t.is_floating_point() else t)(torch.as_tensor(a)))
    def _A_setup():
        B, Sq, D = 32, 80, 448
        Hq, Hkv, Sk, Dh = 16, 8, 512, 256
        assert Hq % Hkv == 0, (Hq, Hkv)
        return {
            "x": _w((lambda *s: torch.randn(*s, dtype=torch.bfloat16) * 0.8)(B, Sq, D)),
            "k_cache": _w((lambda *s: torch.randn(*s, dtype=torch.bfloat16) * 0.8)(B, Hkv, Sk, Dh)),
            "v_cache": _w((lambda *s: torch.randn(*s, dtype=torch.bfloat16) * 0.05)(B, Hkv, Sk, Dh)),
            "wq": _w((lambda *s: torch.randn(*s, dtype=torch.bfloat16) * 0.8)(D, Hq * Dh) * (D ** -0.5)),
            "wo": _w((lambda *s: torch.randn(*s, dtype=torch.bfloat16) * 0.05)(Hq * Dh, D) * ((Hq * Dh) ** -0.5)),
        }
    _A_in = _A_setup()

    def _B_setup():
        B, Sq, D = 32, 80, 448
        H, Sk, Dh = 16, 512, 256
        return {
            "x": _w((lambda *s: torch.randn(*s, dtype=torch.bfloat16) * 0.8)(B, Sq, D)),
            "k_cache": _w((lambda *s: torch.randn(*s, dtype=torch.bfloat16) * 0.8)(B, H, Sk, Dh)),
            "v_cache": _w((lambda *s: torch.randn(*s, dtype=torch.bfloat16) * 0.05)(B, H, Sk, Dh)),
            "wq": _w((lambda *s: torch.randn(*s, dtype=torch.bfloat16) * 0.8)(D, H * Dh) * (D ** -0.5)),
            "wo": _w((lambda *s: torch.randn(*s, dtype=torch.bfloat16) * 0.05)(H * Dh, D) * ((H * Dh) ** -0.5)),
        }
    _B_in = _B_setup()

    return {
        "A_x": _A_in["x"],
        "A_k_cache": _A_in["k_cache"],
        "A_v_cache": _A_in["v_cache"],
        "A_wq": _A_in["wq"],
        "A_wo": _A_in["wo"],
        "B_x": _B_in["x"],
        "B_k_cache": _B_in["k_cache"],
        "B_v_cache": _B_in["v_cache"],
        "B_wq": _B_in["wq"],
        "B_wo": _B_in["wo"],
    }

def reference(A_x, A_k_cache, A_v_cache, A_wq, A_wo, B_x, B_k_cache, B_v_cache, B_wq, B_wo):
    def _A_ref(x, k_cache, v_cache, wq, wo):
        B, Sq, D = x.shape
        Hq, Dh = 16, 256
        Hkv = k_cache.shape[1]
        rep = Hq // Hkv
        q = (lambda x, *d: x.permute(*d))((x @ wq).reshape(B, Sq, Hq, Dh), 0, 2, 1, 3)
        k = (lambda x, n, axis: torch.repeat_interleave(x, n, dim=axis))(k_cache, rep, axis=1)
        v = (lambda x, n, axis: torch.repeat_interleave(x, n, dim=axis))(v_cache, rep, axis=1)
        scores = torch.einsum("bhqd,bhkd->bhqk", q, k) * (Dh ** -0.5)
        p = _softmax(scores, axis=-1)
        o = torch.einsum("bhqk,bhkd->bhqd", p, v)
        return (lambda x, *d: x.permute(*d))(o, 0, 2, 1, 3).reshape(B, Sq, Hq * Dh) @ wo

    def _B_ref(x, k_cache, v_cache, wq, wo):
        B, Sq, D = x.shape
        H, Dh = 16, 256
        q = (lambda x, *d: x.permute(*d))((x @ wq).reshape(B, Sq, H, Dh), 0, 2, 1, 3)
        scores = torch.einsum("bhqd,bhkd->bhqk", q, k_cache) * (Dh ** -0.5)
        p = _softmax(scores, axis=-1)
        o = torch.einsum("bhqk,bhkd->bhqd", p, v_cache)
        return (lambda x, *d: x.permute(*d))(o, 0, 2, 1, 3).reshape(B, Sq, H * Dh) @ wo

    a = _A_ref(A_x, A_k_cache, A_v_cache, A_wq, A_wo)
    b = _B_ref(B_x, B_k_cache, B_v_cache, B_wq, B_wo)
    return torch.concatenate([a, b], axis=-1)
```

```cpp
#include <hip/hip_runtime.h>
#include <hip/hip_bf16.h>
typedef __hip_bfloat16 bf16;
static __device__ __forceinline__ float b2f(unsigned short u){ return __uint_as_float(((unsigned)u)<<16); }
static __device__ __forceinline__ unsigned short f2b(float f){ __bf16 b = (__bf16)f; return __builtin_bit_cast(unsigned short, b); }

__global__ void gemm_naive(const unsigned short* A, const unsigned short* B, unsigned short* C, int M, int N, int K, int ldc){
  __shared__ float As[16][17], Bs[16][17];
  int tx = threadIdx.x & 15, ty = threadIdx.x >> 4;
  int row = blockIdx.y*16+ty, col = blockIdx.x*16+tx;
  float acc = 0;
  for(int k0=0;k0<K;k0+=16){
    As[ty][tx] = b2f(A[(size_t)row*K + k0+tx]);
    Bs[ty][tx] = b2f(B[(size_t)(k0+ty)*N + col]);
    __syncthreads();
    for(int k=0;k<16;k++) acc += As[ty][k]*Bs[k][tx];
    __syncthreads();
  }
  C[(size_t)row*ldc+col] = f2b(acc);
}
__global__ void attn_naive(const unsigned short* Q, const unsigned short* Kc, const unsigned short* Vc, unsigned short* O, int rep){
  int q = blockIdx.x % 80, h = (blockIdx.x/80)%16, b = blockIdx.x/(80*16);
  int Hkv = 16/rep; int kvh = h/rep;
  __shared__ float qs[256], p[512], red[256];
  int t = threadIdx.x;
  qs[t] = b2f(Q[(size_t)(b*80+q)*4096 + h*256 + t]);
  __syncthreads();
  const unsigned short* Kb = Kc + (size_t)(b*Hkv+kvh)*512*256;
  const unsigned short* Vb = Vc + (size_t)(b*Hkv+kvh)*512*256;
  float m = -1e30f;
  for(int j=t;j<512;j+=256){
    float s=0; for(int d=0;d<256;d++) s += qs[d]*b2f(Kb[j*256+d]);
    s *= 0.0625f; p[j]=s; m = fmaxf(m,s);
  }
  red[t]=m; __syncthreads();
  for(int s=128;s>0;s>>=1){ if(t<s) red[t]=fmaxf(red[t],red[t+s]); __syncthreads(); }
  m = red[0]; __syncthreads();
  float l=0;
  for(int j=t;j<512;j+=256){ float e = __expf(p[j]-m); p[j]=e; l+=e; }
  red[t]=l; __syncthreads();
  for(int s=128;s>0;s>>=1){ if(t<s) red[t]+=red[t+s]; __syncthreads(); }
  l = red[0];
  float o=0;
  for(int j=0;j<512;j++) o += b2f(f2b(p[j]))*b2f(Vb[j*256+t]);
  O[(size_t)(b*80+q)*4096 + h*256 + t] = f2b(o/l);
}
extern "C" void kernel_launch(void* const* d_in, const int* in_sizes, int n_in, void* d_out, int out_size, void* d_ws, size_t ws_size, hipStream_t stream){
  unsigned short* ws = (unsigned short*)d_ws;
  unsigned short* out = (unsigned short*)d_out;
  for(int br=0;br<2;br++){
    const unsigned short* x=(const unsigned short*)d_in[br*5+0];
    const unsigned short* k=(const unsigned short*)d_in[br*5+1];
    const unsigned short* v=(const unsigned short*)d_in[br*5+2];
    const unsigned short* wq=(const unsigned short*)d_in[br*5+3];
    const unsigned short* wo=(const unsigned short*)d_in[br*5+4];
    unsigned short* Q = ws + (size_t)br*2*2560*4096;
    unsigned short* O = Q + (size_t)2560*4096;
    gemm_naive<<<dim3(4096/16,2560/16),256,0,stream>>>(x,wq,Q,2560,4096,448,4096);
    attn_naive<<<32*16*80,256,0,stream>>>(Q,k,v,O,br==0?2:1);
    gemm_naive<<<dim3(448/16,2560/16),256,0,stream>>>(O,wo,out+br*448,2560,448,4096,896);
  }
}
```

```cpp
#include <hip/hip_runtime.h>
#include <hip/hip_bf16.h>

namespace {
struct Params {
  const char* in[10];
  char* out;
  char* ws;
};

__global__ __launch_bounds__(256, 1) void fused_attn(Params p) {
  extern __shared__ __attribute__((aligned(16))) char smem[];
  const unsigned bid = blockIdx.x, rank = bid & 7, batch = bid >> 3;
  {
    const unsigned long long t0 = __builtin_amdgcn_s_memrealtime();
    while (__builtin_amdgcn_s_memrealtime() - t0 < (unsigned long long)(((batch >> 1) & 7) * 40)) __builtin_amdgcn_s_sleep(2);
  }
  const unsigned br0 = batch & 1;
  unsigned tid = threadIdx.x;
  const size_t kvA = (size_t)(batch * 8 + rank) * 262144, kvB = (size_t)(batch * 16 + 2 * rank) * 262144;
  const char* i0[5];
  const char* i1[5];
#pragma unroll
  for (int k = 0; k < 5; ++k) {
    i0[k] = br0 ? p.in[5 + k] : p.in[k];
    i1[k] = br0 ? p.in[k] : p.in[5 + k];
  }
  const size_t kv0 = br0 ? kvB : kvA, kv1 = br0 ? kvA : kvB;
  asm volatile(
R"(Lcode0_%=:
s_getpc_b64 s[36:37]
v_and_b32 v16, 63, %[tid]
v_lshrrev_b32 v19, 6, %[tid]
v_lshrrev_b32 v26, 3, v16
v_and_b32 v27, 7, v16
v_and_b32 v28, 1, v19
s_mov_b64 s[52:53], %[x0]
s_and_b32 s53, s53, 0xffff
s_mov_b32 s54, 71680
s_mov_b32 s55, 0x20000
s_mov_b32 s61, %[fl0]
s_bfe_u32 s95, s61, 0x4000b
s_cmp_ge_u32 s95, 14
s_cselect_b32 s93, 14, 7
s_cmp_ge_u32 s95, 7
s_cselect_b32 s93, s93, 0
s_sub_u32 s93, s95, s93
s_lshl_b32 s93, s93, 7
s_and_b32 s95, s95, 7
v_lshl_add_u32 v20, v19, 3, v26
s_mov_b32 s81, 896
v_mul_u32_u24 v20, s81, v20
v_lshrrev_b32 v21, 1, v26
v_lshl_add_u32 v21, v28, 2, v21
v_xor_b32 v21, v21, v27
v_lshl_add_u32 v20, v21, 4, v20
v_accvgpr_write_b32 a241, v20
v_accvgpr_read_b32 v6, a241
s_mov_b32 s94, s93
buffer_load_dwordx4 v[96:99], v6, s[52:55], s94 offen
s_add_u32 s94, s94, 128
s_cmp_eq_u32 s94, 896
s_cselect_b32 s94, 0, s94
v_readfirstlane_b32 s92, v19
buffer_load_dwordx4 v[100:103], v6, s[52:55], s94 offen
s_add_u32 s94, s94, 128
s_cmp_eq_u32 s94, 896
s_cselect_b32 s94, 0, s94
buffer_load_dwordx4 v[104:107], v6, s[52:55], s94 offen
s_add_u32 s94, s94, 128
s_cmp_eq_u32 s94, 896
s_cselect_b32 s94, 0, s94
v_and_b32 v17, 15, v16
buffer_load_dwordx4 v[108:111], v6, s[52:55], s94 offen
s_add_u32 s94, s94, 128
s_cmp_eq_u32 s94, 896
s_cselect_b32 s94, 0, s94
buffer_load_dwordx4 v[112:115], v6, s[52:55], s94 offen
s_add_u32 s94, s94, 128
s_cmp_eq_u32 s94, 896
s_cselect_b32 s94, 0, s94
v_lshrrev_b32 v18, 4, v16
buffer_load_dwordx4 v[116:119], v6, s[52:55], s94 offen
s_add_u32 s94, s94, 128
s_cmp_eq_u32 s94, 896
s_cselect_b32 s94, 0, s94
buffer_load_dwordx4 v[120:123], v6, s[52:55], s94 offen
s_add_u32 s94, s94, 128
s_cmp_eq_u32 s94, 896
s_cselect_b32 s94, 0, s94
v_and_b32 v24, 7, v17
v_add_u32 v6, 0x7000, v6
buffer_load_dwordx4 v[128:131], v6, s[52:55], s94 offen
s_add_u32 s94, s94, 128
s_cmp_eq_u32 s94, 896
s_cselect_b32 s94, 0, s94
buffer_load_dwordx4 v[132:135], v6, s[52:55], s94 offen
v_lshrrev_b32 v25, 3, v17
s_add_u32 s94, s94, 128
s_cmp_eq_u32 s94, 896
s_cselect_b32 s94, 0, s94
s_mov_b64 s[44:45], %[wq0]
s_and_b32 s45, s45, 0xffff
s_mov_b32 s46, 3662848
s_mov_b32 s47, 0x20000
s_mov_b32 s64, 0
s_mov_b32 s65, 8192
s_mov_b32 s66, 16384
s_mov_b32 s67, 24576
s_mov_b32 s68, 32768
s_mov_b32 s69, 40960
s_mov_b32 s70, 49152
s_mov_b32 s71, 57344
v_lshlrev_b32 v20, 4, v24
v_lshl_add_u32 v20, v19, 7, v20
v_lshl_add_u32 v20, v25, 9, v20
v_lshl_add_u32 v20, v18, 16, v20
s_lshl_b32 s81, s93, 12
v_add_u32 v20, s81, v20
v_accvgpr_write_b32 a240, v20
s_bfe_u32 s91, s61, 0x4000b
v_lshl_add_u32 v20, v19, 3, v26
v_min_u32 v20, 27, v20
s_mul_i32 s81, s91, 28
v_add_u32 v20, s81, v20
v_lshlrev_b32 v20, 13, v20
v_lshl_add_u32 v20, v27, 7, v20
v_accvgpr_write_b32 a249, v20
v_min_u32 v20, 55, v16
s_mov_b32 s81, 56
v_mad_u32_u24 v20, v19, s81, v20
s_mul_i32 s81, s91, 224
v_add_u32 v20, s81, v20
v_lshlrev_b32 v20, 7, v20
v_accvgpr_write_b32 a250, v20
s_mov_b64 s[56:57], %[ws]
s_and_b32 s57, s57, 0xffff
s_mov_b32 s58, 42012672
s_mov_b32 s59, 0x20000
s_bfe_u32 s81, s61, 0x5000a
s_lshl_b32 s81, s81, 7
s_add_u32 s81, s81, 65536
v_mov_b32 v29, s81
s_and_b32 s37, s37, 0xffff
s_mov_b32 s38, Lcode1_%=-Lcode0_%=
s_mov_b32 s39, 0x20000
s_bfe_u32 s81, s61, 0x5000a
s_lshl_b32 s81, s81, 12
v_lshlrev_b32 v30, 7, v27
v_lshl_add_u32 v30, v19, 10, v30
v_add_u32 v30, s81, v30
v_lshrrev_b32 v20, 2, v17
v_and_b32 v21, 2, v17
v_lshl_add_u32 v20, v20, 3, v21
v_and_b32 v21, 1, v17
v_lshl_add_u32 v21, v18, 1, v21
v_lshlrev_b32 v21, 4, v21
v_lshl_add_u32 v4, v20, 9, v21
s_lshl_b32 s88, s92, 16
s_mov_b32 s84, 0x05040100
s_mov_b32 s85, 0x07060302
s_mov_b32 s86, 0x3db8aa3b
s_mov_b32 s87, 0x3db8aa3b
s_mov_b32 s90, 0
s_mov_b32 s60, 0
s_mov_b32 s76, 0x55555555
s_mov_b32 s77, 0x55555555
s_not_b64 s[78:79], s[76:77]
s_mov_b64 vcc, s[76:77]
s_mov_b32 s42, 0
s_mov_b32 s43, 0x20000
s_mov_b32 s50, 458752
s_mov_b32 s51, 0x20000
v_lshlrev_b32 v1, 4, v16
v_add_u32 v2, 0x10000, v1
v_add_u32 v3, 0x20000, v1
v_lshrrev_b32 v20, 1, v19
s_mov_b32 s81, 14336
v_mad_u32_u24 v20, v20, s81, v1
v_lshl_add_u32 v20, v28, 10, v20
v_add_u32 v20, 0x14000, v20
v_accvgpr_write_b32 a242, v20
v_lshlrev_b32 v20, 3, v17
v_lshl_add_u32 v20, v19, 7, v20
v_lshl_add_u32 v5, v18, 12, v20
s_mov_b32 s81, 7168
v_mul_u32_u24 v20, s81, v18
s_mov_b32 s81, 224
v_mad_u32_u24 v20, v19, s81, v20
v_lshl_add_u32 v20, v17, 4, v20
v_add_u32 v21, 2, v17
v_and_b32 v21, 16, v21
v_lshl_or_b32 v7, v21, 26, v20
v_lshlrev_b32 v20, 7, v24
v_lshl_add_u32 v20, v25, 10, v20
v_add_u32 v20, 0x14000, v20
v_lshrrev_b32 v21, 1, v17
v_xor_b32 v21, v21, v18
v_lshl_add_u32 v22, v21, 4, v20
v_accvgpr_write_b32 a243, v22
v_xor_b32 v21, 4, v21
v_lshl_add_u32 v22, v21, 4, v20
v_accvgpr_write_b32 a244, v22
v_and_b32 v20, 1, v18
v_lshlrev_b32 v20, 9, v20
v_lshl_add_u32 v20, v17, 4, v20
s_mov_b32 s81, 10240
v_mad_u32_u24 v20, v19, s81, v20
v_lshrrev_b32 v21, 1, v18
s_mov_b32 s81, 40960
v_mad_u32_u24 v20, v21, s81, v20
v_accvgpr_write_b32 a245, v20
v_lshrrev_b32 v20, 1, v18
v_lshl_add_u32 v20, v19, 1, v20
v_subrev_u32 v20, s95, v20
v_and_b32 v20, 7, v20
v_lshlrev_b32 v21, 4, v17
s_mov_b32 s81, 5120
v_mad_u32_u24 v20, v20, s81, v21
v_and_b32 v21, 1, v18
v_lshl_add_u32 v20, v21, 9, v20
v_accvgpr_write_b32 a246, v20
s_mov_b32 s81, 20480
v_mad_u32_u24 v20, v19, s81, v1
v_add_u32 v20, 0x14000, v20
v_accvgpr_write_b32 a247, v20
v_lshlrev_b32 v20, 4, v17
v_lshl_add_u32 v20, v19, 2, v20
v_accvgpr_write_b32 a248, v20
)"
R"(s_mov_b32 s81, 163840
v_mad_u32_u24 v8, v19, s81, v1
v_accvgpr_read_b32 v10, a242
v_add_u32 v11, 0xe000, v10
buffer_load_dwordx4 v[136:139], v6, s[52:55], s94 offen
s_add_u32 s94, s94, 128
s_cmp_eq_u32 s94, 896
s_cselect_b32 s94, 0, s94
buffer_load_dwordx4 v[140:143], v6, s[52:55], s94 offen
s_add_u32 s94, s94, 128
s_cmp_eq_u32 s94, 896
s_cselect_b32 s94, 0, s94
buffer_load_dwordx4 v[144:147], v6, s[52:55], s94 offen
s_add_u32 s94, s94, 128
s_cmp_eq_u32 s94, 896
s_cselect_b32 s94, 0, s94
s_waitcnt vmcnt(8)
ds_write_b128 v10, v[96:99] offset:0
ds_write_b128 v10, v[100:103] offset:2048
ds_write_b128 v10, v[104:107] offset:4096
ds_write_b128 v10, v[108:111] offset:6144
buffer_load_dwordx4 v[148:151], v6, s[52:55], s94 offen
s_add_u32 s94, s94, 128
s_cmp_eq_u32 s94, 896
s_cselect_b32 s94, 0, s94
buffer_load_dwordx4 v[152:155], v6, s[52:55], s94 offen
s_add_u32 s94, s94, 128
s_cmp_eq_u32 s94, 896
s_cselect_b32 s94, 0, s94
v_add_u32 v6, 0x7000, v6
s_waitcnt lgkmcnt(3)
buffer_load_dwordx4 v[96:99], v6, s[52:55], s94 offen
s_add_u32 s94, s94, 128
s_cmp_eq_u32 s94, 896
s_cselect_b32 s94, 0, s94
s_waitcnt vmcnt(8)
ds_write_b128 v10, v[112:115] offset:8192
ds_write_b128 v10, v[116:119] offset:10240
ds_write_b128 v10, v[120:123] offset:12288
s_waitcnt lgkmcnt(5)
buffer_load_dwordx4 v[100:103], v6, s[52:55], s94 offen
s_add_u32 s94, s94, 128
s_cmp_eq_u32 s94, 896
s_cselect_b32 s94, 0, s94
s_waitcnt lgkmcnt(4)
buffer_load_dwordx4 v[104:107], v6, s[52:55], s94 offen
s_add_u32 s94, s94, 128
s_cmp_eq_u32 s94, 896
s_cselect_b32 s94, 0, s94
s_waitcnt lgkmcnt(3)
buffer_load_dwordx4 v[108:111], v6, s[52:55], s94 offen
s_add_u32 s94, s94, 128
s_cmp_eq_u32 s94, 896
s_cselect_b32 s94, 0, s94
s_waitcnt lgkmcnt(2)
buffer_load_dwordx4 v[112:115], v6, s[52:55], s94 offen
s_add_u32 s94, s94, 128
s_cmp_eq_u32 s94, 896
s_cselect_b32 s94, 0, s94
s_waitcnt vmcnt(8)
ds_write_b128 v10, v[128:131] offset:28672
ds_write_b128 v10, v[132:135] offset:30720
ds_write_b128 v10, v[136:139] offset:32768
ds_write_b128 v10, v[140:143] offset:34816
s_waitcnt lgkmcnt(5)
buffer_load_dwordx4 v[116:119], v6, s[52:55], s94 offen
s_add_u32 s94, s94, 128
s_cmp_eq_u32 s94, 896
s_cselect_b32 s94, 0, s94
s_waitcnt lgkmcnt(4)
buffer_load_dwordx4 v[120:123], v6, s[52:55], s94 offen
s_add_u32 s94, s94, 128
s_cmp_eq_u32 s94, 896
s_cselect_b32 s94, 0, s94
v_accvgpr_read_b32 v6, a240
buffer_load_dwordx4 v[32:35], v6, s[44:47], s64 offen
s_waitcnt vmcnt(8)
ds_write_b128 v10, v[144:147] offset:36864
ds_write_b128 v10, v[148:151] offset:38912
ds_write_b128 v10, v[152:155] offset:40960
buffer_load_dwordx4 v[36:39], v6, s[44:47], s65 offen
buffer_load_dwordx4 v[40:43], v6, s[44:47], s66 offen
buffer_load_dwordx4 v[44:47], v6, s[44:47], s67 offen
buffer_load_dwordx4 v[48:51], v6, s[44:47], s68 offen
s_waitcnt vmcnt(8)
ds_write_b128 v11, v[96:99] offset:0
ds_write_b128 v11, v[100:103] offset:2048
ds_write_b128 v11, v[104:107] offset:4096
ds_write_b128 v11, v[108:111] offset:6144
buffer_load_dwordx4 v[52:55], v6, s[44:47], s69 offen
buffer_load_dwordx4 v[56:59], v6, s[44:47], s70 offen
buffer_load_dwordx4 v[60:63], v6, s[44:47], s71 offen
s_waitcnt vmcnt(8)
ds_write_b128 v11, v[112:115] offset:8192
ds_write_b128 v11, v[116:119] offset:10240
buffer_load_dwordx2 a[252:253], v29, s[56:59], 0 offen sc1
buffer_load_dword a255, v30, s[36:39], 0 offen
s_mov_b32 s58, 0
v_add_u32 v6, 0x40000, v6
ds_write_b128 v11, v[120:123] offset:12288
buffer_load_dwordx4 v[64:67], v6, s[44:47], s64 offen
buffer_load_dwordx4 v[68:71], v6, s[44:47], s65 offen
buffer_load_dwordx4 v[72:75], v6, s[44:47], s66 offen
buffer_load_dwordx4 v[76:79], v6, s[44:47], s67 offen
buffer_load_dwordx4 v[80:83], v6, s[44:47], s68 offen
buffer_load_dwordx4 v[84:87], v6, s[44:47], s69 offen
buffer_load_dwordx4 v[88:91], v6, s[44:47], s70 offen
buffer_load_dwordx4 v[92:95], v6, s[44:47], s71 offen
s_mov_b64 s[36:37], %[k0]
s_and_b32 s37, s37, 0xffff
s_lshr_b32 s38, s61, 15
s_lshl_b32 s38, s38, 18
s_add_u32 s38, s38, 262144
Lblock_%=:
s_cmp_eq_u32 s93, 768
s_cselect_b32 s94, -13, 1
s_lshl_b32 s94, s94, 18
v_add_u32 v6, s94, v6
s_nop 7
s_cmp_eq_u32 s90, 0
s_cselect_b64 s[40:41], %[v0], %[v1]
s_cselect_b64 s[48:49], %[wo0], %[wo1]
s_lshr_b32 s89, s61, 15
s_xor_b32 s89, s89, s90
s_lshl_b32 s89, s89, 18
s_and_b32 s41, s41, 0xffff
s_and_b32 s49, s49, 0xffff
s_add_u32 s42, s89, 262144
v_accvgpr_read_b32 v9, a243
v_accvgpr_read_b32 v10, a244
v_add_u32 v11, 0xe000, v9
v_add_u32 v12, 0xe000, v10
buffer_load_dwordx4 v[96:99], v6, s[44:47], s64 offen
s_waitcnt vmcnt(9)
v_perm_b32 v16, v36, v32, s84
v_perm_b32 v17, v44, v40, s84
v_perm_b32 v18, v52, v48, s84
v_perm_b32 v19, v60, v56, s84
buffer_load_dwordx4 v[100:103], v6, s[44:47], s65 offen
buffer_load_dwordx4 v[104:107], v6, s[44:47], s66 offen
buffer_load_dwordx4 v[108:111], v6, s[44:47], s67 offen
buffer_load_dwordx4 v[112:115], v6, s[44:47], s68 offen
buffer_load_dwordx4 v[116:119], v6, s[44:47], s69 offen
buffer_load_dwordx4 v[120:123], v6, s[44:47], s70 offen
buffer_load_dwordx4 v[124:127], v6, s[44:47], s71 offen
v_add_u32 v6, 0x40000, v6
buffer_load_dwordx4 v[128:131], v6, s[44:47], s64 offen
buffer_load_dwordx4 v[132:135], v6, s[44:47], s65 offen
buffer_load_dwordx4 v[136:139], v6, s[44:47], s66 offen
s_waitcnt vmcnt(11)
v_perm_b32 v20, v68, v64, s84
v_perm_b32 v21, v76, v72, s84
v_perm_b32 v22, v84, v80, s84
v_perm_b32 v23, v92, v88, s84
s_waitcnt lgkmcnt(0)
s_barrier
buffer_load_dwordx4 v[140:143], v6, s[44:47], s67 offen
buffer_load_dwordx4 v[144:147], v6, s[44:47], s68 offen
ds_read_b128 a[160:163], v9 offset:0
ds_read_b128 a[164:167], v9 offset:14336
ds_read_b128 a[168:171], v9 offset:28672
ds_read_b128 a[172:175], v9 offset:43008
ds_read_b128 a[176:179], v11 offset:0
ds_read_b128 a[180:183], v10 offset:0
ds_read_b128 a[184:187], v10 offset:14336
ds_read_b128 a[188:191], v10 offset:28672
ds_read_b128 a[192:195], v10 offset:43008
buffer_load_dwordx4 v[148:151], v6, s[44:47], s69 offen
buffer_load_dwordx4 v[152:155], v6, s[44:47], s70 offen
ds_read_b128 a[196:199], v12 offset:0
s_waitcnt lgkmcnt(5)
v_mfma_f32_16x16x32_bf16 a[0:3], v[16:19], a[160:163], 0
s_waitcnt lgkmcnt(0)
v_perm_b32 v24, v36, v32, s85
v_perm_b32 v25, v44, v40, s85
v_mfma_f32_16x16x32_bf16 a[0:3], v[20:23], a[180:183], a[0:3]
v_perm_b32 v26, v52, v48, s85
v_perm_b32 v27, v60, v56, s85
v_mfma_f32_16x16x32_bf16 a[4:7], v[16:19], a[164:167], 0
v_perm_b32 v28, v68, v64, s85
v_perm_b32 v29, v76, v72, s85
v_mfma_f32_16x16x32_bf16 a[4:7], v[20:23], a[184:187], a[4:7]
v_perm_b32 v30, v84, v80, s85
v_perm_b32 v31, v92, v88, s85
v_mfma_f32_16x16x32_bf16 a[8:11], v[16:19], a[168:171], 0
buffer_load_dwordx4 v[156:159], v6, s[44:47], s71 offen
v_mfma_f32_16x16x32_bf16 a[8:11], v[20:23], a[188:191], a[8:11]
s_cmp_eq_u32 s93, 640
s_cselect_b32 s94, -13, 1
s_lshl_b32 s94, s94, 18
v_mfma_f32_16x16x32_bf16 a[12:15], v[16:19], a[172:175], 0
v_add_u32 v6, s94, v6
v_mfma_f32_16x16x32_bf16 a[12:15], v[20:23], a[192:195], a[12:15]
buffer_load_dwordx4 v[160:163], v6, s[44:47], s64 offen
v_mfma_f32_16x16x32_bf16 a[16:19], v[16:19], a[176:179], 0
ds_read_b128 a[200:203], v9 offset:2048
v_mfma_f32_16x16x32_bf16 a[16:19], v[20:23], a[196:199], a[16:19]
ds_read_b128 a[220:223], v10 offset:2048
v_mfma_f32_16x16x32_bf16 a[20:23], v[24:27], a[160:163], 0
v_perm_b32 v16, v37, v33, s84
)"
R"(v_perm_b32 v17, v45, v41, s84
v_mfma_f32_16x16x32_bf16 a[20:23], v[28:31], a[180:183], a[20:23]
v_perm_b32 v18, v53, v49, s84
v_perm_b32 v19, v61, v57, s84
v_mfma_f32_16x16x32_bf16 a[24:27], v[24:27], a[164:167], 0
v_perm_b32 v20, v69, v65, s84
v_perm_b32 v21, v77, v73, s84
v_mfma_f32_16x16x32_bf16 a[24:27], v[28:31], a[184:187], a[24:27]
v_perm_b32 v22, v85, v81, s84
v_perm_b32 v23, v93, v89, s84
v_mfma_f32_16x16x32_bf16 a[28:31], v[24:27], a[168:171], 0
buffer_load_dwordx4 v[164:167], v6, s[44:47], s65 offen
v_mfma_f32_16x16x32_bf16 a[28:31], v[28:31], a[188:191], a[28:31]
buffer_load_dwordx4 v[168:171], v6, s[44:47], s66 offen
v_mfma_f32_16x16x32_bf16 a[32:35], v[24:27], a[172:175], 0
buffer_load_dwordx4 v[172:175], v6, s[44:47], s67 offen
v_mfma_f32_16x16x32_bf16 a[32:35], v[28:31], a[192:195], a[32:35]
buffer_load_dwordx4 v[176:179], v6, s[44:47], s68 offen
v_mfma_f32_16x16x32_bf16 a[36:39], v[24:27], a[176:179], 0
v_mfma_f32_16x16x32_bf16 a[36:39], v[28:31], a[196:199], a[36:39]
buffer_load_dwordx4 v[180:183], v6, s[44:47], s69 offen
v_mfma_f32_16x16x32_bf16 a[40:43], v[16:19], a[160:163], 0
v_perm_b32 v24, v37, v33, s85
v_perm_b32 v25, v45, v41, s85
v_mfma_f32_16x16x32_bf16 a[40:43], v[20:23], a[180:183], a[40:43]
v_perm_b32 v26, v53, v49, s85
v_perm_b32 v27, v61, v57, s85
v_mfma_f32_16x16x32_bf16 a[44:47], v[16:19], a[164:167], 0
v_perm_b32 v28, v69, v65, s85
v_perm_b32 v29, v77, v73, s85
v_mfma_f32_16x16x32_bf16 a[44:47], v[20:23], a[184:187], a[44:47]
v_perm_b32 v30, v85, v81, s85
v_perm_b32 v31, v93, v89, s85
v_mfma_f32_16x16x32_bf16 a[48:51], v[16:19], a[168:171], 0
ds_read_b128 a[204:207], v9 offset:16384
v_mfma_f32_16x16x32_bf16 a[48:51], v[20:23], a[188:191], a[48:51]
ds_read_b128 a[224:227], v10 offset:16384
v_mfma_f32_16x16x32_bf16 a[52:55], v[16:19], a[172:175], 0
buffer_load_dwordx4 v[184:187], v6, s[44:47], s70 offen
v_mfma_f32_16x16x32_bf16 a[52:55], v[20:23], a[192:195], a[52:55]
buffer_load_dwordx4 v[188:191], v6, s[44:47], s71 offen
v_mfma_f32_16x16x32_bf16 a[56:59], v[16:19], a[176:179], 0
v_add_u32 v6, 0x40000, v6
v_mfma_f32_16x16x32_bf16 a[56:59], v[20:23], a[196:199], a[56:59]
buffer_load_dwordx4 v[192:195], v6, s[44:47], s64 offen
v_mfma_f32_16x16x32_bf16 a[60:63], v[24:27], a[160:163], 0
v_perm_b32 v16, v38, v34, s84
v_perm_b32 v17, v46, v42, s84
v_mfma_f32_16x16x32_bf16 a[60:63], v[28:31], a[180:183], a[60:63]
v_perm_b32 v18, v54, v50, s84
v_perm_b32 v19, v62, v58, s84
v_mfma_f32_16x16x32_bf16 a[64:67], v[24:27], a[164:167], 0
v_perm_b32 v20, v70, v66, s84
v_perm_b32 v21, v78, v74, s84
v_mfma_f32_16x16x32_bf16 a[64:67], v[28:31], a[184:187], a[64:67]
v_perm_b32 v22, v86, v82, s84
v_perm_b32 v23, v94, v90, s84
v_mfma_f32_16x16x32_bf16 a[68:71], v[24:27], a[168:171], 0
buffer_load_dwordx4 v[196:199], v6, s[44:47], s65 offen
v_mfma_f32_16x16x32_bf16 a[68:71], v[28:31], a[188:191], a[68:71]
buffer_load_dwordx4 v[200:203], v6, s[44:47], s66 offen
v_mfma_f32_16x16x32_bf16 a[72:75], v[24:27], a[172:175], 0
ds_read_b128 a[208:211], v9 offset:30720
v_mfma_f32_16x16x32_bf16 a[72:75], v[28:31], a[192:195], a[72:75]
ds_read_b128 a[228:231], v10 offset:30720
v_mfma_f32_16x16x32_bf16 a[76:79], v[24:27], a[176:179], 0
buffer_load_dwordx4 v[204:207], v6, s[44:47], s67 offen
v_mfma_f32_16x16x32_bf16 a[76:79], v[28:31], a[196:199], a[76:79]
v_mfma_f32_16x16x32_bf16 a[80:83], v[16:19], a[160:163], 0
v_perm_b32 v24, v38, v34, s85
v_perm_b32 v25, v46, v42, s85
v_mfma_f32_16x16x32_bf16 a[80:83], v[20:23], a[180:183], a[80:83]
v_perm_b32 v26, v54, v50, s85
v_perm_b32 v27, v62, v58, s85
v_mfma_f32_16x16x32_bf16 a[84:87], v[16:19], a[164:167], 0
v_perm_b32 v28, v70, v66, s85
v_perm_b32 v29, v78, v74, s85
v_mfma_f32_16x16x32_bf16 a[84:87], v[20:23], a[184:187], a[84:87]
v_perm_b32 v30, v86, v82, s85
v_perm_b32 v31, v94, v90, s85
v_mfma_f32_16x16x32_bf16 a[88:91], v[16:19], a[168:171], 0
buffer_load_dwordx4 v[208:211], v6, s[44:47], s68 offen
v_mfma_f32_16x16x32_bf16 a[88:91], v[20:23], a[188:191], a[88:91]
buffer_load_dwordx4 v[212:215], v6, s[44:47], s69 offen
v_mfma_f32_16x16x32_bf16 a[92:95], v[16:19], a[172:175], 0
v_mfma_f32_16x16x32_bf16 a[92:95], v[20:23], a[192:195], a[92:95]
v_mfma_f32_16x16x32_bf16 a[96:99], v[16:19], a[176:179], 0
buffer_load_dwordx4 v[216:219], v6, s[44:47], s70 offen
v_mfma_f32_16x16x32_bf16 a[96:99], v[20:23], a[196:199], a[96:99]
v_mfma_f32_16x16x32_bf16 a[100:103], v[24:27], a[160:163], 0
v_perm_b32 v16, v39, v35, s84
v_perm_b32 v17, v47, v43, s84
v_mfma_f32_16x16x32_bf16 a[100:103], v[28:31], a[180:183], a[100:103]
v_perm_b32 v18, v55, v51, s84
v_perm_b32 v19, v63, v59, s84
v_mfma_f32_16x16x32_bf16 a[104:107], v[24:27], a[164:167], 0
ds_read_b128 a[212:215], v9 offset:45056
v_perm_b32 v20, v71, v67, s84
v_mfma_f32_16x16x32_bf16 a[104:107], v[28:31], a[184:187], a[104:107]
ds_read_b128 a[232:235], v10 offset:45056
v_perm_b32 v21, v79, v75, s84
v_mfma_f32_16x16x32_bf16 a[108:111], v[24:27], a[168:171], 0
v_perm_b32 v22, v87, v83, s84
v_perm_b32 v23, v95, v91, s84
v_mfma_f32_16x16x32_bf16 a[108:111], v[28:31], a[188:191], a[108:111]
buffer_load_dwordx4 v[220:223], v6, s[44:47], s71 offen
v_mfma_f32_16x16x32_bf16 a[112:115], v[24:27], a[172:175], 0
s_cmp_eq_u32 s93, 512
s_cselect_b32 s94, -13, 1
s_lshl_b32 s94, s94, 18
v_mfma_f32_16x16x32_bf16 a[112:115], v[28:31], a[192:195], a[112:115]
v_add_u32 v6, s94, v6
v_mfma_f32_16x16x32_bf16 a[116:119], v[24:27], a[176:179], 0
buffer_load_dwordx4 v[224:227], v6, s[44:47], s64 offen
v_mfma_f32_16x16x32_bf16 a[116:119], v[28:31], a[196:199], a[116:119]
buffer_load_dwordx4 v[228:231], v6, s[44:47], s65 offen
v_mfma_f32_16x16x32_bf16 a[120:123], v[16:19], a[160:163], 0
v_perm_b32 v24, v39, v35, s85
v_perm_b32 v25, v47, v43, s85
v_mfma_f32_16x16x32_bf16 a[120:123], v[20:23], a[180:183], a[120:123]
v_perm_b32 v26, v55, v51, s85
v_perm_b32 v27, v63, v59, s85
v_mfma_f32_16x16x32_bf16 a[124:127], v[16:19], a[164:167], 0
v_perm_b32 v28, v71, v67, s85
v_perm_b32 v29, v79, v75, s85
v_mfma_f32_16x16x32_bf16 a[124:127], v[20:23], a[184:187], a[124:127]
v_perm_b32 v30, v87, v83, s85
v_perm_b32 v31, v95, v91, s85
v_mfma_f32_16x16x32_bf16 a[128:131], v[16:19], a[168:171], 0
ds_read_b128 a[216:219], v11 offset:2048
v_mfma_f32_16x16x32_bf16 a[128:131], v[20:23], a[188:191], a[128:131]
ds_read_b128 a[236:239], v12 offset:2048
v_mfma_f32_16x16x32_bf16 a[132:135], v[16:19], a[172:175], 0
buffer_load_dwordx4 v[232:235], v6, s[44:47], s66 offen
v_mfma_f32_16x16x32_bf16 a[132:135], v[20:23], a[192:195], a[132:135]
buffer_load_dwordx4 v[236:239], v6, s[44:47], s67 offen
v_mfma_f32_16x16x32_bf16 a[136:139], v[16:19], a[176:179], 0
buffer_load_dwordx4 v[32:35], v6, s[44:47], s68 offen
v_mfma_f32_16x16x32_bf16 a[136:139], v[20:23], a[196:199], a[136:139]
buffer_load_dwordx4 v[36:39], v6, s[44:47], s69 offen
v_mfma_f32_16x16x32_bf16 a[140:143], v[24:27], a[160:163], 0
s_waitcnt vmcnt(30)
v_perm_b32 v16, v100, v96, s84
v_perm_b32 v17, v108, v104, s84
v_mfma_f32_16x16x32_bf16 a[140:143], v[28:31], a[180:183], a[140:143]
v_perm_b32 v18, v116, v112, s84
v_perm_b32 v19, v124, v120, s84
v_mfma_f32_16x16x32_bf16 a[144:147], v[24:27], a[164:167], 0
s_waitcnt vmcnt(22)
v_perm_b32 v20, v132, v128, s84
v_perm_b32 v21, v140, v136, s84
v_mfma_f32_16x16x32_bf16 a[144:147], v[28:31], a[184:187], a[144:147]
v_perm_b32 v22, v148, v144, s84
v_perm_b32 v23, v156, v152, s84
v_mfma_f32_16x16x32_bf16 a[148:151], v[24:27], a[168:171], 0
buffer_load_dwordx4 v[40:43], v6, s[44:47], s70 offen
v_mfma_f32_16x16x32_bf16 a[148:151], v[28:31], a[188:191], a[148:151]
v_mfma_f32_16x16x32_bf16 a[152:155], v[24:27], a[172:175], 0
buffer_load_dwordx4 v[44:47], v6, s[44:47], s71 offen
v_mfma_f32_16x16x32_bf16 a[152:155], v[28:31], a[192:195], a[152:155]
v_add_u32 v6, 0x40000, v6
v_mfma_f32_16x16x32_bf16 a[156:159], v[24:27], a[176:179], 0
v_mfma_f32_16x16x32_bf16 a[156:159], v[28:31], a[196:199], a[156:159]
buffer_load_dwordx4 v[48:51], v6, s[44:47], s64 offen
s_waitcnt lgkmcnt(1)
v_mfma_f32_16x16x32_bf16 a[0:3], v[16:19], a[200:203], a[0:3]
v_perm_b32 v24, v100, v96, s85
v_perm_b32 v25, v108, v104, s85
v_mfma_f32_16x16x32_bf16 a[0:3], v[20:23], a[220:223], a[0:3]
v_perm_b32 v26, v116, v112, s85
v_perm_b32 v27, v124, v120, s85
v_mfma_f32_16x16x32_bf16 a[4:7], v[16:19], a[204:207], a[4:7]
v_perm_b32 v28, v132, v128, s85
v_perm_b32 v29, v140, v136, s85
v_mfma_f32_16x16x32_bf16 a[4:7], v[20:23], a[224:227], a[4:7]
v_perm_b32 v30, v148, v144, s85
v_perm_b32 v31, v156, v152, s85
v_mfma_f32_16x16x32_bf16 a[8:11], v[16:19], a[208:211], a[8:11]
buffer_load_dwordx4 v[52:55], v6, s[44:47], s65 offen
v_mfma_f32_16x16x32_bf16 a[8:11], v[20:23], a[228:231], a[8:11]
buffer_load_dwordx4 v[56:59], v6, s[44:47], s66 offen
v_mfma_f32_16x16x32_bf16 a[12:15], v[16:19], a[212:215], a[12:15]
ds_read_b128 a[160:163], v9 offset:4096
v_mfma_f32_16x16x32_bf16 a[12:15], v[20:23], a[232:235], a[12:15]
ds_read_b128 a[180:183], v10 offset:4096
v_mfma_f32_16x16x32_bf16 a[16:19], v[16:19], a[216:219], a[16:19]
buffer_load_dwordx4 v[60:63], v6, s[44:47], s67 offen
s_waitcnt lgkmcnt(2)
v_mfma_f32_16x16x32_bf16 a[16:19], v[20:23], a[236:239], a[16:19]
v_mfma_f32_16x16x32_bf16 a[20:23], v[24:27], a[200:203], a[20:23]
v_perm_b32 v16, v101, v97, s84
v_perm_b32 v17, v109, v105, s84
v_mfma_f32_16x16x32_bf16 a[20:23], v[28:31], a[220:223], a[20:23]
v_perm_b32 v18, v117, v113, s84
v_perm_b32 v19, v125, v121, s84
v_mfma_f32_16x16x32_bf16 a[24:27], v[24:27], a[204:207], a[24:27]
v_perm_b32 v20, v133, v129, s84
v_perm_b32 v21, v141, v137, s84
v_mfma_f32_16x16x32_bf16 a[24:27], v[28:31], a[224:227], a[24:27]
v_perm_b32 v22, v149, v145, s84
)"
R"(v_perm_b32 v23, v157, v153, s84
v_mfma_f32_16x16x32_bf16 a[28:31], v[24:27], a[208:211], a[28:31]
buffer_load_dwordx4 v[64:67], v6, s[44:47], s68 offen
v_mfma_f32_16x16x32_bf16 a[28:31], v[28:31], a[228:231], a[28:31]
buffer_load_dwordx4 v[68:71], v6, s[44:47], s69 offen
v_mfma_f32_16x16x32_bf16 a[32:35], v[24:27], a[212:215], a[32:35]
v_mfma_f32_16x16x32_bf16 a[32:35], v[28:31], a[232:235], a[32:35]
v_mfma_f32_16x16x32_bf16 a[36:39], v[24:27], a[216:219], a[36:39]
buffer_load_dwordx4 v[72:75], v6, s[44:47], s70 offen
v_mfma_f32_16x16x32_bf16 a[36:39], v[28:31], a[236:239], a[36:39]
v_mfma_f32_16x16x32_bf16 a[40:43], v[16:19], a[200:203], a[40:43]
v_perm_b32 v24, v101, v97, s85
v_perm_b32 v25, v109, v105, s85
v_mfma_f32_16x16x32_bf16 a[40:43], v[20:23], a[220:223], a[40:43]
v_perm_b32 v26, v117, v113, s85
v_perm_b32 v27, v125, v121, s85
v_mfma_f32_16x16x32_bf16 a[44:47], v[16:19], a[204:207], a[44:47]
ds_read_b128 a[164:167], v9 offset:18432
v_perm_b32 v28, v133, v129, s85
v_mfma_f32_16x16x32_bf16 a[44:47], v[20:23], a[224:227], a[44:47]
ds_read_b128 a[184:187], v10 offset:18432
v_perm_b32 v29, v141, v137, s85
v_mfma_f32_16x16x32_bf16 a[48:51], v[16:19], a[208:211], a[48:51]
v_perm_b32 v30, v149, v145, s85
v_perm_b32 v31, v157, v153, s85
v_mfma_f32_16x16x32_bf16 a[48:51], v[20:23], a[228:231], a[48:51]
buffer_load_dwordx4 v[76:79], v6, s[44:47], s71 offen
v_mfma_f32_16x16x32_bf16 a[52:55], v[16:19], a[212:215], a[52:55]
s_cmp_eq_u32 s93, 384
s_cselect_b32 s94, -13, 1
s_lshl_b32 s94, s94, 18
v_mfma_f32_16x16x32_bf16 a[52:55], v[20:23], a[232:235], a[52:55]
v_add_u32 v6, s94, v6
v_mfma_f32_16x16x32_bf16 a[56:59], v[16:19], a[216:219], a[56:59]
buffer_load_dwordx4 v[80:83], v6, s[44:47], s64 offen
v_mfma_f32_16x16x32_bf16 a[56:59], v[20:23], a[236:239], a[56:59]
buffer_load_dwordx4 v[84:87], v6, s[44:47], s65 offen
v_mfma_f32_16x16x32_bf16 a[60:63], v[24:27], a[200:203], a[60:63]
v_perm_b32 v16, v102, v98, s84
v_perm_b32 v17, v110, v106, s84
v_mfma_f32_16x16x32_bf16 a[60:63], v[28:31], a[220:223], a[60:63]
v_perm_b32 v18, v118, v114, s84
v_perm_b32 v19, v126, v122, s84
v_mfma_f32_16x16x32_bf16 a[64:67], v[24:27], a[204:207], a[64:67]
v_perm_b32 v20, v134, v130, s84
v_perm_b32 v21, v142, v138, s84
v_mfma_f32_16x16x32_bf16 a[64:67], v[28:31], a[224:227], a[64:67]
v_perm_b32 v22, v150, v146, s84
v_perm_b32 v23, v158, v154, s84
v_mfma_f32_16x16x32_bf16 a[68:71], v[24:27], a[208:211], a[68:71]
buffer_load_dwordx4 v[88:91], v6, s[44:47], s66 offen
v_mfma_f32_16x16x32_bf16 a[68:71], v[28:31], a[228:231], a[68:71]
ds_read_b128 a[168:171], v9 offset:32768
v_mfma_f32_16x16x32_bf16 a[72:75], v[24:27], a[212:215], a[72:75]
ds_read_b128 a[188:191], v10 offset:32768
v_mfma_f32_16x16x32_bf16 a[72:75], v[28:31], a[232:235], a[72:75]
buffer_load_dwordx4 v[92:95], v6, s[44:47], s67 offen
v_mfma_f32_16x16x32_bf16 a[76:79], v[24:27], a[216:219], a[76:79]
buffer_load_dwordx4 v[240:243], v6, s[44:47], s68 offen
v_mfma_f32_16x16x32_bf16 a[76:79], v[28:31], a[236:239], a[76:79]
buffer_load_dwordx4 v[244:247], v6, s[44:47], s69 offen
v_mfma_f32_16x16x32_bf16 a[80:83], v[16:19], a[200:203], a[80:83]
v_perm_b32 v24, v102, v98, s85
v_perm_b32 v25, v110, v106, s85
v_mfma_f32_16x16x32_bf16 a[80:83], v[20:23], a[220:223], a[80:83]
v_perm_b32 v26, v118, v114, s85
v_perm_b32 v27, v126, v122, s85
v_mfma_f32_16x16x32_bf16 a[84:87], v[16:19], a[204:207], a[84:87]
v_perm_b32 v28, v134, v130, s85
v_perm_b32 v29, v142, v138, s85
v_mfma_f32_16x16x32_bf16 a[84:87], v[20:23], a[224:227], a[84:87]
v_perm_b32 v30, v150, v146, s85
v_perm_b32 v31, v158, v154, s85
v_mfma_f32_16x16x32_bf16 a[88:91], v[16:19], a[208:211], a[88:91]
buffer_load_dwordx4 v[248:251], v6, s[44:47], s70 offen
v_mfma_f32_16x16x32_bf16 a[88:91], v[20:23], a[228:231], a[88:91]
v_mfma_f32_16x16x32_bf16 a[92:95], v[16:19], a[212:215], a[92:95]
buffer_load_dwordx4 v[252:255], v6, s[44:47], s71 offen
v_mfma_f32_16x16x32_bf16 a[92:95], v[20:23], a[232:235], a[92:95]
v_mfma_f32_16x16x32_bf16 a[96:99], v[16:19], a[216:219], a[96:99]
v_mfma_f32_16x16x32_bf16 a[96:99], v[20:23], a[236:239], a[96:99]
v_mfma_f32_16x16x32_bf16 a[100:103], v[24:27], a[200:203], a[100:103]
v_perm_b32 v16, v103, v99, s84
v_perm_b32 v17, v111, v107, s84
v_mfma_f32_16x16x32_bf16 a[100:103], v[28:31], a[220:223], a[100:103]
v_perm_b32 v18, v119, v115, s84
v_perm_b32 v19, v127, v123, s84
v_mfma_f32_16x16x32_bf16 a[104:107], v[24:27], a[204:207], a[104:107]
ds_read_b128 a[172:175], v9 offset:47104
v_perm_b32 v20, v135, v131, s84
v_mfma_f32_16x16x32_bf16 a[104:107], v[28:31], a[224:227], a[104:107]
ds_read_b128 a[192:195], v10 offset:47104
v_perm_b32 v21, v143, v139, s84
v_mfma_f32_16x16x32_bf16 a[108:111], v[24:27], a[208:211], a[108:111]
v_perm_b32 v22, v151, v147, s84
v_perm_b32 v23, v159, v155, s84
v_mfma_f32_16x16x32_bf16 a[108:111], v[28:31], a[228:231], a[108:111]
v_mfma_f32_16x16x32_bf16 a[112:115], v[24:27], a[212:215], a[112:115]
v_mfma_f32_16x16x32_bf16 a[112:115], v[28:31], a[232:235], a[112:115]
v_mfma_f32_16x16x32_bf16 a[116:119], v[24:27], a[216:219], a[116:119]
v_mfma_f32_16x16x32_bf16 a[116:119], v[28:31], a[236:239], a[116:119]
v_mfma_f32_16x16x32_bf16 a[120:123], v[16:19], a[200:203], a[120:123]
v_perm_b32 v24, v103, v99, s85
v_perm_b32 v25, v111, v107, s85
v_mfma_f32_16x16x32_bf16 a[120:123], v[20:23], a[220:223], a[120:123]
v_perm_b32 v26, v119, v115, s85
v_perm_b32 v27, v127, v123, s85
v_mfma_f32_16x16x32_bf16 a[124:127], v[16:19], a[204:207], a[124:127]
v_perm_b32 v28, v135, v131, s85
v_perm_b32 v29, v143, v139, s85
v_mfma_f32_16x16x32_bf16 a[124:127], v[20:23], a[224:227], a[124:127]
v_perm_b32 v30, v151, v147, s85
v_perm_b32 v31, v159, v155, s85
v_mfma_f32_16x16x32_bf16 a[128:131], v[16:19], a[208:211], a[128:131]
v_add_u32 v6, 0x40000, v6
ds_read_b128 a[176:179], v11 offset:4096
v_mfma_f32_16x16x32_bf16 a[128:131], v[20:23], a[228:231], a[128:131]
buffer_load_dwordx4 v[96:99], v6, s[44:47], s64 offen
ds_read_b128 a[196:199], v12 offset:4096
v_mfma_f32_16x16x32_bf16 a[132:135], v[16:19], a[212:215], a[132:135]
buffer_load_dwordx4 v[100:103], v6, s[44:47], s65 offen
v_mfma_f32_16x16x32_bf16 a[132:135], v[20:23], a[232:235], a[132:135]
buffer_load_dwordx4 v[104:107], v6, s[44:47], s66 offen
v_mfma_f32_16x16x32_bf16 a[136:139], v[16:19], a[216:219], a[136:139]
buffer_load_dwordx4 v[108:111], v6, s[44:47], s67 offen
v_mfma_f32_16x16x32_bf16 a[136:139], v[20:23], a[236:239], a[136:139]
buffer_load_dwordx4 v[112:115], v6, s[44:47], s68 offen
v_mfma_f32_16x16x32_bf16 a[140:143], v[24:27], a[200:203], a[140:143]
s_waitcnt vmcnt(37)
v_perm_b32 v16, v164, v160, s84
v_perm_b32 v17, v172, v168, s84
v_mfma_f32_16x16x32_bf16 a[140:143], v[28:31], a[220:223], a[140:143]
v_perm_b32 v18, v180, v176, s84
v_perm_b32 v19, v188, v184, s84
v_mfma_f32_16x16x32_bf16 a[144:147], v[24:27], a[204:207], a[144:147]
s_waitcnt vmcnt(29)
v_perm_b32 v20, v196, v192, s84
v_perm_b32 v21, v204, v200, s84
v_mfma_f32_16x16x32_bf16 a[144:147], v[28:31], a[224:227], a[144:147]
v_perm_b32 v22, v212, v208, s84
v_perm_b32 v23, v220, v216, s84
v_mfma_f32_16x16x32_bf16 a[148:151], v[24:27], a[208:211], a[148:151]
buffer_load_dwordx4 v[116:119], v6, s[44:47], s69 offen
v_mfma_f32_16x16x32_bf16 a[148:151], v[28:31], a[228:231], a[148:151]
buffer_load_dwordx4 v[120:123], v6, s[44:47], s70 offen
v_mfma_f32_16x16x32_bf16 a[152:155], v[24:27], a[212:215], a[152:155]
buffer_load_dwordx4 v[124:127], v6, s[44:47], s71 offen
v_mfma_f32_16x16x32_bf16 a[152:155], v[28:31], a[232:235], a[152:155]
s_cmp_eq_u32 s93, 256
s_cselect_b32 s94, -13, 1
s_lshl_b32 s94, s94, 18
v_mfma_f32_16x16x32_bf16 a[156:159], v[24:27], a[216:219], a[156:159]
v_add_u32 v6, s94, v6
v_mfma_f32_16x16x32_bf16 a[156:159], v[28:31], a[236:239], a[156:159]
buffer_load_dwordx4 v[128:131], v6, s[44:47], s64 offen
s_waitcnt lgkmcnt(1)
v_mfma_f32_16x16x32_bf16 a[0:3], v[16:19], a[160:163], a[0:3]
v_perm_b32 v24, v164, v160, s85
v_perm_b32 v25, v172, v168, s85
v_mfma_f32_16x16x32_bf16 a[0:3], v[20:23], a[180:183], a[0:3]
v_perm_b32 v26, v180, v176, s85
v_perm_b32 v27, v188, v184, s85
v_mfma_f32_16x16x32_bf16 a[4:7], v[16:19], a[164:167], a[4:7]
v_perm_b32 v28, v196, v192, s85
v_perm_b32 v29, v204, v200, s85
v_mfma_f32_16x16x32_bf16 a[4:7], v[20:23], a[184:187], a[4:7]
v_perm_b32 v30, v212, v208, s85
v_perm_b32 v31, v220, v216, s85
v_mfma_f32_16x16x32_bf16 a[8:11], v[16:19], a[168:171], a[8:11]
buffer_load_dwordx4 v[132:135], v6, s[44:47], s65 offen
v_mfma_f32_16x16x32_bf16 a[8:11], v[20:23], a[188:191], a[8:11]
buffer_load_dwordx4 v[136:139], v6, s[44:47], s66 offen
v_mfma_f32_16x16x32_bf16 a[12:15], v[16:19], a[172:175], a[12:15]
ds_read_b128 a[200:203], v9 offset:6144
v_mfma_f32_16x16x32_bf16 a[12:15], v[20:23], a[192:195], a[12:15]
ds_read_b128 a[220:223], v10 offset:6144
v_mfma_f32_16x16x32_bf16 a[16:19], v[16:19], a[176:179], a[16:19]
buffer_load_dwordx4 v[140:143], v6, s[44:47], s67 offen
s_waitcnt lgkmcnt(2)
v_mfma_f32_16x16x32_bf16 a[16:19], v[20:23], a[196:199], a[16:19]
v_mfma_f32_16x16x32_bf16 a[20:23], v[24:27], a[160:163], a[20:23]
v_perm_b32 v16, v165, v161, s84
v_perm_b32 v17, v173, v169, s84
v_mfma_f32_16x16x32_bf16 a[20:23], v[28:31], a[180:183], a[20:23]
v_perm_b32 v18, v181, v177, s84
v_perm_b32 v19, v189, v185, s84
v_mfma_f32_16x16x32_bf16 a[24:27], v[24:27], a[164:167], a[24:27]
v_perm_b32 v20, v197, v193, s84
v_perm_b32 v21, v205, v201, s84
v_mfma_f32_16x16x32_bf16 a[24:27], v[28:31], a[184:187], a[24:27]
v_perm_b32 v22, v213, v209, s84
v_perm_b32 v23, v221, v217, s84
v_mfma_f32_16x16x32_bf16 a[28:31], v[24:27], a[168:171], a[28:31]
buffer_load_dwordx4 v[144:147], v6, s[44:47], s68 offen
v_mfma_f32_16x16x32_bf16 a[28:31], v[28:31], a[188:191], a[28:31]
v_mfma_f32_16x16x32_bf16 a[32:35], v[24:27], a[172:175], a[32:35]
v_mfma_f32_16x16x32_bf16 a[32:35], v[28:31], a[192:195], a[32:35]
buffer_load_dwordx4 v[148:151], v6, s[44:47], s69 offen
v_mfma_f32_16x16x32_bf16 a[36:39], v[24:27], a[176:179], a[36:39]
v_mfma_f32_16x16x32_bf16 a[36:39], v[28:31], a[196:199], a[36:39]
)"
R"(v_mfma_f32_16x16x32_bf16 a[40:43], v[16:19], a[160:163], a[40:43]
v_perm_b32 v24, v165, v161, s85
v_perm_b32 v25, v173, v169, s85
v_mfma_f32_16x16x32_bf16 a[40:43], v[20:23], a[180:183], a[40:43]
v_perm_b32 v26, v181, v177, s85
v_perm_b32 v27, v189, v185, s85
v_mfma_f32_16x16x32_bf16 a[44:47], v[16:19], a[164:167], a[44:47]
ds_read_b128 a[204:207], v9 offset:20480
v_perm_b32 v28, v197, v193, s85
v_mfma_f32_16x16x32_bf16 a[44:47], v[20:23], a[184:187], a[44:47]
ds_read_b128 a[224:227], v10 offset:20480
v_perm_b32 v29, v205, v201, s85
v_mfma_f32_16x16x32_bf16 a[48:51], v[16:19], a[168:171], a[48:51]
v_perm_b32 v30, v213, v209, s85
v_perm_b32 v31, v221, v217, s85
v_mfma_f32_16x16x32_bf16 a[48:51], v[20:23], a[188:191], a[48:51]
buffer_load_dwordx4 v[152:155], v6, s[44:47], s70 offen
v_mfma_f32_16x16x32_bf16 a[52:55], v[16:19], a[172:175], a[52:55]
buffer_load_dwordx4 v[156:159], v6, s[44:47], s71 offen
v_mfma_f32_16x16x32_bf16 a[52:55], v[20:23], a[192:195], a[52:55]
v_mfma_f32_16x16x32_bf16 a[56:59], v[16:19], a[176:179], a[56:59]
v_mfma_f32_16x16x32_bf16 a[56:59], v[20:23], a[196:199], a[56:59]
v_mfma_f32_16x16x32_bf16 a[60:63], v[24:27], a[160:163], a[60:63]
v_perm_b32 v16, v166, v162, s84
v_perm_b32 v17, v174, v170, s84
v_mfma_f32_16x16x32_bf16 a[60:63], v[28:31], a[180:183], a[60:63]
v_perm_b32 v18, v182, v178, s84
v_perm_b32 v19, v190, v186, s84
v_mfma_f32_16x16x32_bf16 a[64:67], v[24:27], a[164:167], a[64:67]
v_perm_b32 v20, v198, v194, s84
v_perm_b32 v21, v206, v202, s84
v_mfma_f32_16x16x32_bf16 a[64:67], v[28:31], a[184:187], a[64:67]
v_perm_b32 v22, v214, v210, s84
v_perm_b32 v23, v222, v218, s84
v_mfma_f32_16x16x32_bf16 a[68:71], v[24:27], a[168:171], a[68:71]
v_mfma_f32_16x16x32_bf16 a[68:71], v[28:31], a[188:191], a[68:71]
ds_read_b128 a[208:211], v9 offset:34816
v_mfma_f32_16x16x32_bf16 a[72:75], v[24:27], a[172:175], a[72:75]
ds_read_b128 a[228:231], v10 offset:34816
v_mfma_f32_16x16x32_bf16 a[72:75], v[28:31], a[192:195], a[72:75]
v_mfma_f32_16x16x32_bf16 a[76:79], v[24:27], a[176:179], a[76:79]
v_mfma_f32_16x16x32_bf16 a[76:79], v[28:31], a[196:199], a[76:79]
v_mfma_f32_16x16x32_bf16 a[80:83], v[16:19], a[160:163], a[80:83]
v_perm_b32 v24, v166, v162, s85
v_perm_b32 v25, v174, v170, s85
v_mfma_f32_16x16x32_bf16 a[80:83], v[20:23], a[180:183], a[80:83]
v_perm_b32 v26, v182, v178, s85
v_perm_b32 v27, v190, v186, s85
v_mfma_f32_16x16x32_bf16 a[84:87], v[16:19], a[164:167], a[84:87]
v_perm_b32 v28, v198, v194, s85
v_perm_b32 v29, v206, v202, s85
v_mfma_f32_16x16x32_bf16 a[84:87], v[20:23], a[184:187], a[84:87]
v_perm_b32 v30, v214, v210, s85
v_perm_b32 v31, v222, v218, s85
v_mfma_f32_16x16x32_bf16 a[88:91], v[16:19], a[168:171], a[88:91]
v_mfma_f32_16x16x32_bf16 a[88:91], v[20:23], a[188:191], a[88:91]
v_mfma_f32_16x16x32_bf16 a[92:95], v[16:19], a[172:175], a[92:95]
v_mfma_f32_16x16x32_bf16 a[92:95], v[20:23], a[192:195], a[92:95]
v_mfma_f32_16x16x32_bf16 a[96:99], v[16:19], a[176:179], a[96:99]
v_mfma_f32_16x16x32_bf16 a[96:99], v[20:23], a[196:199], a[96:99]
v_mfma_f32_16x16x32_bf16 a[100:103], v[24:27], a[160:163], a[100:103]
v_perm_b32 v16, v167, v163, s84
v_perm_b32 v17, v175, v171, s84
v_mfma_f32_16x16x32_bf16 a[100:103], v[28:31], a[180:183], a[100:103]
v_perm_b32 v18, v183, v179, s84
v_perm_b32 v19, v191, v187, s84
v_mfma_f32_16x16x32_bf16 a[104:107], v[24:27], a[164:167], a[104:107]
ds_read_b128 a[212:215], v9 offset:49152
v_perm_b32 v20, v199, v195, s84
v_mfma_f32_16x16x32_bf16 a[104:107], v[28:31], a[184:187], a[104:107]
ds_read_b128 a[232:235], v10 offset:49152
v_perm_b32 v21, v207, v203, s84
v_mfma_f32_16x16x32_bf16 a[108:111], v[24:27], a[168:171], a[108:111]
v_perm_b32 v22, v215, v211, s84
v_perm_b32 v23, v223, v219, s84
v_mfma_f32_16x16x32_bf16 a[108:111], v[28:31], a[188:191], a[108:111]
v_mfma_f32_16x16x32_bf16 a[112:115], v[24:27], a[172:175], a[112:115]
v_mfma_f32_16x16x32_bf16 a[112:115], v[28:31], a[192:195], a[112:115]
v_mfma_f32_16x16x32_bf16 a[116:119], v[24:27], a[176:179], a[116:119]
v_mfma_f32_16x16x32_bf16 a[116:119], v[28:31], a[196:199], a[116:119]
v_mfma_f32_16x16x32_bf16 a[120:123], v[16:19], a[160:163], a[120:123]
v_perm_b32 v24, v167, v163, s85
v_perm_b32 v25, v175, v171, s85
v_mfma_f32_16x16x32_bf16 a[120:123], v[20:23], a[180:183], a[120:123]
v_perm_b32 v26, v183, v179, s85
v_perm_b32 v27, v191, v187, s85
v_mfma_f32_16x16x32_bf16 a[124:127], v[16:19], a[164:167], a[124:127]
v_perm_b32 v28, v199, v195, s85
v_perm_b32 v29, v207, v203, s85
v_mfma_f32_16x16x32_bf16 a[124:127], v[20:23], a[184:187], a[124:127]
v_perm_b32 v30, v215, v211, s85
v_perm_b32 v31, v223, v219, s85
v_mfma_f32_16x16x32_bf16 a[128:131], v[16:19], a[168:171], a[128:131]
v_add_u32 v6, 0x40000, v6
ds_read_b128 a[216:219], v11 offset:6144
v_mfma_f32_16x16x32_bf16 a[128:131], v[20:23], a[188:191], a[128:131]
buffer_load_dwordx4 v[160:163], v6, s[44:47], s64 offen
ds_read_b128 a[236:239], v12 offset:6144
v_mfma_f32_16x16x32_bf16 a[132:135], v[16:19], a[172:175], a[132:135]
buffer_load_dwordx4 v[164:167], v6, s[44:47], s65 offen
v_mfma_f32_16x16x32_bf16 a[132:135], v[20:23], a[192:195], a[132:135]
buffer_load_dwordx4 v[168:171], v6, s[44:47], s66 offen
v_mfma_f32_16x16x32_bf16 a[136:139], v[16:19], a[176:179], a[136:139]
buffer_load_dwordx4 v[172:175], v6, s[44:47], s67 offen
v_mfma_f32_16x16x32_bf16 a[136:139], v[20:23], a[196:199], a[136:139]
buffer_load_dwordx4 v[176:179], v6, s[44:47], s68 offen
v_mfma_f32_16x16x32_bf16 a[140:143], v[24:27], a[160:163], a[140:143]
s_waitcnt vmcnt(37)
v_perm_b32 v16, v228, v224, s84
v_perm_b32 v17, v236, v232, s84
v_mfma_f32_16x16x32_bf16 a[140:143], v[28:31], a[180:183], a[140:143]
v_perm_b32 v18, v36, v32, s84
v_perm_b32 v19, v44, v40, s84
v_mfma_f32_16x16x32_bf16 a[144:147], v[24:27], a[164:167], a[144:147]
s_waitcnt vmcnt(29)
v_perm_b32 v20, v52, v48, s84
v_perm_b32 v21, v60, v56, s84
v_mfma_f32_16x16x32_bf16 a[144:147], v[28:31], a[184:187], a[144:147]
v_perm_b32 v22, v68, v64, s84
v_perm_b32 v23, v76, v72, s84
v_mfma_f32_16x16x32_bf16 a[148:151], v[24:27], a[168:171], a[148:151]
buffer_load_dwordx4 v[180:183], v6, s[44:47], s69 offen
v_mfma_f32_16x16x32_bf16 a[148:151], v[28:31], a[188:191], a[148:151]
buffer_load_dwordx4 v[184:187], v6, s[44:47], s70 offen
v_mfma_f32_16x16x32_bf16 a[152:155], v[24:27], a[172:175], a[152:155]
buffer_load_dwordx4 v[188:191], v6, s[44:47], s71 offen
v_mfma_f32_16x16x32_bf16 a[152:155], v[28:31], a[192:195], a[152:155]
s_cmp_eq_u32 s93, 128
s_cselect_b32 s94, -13, 1
s_lshl_b32 s94, s94, 18
v_mfma_f32_16x16x32_bf16 a[156:159], v[24:27], a[176:179], a[156:159]
v_add_u32 v6, s94, v6
v_mfma_f32_16x16x32_bf16 a[156:159], v[28:31], a[196:199], a[156:159]
buffer_load_dwordx4 v[192:195], v6, s[44:47], s64 offen
s_waitcnt lgkmcnt(1)
v_mfma_f32_16x16x32_bf16 a[0:3], v[16:19], a[200:203], a[0:3]
v_perm_b32 v24, v228, v224, s85
v_perm_b32 v25, v236, v232, s85
v_mfma_f32_16x16x32_bf16 a[0:3], v[20:23], a[220:223], a[0:3]
v_perm_b32 v26, v36, v32, s85
v_perm_b32 v27, v44, v40, s85
v_mfma_f32_16x16x32_bf16 a[4:7], v[16:19], a[204:207], a[4:7]
v_perm_b32 v28, v52, v48, s85
v_perm_b32 v29, v60, v56, s85
v_mfma_f32_16x16x32_bf16 a[4:7], v[20:23], a[224:227], a[4:7]
v_perm_b32 v30, v68, v64, s85
v_perm_b32 v31, v76, v72, s85
v_mfma_f32_16x16x32_bf16 a[8:11], v[16:19], a[208:211], a[8:11]
buffer_load_dwordx4 v[196:199], v6, s[44:47], s65 offen
v_mfma_f32_16x16x32_bf16 a[8:11], v[20:23], a[228:231], a[8:11]
buffer_load_dwordx4 v[200:203], v6, s[44:47], s66 offen
v_mfma_f32_16x16x32_bf16 a[12:15], v[16:19], a[212:215], a[12:15]
ds_read_b128 a[160:163], v9 offset:8192
v_mfma_f32_16x16x32_bf16 a[12:15], v[20:23], a[232:235], a[12:15]
ds_read_b128 a[180:183], v10 offset:8192
v_mfma_f32_16x16x32_bf16 a[16:19], v[16:19], a[216:219], a[16:19]
buffer_load_dwordx4 v[204:207], v6, s[44:47], s67 offen
s_waitcnt lgkmcnt(2)
v_mfma_f32_16x16x32_bf16 a[16:19], v[20:23], a[236:239], a[16:19]
v_mfma_f32_16x16x32_bf16 a[20:23], v[24:27], a[200:203], a[20:23]
v_perm_b32 v16, v229, v225, s84
v_perm_b32 v17, v237, v233, s84
v_mfma_f32_16x16x32_bf16 a[20:23], v[28:31], a[220:223], a[20:23]
v_perm_b32 v18, v37, v33, s84
v_perm_b32 v19, v45, v41, s84
v_mfma_f32_16x16x32_bf16 a[24:27], v[24:27], a[204:207], a[24:27]
v_perm_b32 v20, v53, v49, s84
v_perm_b32 v21, v61, v57, s84
v_mfma_f32_16x16x32_bf16 a[24:27], v[28:31], a[224:227], a[24:27]
v_perm_b32 v22, v69, v65, s84
v_perm_b32 v23, v77, v73, s84
v_mfma_f32_16x16x32_bf16 a[28:31], v[24:27], a[208:211], a[28:31]
buffer_load_dwordx4 v[208:211], v6, s[44:47], s68 offen
v_mfma_f32_16x16x32_bf16 a[28:31], v[28:31], a[228:231], a[28:31]
v_mfma_f32_16x16x32_bf16 a[32:35], v[24:27], a[212:215], a[32:35]
v_mfma_f32_16x16x32_bf16 a[32:35], v[28:31], a[232:235], a[32:35]
buffer_load_dwordx4 v[212:215], v6, s[44:47], s69 offen
v_mfma_f32_16x16x32_bf16 a[36:39], v[24:27], a[216:219], a[36:39]
v_mfma_f32_16x16x32_bf16 a[36:39], v[28:31], a[236:239], a[36:39]
v_mfma_f32_16x16x32_bf16 a[40:43], v[16:19], a[200:203], a[40:43]
v_perm_b32 v24, v229, v225, s85
v_perm_b32 v25, v237, v233, s85
v_mfma_f32_16x16x32_bf16 a[40:43], v[20:23], a[220:223], a[40:43]
v_perm_b32 v26, v37, v33, s85
v_perm_b32 v27, v45, v41, s85
v_mfma_f32_16x16x32_bf16 a[44:47], v[16:19], a[204:207], a[44:47]
ds_read_b128 a[164:167], v9 offset:22528
v_perm_b32 v28, v53, v49, s85
v_mfma_f32_16x16x32_bf16 a[44:47], v[20:23], a[224:227], a[44:47]
ds_read_b128 a[184:187], v10 offset:22528
v_perm_b32 v29, v61, v57, s85
v_mfma_f32_16x16x32_bf16 a[48:51], v[16:19], a[208:211], a[48:51]
v_perm_b32 v30, v69, v65, s85
v_perm_b32 v31, v77, v73, s85
v_mfma_f32_16x16x32_bf16 a[48:51], v[20:23], a[228:231], a[48:51]
buffer_load_dwordx4 v[216:219], v6, s[44:47], s70 offen
v_mfma_f32_16x16x32_bf16 a[52:55], v[16:19], a[212:215], a[52:55]
buffer_load_dwordx4 v[220:223], v6, s[44:47], s71 offen
v_mfma_f32_16x16x32_bf16 a[52:55], v[20:23], a[232:235], a[52:55]
v_mfma_f32_16x16x32_bf16 a[56:59], v[16:19], a[216:219], a[56:59]
)"
R"(v_mfma_f32_16x16x32_bf16 a[56:59], v[20:23], a[236:239], a[56:59]
v_mfma_f32_16x16x32_bf16 a[60:63], v[24:27], a[200:203], a[60:63]
v_perm_b32 v16, v230, v226, s84
v_perm_b32 v17, v238, v234, s84
v_mfma_f32_16x16x32_bf16 a[60:63], v[28:31], a[220:223], a[60:63]
v_perm_b32 v18, v38, v34, s84
v_perm_b32 v19, v46, v42, s84
v_mfma_f32_16x16x32_bf16 a[64:67], v[24:27], a[204:207], a[64:67]
v_perm_b32 v20, v54, v50, s84
v_perm_b32 v21, v62, v58, s84
v_mfma_f32_16x16x32_bf16 a[64:67], v[28:31], a[224:227], a[64:67]
v_perm_b32 v22, v70, v66, s84
v_perm_b32 v23, v78, v74, s84
v_mfma_f32_16x16x32_bf16 a[68:71], v[24:27], a[208:211], a[68:71]
v_mfma_f32_16x16x32_bf16 a[68:71], v[28:31], a[228:231], a[68:71]
ds_read_b128 a[168:171], v9 offset:36864
v_mfma_f32_16x16x32_bf16 a[72:75], v[24:27], a[212:215], a[72:75]
ds_read_b128 a[188:191], v10 offset:36864
v_mfma_f32_16x16x32_bf16 a[72:75], v[28:31], a[232:235], a[72:75]
v_mfma_f32_16x16x32_bf16 a[76:79], v[24:27], a[216:219], a[76:79]
v_mfma_f32_16x16x32_bf16 a[76:79], v[28:31], a[236:239], a[76:79]
v_mfma_f32_16x16x32_bf16 a[80:83], v[16:19], a[200:203], a[80:83]
v_perm_b32 v24, v230, v226, s85
v_perm_b32 v25, v238, v234, s85
v_mfma_f32_16x16x32_bf16 a[80:83], v[20:23], a[220:223], a[80:83]
v_perm_b32 v26, v38, v34, s85
v_perm_b32 v27, v46, v42, s85
v_mfma_f32_16x16x32_bf16 a[84:87], v[16:19], a[204:207], a[84:87]
v_perm_b32 v28, v54, v50, s85
v_perm_b32 v29, v62, v58, s85
v_mfma_f32_16x16x32_bf16 a[84:87], v[20:23], a[224:227], a[84:87]
v_perm_b32 v30, v70, v66, s85
v_perm_b32 v31, v78, v74, s85
v_mfma_f32_16x16x32_bf16 a[88:91], v[16:19], a[208:211], a[88:91]
v_mfma_f32_16x16x32_bf16 a[88:91], v[20:23], a[228:231], a[88:91]
v_mfma_f32_16x16x32_bf16 a[92:95], v[16:19], a[212:215], a[92:95]
v_mfma_f32_16x16x32_bf16 a[92:95], v[20:23], a[232:235], a[92:95]
v_mfma_f32_16x16x32_bf16 a[96:99], v[16:19], a[216:219], a[96:99]
v_mfma_f32_16x16x32_bf16 a[96:99], v[20:23], a[236:239], a[96:99]
v_mfma_f32_16x16x32_bf16 a[100:103], v[24:27], a[200:203], a[100:103]
v_perm_b32 v16, v231, v227, s84
v_perm_b32 v17, v239, v235, s84
v_mfma_f32_16x16x32_bf16 a[100:103], v[28:31], a[220:223], a[100:103]
v_perm_b32 v18, v39, v35, s84
v_perm_b32 v19, v47, v43, s84
v_mfma_f32_16x16x32_bf16 a[104:107], v[24:27], a[204:207], a[104:107]
ds_read_b128 a[172:175], v9 offset:51200
v_perm_b32 v20, v55, v51, s84
v_mfma_f32_16x16x32_bf16 a[104:107], v[28:31], a[224:227], a[104:107]
ds_read_b128 a[192:195], v10 offset:51200
v_perm_b32 v21, v63, v59, s84
v_mfma_f32_16x16x32_bf16 a[108:111], v[24:27], a[208:211], a[108:111]
v_perm_b32 v22, v71, v67, s84
v_perm_b32 v23, v79, v75, s84
v_mfma_f32_16x16x32_bf16 a[108:111], v[28:31], a[228:231], a[108:111]
v_mfma_f32_16x16x32_bf16 a[112:115], v[24:27], a[212:215], a[112:115]
v_mfma_f32_16x16x32_bf16 a[112:115], v[28:31], a[232:235], a[112:115]
v_mfma_f32_16x16x32_bf16 a[116:119], v[24:27], a[216:219], a[116:119]
v_mfma_f32_16x16x32_bf16 a[116:119], v[28:31], a[236:239], a[116:119]
v_mfma_f32_16x16x32_bf16 a[120:123], v[16:19], a[200:203], a[120:123]
v_perm_b32 v24, v231, v227, s85
v_perm_b32 v25, v239, v235, s85
v_mfma_f32_16x16x32_bf16 a[120:123], v[20:23], a[220:223], a[120:123]
v_perm_b32 v26, v39, v35, s85
v_perm_b32 v27, v47, v43, s85
v_mfma_f32_16x16x32_bf16 a[124:127], v[16:19], a[204:207], a[124:127]
v_perm_b32 v28, v55, v51, s85
v_perm_b32 v29, v63, v59, s85
v_mfma_f32_16x16x32_bf16 a[124:127], v[20:23], a[224:227], a[124:127]
v_perm_b32 v30, v71, v67, s85
v_perm_b32 v31, v79, v75, s85
v_mfma_f32_16x16x32_bf16 a[128:131], v[16:19], a[208:211], a[128:131]
v_add_u32 v6, 0x40000, v6
ds_read_b128 a[176:179], v11 offset:8192
v_mfma_f32_16x16x32_bf16 a[128:131], v[20:23], a[228:231], a[128:131]
buffer_load_dwordx4 v[224:227], v6, s[44:47], s64 offen
ds_read_b128 a[196:199], v12 offset:8192
v_mfma_f32_16x16x32_bf16 a[132:135], v[16:19], a[212:215], a[132:135]
buffer_load_dwordx4 v[228:231], v6, s[44:47], s65 offen
v_mfma_f32_16x16x32_bf16 a[132:135], v[20:23], a[232:235], a[132:135]
buffer_load_dwordx4 v[232:235], v6, s[44:47], s66 offen
v_mfma_f32_16x16x32_bf16 a[136:139], v[16:19], a[216:219], a[136:139]
buffer_load_dwordx4 v[236:239], v6, s[44:47], s67 offen
v_mfma_f32_16x16x32_bf16 a[136:139], v[20:23], a[236:239], a[136:139]
buffer_load_dwordx4 v[32:35], v6, s[44:47], s68 offen
v_mfma_f32_16x16x32_bf16 a[140:143], v[24:27], a[200:203], a[140:143]
s_waitcnt vmcnt(37)
v_perm_b32 v16, v84, v80, s84
v_perm_b32 v17, v92, v88, s84
v_mfma_f32_16x16x32_bf16 a[140:143], v[28:31], a[220:223], a[140:143]
v_perm_b32 v18, v244, v240, s84
v_perm_b32 v19, v252, v248, s84
v_mfma_f32_16x16x32_bf16 a[144:147], v[24:27], a[204:207], a[144:147]
s_waitcnt vmcnt(29)
v_perm_b32 v20, v100, v96, s84
v_perm_b32 v21, v108, v104, s84
v_mfma_f32_16x16x32_bf16 a[144:147], v[28:31], a[224:227], a[144:147]
v_perm_b32 v22, v116, v112, s84
v_perm_b32 v23, v124, v120, s84
v_mfma_f32_16x16x32_bf16 a[148:151], v[24:27], a[208:211], a[148:151]
buffer_load_dwordx4 v[36:39], v6, s[44:47], s69 offen
v_mfma_f32_16x16x32_bf16 a[148:151], v[28:31], a[228:231], a[148:151]
buffer_load_dwordx4 v[40:43], v6, s[44:47], s70 offen
v_mfma_f32_16x16x32_bf16 a[152:155], v[24:27], a[212:215], a[152:155]
buffer_load_dwordx4 v[44:47], v6, s[44:47], s71 offen
v_mfma_f32_16x16x32_bf16 a[152:155], v[28:31], a[232:235], a[152:155]
s_waitcnt vmcnt(0)
v_mfma_f32_16x16x32_bf16 a[156:159], v[24:27], a[216:219], a[156:159]
s_mov_b32 s72, s88
v_mfma_f32_16x16x32_bf16 a[156:159], v[28:31], a[236:239], a[156:159]
s_add_u32 s73, s72, s66
s_waitcnt lgkmcnt(1)
v_mfma_f32_16x16x32_bf16 a[0:3], v[16:19], a[160:163], a[0:3]
s_add_u32 s74, s72, s68
v_perm_b32 v24, v84, v80, s85
v_perm_b32 v25, v92, v88, s85
v_mfma_f32_16x16x32_bf16 a[0:3], v[20:23], a[180:183], a[0:3]
s_add_u32 s75, s72, s70
v_perm_b32 v26, v244, v240, s85
v_perm_b32 v27, v252, v248, s85
v_mfma_f32_16x16x32_bf16 a[4:7], v[16:19], a[164:167], a[4:7]
v_perm_b32 v28, v100, v96, s85
v_perm_b32 v29, v108, v104, s85
v_mfma_f32_16x16x32_bf16 a[4:7], v[20:23], a[184:187], a[4:7]
v_perm_b32 v30, v116, v112, s85
v_perm_b32 v31, v124, v120, s85
v_mfma_f32_16x16x32_bf16 a[8:11], v[16:19], a[168:171], a[8:11]
buffer_load_dwordx4 v[48:51], v4, s[36:39], s72 offen nt
v_mfma_f32_16x16x32_bf16 a[8:11], v[20:23], a[188:191], a[8:11]
buffer_load_dwordx4 v[52:55], v4, s[36:39], s72 offen offset:512 nt
v_mfma_f32_16x16x32_bf16 a[12:15], v[16:19], a[172:175], a[12:15]
buffer_load_dwordx4 v[56:59], v4, s[36:39], s72 offen offset:2048 nt
ds_read_b128 a[200:203], v9 offset:10240
v_mfma_f32_16x16x32_bf16 a[12:15], v[20:23], a[192:195], a[12:15]
buffer_load_dwordx4 v[60:63], v4, s[36:39], s72 offen offset:2560 nt
ds_read_b128 a[220:223], v10 offset:10240
v_mfma_f32_16x16x32_bf16 a[16:19], v[16:19], a[176:179], a[16:19]
buffer_load_dwordx4 v[64:67], v4, s[36:39], s72 offen offset:128 nt
s_waitcnt lgkmcnt(2)
v_mfma_f32_16x16x32_bf16 a[16:19], v[20:23], a[196:199], a[16:19]
buffer_load_dwordx4 v[68:71], v4, s[36:39], s72 offen offset:640 nt
v_mfma_f32_16x16x32_bf16 a[20:23], v[24:27], a[160:163], a[20:23]
v_perm_b32 v16, v85, v81, s84
v_perm_b32 v17, v93, v89, s84
v_mfma_f32_16x16x32_bf16 a[20:23], v[28:31], a[180:183], a[20:23]
v_perm_b32 v18, v245, v241, s84
v_perm_b32 v19, v253, v249, s84
v_mfma_f32_16x16x32_bf16 a[24:27], v[24:27], a[164:167], a[24:27]
v_perm_b32 v20, v101, v97, s84
v_perm_b32 v21, v109, v105, s84
v_mfma_f32_16x16x32_bf16 a[24:27], v[28:31], a[184:187], a[24:27]
v_perm_b32 v22, v117, v113, s84
v_perm_b32 v23, v125, v121, s84
v_mfma_f32_16x16x32_bf16 a[28:31], v[24:27], a[168:171], a[28:31]
buffer_load_dwordx4 v[72:75], v4, s[36:39], s72 offen offset:2176 nt
v_mfma_f32_16x16x32_bf16 a[28:31], v[28:31], a[188:191], a[28:31]
buffer_load_dwordx4 v[76:79], v4, s[36:39], s72 offen offset:2688 nt
v_mfma_f32_16x16x32_bf16 a[32:35], v[24:27], a[172:175], a[32:35]
v_mfma_f32_16x16x32_bf16 a[32:35], v[28:31], a[192:195], a[32:35]
v_mfma_f32_16x16x32_bf16 a[36:39], v[24:27], a[176:179], a[36:39]
v_mfma_f32_16x16x32_bf16 a[36:39], v[28:31], a[196:199], a[36:39]
v_mfma_f32_16x16x32_bf16 a[40:43], v[16:19], a[160:163], a[40:43]
v_perm_b32 v24, v85, v81, s85
v_perm_b32 v25, v93, v89, s85
v_mfma_f32_16x16x32_bf16 a[40:43], v[20:23], a[180:183], a[40:43]
v_perm_b32 v26, v245, v241, s85
v_perm_b32 v27, v253, v249, s85
v_mfma_f32_16x16x32_bf16 a[44:47], v[16:19], a[164:167], a[44:47]
ds_read_b128 a[204:207], v9 offset:24576
v_perm_b32 v28, v101, v97, s85
v_mfma_f32_16x16x32_bf16 a[44:47], v[20:23], a[184:187], a[44:47]
ds_read_b128 a[224:227], v10 offset:24576
v_perm_b32 v29, v109, v105, s85
v_mfma_f32_16x16x32_bf16 a[48:51], v[16:19], a[168:171], a[48:51]
v_perm_b32 v30, v117, v113, s85
v_perm_b32 v31, v125, v121, s85
v_mfma_f32_16x16x32_bf16 a[48:51], v[20:23], a[188:191], a[48:51]
v_mfma_f32_16x16x32_bf16 a[52:55], v[16:19], a[172:175], a[52:55]
v_mfma_f32_16x16x32_bf16 a[52:55], v[20:23], a[192:195], a[52:55]
v_mfma_f32_16x16x32_bf16 a[56:59], v[16:19], a[176:179], a[56:59]
v_mfma_f32_16x16x32_bf16 a[56:59], v[20:23], a[196:199], a[56:59]
v_mfma_f32_16x16x32_bf16 a[60:63], v[24:27], a[160:163], a[60:63]
v_perm_b32 v16, v86, v82, s84
v_perm_b32 v17, v94, v90, s84
v_mfma_f32_16x16x32_bf16 a[60:63], v[28:31], a[180:183], a[60:63]
v_perm_b32 v18, v246, v242, s84
v_perm_b32 v19, v254, v250, s84
v_mfma_f32_16x16x32_bf16 a[64:67], v[24:27], a[164:167], a[64:67]
v_perm_b32 v20, v102, v98, s84
v_perm_b32 v21, v110, v106, s84
v_mfma_f32_16x16x32_bf16 a[64:67], v[28:31], a[184:187], a[64:67]
v_perm_b32 v22, v118, v114, s84
v_perm_b32 v23, v126, v122, s84
v_mfma_f32_16x16x32_bf16 a[68:71], v[24:27], a[168:171], a[68:71]
v_mfma_f32_16x16x32_bf16 a[68:71], v[28:31], a[188:191], a[68:71]
ds_read_b128 a[208:211], v9 offset:38912
v_mfma_f32_16x16x32_bf16 a[72:75], v[24:27], a[172:175], a[72:75]
ds_read_b128 a[228:231], v10 offset:38912
v_mfma_f32_16x16x32_bf16 a[72:75], v[28:31], a[192:195], a[72:75]
v_mfma_f32_16x16x32_bf16 a[76:79], v[24:27], a[176:179], a[76:79]
)"
R"(v_mfma_f32_16x16x32_bf16 a[76:79], v[28:31], a[196:199], a[76:79]
v_mfma_f32_16x16x32_bf16 a[80:83], v[16:19], a[160:163], a[80:83]
v_perm_b32 v24, v86, v82, s85
v_perm_b32 v25, v94, v90, s85
v_mfma_f32_16x16x32_bf16 a[80:83], v[20:23], a[180:183], a[80:83]
v_perm_b32 v26, v246, v242, s85
v_perm_b32 v27, v254, v250, s85
v_mfma_f32_16x16x32_bf16 a[84:87], v[16:19], a[164:167], a[84:87]
v_perm_b32 v28, v102, v98, s85
v_perm_b32 v29, v110, v106, s85
v_mfma_f32_16x16x32_bf16 a[84:87], v[20:23], a[184:187], a[84:87]
v_perm_b32 v30, v118, v114, s85
v_perm_b32 v31, v126, v122, s85
v_mfma_f32_16x16x32_bf16 a[88:91], v[16:19], a[168:171], a[88:91]
v_mfma_f32_16x16x32_bf16 a[88:91], v[20:23], a[188:191], a[88:91]
v_mfma_f32_16x16x32_bf16 a[92:95], v[16:19], a[172:175], a[92:95]
v_mfma_f32_16x16x32_bf16 a[92:95], v[20:23], a[192:195], a[92:95]
v_mfma_f32_16x16x32_bf16 a[96:99], v[16:19], a[176:179], a[96:99]
v_mfma_f32_16x16x32_bf16 a[96:99], v[20:23], a[196:199], a[96:99]
v_mfma_f32_16x16x32_bf16 a[100:103], v[24:27], a[160:163], a[100:103]
v_perm_b32 v16, v87, v83, s84
v_perm_b32 v17, v95, v91, s84
v_mfma_f32_16x16x32_bf16 a[100:103], v[28:31], a[180:183], a[100:103]
v_perm_b32 v18, v247, v243, s84
v_perm_b32 v19, v255, v251, s84
v_mfma_f32_16x16x32_bf16 a[104:107], v[24:27], a[164:167], a[104:107]
ds_read_b128 a[212:215], v9 offset:53248
v_perm_b32 v20, v103, v99, s84
v_mfma_f32_16x16x32_bf16 a[104:107], v[28:31], a[184:187], a[104:107]
ds_read_b128 a[232:235], v10 offset:53248
v_perm_b32 v21, v111, v107, s84
v_mfma_f32_16x16x32_bf16 a[108:111], v[24:27], a[168:171], a[108:111]
v_perm_b32 v22, v119, v115, s84
v_perm_b32 v23, v127, v123, s84
v_mfma_f32_16x16x32_bf16 a[108:111], v[28:31], a[188:191], a[108:111]
v_mfma_f32_16x16x32_bf16 a[112:115], v[24:27], a[172:175], a[112:115]
v_mfma_f32_16x16x32_bf16 a[112:115], v[28:31], a[192:195], a[112:115]
v_mfma_f32_16x16x32_bf16 a[116:119], v[24:27], a[176:179], a[116:119]
v_mfma_f32_16x16x32_bf16 a[116:119], v[28:31], a[196:199], a[116:119]
v_mfma_f32_16x16x32_bf16 a[120:123], v[16:19], a[160:163], a[120:123]
v_perm_b32 v24, v87, v83, s85
v_perm_b32 v25, v95, v91, s85
v_mfma_f32_16x16x32_bf16 a[120:123], v[20:23], a[180:183], a[120:123]
v_perm_b32 v26, v247, v243, s85
v_perm_b32 v27, v255, v251, s85
v_mfma_f32_16x16x32_bf16 a[124:127], v[16:19], a[164:167], a[124:127]
v_perm_b32 v28, v103, v99, s85
v_perm_b32 v29, v111, v107, s85
v_mfma_f32_16x16x32_bf16 a[124:127], v[20:23], a[184:187], a[124:127]
v_perm_b32 v30, v119, v115, s85
v_perm_b32 v31, v127, v123, s85
v_mfma_f32_16x16x32_bf16 a[128:131], v[16:19], a[168:171], a[128:131]
ds_read_b128 a[216:219], v11 offset:10240
v_mfma_f32_16x16x32_bf16 a[128:131], v[20:23], a[188:191], a[128:131]
ds_read_b128 a[236:239], v12 offset:10240
v_mfma_f32_16x16x32_bf16 a[132:135], v[16:19], a[172:175], a[132:135]
buffer_load_dwordx4 v[80:83], v4, s[36:39], s73 offen nt
v_mfma_f32_16x16x32_bf16 a[132:135], v[20:23], a[192:195], a[132:135]
buffer_load_dwordx4 v[84:87], v4, s[36:39], s73 offen offset:512 nt
v_mfma_f32_16x16x32_bf16 a[136:139], v[16:19], a[176:179], a[136:139]
buffer_load_dwordx4 v[88:91], v4, s[36:39], s73 offen offset:2048 nt
v_mfma_f32_16x16x32_bf16 a[136:139], v[20:23], a[196:199], a[136:139]
buffer_load_dwordx4 v[92:95], v4, s[36:39], s73 offen offset:2560 nt
v_mfma_f32_16x16x32_bf16 a[140:143], v[24:27], a[160:163], a[140:143]
v_perm_b32 v16, v132, v128, s84
v_perm_b32 v17, v140, v136, s84
v_mfma_f32_16x16x32_bf16 a[140:143], v[28:31], a[180:183], a[140:143]
v_perm_b32 v18, v148, v144, s84
v_perm_b32 v19, v156, v152, s84
v_mfma_f32_16x16x32_bf16 a[144:147], v[24:27], a[164:167], a[144:147]
v_perm_b32 v20, v164, v160, s84
v_perm_b32 v21, v172, v168, s84
v_mfma_f32_16x16x32_bf16 a[144:147], v[28:31], a[184:187], a[144:147]
v_perm_b32 v22, v180, v176, s84
v_perm_b32 v23, v188, v184, s84
v_mfma_f32_16x16x32_bf16 a[148:151], v[24:27], a[168:171], a[148:151]
v_mfma_f32_16x16x32_bf16 a[148:151], v[28:31], a[188:191], a[148:151]
v_mfma_f32_16x16x32_bf16 a[152:155], v[24:27], a[172:175], a[152:155]
v_mfma_f32_16x16x32_bf16 a[152:155], v[28:31], a[192:195], a[152:155]
v_mfma_f32_16x16x32_bf16 a[156:159], v[24:27], a[176:179], a[156:159]
v_mfma_f32_16x16x32_bf16 a[156:159], v[28:31], a[196:199], a[156:159]
s_waitcnt lgkmcnt(1)
v_mfma_f32_16x16x32_bf16 a[0:3], v[16:19], a[200:203], a[0:3]
v_perm_b32 v24, v132, v128, s85
v_perm_b32 v25, v140, v136, s85
v_mfma_f32_16x16x32_bf16 a[0:3], v[20:23], a[220:223], a[0:3]
v_perm_b32 v26, v148, v144, s85
v_perm_b32 v27, v156, v152, s85
v_mfma_f32_16x16x32_bf16 a[4:7], v[16:19], a[204:207], a[4:7]
v_perm_b32 v28, v164, v160, s85
v_perm_b32 v29, v172, v168, s85
v_mfma_f32_16x16x32_bf16 a[4:7], v[20:23], a[224:227], a[4:7]
v_perm_b32 v30, v180, v176, s85
v_perm_b32 v31, v188, v184, s85
v_mfma_f32_16x16x32_bf16 a[8:11], v[16:19], a[208:211], a[8:11]
v_mfma_f32_16x16x32_bf16 a[8:11], v[20:23], a[228:231], a[8:11]
v_mfma_f32_16x16x32_bf16 a[12:15], v[16:19], a[212:215], a[12:15]
ds_read_b128 a[160:163], v9 offset:12288
v_mfma_f32_16x16x32_bf16 a[12:15], v[20:23], a[232:235], a[12:15]
ds_read_b128 a[180:183], v10 offset:12288
v_mfma_f32_16x16x32_bf16 a[16:19], v[16:19], a[216:219], a[16:19]
buffer_load_dwordx4 v[240:243], v4, s[36:39], s73 offen offset:128 nt
s_waitcnt lgkmcnt(2)
v_mfma_f32_16x16x32_bf16 a[16:19], v[20:23], a[236:239], a[16:19]
v_mfma_f32_16x16x32_bf16 a[20:23], v[24:27], a[200:203], a[20:23]
v_perm_b32 v16, v133, v129, s84
v_perm_b32 v17, v141, v137, s84
v_mfma_f32_16x16x32_bf16 a[20:23], v[28:31], a[220:223], a[20:23]
v_perm_b32 v18, v149, v145, s84
v_perm_b32 v19, v157, v153, s84
v_mfma_f32_16x16x32_bf16 a[24:27], v[24:27], a[204:207], a[24:27]
v_perm_b32 v20, v165, v161, s84
v_perm_b32 v21, v173, v169, s84
v_mfma_f32_16x16x32_bf16 a[24:27], v[28:31], a[224:227], a[24:27]
v_perm_b32 v22, v181, v177, s84
v_perm_b32 v23, v189, v185, s84
v_mfma_f32_16x16x32_bf16 a[28:31], v[24:27], a[208:211], a[28:31]
buffer_load_dwordx4 v[244:247], v4, s[36:39], s73 offen offset:640 nt
v_mfma_f32_16x16x32_bf16 a[28:31], v[28:31], a[228:231], a[28:31]
v_mfma_f32_16x16x32_bf16 a[32:35], v[24:27], a[212:215], a[32:35]
v_mfma_f32_16x16x32_bf16 a[32:35], v[28:31], a[232:235], a[32:35]
v_mfma_f32_16x16x32_bf16 a[36:39], v[24:27], a[216:219], a[36:39]
v_mfma_f32_16x16x32_bf16 a[36:39], v[28:31], a[236:239], a[36:39]
v_mfma_f32_16x16x32_bf16 a[40:43], v[16:19], a[200:203], a[40:43]
v_perm_b32 v24, v133, v129, s85
v_perm_b32 v25, v141, v137, s85
v_mfma_f32_16x16x32_bf16 a[40:43], v[20:23], a[220:223], a[40:43]
v_perm_b32 v26, v149, v145, s85
v_perm_b32 v27, v157, v153, s85
v_mfma_f32_16x16x32_bf16 a[44:47], v[16:19], a[204:207], a[44:47]
ds_read_b128 a[164:167], v9 offset:26624
v_perm_b32 v28, v165, v161, s85
v_mfma_f32_16x16x32_bf16 a[44:47], v[20:23], a[224:227], a[44:47]
ds_read_b128 a[184:187], v10 offset:26624
v_perm_b32 v29, v173, v169, s85
v_mfma_f32_16x16x32_bf16 a[48:51], v[16:19], a[208:211], a[48:51]
v_perm_b32 v30, v181, v177, s85
v_perm_b32 v31, v189, v185, s85
v_mfma_f32_16x16x32_bf16 a[48:51], v[20:23], a[228:231], a[48:51]
buffer_load_dwordx4 v[248:251], v4, s[36:39], s73 offen offset:2176 nt
v_mfma_f32_16x16x32_bf16 a[52:55], v[16:19], a[212:215], a[52:55]
v_mfma_f32_16x16x32_bf16 a[52:55], v[20:23], a[232:235], a[52:55]
v_mfma_f32_16x16x32_bf16 a[56:59], v[16:19], a[216:219], a[56:59]
v_mfma_f32_16x16x32_bf16 a[56:59], v[20:23], a[236:239], a[56:59]
v_mfma_f32_16x16x32_bf16 a[60:63], v[24:27], a[200:203], a[60:63]
v_perm_b32 v16, v134, v130, s84
v_perm_b32 v17, v142, v138, s84
v_mfma_f32_16x16x32_bf16 a[60:63], v[28:31], a[220:223], a[60:63]
v_perm_b32 v18, v150, v146, s84
v_perm_b32 v19, v158, v154, s84
v_mfma_f32_16x16x32_bf16 a[64:67], v[24:27], a[204:207], a[64:67]
v_perm_b32 v20, v166, v162, s84
v_perm_b32 v21, v174, v170, s84
v_mfma_f32_16x16x32_bf16 a[64:67], v[28:31], a[224:227], a[64:67]
v_perm_b32 v22, v182, v178, s84
v_perm_b32 v23, v190, v186, s84
v_mfma_f32_16x16x32_bf16 a[68:71], v[24:27], a[208:211], a[68:71]
buffer_load_dwordx4 v[252:255], v4, s[36:39], s73 offen offset:2688 nt
v_mfma_f32_16x16x32_bf16 a[68:71], v[28:31], a[228:231], a[68:71]
ds_read_b128 a[168:171], v9 offset:40960
v_mfma_f32_16x16x32_bf16 a[72:75], v[24:27], a[212:215], a[72:75]
ds_read_b128 a[188:191], v10 offset:40960
v_mfma_f32_16x16x32_bf16 a[72:75], v[28:31], a[232:235], a[72:75]
v_mfma_f32_16x16x32_bf16 a[76:79], v[24:27], a[216:219], a[76:79]
v_mfma_f32_16x16x32_bf16 a[76:79], v[28:31], a[236:239], a[76:79]
v_mfma_f32_16x16x32_bf16 a[80:83], v[16:19], a[200:203], a[80:83]
v_perm_b32 v24, v134, v130, s85
v_perm_b32 v25, v142, v138, s85
v_mfma_f32_16x16x32_bf16 a[80:83], v[20:23], a[220:223], a[80:83]
v_perm_b32 v26, v150, v146, s85
v_perm_b32 v27, v158, v154, s85
v_mfma_f32_16x16x32_bf16 a[84:87], v[16:19], a[204:207], a[84:87]
v_perm_b32 v28, v166, v162, s85
v_perm_b32 v29, v174, v170, s85
v_mfma_f32_16x16x32_bf16 a[84:87], v[20:23], a[224:227], a[84:87]
v_perm_b32 v30, v182, v178, s85
v_perm_b32 v31, v190, v186, s85
v_mfma_f32_16x16x32_bf16 a[88:91], v[16:19], a[208:211], a[88:91]
v_mfma_f32_16x16x32_bf16 a[88:91], v[20:23], a[228:231], a[88:91]
v_mfma_f32_16x16x32_bf16 a[92:95], v[16:19], a[212:215], a[92:95]
v_mfma_f32_16x16x32_bf16 a[92:95], v[20:23], a[232:235], a[92:95]
v_mfma_f32_16x16x32_bf16 a[96:99], v[16:19], a[216:219], a[96:99]
v_mfma_f32_16x16x32_bf16 a[96:99], v[20:23], a[236:239], a[96:99]
v_mfma_f32_16x16x32_bf16 a[100:103], v[24:27], a[200:203], a[100:103]
v_perm_b32 v16, v135, v131, s84
v_perm_b32 v17, v143, v139, s84
v_mfma_f32_16x16x32_bf16 a[100:103], v[28:31], a[220:223], a[100:103]
v_perm_b32 v18, v151, v147, s84
v_perm_b32 v19, v159, v155, s84
v_mfma_f32_16x16x32_bf16 a[104:107], v[24:27], a[204:207], a[104:107]
ds_read_b128 a[172:175], v9 offset:55296
v_perm_b32 v20, v167, v163, s84
v_mfma_f32_16x16x32_bf16 a[104:107], v[28:31], a[224:227], a[104:107]
ds_read_b128 a[192:195], v10 offset:55296
v_perm_b32 v21, v175, v171, s84
v_mfma_f32_16x16x32_bf16 a[108:111], v[24:27], a[208:211], a[108:111]
v_perm_b32 v22, v183, v179, s84
v_perm_b32 v23, v191, v187, s84
v_mfma_f32_16x16x32_bf16 a[108:111], v[28:31], a[228:231], a[108:111]
v_mfma_f32_16x16x32_bf16 a[112:115], v[24:27], a[212:215], a[112:115]
)"
R"(v_mfma_f32_16x16x32_bf16 a[112:115], v[28:31], a[232:235], a[112:115]
v_mfma_f32_16x16x32_bf16 a[116:119], v[24:27], a[216:219], a[116:119]
v_mfma_f32_16x16x32_bf16 a[116:119], v[28:31], a[236:239], a[116:119]
v_mfma_f32_16x16x32_bf16 a[120:123], v[16:19], a[200:203], a[120:123]
v_perm_b32 v24, v135, v131, s85
v_perm_b32 v25, v143, v139, s85
v_mfma_f32_16x16x32_bf16 a[120:123], v[20:23], a[220:223], a[120:123]
v_perm_b32 v26, v151, v147, s85
v_perm_b32 v27, v159, v155, s85
v_mfma_f32_16x16x32_bf16 a[124:127], v[16:19], a[204:207], a[124:127]
v_perm_b32 v28, v167, v163, s85
v_perm_b32 v29, v175, v171, s85
v_mfma_f32_16x16x32_bf16 a[124:127], v[20:23], a[224:227], a[124:127]
v_perm_b32 v30, v183, v179, s85
v_perm_b32 v31, v191, v187, s85
v_mfma_f32_16x16x32_bf16 a[128:131], v[16:19], a[208:211], a[128:131]
ds_read_b128 a[176:179], v11 offset:12288
v_mfma_f32_16x16x32_bf16 a[128:131], v[20:23], a[228:231], a[128:131]
ds_read_b128 a[196:199], v12 offset:12288
v_mfma_f32_16x16x32_bf16 a[132:135], v[16:19], a[212:215], a[132:135]
buffer_load_dwordx4 v[176:179], v4, s[36:39], s72 offen offset:256 nt
v_mfma_f32_16x16x32_bf16 a[132:135], v[20:23], a[232:235], a[132:135]
buffer_load_dwordx4 v[180:183], v4, s[36:39], s72 offen offset:768 nt
v_mfma_f32_16x16x32_bf16 a[136:139], v[16:19], a[216:219], a[136:139]
buffer_load_dwordx4 v[184:187], v4, s[36:39], s72 offen offset:2304 nt
v_mfma_f32_16x16x32_bf16 a[136:139], v[20:23], a[236:239], a[136:139]
buffer_load_dwordx4 v[188:191], v4, s[36:39], s72 offen offset:2816 nt
v_mfma_f32_16x16x32_bf16 a[140:143], v[24:27], a[200:203], a[140:143]
v_perm_b32 v16, v196, v192, s84
v_perm_b32 v17, v204, v200, s84
v_mfma_f32_16x16x32_bf16 a[140:143], v[28:31], a[220:223], a[140:143]
v_perm_b32 v18, v212, v208, s84
v_perm_b32 v19, v220, v216, s84
v_mfma_f32_16x16x32_bf16 a[144:147], v[24:27], a[204:207], a[144:147]
v_perm_b32 v20, v228, v224, s84
v_perm_b32 v21, v236, v232, s84
v_mfma_f32_16x16x32_bf16 a[144:147], v[28:31], a[224:227], a[144:147]
v_perm_b32 v22, v36, v32, s84
v_perm_b32 v23, v44, v40, s84
v_mfma_f32_16x16x32_bf16 a[148:151], v[24:27], a[208:211], a[148:151]
v_mfma_f32_16x16x32_bf16 a[148:151], v[28:31], a[228:231], a[148:151]
v_mfma_f32_16x16x32_bf16 a[152:155], v[24:27], a[212:215], a[152:155]
v_mfma_f32_16x16x32_bf16 a[152:155], v[28:31], a[232:235], a[152:155]
v_mfma_f32_16x16x32_bf16 a[156:159], v[24:27], a[216:219], a[156:159]
v_mfma_f32_16x16x32_bf16 a[156:159], v[28:31], a[236:239], a[156:159]
s_waitcnt lgkmcnt(1)
v_mfma_f32_16x16x32_bf16 a[0:3], v[16:19], a[160:163], a[0:3]
v_perm_b32 v24, v196, v192, s85
v_perm_b32 v25, v204, v200, s85
v_mfma_f32_16x16x32_bf16 a[0:3], v[20:23], a[180:183], a[0:3]
v_perm_b32 v26, v212, v208, s85
v_perm_b32 v27, v220, v216, s85
v_mfma_f32_16x16x32_bf16 a[4:7], v[16:19], a[164:167], a[4:7]
v_perm_b32 v28, v228, v224, s85
v_perm_b32 v29, v236, v232, s85
v_mfma_f32_16x16x32_bf16 a[4:7], v[20:23], a[184:187], a[4:7]
v_perm_b32 v30, v36, v32, s85
v_perm_b32 v31, v44, v40, s85
v_mfma_f32_16x16x32_bf16 a[8:11], v[16:19], a[168:171], a[8:11]
v_mfma_f32_16x16x32_bf16 a[8:11], v[20:23], a[188:191], a[8:11]
v_mfma_f32_16x16x32_bf16 a[12:15], v[16:19], a[172:175], a[12:15]
v_mfma_f32_16x16x32_bf16 a[12:15], v[20:23], a[192:195], a[12:15]
v_mfma_f32_16x16x32_bf16 a[16:19], v[16:19], a[176:179], a[16:19]
s_waitcnt lgkmcnt(0)
v_mfma_f32_16x16x32_bf16 a[16:19], v[20:23], a[196:199], a[16:19]
v_mfma_f32_16x16x32_bf16 a[20:23], v[24:27], a[160:163], a[20:23]
v_perm_b32 v16, v197, v193, s84
v_perm_b32 v17, v205, v201, s84
v_mfma_f32_16x16x32_bf16 a[20:23], v[28:31], a[180:183], a[20:23]
v_perm_b32 v18, v213, v209, s84
v_perm_b32 v19, v221, v217, s84
v_mfma_f32_16x16x32_bf16 a[24:27], v[24:27], a[164:167], a[24:27]
v_perm_b32 v20, v229, v225, s84
v_perm_b32 v21, v237, v233, s84
v_mfma_f32_16x16x32_bf16 a[24:27], v[28:31], a[184:187], a[24:27]
v_perm_b32 v22, v37, v33, s84
v_perm_b32 v23, v45, v41, s84
v_mfma_f32_16x16x32_bf16 a[28:31], v[24:27], a[168:171], a[28:31]
v_mfma_f32_16x16x32_bf16 a[28:31], v[28:31], a[188:191], a[28:31]
v_mfma_f32_16x16x32_bf16 a[32:35], v[24:27], a[172:175], a[32:35]
v_mfma_f32_16x16x32_bf16 a[32:35], v[28:31], a[192:195], a[32:35]
v_mfma_f32_16x16x32_bf16 a[36:39], v[24:27], a[176:179], a[36:39]
v_mfma_f32_16x16x32_bf16 a[36:39], v[28:31], a[196:199], a[36:39]
v_mfma_f32_16x16x32_bf16 a[40:43], v[16:19], a[160:163], a[40:43]
v_perm_b32 v24, v197, v193, s85
v_perm_b32 v25, v205, v201, s85
v_mfma_f32_16x16x32_bf16 a[40:43], v[20:23], a[180:183], a[40:43]
v_perm_b32 v26, v213, v209, s85
v_perm_b32 v27, v221, v217, s85
v_mfma_f32_16x16x32_bf16 a[44:47], v[16:19], a[164:167], a[44:47]
v_perm_b32 v28, v229, v225, s85
v_perm_b32 v29, v237, v233, s85
v_mfma_f32_16x16x32_bf16 a[44:47], v[20:23], a[184:187], a[44:47]
v_perm_b32 v30, v37, v33, s85
v_perm_b32 v31, v45, v41, s85
v_mfma_f32_16x16x32_bf16 a[48:51], v[16:19], a[168:171], a[48:51]
v_mfma_f32_16x16x32_bf16 a[48:51], v[20:23], a[188:191], a[48:51]
v_mfma_f32_16x16x32_bf16 a[52:55], v[16:19], a[172:175], a[52:55]
v_mfma_f32_16x16x32_bf16 a[52:55], v[20:23], a[192:195], a[52:55]
v_mfma_f32_16x16x32_bf16 a[56:59], v[16:19], a[176:179], a[56:59]
v_mfma_f32_16x16x32_bf16 a[56:59], v[20:23], a[196:199], a[56:59]
v_mfma_f32_16x16x32_bf16 a[60:63], v[24:27], a[160:163], a[60:63]
v_perm_b32 v16, v198, v194, s84
v_perm_b32 v17, v206, v202, s84
v_mfma_f32_16x16x32_bf16 a[60:63], v[28:31], a[180:183], a[60:63]
v_perm_b32 v18, v214, v210, s84
v_perm_b32 v19, v222, v218, s84
v_mfma_f32_16x16x32_bf16 a[64:67], v[24:27], a[164:167], a[64:67]
v_perm_b32 v20, v230, v226, s84
v_perm_b32 v21, v238, v234, s84
v_mfma_f32_16x16x32_bf16 a[64:67], v[28:31], a[184:187], a[64:67]
v_perm_b32 v22, v38, v34, s84
v_perm_b32 v23, v46, v42, s84
v_mfma_f32_16x16x32_bf16 a[68:71], v[24:27], a[168:171], a[68:71]
v_mfma_f32_16x16x32_bf16 a[68:71], v[28:31], a[188:191], a[68:71]
v_mfma_f32_16x16x32_bf16 a[72:75], v[24:27], a[172:175], a[72:75]
v_mfma_f32_16x16x32_bf16 a[72:75], v[28:31], a[192:195], a[72:75]
v_mfma_f32_16x16x32_bf16 a[76:79], v[24:27], a[176:179], a[76:79]
v_mfma_f32_16x16x32_bf16 a[76:79], v[28:31], a[196:199], a[76:79]
v_mfma_f32_16x16x32_bf16 a[80:83], v[16:19], a[160:163], a[80:83]
v_perm_b32 v24, v198, v194, s85
v_perm_b32 v25, v206, v202, s85
v_mfma_f32_16x16x32_bf16 a[80:83], v[20:23], a[180:183], a[80:83]
v_perm_b32 v26, v214, v210, s85
v_perm_b32 v27, v222, v218, s85
v_mfma_f32_16x16x32_bf16 a[84:87], v[16:19], a[164:167], a[84:87]
v_perm_b32 v28, v230, v226, s85
v_perm_b32 v29, v238, v234, s85
v_mfma_f32_16x16x32_bf16 a[84:87], v[20:23], a[184:187], a[84:87]
v_perm_b32 v30, v38, v34, s85
v_perm_b32 v31, v46, v42, s85
v_mfma_f32_16x16x32_bf16 a[88:91], v[16:19], a[168:171], a[88:91]
v_mfma_f32_16x16x32_bf16 a[88:91], v[20:23], a[188:191], a[88:91]
v_mfma_f32_16x16x32_bf16 a[92:95], v[16:19], a[172:175], a[92:95]
v_mfma_f32_16x16x32_bf16 a[92:95], v[20:23], a[192:195], a[92:95]
v_mfma_f32_16x16x32_bf16 a[96:99], v[16:19], a[176:179], a[96:99]
v_mfma_f32_16x16x32_bf16 a[96:99], v[20:23], a[196:199], a[96:99]
v_mfma_f32_16x16x32_bf16 a[100:103], v[24:27], a[160:163], a[100:103]
v_perm_b32 v16, v199, v195, s84
v_perm_b32 v17, v207, v203, s84
v_mfma_f32_16x16x32_bf16 a[100:103], v[28:31], a[180:183], a[100:103]
v_perm_b32 v18, v215, v211, s84
v_perm_b32 v19, v223, v219, s84
v_mfma_f32_16x16x32_bf16 a[104:107], v[24:27], a[164:167], a[104:107]
v_perm_b32 v20, v231, v227, s84
v_perm_b32 v21, v239, v235, s84
v_mfma_f32_16x16x32_bf16 a[104:107], v[28:31], a[184:187], a[104:107]
v_perm_b32 v22, v39, v35, s84
v_perm_b32 v23, v47, v43, s84
v_mfma_f32_16x16x32_bf16 a[108:111], v[24:27], a[168:171], a[108:111]
v_mfma_f32_16x16x32_bf16 a[108:111], v[28:31], a[188:191], a[108:111]
v_mfma_f32_16x16x32_bf16 a[112:115], v[24:27], a[172:175], a[112:115]
v_mfma_f32_16x16x32_bf16 a[112:115], v[28:31], a[192:195], a[112:115]
v_mfma_f32_16x16x32_bf16 a[116:119], v[24:27], a[176:179], a[116:119]
v_mfma_f32_16x16x32_bf16 a[116:119], v[28:31], a[196:199], a[116:119]
v_mfma_f32_16x16x32_bf16 a[120:123], v[16:19], a[160:163], a[120:123]
v_perm_b32 v24, v199, v195, s85
v_perm_b32 v25, v207, v203, s85
v_mfma_f32_16x16x32_bf16 a[120:123], v[20:23], a[180:183], a[120:123]
v_perm_b32 v26, v215, v211, s85
v_perm_b32 v27, v223, v219, s85
v_mfma_f32_16x16x32_bf16 a[124:127], v[16:19], a[164:167], a[124:127]
v_perm_b32 v28, v231, v227, s85
v_perm_b32 v29, v239, v235, s85
v_mfma_f32_16x16x32_bf16 a[124:127], v[20:23], a[184:187], a[124:127]
v_perm_b32 v30, v39, v35, s85
v_perm_b32 v31, v47, v43, s85
v_mfma_f32_16x16x32_bf16 a[128:131], v[16:19], a[168:171], a[128:131]
buffer_load_dwordx4 v[192:195], v4, s[36:39], s72 offen offset:384 nt
v_mfma_f32_16x16x32_bf16 a[128:131], v[20:23], a[188:191], a[128:131]
buffer_load_dwordx4 v[196:199], v4, s[36:39], s72 offen offset:896 nt
v_mfma_f32_16x16x32_bf16 a[132:135], v[16:19], a[172:175], a[132:135]
buffer_load_dwordx4 v[200:203], v4, s[36:39], s72 offen offset:2432 nt
v_mfma_f32_16x16x32_bf16 a[132:135], v[20:23], a[192:195], a[132:135]
buffer_load_dwordx4 v[204:207], v4, s[36:39], s72 offen offset:2944 nt
v_mfma_f32_16x16x32_bf16 a[136:139], v[16:19], a[176:179], a[136:139]
buffer_load_dwordx4 v[32:35], v4, s[36:39], s73 offen offset:256 nt
v_mfma_f32_16x16x32_bf16 a[136:139], v[20:23], a[196:199], a[136:139]
buffer_load_dwordx4 v[36:39], v4, s[36:39], s73 offen offset:768 nt
v_mfma_f32_16x16x32_bf16 a[140:143], v[24:27], a[160:163], a[140:143]
buffer_load_dwordx4 v[40:43], v4, s[36:39], s73 offen offset:2304 nt
v_mfma_f32_16x16x32_bf16 a[140:143], v[28:31], a[180:183], a[140:143]
buffer_load_dwordx4 v[44:47], v4, s[36:39], s73 offen offset:2816 nt
v_mfma_f32_16x16x32_bf16 a[144:147], v[24:27], a[164:167], a[144:147]
buffer_load_dwordx4 v[208:211], v4, s[36:39], s73 offen offset:384 nt
v_mfma_f32_16x16x32_bf16 a[144:147], v[28:31], a[184:187], a[144:147]
v_mfma_f32_16x16x32_bf16 a[148:151], v[24:27], a[168:171], a[148:151]
v_mfma_f32_16x16x32_bf16 a[148:151], v[28:31], a[188:191], a[148:151]
v_mfma_f32_16x16x32_bf16 a[152:155], v[24:27], a[172:175], a[152:155]
buffer_load_dwordx4 v[212:215], v4, s[36:39], s73 offen offset:896 nt
v_mfma_f32_16x16x32_bf16 a[152:155], v[28:31], a[192:195], a[152:155]
v_mfma_f32_16x16x32_bf16 a[156:159], v[24:27], a[176:179], a[156:159]
v_mfma_f32_16x16x32_bf16 a[156:159], v[28:31], a[196:199], a[156:159]
v_accvgpr_read_b32 v9, a245
v_accvgpr_read_b32 v16, a0
v_accvgpr_read_b32 v17, a20
v_accvgpr_read_b32 v18, a40
v_accvgpr_read_b32 v19, a60
v_accvgpr_read_b32 v20, a80
v_accvgpr_read_b32 v21, a100
)"
R"(v_accvgpr_read_b32 v22, a120
v_accvgpr_read_b32 v23, a140
v_cvt_pk_bf16_f32 v24, v16, v17
v_cvt_pk_bf16_f32 v25, v18, v19
v_cvt_pk_bf16_f32 v26, v20, v21
v_cvt_pk_bf16_f32 v27, v22, v23
ds_write_b128 v9, v[24:27] offset:0
v_accvgpr_read_b32 v16, a1
v_accvgpr_read_b32 v17, a21
v_accvgpr_read_b32 v18, a41
v_accvgpr_read_b32 v19, a61
v_accvgpr_read_b32 v20, a81
v_accvgpr_read_b32 v21, a101
v_accvgpr_read_b32 v22, a121
v_accvgpr_read_b32 v23, a141
v_cvt_pk_bf16_f32 v28, v16, v17
v_cvt_pk_bf16_f32 v29, v18, v19
v_cvt_pk_bf16_f32 v30, v20, v21
v_cvt_pk_bf16_f32 v31, v22, v23
ds_write_b128 v9, v[28:31] offset:5120
v_accvgpr_read_b32 v16, a2
v_accvgpr_read_b32 v17, a22
v_accvgpr_read_b32 v18, a42
v_accvgpr_read_b32 v19, a62
v_accvgpr_read_b32 v20, a82
v_accvgpr_read_b32 v21, a102
v_accvgpr_read_b32 v22, a122
v_accvgpr_read_b32 v23, a142
v_cvt_pk_bf16_f32 v24, v16, v17
v_cvt_pk_bf16_f32 v25, v18, v19
v_cvt_pk_bf16_f32 v26, v20, v21
v_cvt_pk_bf16_f32 v27, v22, v23
ds_write_b128 v9, v[24:27] offset:256
buffer_load_dwordx4 v[216:219], v4, s[36:39], s73 offen offset:2432 nt
buffer_load_dwordx4 v[220:223], v4, s[36:39], s73 offen offset:2944 nt
v_accvgpr_read_b32 v16, a3
v_accvgpr_read_b32 v17, a23
v_accvgpr_read_b32 v18, a43
v_accvgpr_read_b32 v19, a63
v_accvgpr_read_b32 v20, a83
v_accvgpr_read_b32 v21, a103
v_accvgpr_read_b32 v22, a123
v_accvgpr_read_b32 v23, a143
v_cvt_pk_bf16_f32 v28, v16, v17
v_cvt_pk_bf16_f32 v29, v18, v19
v_cvt_pk_bf16_f32 v30, v20, v21
v_cvt_pk_bf16_f32 v31, v22, v23
ds_write_b128 v9, v[28:31] offset:5376
v_accvgpr_read_b32 v16, a4
v_accvgpr_read_b32 v17, a24
v_accvgpr_read_b32 v18, a44
v_accvgpr_read_b32 v19, a64
v_accvgpr_read_b32 v20, a84
v_accvgpr_read_b32 v21, a104
v_accvgpr_read_b32 v22, a124
v_accvgpr_read_b32 v23, a144
v_cvt_pk_bf16_f32 v24, v16, v17
v_cvt_pk_bf16_f32 v25, v18, v19
v_cvt_pk_bf16_f32 v26, v20, v21
v_cvt_pk_bf16_f32 v27, v22, v23
ds_write_b128 v9, v[24:27] offset:1024
v_accvgpr_read_b32 v16, a5
v_accvgpr_read_b32 v17, a25
v_accvgpr_read_b32 v18, a45
v_accvgpr_read_b32 v19, a65
v_accvgpr_read_b32 v20, a85
v_accvgpr_read_b32 v21, a105
v_accvgpr_read_b32 v22, a125
v_accvgpr_read_b32 v23, a145
v_cvt_pk_bf16_f32 v28, v16, v17
v_cvt_pk_bf16_f32 v29, v18, v19
v_cvt_pk_bf16_f32 v30, v20, v21
v_cvt_pk_bf16_f32 v31, v22, v23
ds_write_b128 v9, v[28:31] offset:6144
v_accvgpr_read_b32 v16, a6
v_accvgpr_read_b32 v17, a26
v_accvgpr_read_b32 v18, a46
v_accvgpr_read_b32 v19, a66
v_accvgpr_read_b32 v20, a86
v_accvgpr_read_b32 v21, a106
v_accvgpr_read_b32 v22, a126
v_accvgpr_read_b32 v23, a146
v_cvt_pk_bf16_f32 v24, v16, v17
v_cvt_pk_bf16_f32 v25, v18, v19
v_cvt_pk_bf16_f32 v26, v20, v21
v_cvt_pk_bf16_f32 v27, v22, v23
ds_write_b128 v9, v[24:27] offset:1280
v_accvgpr_read_b32 v16, a7
v_accvgpr_read_b32 v17, a27
v_accvgpr_read_b32 v18, a47
v_accvgpr_read_b32 v19, a67
v_accvgpr_read_b32 v20, a87
v_accvgpr_read_b32 v21, a107
v_accvgpr_read_b32 v22, a127
v_accvgpr_read_b32 v23, a147
v_cvt_pk_bf16_f32 v28, v16, v17
v_cvt_pk_bf16_f32 v29, v18, v19
v_cvt_pk_bf16_f32 v30, v20, v21
v_cvt_pk_bf16_f32 v31, v22, v23
ds_write_b128 v9, v[28:31] offset:6400
v_accvgpr_read_b32 v16, a8
v_accvgpr_read_b32 v17, a28
v_accvgpr_read_b32 v18, a48
v_accvgpr_read_b32 v19, a68
v_accvgpr_read_b32 v20, a88
v_accvgpr_read_b32 v21, a108
v_accvgpr_read_b32 v22, a128
v_accvgpr_read_b32 v23, a148
v_cvt_pk_bf16_f32 v24, v16, v17
v_cvt_pk_bf16_f32 v25, v18, v19
v_cvt_pk_bf16_f32 v26, v20, v21
v_cvt_pk_bf16_f32 v27, v22, v23
ds_write_b128 v9, v[24:27] offset:2048
v_accvgpr_read_b32 v16, a9
v_accvgpr_read_b32 v17, a29
v_accvgpr_read_b32 v18, a49
v_accvgpr_read_b32 v19, a69
v_accvgpr_read_b32 v20, a89
v_accvgpr_read_b32 v21, a109
v_accvgpr_read_b32 v22, a129
v_accvgpr_read_b32 v23, a149
v_cvt_pk_bf16_f32 v28, v16, v17
v_cvt_pk_bf16_f32 v29, v18, v19
v_cvt_pk_bf16_f32 v30, v20, v21
v_cvt_pk_bf16_f32 v31, v22, v23
ds_write_b128 v9, v[28:31] offset:7168
v_accvgpr_read_b32 v16, a10
v_accvgpr_read_b32 v17, a30
v_accvgpr_read_b32 v18, a50
v_accvgpr_read_b32 v19, a70
v_accvgpr_read_b32 v20, a90
v_accvgpr_read_b32 v21, a110
v_accvgpr_read_b32 v22, a130
v_accvgpr_read_b32 v23, a150
v_cvt_pk_bf16_f32 v24, v16, v17
v_cvt_pk_bf16_f32 v25, v18, v19
v_cvt_pk_bf16_f32 v26, v20, v21
v_cvt_pk_bf16_f32 v27, v22, v23
ds_write_b128 v9, v[24:27] offset:2304
v_accvgpr_read_b32 v16, a11
v_accvgpr_read_b32 v17, a31
v_accvgpr_read_b32 v18, a51
v_accvgpr_read_b32 v19, a71
v_accvgpr_read_b32 v20, a91
v_accvgpr_read_b32 v21, a111
v_accvgpr_read_b32 v22, a131
v_accvgpr_read_b32 v23, a151
v_cvt_pk_bf16_f32 v28, v16, v17
v_cvt_pk_bf16_f32 v29, v18, v19
v_cvt_pk_bf16_f32 v30, v20, v21
v_cvt_pk_bf16_f32 v31, v22, v23
ds_write_b128 v9, v[28:31] offset:7424
v_accvgpr_read_b32 v16, a12
v_accvgpr_read_b32 v17, a32
v_accvgpr_read_b32 v18, a52
v_accvgpr_read_b32 v19, a72
v_accvgpr_read_b32 v20, a92
v_accvgpr_read_b32 v21, a112
v_accvgpr_read_b32 v22, a132
v_accvgpr_read_b32 v23, a152
v_cvt_pk_bf16_f32 v24, v16, v17
v_cvt_pk_bf16_f32 v25, v18, v19
v_cvt_pk_bf16_f32 v26, v20, v21
v_cvt_pk_bf16_f32 v27, v22, v23
ds_write_b128 v9, v[24:27] offset:3072
v_accvgpr_read_b32 v16, a13
v_accvgpr_read_b32 v17, a33
v_accvgpr_read_b32 v18, a53
v_accvgpr_read_b32 v19, a73
v_accvgpr_read_b32 v20, a93
v_accvgpr_read_b32 v21, a113
v_accvgpr_read_b32 v22, a133
v_accvgpr_read_b32 v23, a153
v_cvt_pk_bf16_f32 v28, v16, v17
v_cvt_pk_bf16_f32 v29, v18, v19
v_cvt_pk_bf16_f32 v30, v20, v21
v_cvt_pk_bf16_f32 v31, v22, v23
ds_write_b128 v9, v[28:31] offset:8192
v_accvgpr_read_b32 v16, a14
v_accvgpr_read_b32 v17, a34
v_accvgpr_read_b32 v18, a54
v_accvgpr_read_b32 v19, a74
v_accvgpr_read_b32 v20, a94
v_accvgpr_read_b32 v21, a114
v_accvgpr_read_b32 v22, a134
v_accvgpr_read_b32 v23, a154
v_cvt_pk_bf16_f32 v24, v16, v17
v_cvt_pk_bf16_f32 v25, v18, v19
v_cvt_pk_bf16_f32 v26, v20, v21
v_cvt_pk_bf16_f32 v27, v22, v23
s_waitcnt lgkmcnt(10)
ds_write_b128 v9, v[24:27] offset:3328
v_accvgpr_read_b32 v16, a15
v_accvgpr_read_b32 v17, a35
v_accvgpr_read_b32 v18, a55
v_accvgpr_read_b32 v19, a75
v_accvgpr_read_b32 v20, a95
v_accvgpr_read_b32 v21, a115
v_accvgpr_read_b32 v22, a135
v_accvgpr_read_b32 v23, a155
)"
R"(v_cvt_pk_bf16_f32 v28, v16, v17
v_cvt_pk_bf16_f32 v29, v18, v19
v_cvt_pk_bf16_f32 v30, v20, v21
v_cvt_pk_bf16_f32 v31, v22, v23
ds_write_b128 v9, v[28:31] offset:8448
v_accvgpr_read_b32 v16, a16
v_accvgpr_read_b32 v17, a36
v_accvgpr_read_b32 v18, a56
v_accvgpr_read_b32 v19, a76
v_accvgpr_read_b32 v20, a96
v_accvgpr_read_b32 v21, a116
v_accvgpr_read_b32 v22, a136
v_accvgpr_read_b32 v23, a156
v_cvt_pk_bf16_f32 v24, v16, v17
v_cvt_pk_bf16_f32 v25, v18, v19
v_cvt_pk_bf16_f32 v26, v20, v21
v_cvt_pk_bf16_f32 v27, v22, v23
ds_write_b128 v9, v[24:27] offset:4096
v_accvgpr_read_b32 v16, a17
v_accvgpr_read_b32 v17, a37
v_accvgpr_read_b32 v18, a57
v_accvgpr_read_b32 v19, a77
v_accvgpr_read_b32 v20, a97
v_accvgpr_read_b32 v21, a117
v_accvgpr_read_b32 v22, a137
v_accvgpr_read_b32 v23, a157
v_cvt_pk_bf16_f32 v28, v16, v17
v_cvt_pk_bf16_f32 v29, v18, v19
v_cvt_pk_bf16_f32 v30, v20, v21
v_cvt_pk_bf16_f32 v31, v22, v23
ds_write_b128 v9, v[28:31] offset:9216
v_accvgpr_read_b32 v16, a18
v_accvgpr_read_b32 v17, a38
v_accvgpr_read_b32 v18, a58
v_accvgpr_read_b32 v19, a78
v_accvgpr_read_b32 v20, a98
v_accvgpr_read_b32 v21, a118
v_accvgpr_read_b32 v22, a138
v_accvgpr_read_b32 v23, a158
v_cvt_pk_bf16_f32 v24, v16, v17
v_cvt_pk_bf16_f32 v25, v18, v19
v_cvt_pk_bf16_f32 v26, v20, v21
v_cvt_pk_bf16_f32 v27, v22, v23
s_waitcnt lgkmcnt(10)
ds_write_b128 v9, v[24:27] offset:4352
v_accvgpr_read_b32 v16, a19
v_accvgpr_read_b32 v17, a39
v_accvgpr_read_b32 v18, a59
v_accvgpr_read_b32 v19, a79
v_accvgpr_read_b32 v20, a99
v_accvgpr_read_b32 v21, a119
v_accvgpr_read_b32 v22, a139
v_accvgpr_read_b32 v23, a159
v_cvt_pk_bf16_f32 v28, v16, v17
v_cvt_pk_bf16_f32 v29, v18, v19
v_cvt_pk_bf16_f32 v30, v20, v21
v_cvt_pk_bf16_f32 v31, v22, v23
ds_write_b128 v9, v[28:31] offset:9472
s_waitcnt vmcnt(28)
v_cndmask_b32_dpp v16, v52, v48, vcc quad_perm:[1,0,3,2] row_mask:0xf bank_mask:0xf
v_cndmask_b32_dpp v17, v53, v49, vcc quad_perm:[1,0,3,2] row_mask:0xf bank_mask:0xf
v_cndmask_b32_dpp v18, v54, v50, vcc quad_perm:[1,0,3,2] row_mask:0xf bank_mask:0xf
v_cndmask_b32_dpp v19, v55, v51, vcc quad_perm:[1,0,3,2] row_mask:0xf bank_mask:0xf
s_mov_b64 vcc, s[78:79]
v_cndmask_b32_dpp v52, v48, v52, vcc quad_perm:[1,0,3,2] row_mask:0xf bank_mask:0xf
v_cndmask_b32_dpp v53, v49, v53, vcc quad_perm:[1,0,3,2] row_mask:0xf bank_mask:0xf
v_cndmask_b32_dpp v54, v50, v54, vcc quad_perm:[1,0,3,2] row_mask:0xf bank_mask:0xf
v_cndmask_b32_dpp v55, v51, v55, vcc quad_perm:[1,0,3,2] row_mask:0xf bank_mask:0xf
s_waitcnt lgkmcnt(0)
s_barrier
s_cmp_lg_u32 s89, 0
s_cbranch_scc1 LbodyB_%=
ds_read_b128 a[160:163], v1 offset:0
ds_read_b128 a[164:167], v1 offset:1024
ds_read_b128 a[168:171], v1 offset:2048
ds_read_b128 a[172:175], v1 offset:3072
ds_read_b128 a[176:179], v1 offset:4096
ds_read_b128 a[180:183], v1 offset:40960
ds_read_b128 a[184:187], v1 offset:41984
ds_read_b128 a[188:191], v1 offset:43008
ds_read_b128 a[192:195], v1 offset:44032
ds_read_b128 a[196:199], v1 offset:45056
ds_read_b128 a[200:203], v1 offset:5120
ds_read_b128 a[204:207], v1 offset:6144
ds_read_b128 a[208:211], v1 offset:7168
ds_read_b128 a[212:215], v1 offset:8192
s_waitcnt lgkmcnt(10)
ds_read_b128 a[216:219], v1 offset:9216
ds_read_b128 a[220:223], v1 offset:46080
ds_read_b128 a[224:227], v1 offset:47104
ds_read_b128 a[228:231], v1 offset:48128
s_waitcnt lgkmcnt(10)
ds_read_b128 a[232:235], v1 offset:49152
ds_read_b128 a[236:239], v1 offset:50176
v_mfma_f32_16x16x32_bf16 a[0:3], v[16:19], a[160:163], 0
s_waitcnt lgkmcnt(0)
v_cndmask_b32_dpp v20, v56, v60, vcc quad_perm:[1,0,3,2] row_mask:0xf bank_mask:0xf
v_cndmask_b32_dpp v21, v57, v61, vcc quad_perm:[1,0,3,2] row_mask:0xf bank_mask:0xf
v_mfma_f32_16x16x32_bf16 a[0:3], v[52:55], a[200:203], a[0:3]
v_cndmask_b32_dpp v22, v58, v62, vcc quad_perm:[1,0,3,2] row_mask:0xf bank_mask:0xf
v_cndmask_b32_dpp v23, v59, v63, vcc quad_perm:[1,0,3,2] row_mask:0xf bank_mask:0xf
v_mfma_f32_16x16x32_bf16 a[16:19], v[16:19], a[164:167], 0
s_mov_b64 vcc, s[76:77]
v_cndmask_b32_dpp v56, v60, v56, vcc quad_perm:[1,0,3,2] row_mask:0xf bank_mask:0xf
v_cndmask_b32_dpp v57, v61, v57, vcc quad_perm:[1,0,3,2] row_mask:0xf bank_mask:0xf
v_mfma_f32_16x16x32_bf16 a[16:19], v[52:55], a[204:207], a[16:19]
v_cndmask_b32_dpp v58, v62, v58, vcc quad_perm:[1,0,3,2] row_mask:0xf bank_mask:0xf
v_cndmask_b32_dpp v59, v63, v59, vcc quad_perm:[1,0,3,2] row_mask:0xf bank_mask:0xf
v_mfma_f32_16x16x32_bf16 a[32:35], v[16:19], a[168:171], 0
v_mfma_f32_16x16x32_bf16 a[32:35], v[52:55], a[208:211], a[32:35]
v_mfma_f32_16x16x32_bf16 a[48:51], v[16:19], a[172:175], 0
v_mfma_f32_16x16x32_bf16 a[48:51], v[52:55], a[212:215], a[48:51]
v_mfma_f32_16x16x32_bf16 a[64:67], v[16:19], a[176:179], 0
v_mfma_f32_16x16x32_bf16 a[64:67], v[52:55], a[216:219], a[64:67]
v_mfma_f32_16x16x32_bf16 a[80:83], v[16:19], a[180:183], 0
v_mfma_f32_16x16x32_bf16 a[80:83], v[52:55], a[220:223], a[80:83]
v_mfma_f32_16x16x32_bf16 a[96:99], v[16:19], a[184:187], 0
v_mfma_f32_16x16x32_bf16 a[96:99], v[52:55], a[224:227], a[96:99]
v_mfma_f32_16x16x32_bf16 a[112:115], v[16:19], a[188:191], 0
v_mfma_f32_16x16x32_bf16 a[112:115], v[52:55], a[228:231], a[112:115]
v_mfma_f32_16x16x32_bf16 a[128:131], v[16:19], a[192:195], 0
v_mfma_f32_16x16x32_bf16 a[128:131], v[52:55], a[232:235], a[128:131]
v_mfma_f32_16x16x32_bf16 a[144:147], v[16:19], a[196:199], 0
v_mfma_f32_16x16x32_bf16 a[144:147], v[52:55], a[236:239], a[144:147]
v_mfma_f32_16x16x32_bf16 a[4:7], v[56:59], a[160:163], 0
s_waitcnt vmcnt(20)
v_cndmask_b32_dpp v16, v84, v80, vcc quad_perm:[1,0,3,2] row_mask:0xf bank_mask:0xf
v_cndmask_b32_dpp v17, v85, v81, vcc quad_perm:[1,0,3,2] row_mask:0xf bank_mask:0xf
v_mfma_f32_16x16x32_bf16 a[4:7], v[20:23], a[200:203], a[4:7]
v_cndmask_b32_dpp v18, v86, v82, vcc quad_perm:[1,0,3,2] row_mask:0xf bank_mask:0xf
v_cndmask_b32_dpp v19, v87, v83, vcc quad_perm:[1,0,3,2] row_mask:0xf bank_mask:0xf
v_mfma_f32_16x16x32_bf16 a[20:23], v[56:59], a[164:167], 0
s_mov_b64 vcc, s[78:79]
v_cndmask_b32_dpp v84, v80, v84, vcc quad_perm:[1,0,3,2] row_mask:0xf bank_mask:0xf
v_cndmask_b32_dpp v85, v81, v85, vcc quad_perm:[1,0,3,2] row_mask:0xf bank_mask:0xf
v_mfma_f32_16x16x32_bf16 a[20:23], v[20:23], a[204:207], a[20:23]
v_cndmask_b32_dpp v86, v82, v86, vcc quad_perm:[1,0,3,2] row_mask:0xf bank_mask:0xf
v_cndmask_b32_dpp v87, v83, v87, vcc quad_perm:[1,0,3,2] row_mask:0xf bank_mask:0xf
v_mfma_f32_16x16x32_bf16 a[36:39], v[56:59], a[168:171], 0
v_mfma_f32_16x16x32_bf16 a[36:39], v[20:23], a[208:211], a[36:39]
v_mfma_f32_16x16x32_bf16 a[52:55], v[56:59], a[172:175], 0
v_mfma_f32_16x16x32_bf16 a[52:55], v[20:23], a[212:215], a[52:55]
v_mfma_f32_16x16x32_bf16 a[68:71], v[56:59], a[176:179], 0
v_mfma_f32_16x16x32_bf16 a[68:71], v[20:23], a[216:219], a[68:71]
v_mfma_f32_16x16x32_bf16 a[84:87], v[56:59], a[180:183], 0
v_mfma_f32_16x16x32_bf16 a[84:87], v[20:23], a[220:223], a[84:87]
v_mfma_f32_16x16x32_bf16 a[100:103], v[56:59], a[184:187], 0
v_mfma_f32_16x16x32_bf16 a[100:103], v[20:23], a[224:227], a[100:103]
v_mfma_f32_16x16x32_bf16 a[116:119], v[56:59], a[188:191], 0
v_mfma_f32_16x16x32_bf16 a[116:119], v[20:23], a[228:231], a[116:119]
v_mfma_f32_16x16x32_bf16 a[132:135], v[56:59], a[192:195], 0
v_mfma_f32_16x16x32_bf16 a[132:135], v[20:23], a[232:235], a[132:135]
v_mfma_f32_16x16x32_bf16 a[148:151], v[56:59], a[196:199], 0
buffer_load_dwordx4 v[48:51], v4, s[36:39], s74 offen nt
v_mfma_f32_16x16x32_bf16 a[148:151], v[20:23], a[236:239], a[148:151]
buffer_load_dwordx4 v[52:55], v4, s[36:39], s74 offen offset:512 nt
v_mfma_f32_16x16x32_bf16 a[8:11], v[16:19], a[160:163], 0
v_cndmask_b32_dpp v20, v88, v92, vcc quad_perm:[1,0,3,2] row_mask:0xf bank_mask:0xf
v_cndmask_b32_dpp v21, v89, v93, vcc quad_perm:[1,0,3,2] row_mask:0xf bank_mask:0xf
v_mfma_f32_16x16x32_bf16 a[8:11], v[84:87], a[200:203], a[8:11]
v_cndmask_b32_dpp v22, v90, v94, vcc quad_perm:[1,0,3,2] row_mask:0xf bank_mask:0xf
v_cndmask_b32_dpp v23, v91, v95, vcc quad_perm:[1,0,3,2] row_mask:0xf bank_mask:0xf
v_mfma_f32_16x16x32_bf16 a[24:27], v[16:19], a[164:167], 0
s_mov_b64 vcc, s[76:77]
v_cndmask_b32_dpp v88, v92, v88, vcc quad_perm:[1,0,3,2] row_mask:0xf bank_mask:0xf
v_cndmask_b32_dpp v89, v93, v89, vcc quad_perm:[1,0,3,2] row_mask:0xf bank_mask:0xf
v_mfma_f32_16x16x32_bf16 a[24:27], v[84:87], a[204:207], a[24:27]
v_cndmask_b32_dpp v90, v94, v90, vcc quad_perm:[1,0,3,2] row_mask:0xf bank_mask:0xf
v_cndmask_b32_dpp v91, v95, v91, vcc quad_perm:[1,0,3,2] row_mask:0xf bank_mask:0xf
v_mfma_f32_16x16x32_bf16 a[40:43], v[16:19], a[168:171], 0
buffer_load_dwordx4 v[56:59], v4, s[36:39], s74 offen offset:2048 nt
v_mfma_f32_16x16x32_bf16 a[40:43], v[84:87], a[208:211], a[40:43]
buffer_load_dwordx4 v[60:63], v4, s[36:39], s74 offen offset:2560 nt
v_mfma_f32_16x16x32_bf16 a[56:59], v[16:19], a[172:175], 0
v_mfma_f32_16x16x32_bf16 a[56:59], v[84:87], a[212:215], a[56:59]
v_mfma_f32_16x16x32_bf16 a[72:75], v[16:19], a[176:179], 0
v_mfma_f32_16x16x32_bf16 a[72:75], v[84:87], a[216:219], a[72:75]
v_mfma_f32_16x16x32_bf16 a[88:91], v[16:19], a[180:183], 0
v_mfma_f32_16x16x32_bf16 a[88:91], v[84:87], a[220:223], a[88:91]
v_mfma_f32_16x16x32_bf16 a[104:107], v[16:19], a[184:187], 0
v_mfma_f32_16x16x32_bf16 a[104:107], v[84:87], a[224:227], a[104:107]
v_mfma_f32_16x16x32_bf16 a[120:123], v[16:19], a[188:191], 0
v_mfma_f32_16x16x32_bf16 a[120:123], v[84:87], a[228:231], a[120:123]
v_mfma_f32_16x16x32_bf16 a[136:139], v[16:19], a[192:195], 0
v_mfma_f32_16x16x32_bf16 a[136:139], v[84:87], a[232:235], a[136:139]
v_mfma_f32_16x16x32_bf16 a[152:155], v[16:19], a[196:199], 0
v_mfma_f32_16x16x32_bf16 a[152:155], v[84:87], a[236:239], a[152:155]
v_mfma_f32_16x16x32_bf16 a[12:15], v[88:91], a[160:163], 0
v_cndmask_b32_dpp v16, v68, v64, vcc quad_perm:[1,0,3,2] row_mask:0xf bank_mask:0xf
v_cndmask_b32_dpp v17, v69, v65, vcc quad_perm:[1,0,3,2] row_mask:0xf bank_mask:0xf
v_mfma_f32_16x16x32_bf16 a[12:15], v[20:23], a[200:203], a[12:15]
v_cndmask_b32_dpp v18, v70, v66, vcc quad_perm:[1,0,3,2] row_mask:0xf bank_mask:0xf
v_cndmask_b32_dpp v19, v71, v67, vcc quad_perm:[1,0,3,2] row_mask:0xf bank_mask:0xf
v_mfma_f32_16x16x32_bf16 a[28:31], v[88:91], a[164:167], 0
ds_read_b128 a[160:163], v1 offset:10240
s_mov_b64 vcc, s[78:79]
v_cndmask_b32_dpp v68, v64, v68, vcc quad_perm:[1,0,3,2] row_mask:0xf bank_mask:0xf
v_mfma_f32_16x16x32_bf16 a[28:31], v[20:23], a[204:207], a[28:31]
ds_read_b128 a[200:203], v1 offset:15360
v_cndmask_b32_dpp v69, v65, v69, vcc quad_perm:[1,0,3,2] row_mask:0xf bank_mask:0xf
)"
R"(v_mfma_f32_16x16x32_bf16 a[44:47], v[88:91], a[168:171], 0
ds_read_b128 a[164:167], v1 offset:11264
v_cndmask_b32_dpp v70, v66, v70, vcc quad_perm:[1,0,3,2] row_mask:0xf bank_mask:0xf
v_mfma_f32_16x16x32_bf16 a[44:47], v[20:23], a[208:211], a[44:47]
ds_read_b128 a[204:207], v1 offset:16384
v_cndmask_b32_dpp v71, v67, v71, vcc quad_perm:[1,0,3,2] row_mask:0xf bank_mask:0xf
v_mfma_f32_16x16x32_bf16 a[60:63], v[88:91], a[172:175], 0
ds_read_b128 a[168:171], v1 offset:12288
v_mfma_f32_16x16x32_bf16 a[60:63], v[20:23], a[212:215], a[60:63]
ds_read_b128 a[208:211], v1 offset:17408
v_mfma_f32_16x16x32_bf16 a[76:79], v[88:91], a[176:179], 0
ds_read_b128 a[172:175], v1 offset:13312
v_mfma_f32_16x16x32_bf16 a[76:79], v[20:23], a[216:219], a[76:79]
ds_read_b128 a[212:215], v1 offset:18432
v_mfma_f32_16x16x32_bf16 a[92:95], v[88:91], a[180:183], 0
ds_read_b128 a[176:179], v1 offset:14336
v_mfma_f32_16x16x32_bf16 a[92:95], v[20:23], a[220:223], a[92:95]
ds_read_b128 a[216:219], v1 offset:19456
v_mfma_f32_16x16x32_bf16 a[108:111], v[88:91], a[184:187], 0
ds_read_b128 a[180:183], v1 offset:51200
v_mfma_f32_16x16x32_bf16 a[108:111], v[20:23], a[224:227], a[108:111]
ds_read_b128 a[220:223], v1 offset:56320
v_mfma_f32_16x16x32_bf16 a[124:127], v[88:91], a[188:191], 0
ds_read_b128 a[184:187], v1 offset:52224
v_mfma_f32_16x16x32_bf16 a[124:127], v[20:23], a[228:231], a[124:127]
ds_read_b128 a[224:227], v1 offset:57344
v_mfma_f32_16x16x32_bf16 a[140:143], v[88:91], a[192:195], 0
s_waitcnt lgkmcnt(10)
ds_read_b128 a[188:191], v1 offset:53248
v_mfma_f32_16x16x32_bf16 a[140:143], v[20:23], a[232:235], a[140:143]
ds_read_b128 a[228:231], v1 offset:58368
v_mfma_f32_16x16x32_bf16 a[156:159], v[88:91], a[196:199], 0
buffer_load_dwordx4 v[80:83], v4, s[36:39], s74 offen offset:128 nt
ds_read_b128 a[192:195], v1 offset:54272
v_mfma_f32_16x16x32_bf16 a[156:159], v[20:23], a[236:239], a[156:159]
buffer_load_dwordx4 v[84:87], v4, s[36:39], s74 offen offset:640 nt
ds_read_b128 a[232:235], v1 offset:59392
v_mfma_f32_16x16x32_bf16 a[0:3], v[16:19], a[160:163], a[0:3]
s_waitcnt lgkmcnt(10)
ds_read_b128 a[196:199], v1 offset:55296
v_cndmask_b32_dpp v20, v72, v76, vcc quad_perm:[1,0,3,2] row_mask:0xf bank_mask:0xf
v_mfma_f32_16x16x32_bf16 a[0:3], v[68:71], a[200:203], a[0:3]
ds_read_b128 a[236:239], v1 offset:60416
v_cndmask_b32_dpp v21, v73, v77, vcc quad_perm:[1,0,3,2] row_mask:0xf bank_mask:0xf
v_mfma_f32_16x16x32_bf16 a[16:19], v[16:19], a[164:167], a[16:19]
v_cndmask_b32_dpp v22, v74, v78, vcc quad_perm:[1,0,3,2] row_mask:0xf bank_mask:0xf
v_cndmask_b32_dpp v23, v75, v79, vcc quad_perm:[1,0,3,2] row_mask:0xf bank_mask:0xf
v_mfma_f32_16x16x32_bf16 a[16:19], v[68:71], a[204:207], a[16:19]
s_mov_b64 vcc, s[76:77]
v_cndmask_b32_dpp v72, v76, v72, vcc quad_perm:[1,0,3,2] row_mask:0xf bank_mask:0xf
v_cndmask_b32_dpp v73, v77, v73, vcc quad_perm:[1,0,3,2] row_mask:0xf bank_mask:0xf
v_mfma_f32_16x16x32_bf16 a[32:35], v[16:19], a[168:171], a[32:35]
v_cndmask_b32_dpp v74, v78, v74, vcc quad_perm:[1,0,3,2] row_mask:0xf bank_mask:0xf
v_cndmask_b32_dpp v75, v79, v75, vcc quad_perm:[1,0,3,2] row_mask:0xf bank_mask:0xf
v_mfma_f32_16x16x32_bf16 a[32:35], v[68:71], a[208:211], a[32:35]
buffer_load_dwordx4 v[88:91], v4, s[36:39], s74 offen offset:2176 nt
v_mfma_f32_16x16x32_bf16 a[48:51], v[16:19], a[172:175], a[48:51]
buffer_load_dwordx4 v[92:95], v4, s[36:39], s74 offen offset:2688 nt
v_mfma_f32_16x16x32_bf16 a[48:51], v[68:71], a[212:215], a[48:51]
s_waitcnt lgkmcnt(11)
v_mfma_f32_16x16x32_bf16 a[64:67], v[16:19], a[176:179], a[64:67]
s_waitcnt lgkmcnt(10)
v_mfma_f32_16x16x32_bf16 a[64:67], v[68:71], a[216:219], a[64:67]
s_waitcnt lgkmcnt(9)
v_mfma_f32_16x16x32_bf16 a[80:83], v[16:19], a[180:183], a[80:83]
s_waitcnt lgkmcnt(8)
v_mfma_f32_16x16x32_bf16 a[80:83], v[68:71], a[220:223], a[80:83]
s_waitcnt lgkmcnt(7)
v_mfma_f32_16x16x32_bf16 a[96:99], v[16:19], a[184:187], a[96:99]
s_waitcnt lgkmcnt(6)
v_mfma_f32_16x16x32_bf16 a[96:99], v[68:71], a[224:227], a[96:99]
s_waitcnt lgkmcnt(5)
v_mfma_f32_16x16x32_bf16 a[112:115], v[16:19], a[188:191], a[112:115]
s_waitcnt lgkmcnt(4)
v_mfma_f32_16x16x32_bf16 a[112:115], v[68:71], a[228:231], a[112:115]
s_waitcnt lgkmcnt(3)
v_mfma_f32_16x16x32_bf16 a[128:131], v[16:19], a[192:195], a[128:131]
s_waitcnt lgkmcnt(2)
v_mfma_f32_16x16x32_bf16 a[128:131], v[68:71], a[232:235], a[128:131]
s_waitcnt lgkmcnt(1)
v_mfma_f32_16x16x32_bf16 a[144:147], v[16:19], a[196:199], a[144:147]
s_waitcnt lgkmcnt(0)
v_mfma_f32_16x16x32_bf16 a[144:147], v[68:71], a[236:239], a[144:147]
v_mfma_f32_16x16x32_bf16 a[4:7], v[72:75], a[160:163], a[4:7]
s_waitcnt vmcnt(24)
v_cndmask_b32_dpp v16, v244, v240, vcc quad_perm:[1,0,3,2] row_mask:0xf bank_mask:0xf
v_cndmask_b32_dpp v17, v245, v241, vcc quad_perm:[1,0,3,2] row_mask:0xf bank_mask:0xf
v_mfma_f32_16x16x32_bf16 a[4:7], v[20:23], a[200:203], a[4:7]
v_cndmask_b32_dpp v18, v246, v242, vcc quad_perm:[1,0,3,2] row_mask:0xf bank_mask:0xf
v_cndmask_b32_dpp v19, v247, v243, vcc quad_perm:[1,0,3,2] row_mask:0xf bank_mask:0xf
v_mfma_f32_16x16x32_bf16 a[20:23], v[72:75], a[164:167], a[20:23]
s_mov_b64 vcc, s[78:79]
v_cndmask_b32_dpp v244, v240, v244, vcc quad_perm:[1,0,3,2] row_mask:0xf bank_mask:0xf
v_cndmask_b32_dpp v245, v241, v245, vcc quad_perm:[1,0,3,2] row_mask:0xf bank_mask:0xf
v_mfma_f32_16x16x32_bf16 a[20:23], v[20:23], a[204:207], a[20:23]
v_cndmask_b32_dpp v246, v242, v246, vcc quad_perm:[1,0,3,2] row_mask:0xf bank_mask:0xf
v_cndmask_b32_dpp v247, v243, v247, vcc quad_perm:[1,0,3,2] row_mask:0xf bank_mask:0xf
v_mfma_f32_16x16x32_bf16 a[36:39], v[72:75], a[168:171], a[36:39]
v_mfma_f32_16x16x32_bf16 a[36:39], v[20:23], a[208:211], a[36:39]
v_mfma_f32_16x16x32_bf16 a[52:55], v[72:75], a[172:175], a[52:55]
v_mfma_f32_16x16x32_bf16 a[52:55], v[20:23], a[212:215], a[52:55]
v_mfma_f32_16x16x32_bf16 a[68:71], v[72:75], a[176:179], a[68:71]
v_mfma_f32_16x16x32_bf16 a[68:71], v[20:23], a[216:219], a[68:71]
v_mfma_f32_16x16x32_bf16 a[84:87], v[72:75], a[180:183], a[84:87]
v_mfma_f32_16x16x32_bf16 a[84:87], v[20:23], a[220:223], a[84:87]
v_mfma_f32_16x16x32_bf16 a[100:103], v[72:75], a[184:187], a[100:103]
v_mfma_f32_16x16x32_bf16 a[100:103], v[20:23], a[224:227], a[100:103]
v_mfma_f32_16x16x32_bf16 a[116:119], v[72:75], a[188:191], a[116:119]
v_mfma_f32_16x16x32_bf16 a[116:119], v[20:23], a[228:231], a[116:119]
v_mfma_f32_16x16x32_bf16 a[132:135], v[72:75], a[192:195], a[132:135]
v_mfma_f32_16x16x32_bf16 a[132:135], v[20:23], a[232:235], a[132:135]
v_mfma_f32_16x16x32_bf16 a[148:151], v[72:75], a[196:199], a[148:151]
buffer_load_dwordx4 v[64:67], v4, s[36:39], s75 offen nt
v_mfma_f32_16x16x32_bf16 a[148:151], v[20:23], a[236:239], a[148:151]
buffer_load_dwordx4 v[68:71], v4, s[36:39], s75 offen offset:512 nt
v_mfma_f32_16x16x32_bf16 a[8:11], v[16:19], a[160:163], a[8:11]
v_cndmask_b32_dpp v20, v248, v252, vcc quad_perm:[1,0,3,2] row_mask:0xf bank_mask:0xf
v_cndmask_b32_dpp v21, v249, v253, vcc quad_perm:[1,0,3,2] row_mask:0xf bank_mask:0xf
v_mfma_f32_16x16x32_bf16 a[8:11], v[244:247], a[200:203], a[8:11]
v_cndmask_b32_dpp v22, v250, v254, vcc quad_perm:[1,0,3,2] row_mask:0xf bank_mask:0xf
v_cndmask_b32_dpp v23, v251, v255, vcc quad_perm:[1,0,3,2] row_mask:0xf bank_mask:0xf
v_mfma_f32_16x16x32_bf16 a[24:27], v[16:19], a[164:167], a[24:27]
s_mov_b64 vcc, s[76:77]
v_cndmask_b32_dpp v248, v252, v248, vcc quad_perm:[1,0,3,2] row_mask:0xf bank_mask:0xf
v_cndmask_b32_dpp v249, v253, v249, vcc quad_perm:[1,0,3,2] row_mask:0xf bank_mask:0xf
v_mfma_f32_16x16x32_bf16 a[24:27], v[244:247], a[204:207], a[24:27]
v_cndmask_b32_dpp v250, v254, v250, vcc quad_perm:[1,0,3,2] row_mask:0xf bank_mask:0xf
v_cndmask_b32_dpp v251, v255, v251, vcc quad_perm:[1,0,3,2] row_mask:0xf bank_mask:0xf
v_mfma_f32_16x16x32_bf16 a[40:43], v[16:19], a[168:171], a[40:43]
buffer_load_dwordx4 v[72:75], v4, s[36:39], s75 offen offset:2048 nt
v_mfma_f32_16x16x32_bf16 a[40:43], v[244:247], a[208:211], a[40:43]
buffer_load_dwordx4 v[76:79], v4, s[36:39], s75 offen offset:2560 nt
v_mfma_f32_16x16x32_bf16 a[56:59], v[16:19], a[172:175], a[56:59]
v_mfma_f32_16x16x32_bf16 a[56:59], v[244:247], a[212:215], a[56:59]
v_mfma_f32_16x16x32_bf16 a[72:75], v[16:19], a[176:179], a[72:75]
v_mfma_f32_16x16x32_bf16 a[72:75], v[244:247], a[216:219], a[72:75]
v_mfma_f32_16x16x32_bf16 a[88:91], v[16:19], a[180:183], a[88:91]
v_mfma_f32_16x16x32_bf16 a[88:91], v[244:247], a[220:223], a[88:91]
v_mfma_f32_16x16x32_bf16 a[104:107], v[16:19], a[184:187], a[104:107]
v_mfma_f32_16x16x32_bf16 a[104:107], v[244:247], a[224:227], a[104:107]
v_mfma_f32_16x16x32_bf16 a[120:123], v[16:19], a[188:191], a[120:123]
v_mfma_f32_16x16x32_bf16 a[120:123], v[244:247], a[228:231], a[120:123]
v_mfma_f32_16x16x32_bf16 a[136:139], v[16:19], a[192:195], a[136:139]
v_mfma_f32_16x16x32_bf16 a[136:139], v[244:247], a[232:235], a[136:139]
v_mfma_f32_16x16x32_bf16 a[152:155], v[16:19], a[196:199], a[152:155]
v_mfma_f32_16x16x32_bf16 a[152:155], v[244:247], a[236:239], a[152:155]
v_mfma_f32_16x16x32_bf16 a[12:15], v[248:251], a[160:163], a[12:15]
s_waitcnt vmcnt(24)
v_cndmask_b32_dpp v16, v180, v176, vcc quad_perm:[1,0,3,2] row_mask:0xf bank_mask:0xf
v_cndmask_b32_dpp v17, v181, v177, vcc quad_perm:[1,0,3,2] row_mask:0xf bank_mask:0xf
v_mfma_f32_16x16x32_bf16 a[12:15], v[20:23], a[200:203], a[12:15]
v_cndmask_b32_dpp v18, v182, v178, vcc quad_perm:[1,0,3,2] row_mask:0xf bank_mask:0xf
v_cndmask_b32_dpp v19, v183, v179, vcc quad_perm:[1,0,3,2] row_mask:0xf bank_mask:0xf
v_mfma_f32_16x16x32_bf16 a[28:31], v[248:251], a[164:167], a[28:31]
ds_read_b128 a[160:163], v1 offset:20480
s_mov_b64 vcc, s[78:79]
v_cndmask_b32_dpp v180, v176, v180, vcc quad_perm:[1,0,3,2] row_mask:0xf bank_mask:0xf
v_mfma_f32_16x16x32_bf16 a[28:31], v[20:23], a[204:207], a[28:31]
ds_read_b128 a[200:203], v1 offset:25600
v_cndmask_b32_dpp v181, v177, v181, vcc quad_perm:[1,0,3,2] row_mask:0xf bank_mask:0xf
v_mfma_f32_16x16x32_bf16 a[44:47], v[248:251], a[168:171], a[44:47]
ds_read_b128 a[164:167], v1 offset:21504
v_cndmask_b32_dpp v182, v178, v182, vcc quad_perm:[1,0,3,2] row_mask:0xf bank_mask:0xf
v_mfma_f32_16x16x32_bf16 a[44:47], v[20:23], a[208:211], a[44:47]
ds_read_b128 a[204:207], v1 offset:26624
v_cndmask_b32_dpp v183, v179, v183, vcc quad_perm:[1,0,3,2] row_mask:0xf bank_mask:0xf
v_mfma_f32_16x16x32_bf16 a[60:63], v[248:251], a[172:175], a[60:63]
ds_read_b128 a[168:171], v1 offset:22528
v_mfma_f32_16x16x32_bf16 a[60:63], v[20:23], a[212:215], a[60:63]
ds_read_b128 a[208:211], v1 offset:27648
v_mfma_f32_16x16x32_bf16 a[76:79], v[248:251], a[176:179], a[76:79]
ds_read_b128 a[172:175], v1 offset:23552
v_mfma_f32_16x16x32_bf16 a[76:79], v[20:23], a[216:219], a[76:79]
ds_read_b128 a[212:215], v1 offset:28672
v_mfma_f32_16x16x32_bf16 a[92:95], v[248:251], a[180:183], a[92:95]
ds_read_b128 a[176:179], v1 offset:24576
v_mfma_f32_16x16x32_bf16 a[92:95], v[20:23], a[220:223], a[92:95]
ds_read_b128 a[216:219], v1 offset:29696
v_mfma_f32_16x16x32_bf16 a[108:111], v[248:251], a[184:187], a[108:111]
ds_read_b128 a[180:183], v1 offset:61440
v_mfma_f32_16x16x32_bf16 a[108:111], v[20:23], a[224:227], a[108:111]
ds_read_b128 a[220:223], v2 offset:1024
v_mfma_f32_16x16x32_bf16 a[124:127], v[248:251], a[188:191], a[124:127]
ds_read_b128 a[184:187], v1 offset:62464
v_mfma_f32_16x16x32_bf16 a[124:127], v[20:23], a[228:231], a[124:127]
ds_read_b128 a[224:227], v2 offset:2048
v_mfma_f32_16x16x32_bf16 a[140:143], v[248:251], a[192:195], a[140:143]
s_waitcnt lgkmcnt(10)
ds_read_b128 a[188:191], v1 offset:63488
v_mfma_f32_16x16x32_bf16 a[140:143], v[20:23], a[232:235], a[140:143]
ds_read_b128 a[228:231], v2 offset:3072
v_mfma_f32_16x16x32_bf16 a[156:159], v[248:251], a[196:199], a[156:159]
ds_read_b128 a[192:195], v1 offset:64512
v_mfma_f32_16x16x32_bf16 a[156:159], v[20:23], a[236:239], a[156:159]
ds_read_b128 a[232:235], v2 offset:4096
v_mfma_f32_16x16x32_bf16 a[0:3], v[16:19], a[160:163], a[0:3]
s_waitcnt lgkmcnt(10)
ds_read_b128 a[196:199], v2 offset:0
v_cndmask_b32_dpp v20, v184, v188, vcc quad_perm:[1,0,3,2] row_mask:0xf bank_mask:0xf
v_mfma_f32_16x16x32_bf16 a[0:3], v[180:183], a[200:203], a[0:3]
)"
R"(ds_read_b128 a[236:239], v2 offset:5120
v_cndmask_b32_dpp v21, v185, v189, vcc quad_perm:[1,0,3,2] row_mask:0xf bank_mask:0xf
v_mfma_f32_16x16x32_bf16 a[16:19], v[16:19], a[164:167], a[16:19]
v_cndmask_b32_dpp v22, v186, v190, vcc quad_perm:[1,0,3,2] row_mask:0xf bank_mask:0xf
v_cndmask_b32_dpp v23, v187, v191, vcc quad_perm:[1,0,3,2] row_mask:0xf bank_mask:0xf
v_mfma_f32_16x16x32_bf16 a[16:19], v[180:183], a[204:207], a[16:19]
s_mov_b64 vcc, s[76:77]
v_cndmask_b32_dpp v184, v188, v184, vcc quad_perm:[1,0,3,2] row_mask:0xf bank_mask:0xf
v_cndmask_b32_dpp v185, v189, v185, vcc quad_perm:[1,0,3,2] row_mask:0xf bank_mask:0xf
v_mfma_f32_16x16x32_bf16 a[32:35], v[16:19], a[168:171], a[32:35]
v_cndmask_b32_dpp v186, v190, v186, vcc quad_perm:[1,0,3,2] row_mask:0xf bank_mask:0xf
v_cndmask_b32_dpp v187, v191, v187, vcc quad_perm:[1,0,3,2] row_mask:0xf bank_mask:0xf
v_mfma_f32_16x16x32_bf16 a[32:35], v[180:183], a[208:211], a[32:35]
v_mfma_f32_16x16x32_bf16 a[48:51], v[16:19], a[172:175], a[48:51]
v_mfma_f32_16x16x32_bf16 a[48:51], v[180:183], a[212:215], a[48:51]
s_waitcnt lgkmcnt(11)
v_mfma_f32_16x16x32_bf16 a[64:67], v[16:19], a[176:179], a[64:67]
s_waitcnt lgkmcnt(10)
v_mfma_f32_16x16x32_bf16 a[64:67], v[180:183], a[216:219], a[64:67]
s_waitcnt lgkmcnt(9)
v_mfma_f32_16x16x32_bf16 a[80:83], v[16:19], a[180:183], a[80:83]
s_waitcnt lgkmcnt(8)
v_mfma_f32_16x16x32_bf16 a[80:83], v[180:183], a[220:223], a[80:83]
s_waitcnt lgkmcnt(7)
v_mfma_f32_16x16x32_bf16 a[96:99], v[16:19], a[184:187], a[96:99]
s_waitcnt lgkmcnt(6)
v_mfma_f32_16x16x32_bf16 a[96:99], v[180:183], a[224:227], a[96:99]
s_waitcnt lgkmcnt(5)
v_mfma_f32_16x16x32_bf16 a[112:115], v[16:19], a[188:191], a[112:115]
s_waitcnt lgkmcnt(4)
v_mfma_f32_16x16x32_bf16 a[112:115], v[180:183], a[228:231], a[112:115]
s_waitcnt lgkmcnt(3)
v_mfma_f32_16x16x32_bf16 a[128:131], v[16:19], a[192:195], a[128:131]
s_waitcnt lgkmcnt(2)
v_mfma_f32_16x16x32_bf16 a[128:131], v[180:183], a[232:235], a[128:131]
s_waitcnt lgkmcnt(1)
v_mfma_f32_16x16x32_bf16 a[144:147], v[16:19], a[196:199], a[144:147]
s_waitcnt lgkmcnt(0)
v_mfma_f32_16x16x32_bf16 a[144:147], v[180:183], a[236:239], a[144:147]
v_mfma_f32_16x16x32_bf16 a[4:7], v[184:187], a[160:163], a[4:7]
s_waitcnt vmcnt(16)
v_cndmask_b32_dpp v16, v36, v32, vcc quad_perm:[1,0,3,2] row_mask:0xf bank_mask:0xf
v_cndmask_b32_dpp v17, v37, v33, vcc quad_perm:[1,0,3,2] row_mask:0xf bank_mask:0xf
v_mfma_f32_16x16x32_bf16 a[4:7], v[20:23], a[200:203], a[4:7]
v_cndmask_b32_dpp v18, v38, v34, vcc quad_perm:[1,0,3,2] row_mask:0xf bank_mask:0xf
v_cndmask_b32_dpp v19, v39, v35, vcc quad_perm:[1,0,3,2] row_mask:0xf bank_mask:0xf
v_mfma_f32_16x16x32_bf16 a[20:23], v[184:187], a[164:167], a[20:23]
s_mov_b64 vcc, s[78:79]
v_cndmask_b32_dpp v36, v32, v36, vcc quad_perm:[1,0,3,2] row_mask:0xf bank_mask:0xf
v_cndmask_b32_dpp v37, v33, v37, vcc quad_perm:[1,0,3,2] row_mask:0xf bank_mask:0xf
v_mfma_f32_16x16x32_bf16 a[20:23], v[20:23], a[204:207], a[20:23]
v_cndmask_b32_dpp v38, v34, v38, vcc quad_perm:[1,0,3,2] row_mask:0xf bank_mask:0xf
v_cndmask_b32_dpp v39, v35, v39, vcc quad_perm:[1,0,3,2] row_mask:0xf bank_mask:0xf
v_mfma_f32_16x16x32_bf16 a[36:39], v[184:187], a[168:171], a[36:39]
v_mfma_f32_16x16x32_bf16 a[36:39], v[20:23], a[208:211], a[36:39]
v_mfma_f32_16x16x32_bf16 a[52:55], v[184:187], a[172:175], a[52:55]
v_mfma_f32_16x16x32_bf16 a[52:55], v[20:23], a[212:215], a[52:55]
v_mfma_f32_16x16x32_bf16 a[68:71], v[184:187], a[176:179], a[68:71]
v_mfma_f32_16x16x32_bf16 a[68:71], v[20:23], a[216:219], a[68:71]
v_mfma_f32_16x16x32_bf16 a[84:87], v[184:187], a[180:183], a[84:87]
v_mfma_f32_16x16x32_bf16 a[84:87], v[20:23], a[220:223], a[84:87]
v_mfma_f32_16x16x32_bf16 a[100:103], v[184:187], a[184:187], a[100:103]
v_mfma_f32_16x16x32_bf16 a[100:103], v[20:23], a[224:227], a[100:103]
v_mfma_f32_16x16x32_bf16 a[116:119], v[184:187], a[188:191], a[116:119]
v_mfma_f32_16x16x32_bf16 a[116:119], v[20:23], a[228:231], a[116:119]
v_mfma_f32_16x16x32_bf16 a[132:135], v[184:187], a[192:195], a[132:135]
v_mfma_f32_16x16x32_bf16 a[132:135], v[20:23], a[232:235], a[132:135]
v_mfma_f32_16x16x32_bf16 a[148:151], v[184:187], a[196:199], a[148:151]
v_mfma_f32_16x16x32_bf16 a[148:151], v[20:23], a[236:239], a[148:151]
v_mfma_f32_16x16x32_bf16 a[8:11], v[16:19], a[160:163], a[8:11]
v_cndmask_b32_dpp v20, v40, v44, vcc quad_perm:[1,0,3,2] row_mask:0xf bank_mask:0xf
v_cndmask_b32_dpp v21, v41, v45, vcc quad_perm:[1,0,3,2] row_mask:0xf bank_mask:0xf
v_mfma_f32_16x16x32_bf16 a[8:11], v[36:39], a[200:203], a[8:11]
v_cndmask_b32_dpp v22, v42, v46, vcc quad_perm:[1,0,3,2] row_mask:0xf bank_mask:0xf
v_cndmask_b32_dpp v23, v43, v47, vcc quad_perm:[1,0,3,2] row_mask:0xf bank_mask:0xf
v_mfma_f32_16x16x32_bf16 a[24:27], v[16:19], a[164:167], a[24:27]
s_mov_b64 vcc, s[76:77]
v_cndmask_b32_dpp v40, v44, v40, vcc quad_perm:[1,0,3,2] row_mask:0xf bank_mask:0xf
v_cndmask_b32_dpp v41, v45, v41, vcc quad_perm:[1,0,3,2] row_mask:0xf bank_mask:0xf
v_mfma_f32_16x16x32_bf16 a[24:27], v[36:39], a[204:207], a[24:27]
v_cndmask_b32_dpp v42, v46, v42, vcc quad_perm:[1,0,3,2] row_mask:0xf bank_mask:0xf
v_cndmask_b32_dpp v43, v47, v43, vcc quad_perm:[1,0,3,2] row_mask:0xf bank_mask:0xf
v_mfma_f32_16x16x32_bf16 a[40:43], v[16:19], a[168:171], a[40:43]
v_mfma_f32_16x16x32_bf16 a[40:43], v[36:39], a[208:211], a[40:43]
v_mfma_f32_16x16x32_bf16 a[56:59], v[16:19], a[172:175], a[56:59]
v_mfma_f32_16x16x32_bf16 a[56:59], v[36:39], a[212:215], a[56:59]
v_mfma_f32_16x16x32_bf16 a[72:75], v[16:19], a[176:179], a[72:75]
v_mfma_f32_16x16x32_bf16 a[72:75], v[36:39], a[216:219], a[72:75]
v_mfma_f32_16x16x32_bf16 a[88:91], v[16:19], a[180:183], a[88:91]
v_mfma_f32_16x16x32_bf16 a[88:91], v[36:39], a[220:223], a[88:91]
v_mfma_f32_16x16x32_bf16 a[104:107], v[16:19], a[184:187], a[104:107]
v_mfma_f32_16x16x32_bf16 a[104:107], v[36:39], a[224:227], a[104:107]
v_mfma_f32_16x16x32_bf16 a[120:123], v[16:19], a[188:191], a[120:123]
v_mfma_f32_16x16x32_bf16 a[120:123], v[36:39], a[228:231], a[120:123]
v_mfma_f32_16x16x32_bf16 a[136:139], v[16:19], a[192:195], a[136:139]
v_mfma_f32_16x16x32_bf16 a[136:139], v[36:39], a[232:235], a[136:139]
v_mfma_f32_16x16x32_bf16 a[152:155], v[16:19], a[196:199], a[152:155]
v_mfma_f32_16x16x32_bf16 a[152:155], v[36:39], a[236:239], a[152:155]
v_mfma_f32_16x16x32_bf16 a[12:15], v[40:43], a[160:163], a[12:15]
v_cndmask_b32_dpp v16, v196, v192, vcc quad_perm:[1,0,3,2] row_mask:0xf bank_mask:0xf
v_cndmask_b32_dpp v17, v197, v193, vcc quad_perm:[1,0,3,2] row_mask:0xf bank_mask:0xf
v_mfma_f32_16x16x32_bf16 a[12:15], v[20:23], a[200:203], a[12:15]
v_cndmask_b32_dpp v18, v198, v194, vcc quad_perm:[1,0,3,2] row_mask:0xf bank_mask:0xf
v_cndmask_b32_dpp v19, v199, v195, vcc quad_perm:[1,0,3,2] row_mask:0xf bank_mask:0xf
v_mfma_f32_16x16x32_bf16 a[28:31], v[40:43], a[164:167], a[28:31]
ds_read_b128 a[160:163], v1 offset:30720
s_mov_b64 vcc, s[78:79]
v_cndmask_b32_dpp v196, v192, v196, vcc quad_perm:[1,0,3,2] row_mask:0xf bank_mask:0xf
v_mfma_f32_16x16x32_bf16 a[28:31], v[20:23], a[204:207], a[28:31]
ds_read_b128 a[200:203], v1 offset:35840
v_cndmask_b32_dpp v197, v193, v197, vcc quad_perm:[1,0,3,2] row_mask:0xf bank_mask:0xf
v_mfma_f32_16x16x32_bf16 a[44:47], v[40:43], a[168:171], a[44:47]
ds_read_b128 a[164:167], v1 offset:31744
v_cndmask_b32_dpp v198, v194, v198, vcc quad_perm:[1,0,3,2] row_mask:0xf bank_mask:0xf
v_mfma_f32_16x16x32_bf16 a[44:47], v[20:23], a[208:211], a[44:47]
ds_read_b128 a[204:207], v1 offset:36864
v_cndmask_b32_dpp v199, v195, v199, vcc quad_perm:[1,0,3,2] row_mask:0xf bank_mask:0xf
v_mfma_f32_16x16x32_bf16 a[60:63], v[40:43], a[172:175], a[60:63]
ds_read_b128 a[168:171], v1 offset:32768
v_mfma_f32_16x16x32_bf16 a[60:63], v[20:23], a[212:215], a[60:63]
ds_read_b128 a[208:211], v1 offset:37888
v_mfma_f32_16x16x32_bf16 a[76:79], v[40:43], a[176:179], a[76:79]
ds_read_b128 a[172:175], v1 offset:33792
v_mfma_f32_16x16x32_bf16 a[76:79], v[20:23], a[216:219], a[76:79]
ds_read_b128 a[212:215], v1 offset:38912
v_mfma_f32_16x16x32_bf16 a[92:95], v[40:43], a[180:183], a[92:95]
ds_read_b128 a[176:179], v1 offset:34816
v_mfma_f32_16x16x32_bf16 a[92:95], v[20:23], a[220:223], a[92:95]
ds_read_b128 a[216:219], v1 offset:39936
v_mfma_f32_16x16x32_bf16 a[108:111], v[40:43], a[184:187], a[108:111]
ds_read_b128 a[180:183], v2 offset:6144
v_mfma_f32_16x16x32_bf16 a[108:111], v[20:23], a[224:227], a[108:111]
ds_read_b128 a[220:223], v2 offset:11264
v_mfma_f32_16x16x32_bf16 a[124:127], v[40:43], a[188:191], a[124:127]
ds_read_b128 a[184:187], v2 offset:7168
v_mfma_f32_16x16x32_bf16 a[124:127], v[20:23], a[228:231], a[124:127]
ds_read_b128 a[224:227], v2 offset:12288
v_mfma_f32_16x16x32_bf16 a[140:143], v[40:43], a[192:195], a[140:143]
s_waitcnt lgkmcnt(10)
ds_read_b128 a[188:191], v2 offset:8192
v_mfma_f32_16x16x32_bf16 a[140:143], v[20:23], a[232:235], a[140:143]
ds_read_b128 a[228:231], v2 offset:13312
v_mfma_f32_16x16x32_bf16 a[156:159], v[40:43], a[196:199], a[156:159]
buffer_load_dwordx4 v[32:35], v4, s[36:39], s75 offen offset:128 nt
ds_read_b128 a[192:195], v2 offset:9216
v_mfma_f32_16x16x32_bf16 a[156:159], v[20:23], a[236:239], a[156:159]
buffer_load_dwordx4 v[36:39], v4, s[36:39], s75 offen offset:640 nt
ds_read_b128 a[232:235], v2 offset:14336
v_mfma_f32_16x16x32_bf16 a[0:3], v[16:19], a[160:163], a[0:3]
s_waitcnt lgkmcnt(10)
ds_read_b128 a[196:199], v2 offset:10240
v_cndmask_b32_dpp v20, v200, v204, vcc quad_perm:[1,0,3,2] row_mask:0xf bank_mask:0xf
v_mfma_f32_16x16x32_bf16 a[0:3], v[196:199], a[200:203], a[0:3]
ds_read_b128 a[236:239], v2 offset:15360
v_cndmask_b32_dpp v21, v201, v205, vcc quad_perm:[1,0,3,2] row_mask:0xf bank_mask:0xf
v_mfma_f32_16x16x32_bf16 a[16:19], v[16:19], a[164:167], a[16:19]
v_cndmask_b32_dpp v22, v202, v206, vcc quad_perm:[1,0,3,2] row_mask:0xf bank_mask:0xf
v_cndmask_b32_dpp v23, v203, v207, vcc quad_perm:[1,0,3,2] row_mask:0xf bank_mask:0xf
v_mfma_f32_16x16x32_bf16 a[16:19], v[196:199], a[204:207], a[16:19]
s_mov_b64 vcc, s[76:77]
v_cndmask_b32_dpp v200, v204, v200, vcc quad_perm:[1,0,3,2] row_mask:0xf bank_mask:0xf
v_cndmask_b32_dpp v201, v205, v201, vcc quad_perm:[1,0,3,2] row_mask:0xf bank_mask:0xf
v_mfma_f32_16x16x32_bf16 a[32:35], v[16:19], a[168:171], a[32:35]
v_cndmask_b32_dpp v202, v206, v202, vcc quad_perm:[1,0,3,2] row_mask:0xf bank_mask:0xf
v_cndmask_b32_dpp v203, v207, v203, vcc quad_perm:[1,0,3,2] row_mask:0xf bank_mask:0xf
v_mfma_f32_16x16x32_bf16 a[32:35], v[196:199], a[208:211], a[32:35]
buffer_load_dwordx4 v[40:43], v4, s[36:39], s75 offen offset:2176 nt
v_mfma_f32_16x16x32_bf16 a[48:51], v[16:19], a[172:175], a[48:51]
buffer_load_dwordx4 v[44:47], v4, s[36:39], s75 offen offset:2688 nt
v_mfma_f32_16x16x32_bf16 a[48:51], v[196:199], a[212:215], a[48:51]
s_waitcnt lgkmcnt(11)
v_mfma_f32_16x16x32_bf16 a[64:67], v[16:19], a[176:179], a[64:67]
s_waitcnt lgkmcnt(10)
v_mfma_f32_16x16x32_bf16 a[64:67], v[196:199], a[216:219], a[64:67]
s_waitcnt lgkmcnt(9)
v_mfma_f32_16x16x32_bf16 a[80:83], v[16:19], a[180:183], a[80:83]
s_waitcnt lgkmcnt(8)
v_mfma_f32_16x16x32_bf16 a[80:83], v[196:199], a[220:223], a[80:83]
s_waitcnt lgkmcnt(7)
v_mfma_f32_16x16x32_bf16 a[96:99], v[16:19], a[184:187], a[96:99]
s_waitcnt lgkmcnt(6)
v_mfma_f32_16x16x32_bf16 a[96:99], v[196:199], a[224:227], a[96:99]
s_waitcnt lgkmcnt(5)
v_mfma_f32_16x16x32_bf16 a[112:115], v[16:19], a[188:191], a[112:115]
s_waitcnt lgkmcnt(4)
v_mfma_f32_16x16x32_bf16 a[112:115], v[196:199], a[228:231], a[112:115]
s_waitcnt lgkmcnt(3)
v_mfma_f32_16x16x32_bf16 a[128:131], v[16:19], a[192:195], a[128:131]
s_waitcnt lgkmcnt(2)
v_mfma_f32_16x16x32_bf16 a[128:131], v[196:199], a[232:235], a[128:131]
s_waitcnt lgkmcnt(1)
v_mfma_f32_16x16x32_bf16 a[144:147], v[16:19], a[196:199], a[144:147]
s_waitcnt lgkmcnt(0)
v_mfma_f32_16x16x32_bf16 a[144:147], v[196:199], a[236:239], a[144:147]
v_mfma_f32_16x16x32_bf16 a[4:7], v[200:203], a[160:163], a[4:7]
s_waitcnt vmcnt(16)
v_cndmask_b32_dpp v16, v212, v208, vcc quad_perm:[1,0,3,2] row_mask:0xf bank_mask:0xf
v_cndmask_b32_dpp v17, v213, v209, vcc quad_perm:[1,0,3,2] row_mask:0xf bank_mask:0xf
v_mfma_f32_16x16x32_bf16 a[4:7], v[20:23], a[200:203], a[4:7]
v_cndmask_b32_dpp v18, v214, v210, vcc quad_perm:[1,0,3,2] row_mask:0xf bank_mask:0xf
)"
R"(v_cndmask_b32_dpp v19, v215, v211, vcc quad_perm:[1,0,3,2] row_mask:0xf bank_mask:0xf
v_mfma_f32_16x16x32_bf16 a[20:23], v[200:203], a[164:167], a[20:23]
s_mov_b64 vcc, s[78:79]
v_cndmask_b32_dpp v212, v208, v212, vcc quad_perm:[1,0,3,2] row_mask:0xf bank_mask:0xf
v_cndmask_b32_dpp v213, v209, v213, vcc quad_perm:[1,0,3,2] row_mask:0xf bank_mask:0xf
v_mfma_f32_16x16x32_bf16 a[20:23], v[20:23], a[204:207], a[20:23]
v_cndmask_b32_dpp v214, v210, v214, vcc quad_perm:[1,0,3,2] row_mask:0xf bank_mask:0xf
v_cndmask_b32_dpp v215, v211, v215, vcc quad_perm:[1,0,3,2] row_mask:0xf bank_mask:0xf
v_mfma_f32_16x16x32_bf16 a[36:39], v[200:203], a[168:171], a[36:39]
v_mfma_f32_16x16x32_bf16 a[36:39], v[20:23], a[208:211], a[36:39]
v_mfma_f32_16x16x32_bf16 a[52:55], v[200:203], a[172:175], a[52:55]
v_mfma_f32_16x16x32_bf16 a[52:55], v[20:23], a[212:215], a[52:55]
v_mfma_f32_16x16x32_bf16 a[68:71], v[200:203], a[176:179], a[68:71]
v_mfma_f32_16x16x32_bf16 a[68:71], v[20:23], a[216:219], a[68:71]
v_mfma_f32_16x16x32_bf16 a[84:87], v[200:203], a[180:183], a[84:87]
v_mfma_f32_16x16x32_bf16 a[84:87], v[20:23], a[220:223], a[84:87]
v_mfma_f32_16x16x32_bf16 a[100:103], v[200:203], a[184:187], a[100:103]
v_mfma_f32_16x16x32_bf16 a[100:103], v[20:23], a[224:227], a[100:103]
v_mfma_f32_16x16x32_bf16 a[116:119], v[200:203], a[188:191], a[116:119]
v_mfma_f32_16x16x32_bf16 a[116:119], v[20:23], a[228:231], a[116:119]
v_mfma_f32_16x16x32_bf16 a[132:135], v[200:203], a[192:195], a[132:135]
v_mfma_f32_16x16x32_bf16 a[132:135], v[20:23], a[232:235], a[132:135]
v_mfma_f32_16x16x32_bf16 a[148:151], v[200:203], a[196:199], a[148:151]
v_mfma_f32_16x16x32_bf16 a[148:151], v[20:23], a[236:239], a[148:151]
v_mfma_f32_16x16x32_bf16 a[8:11], v[16:19], a[160:163], a[8:11]
v_cndmask_b32_dpp v20, v216, v220, vcc quad_perm:[1,0,3,2] row_mask:0xf bank_mask:0xf
v_cndmask_b32_dpp v21, v217, v221, vcc quad_perm:[1,0,3,2] row_mask:0xf bank_mask:0xf
v_mfma_f32_16x16x32_bf16 a[8:11], v[212:215], a[200:203], a[8:11]
v_cndmask_b32_dpp v22, v218, v222, vcc quad_perm:[1,0,3,2] row_mask:0xf bank_mask:0xf
v_cndmask_b32_dpp v23, v219, v223, vcc quad_perm:[1,0,3,2] row_mask:0xf bank_mask:0xf
v_mfma_f32_16x16x32_bf16 a[24:27], v[16:19], a[164:167], a[24:27]
s_mov_b64 vcc, s[76:77]
v_cndmask_b32_dpp v216, v220, v216, vcc quad_perm:[1,0,3,2] row_mask:0xf bank_mask:0xf
v_cndmask_b32_dpp v217, v221, v217, vcc quad_perm:[1,0,3,2] row_mask:0xf bank_mask:0xf
v_mfma_f32_16x16x32_bf16 a[24:27], v[212:215], a[204:207], a[24:27]
v_cndmask_b32_dpp v218, v222, v218, vcc quad_perm:[1,0,3,2] row_mask:0xf bank_mask:0xf
v_cndmask_b32_dpp v219, v223, v219, vcc quad_perm:[1,0,3,2] row_mask:0xf bank_mask:0xf
v_mfma_f32_16x16x32_bf16 a[40:43], v[16:19], a[168:171], a[40:43]
v_mfma_f32_16x16x32_bf16 a[40:43], v[212:215], a[208:211], a[40:43]
v_mfma_f32_16x16x32_bf16 a[56:59], v[16:19], a[172:175], a[56:59]
v_mfma_f32_16x16x32_bf16 a[56:59], v[212:215], a[212:215], a[56:59]
v_mfma_f32_16x16x32_bf16 a[72:75], v[16:19], a[176:179], a[72:75]
v_mfma_f32_16x16x32_bf16 a[72:75], v[212:215], a[216:219], a[72:75]
v_mfma_f32_16x16x32_bf16 a[88:91], v[16:19], a[180:183], a[88:91]
v_mfma_f32_16x16x32_bf16 a[88:91], v[212:215], a[220:223], a[88:91]
v_mfma_f32_16x16x32_bf16 a[104:107], v[16:19], a[184:187], a[104:107]
v_mfma_f32_16x16x32_bf16 a[104:107], v[212:215], a[224:227], a[104:107]
v_mfma_f32_16x16x32_bf16 a[120:123], v[16:19], a[188:191], a[120:123]
v_mfma_f32_16x16x32_bf16 a[120:123], v[212:215], a[228:231], a[120:123]
v_mfma_f32_16x16x32_bf16 a[136:139], v[16:19], a[192:195], a[136:139]
v_mfma_f32_16x16x32_bf16 a[136:139], v[212:215], a[232:235], a[136:139]
v_mfma_f32_16x16x32_bf16 a[152:155], v[16:19], a[196:199], a[152:155]
v_mfma_f32_16x16x32_bf16 a[152:155], v[212:215], a[236:239], a[152:155]
v_mfma_f32_16x16x32_bf16 a[12:15], v[216:219], a[160:163], a[12:15]
s_waitcnt vmcnt(12)
v_cndmask_b32_dpp v16, v52, v48, vcc quad_perm:[1,0,3,2] row_mask:0xf bank_mask:0xf
v_cndmask_b32_dpp v17, v53, v49, vcc quad_perm:[1,0,3,2] row_mask:0xf bank_mask:0xf
v_mfma_f32_16x16x32_bf16 a[12:15], v[20:23], a[200:203], a[12:15]
v_cndmask_b32_dpp v18, v54, v50, vcc quad_perm:[1,0,3,2] row_mask:0xf bank_mask:0xf
v_cndmask_b32_dpp v19, v55, v51, vcc quad_perm:[1,0,3,2] row_mask:0xf bank_mask:0xf
v_mfma_f32_16x16x32_bf16 a[28:31], v[216:219], a[164:167], a[28:31]
ds_read_b128 a[160:163], v1 offset:0
s_mov_b64 vcc, s[78:79]
v_cndmask_b32_dpp v52, v48, v52, vcc quad_perm:[1,0,3,2] row_mask:0xf bank_mask:0xf
v_mfma_f32_16x16x32_bf16 a[28:31], v[20:23], a[204:207], a[28:31]
ds_read_b128 a[200:203], v1 offset:5120
v_cndmask_b32_dpp v53, v49, v53, vcc quad_perm:[1,0,3,2] row_mask:0xf bank_mask:0xf
v_mfma_f32_16x16x32_bf16 a[44:47], v[216:219], a[168:171], a[44:47]
ds_read_b128 a[164:167], v1 offset:1024
v_cndmask_b32_dpp v54, v50, v54, vcc quad_perm:[1,0,3,2] row_mask:0xf bank_mask:0xf
v_mfma_f32_16x16x32_bf16 a[44:47], v[20:23], a[208:211], a[44:47]
ds_read_b128 a[204:207], v1 offset:6144
v_cndmask_b32_dpp v55, v51, v55, vcc quad_perm:[1,0,3,2] row_mask:0xf bank_mask:0xf
v_mfma_f32_16x16x32_bf16 a[60:63], v[216:219], a[172:175], a[60:63]
ds_read_b128 a[168:171], v1 offset:2048
v_mfma_f32_16x16x32_bf16 a[60:63], v[20:23], a[212:215], a[60:63]
ds_read_b128 a[208:211], v1 offset:7168
v_mfma_f32_16x16x32_bf16 a[76:79], v[216:219], a[176:179], a[76:79]
ds_read_b128 a[172:175], v1 offset:3072
v_mfma_f32_16x16x32_bf16 a[76:79], v[20:23], a[216:219], a[76:79]
ds_read_b128 a[212:215], v1 offset:8192
v_mfma_f32_16x16x32_bf16 a[92:95], v[216:219], a[180:183], a[92:95]
ds_read_b128 a[176:179], v1 offset:4096
v_mfma_f32_16x16x32_bf16 a[92:95], v[20:23], a[220:223], a[92:95]
ds_read_b128 a[216:219], v1 offset:9216
v_mfma_f32_16x16x32_bf16 a[108:111], v[216:219], a[184:187], a[108:111]
ds_read_b128 a[180:183], v1 offset:40960
v_mfma_f32_16x16x32_bf16 a[108:111], v[20:23], a[224:227], a[108:111]
ds_read_b128 a[220:223], v1 offset:46080
v_mfma_f32_16x16x32_bf16 a[124:127], v[216:219], a[188:191], a[124:127]
ds_read_b128 a[184:187], v1 offset:41984
v_mfma_f32_16x16x32_bf16 a[124:127], v[20:23], a[228:231], a[124:127]
ds_read_b128 a[224:227], v1 offset:47104
v_mfma_f32_16x16x32_bf16 a[140:143], v[216:219], a[192:195], a[140:143]
s_waitcnt lgkmcnt(10)
ds_read_b128 a[188:191], v1 offset:43008
v_mfma_f32_16x16x32_bf16 a[140:143], v[20:23], a[232:235], a[140:143]
ds_read_b128 a[228:231], v1 offset:48128
v_mfma_f32_16x16x32_bf16 a[156:159], v[216:219], a[196:199], a[156:159]
ds_read_b128 a[192:195], v1 offset:44032
v_mfma_f32_16x16x32_bf16 a[156:159], v[20:23], a[236:239], a[156:159]
ds_read_b128 a[232:235], v1 offset:49152
v_mfma_f32_16x16x32_bf16 v[96:99], v[16:19], a[160:163], 0
s_waitcnt lgkmcnt(10)
ds_read_b128 a[196:199], v1 offset:45056
v_cndmask_b32_dpp v20, v56, v60, vcc quad_perm:[1,0,3,2] row_mask:0xf bank_mask:0xf
v_mfma_f32_16x16x32_bf16 v[96:99], v[52:55], a[200:203], v[96:99]
ds_read_b128 a[236:239], v1 offset:50176
v_cndmask_b32_dpp v21, v57, v61, vcc quad_perm:[1,0,3,2] row_mask:0xf bank_mask:0xf
v_mfma_f32_16x16x32_bf16 v[112:115], v[16:19], a[164:167], 0
v_cndmask_b32_dpp v22, v58, v62, vcc quad_perm:[1,0,3,2] row_mask:0xf bank_mask:0xf
v_cndmask_b32_dpp v23, v59, v63, vcc quad_perm:[1,0,3,2] row_mask:0xf bank_mask:0xf
v_mfma_f32_16x16x32_bf16 v[112:115], v[52:55], a[204:207], v[112:115]
s_mov_b64 vcc, s[76:77]
v_cndmask_b32_dpp v56, v60, v56, vcc quad_perm:[1,0,3,2] row_mask:0xf bank_mask:0xf
v_cndmask_b32_dpp v57, v61, v57, vcc quad_perm:[1,0,3,2] row_mask:0xf bank_mask:0xf
v_mfma_f32_16x16x32_bf16 v[128:131], v[16:19], a[168:171], 0
v_cndmask_b32_dpp v58, v62, v58, vcc quad_perm:[1,0,3,2] row_mask:0xf bank_mask:0xf
v_cndmask_b32_dpp v59, v63, v59, vcc quad_perm:[1,0,3,2] row_mask:0xf bank_mask:0xf
v_mfma_f32_16x16x32_bf16 v[128:131], v[52:55], a[208:211], v[128:131]
v_mfma_f32_16x16x32_bf16 v[144:147], v[16:19], a[172:175], 0
v_mfma_f32_16x16x32_bf16 v[144:147], v[52:55], a[212:215], v[144:147]
s_waitcnt lgkmcnt(11)
v_mfma_f32_16x16x32_bf16 v[160:163], v[16:19], a[176:179], 0
s_waitcnt lgkmcnt(10)
v_mfma_f32_16x16x32_bf16 v[160:163], v[52:55], a[216:219], v[160:163]
s_waitcnt lgkmcnt(9)
v_mfma_f32_16x16x32_bf16 v[176:179], v[16:19], a[180:183], 0
s_waitcnt lgkmcnt(8)
v_mfma_f32_16x16x32_bf16 v[176:179], v[52:55], a[220:223], v[176:179]
s_waitcnt lgkmcnt(7)
v_mfma_f32_16x16x32_bf16 v[192:195], v[16:19], a[184:187], 0
s_waitcnt lgkmcnt(6)
v_mfma_f32_16x16x32_bf16 v[192:195], v[52:55], a[224:227], v[192:195]
s_waitcnt lgkmcnt(5)
v_mfma_f32_16x16x32_bf16 v[208:211], v[16:19], a[188:191], 0
s_waitcnt lgkmcnt(4)
v_mfma_f32_16x16x32_bf16 v[208:211], v[52:55], a[228:231], v[208:211]
s_waitcnt lgkmcnt(3)
v_mfma_f32_16x16x32_bf16 v[224:227], v[16:19], a[192:195], 0
s_waitcnt lgkmcnt(2)
v_mfma_f32_16x16x32_bf16 v[224:227], v[52:55], a[232:235], v[224:227]
s_waitcnt lgkmcnt(1)
v_mfma_f32_16x16x32_bf16 v[240:243], v[16:19], a[196:199], 0
s_waitcnt lgkmcnt(0)
v_mfma_f32_16x16x32_bf16 v[240:243], v[52:55], a[236:239], v[240:243]
v_mfma_f32_16x16x32_bf16 v[100:103], v[56:59], a[160:163], 0
s_waitcnt vmcnt(4)
v_cndmask_b32_dpp v16, v68, v64, vcc quad_perm:[1,0,3,2] row_mask:0xf bank_mask:0xf
v_cndmask_b32_dpp v17, v69, v65, vcc quad_perm:[1,0,3,2] row_mask:0xf bank_mask:0xf
v_mfma_f32_16x16x32_bf16 v[100:103], v[20:23], a[200:203], v[100:103]
v_cndmask_b32_dpp v18, v70, v66, vcc quad_perm:[1,0,3,2] row_mask:0xf bank_mask:0xf
v_cndmask_b32_dpp v19, v71, v67, vcc quad_perm:[1,0,3,2] row_mask:0xf bank_mask:0xf
v_mfma_f32_16x16x32_bf16 v[116:119], v[56:59], a[164:167], 0
s_mov_b64 vcc, s[78:79]
v_cndmask_b32_dpp v68, v64, v68, vcc quad_perm:[1,0,3,2] row_mask:0xf bank_mask:0xf
v_cndmask_b32_dpp v69, v65, v69, vcc quad_perm:[1,0,3,2] row_mask:0xf bank_mask:0xf
v_mfma_f32_16x16x32_bf16 v[116:119], v[20:23], a[204:207], v[116:119]
v_cndmask_b32_dpp v70, v66, v70, vcc quad_perm:[1,0,3,2] row_mask:0xf bank_mask:0xf
v_cndmask_b32_dpp v71, v67, v71, vcc quad_perm:[1,0,3,2] row_mask:0xf bank_mask:0xf
v_mfma_f32_16x16x32_bf16 v[132:135], v[56:59], a[168:171], 0
v_mfma_f32_16x16x32_bf16 v[132:135], v[20:23], a[208:211], v[132:135]
v_accvgpr_read_b32 v11, a0
v_accvgpr_read_b32 v12, a1
v_mfma_f32_16x16x32_bf16 v[148:151], v[56:59], a[172:175], 0
v_max_f32 v24, v11, v12
v_accvgpr_read_b32 v13, a2
v_mfma_f32_16x16x32_bf16 v[148:151], v[20:23], a[212:215], v[148:151]
v_accvgpr_read_b32 v14, a3
v_max3_f32 v24, v24, v13, v14
v_mfma_f32_16x16x32_bf16 v[164:167], v[56:59], a[176:179], 0
v_accvgpr_read_b32 v11, a4
v_accvgpr_read_b32 v12, a5
v_mfma_f32_16x16x32_bf16 v[164:167], v[20:23], a[216:219], v[164:167]
v_max3_f32 v24, v24, v11, v12
v_accvgpr_read_b32 v13, a6
v_mfma_f32_16x16x32_bf16 v[180:183], v[56:59], a[180:183], 0
v_accvgpr_read_b32 v14, a7
v_max3_f32 v24, v24, v13, v14
v_mfma_f32_16x16x32_bf16 v[180:183], v[20:23], a[220:223], v[180:183]
v_accvgpr_read_b32 v11, a8
v_accvgpr_read_b32 v12, a9
v_mfma_f32_16x16x32_bf16 v[196:199], v[56:59], a[184:187], 0
v_mfma_f32_16x16x32_bf16 v[196:199], v[20:23], a[224:227], v[196:199]
v_max3_f32 v24, v24, v11, v12
v_accvgpr_read_b32 v13, a10
v_mfma_f32_16x16x32_bf16 v[212:215], v[56:59], a[188:191], 0
v_accvgpr_read_b32 v14, a11
v_max3_f32 v24, v24, v13, v14
v_mfma_f32_16x16x32_bf16 v[212:215], v[20:23], a[228:231], v[212:215]
v_accvgpr_read_b32 v11, a12
v_accvgpr_read_b32 v12, a13
v_mfma_f32_16x16x32_bf16 v[228:231], v[56:59], a[192:195], 0
v_max3_f32 v24, v24, v11, v12
v_accvgpr_read_b32 v13, a14
v_mfma_f32_16x16x32_bf16 v[228:231], v[20:23], a[232:235], v[228:231]
v_accvgpr_read_b32 v14, a15
v_max3_f32 v24, v24, v13, v14
v_mfma_f32_16x16x32_bf16 v[244:247], v[56:59], a[196:199], 0
v_accvgpr_read_b32 v11, a16
)"
R"(v_accvgpr_read_b32 v12, a17
v_mfma_f32_16x16x32_bf16 v[244:247], v[20:23], a[236:239], v[244:247]
v_max_f32 v25, v11, v12
v_accvgpr_read_b32 v13, a18
v_mfma_f32_16x16x32_bf16 v[104:107], v[16:19], a[160:163], 0
v_cndmask_b32_dpp v20, v72, v76, vcc quad_perm:[1,0,3,2] row_mask:0xf bank_mask:0xf
v_cndmask_b32_dpp v21, v73, v77, vcc quad_perm:[1,0,3,2] row_mask:0xf bank_mask:0xf
v_mfma_f32_16x16x32_bf16 v[104:107], v[68:71], a[200:203], v[104:107]
v_cndmask_b32_dpp v22, v74, v78, vcc quad_perm:[1,0,3,2] row_mask:0xf bank_mask:0xf
v_cndmask_b32_dpp v23, v75, v79, vcc quad_perm:[1,0,3,2] row_mask:0xf bank_mask:0xf
v_mfma_f32_16x16x32_bf16 v[120:123], v[16:19], a[164:167], 0
s_mov_b64 vcc, s[76:77]
v_cndmask_b32_dpp v72, v76, v72, vcc quad_perm:[1,0,3,2] row_mask:0xf bank_mask:0xf
v_cndmask_b32_dpp v73, v77, v73, vcc quad_perm:[1,0,3,2] row_mask:0xf bank_mask:0xf
v_mfma_f32_16x16x32_bf16 v[120:123], v[68:71], a[204:207], v[120:123]
v_cndmask_b32_dpp v74, v78, v74, vcc quad_perm:[1,0,3,2] row_mask:0xf bank_mask:0xf
v_cndmask_b32_dpp v75, v79, v75, vcc quad_perm:[1,0,3,2] row_mask:0xf bank_mask:0xf
v_mfma_f32_16x16x32_bf16 v[136:139], v[16:19], a[168:171], 0
v_accvgpr_read_b32 v14, a19
v_max3_f32 v25, v25, v13, v14
v_mfma_f32_16x16x32_bf16 v[136:139], v[68:71], a[208:211], v[136:139]
v_accvgpr_read_b32 v11, a20
v_accvgpr_read_b32 v12, a21
v_mfma_f32_16x16x32_bf16 v[152:155], v[16:19], a[172:175], 0
v_max3_f32 v25, v25, v11, v12
v_accvgpr_read_b32 v13, a22
v_mfma_f32_16x16x32_bf16 v[152:155], v[68:71], a[212:215], v[152:155]
v_accvgpr_read_b32 v14, a23
v_max3_f32 v25, v25, v13, v14
v_mfma_f32_16x16x32_bf16 v[168:171], v[16:19], a[176:179], 0
buffer_load_dwordx4 v[48:51], v4, s[36:39], s74 offen offset:256 nt
v_mfma_f32_16x16x32_bf16 v[168:171], v[68:71], a[216:219], v[168:171]
v_accvgpr_read_b32 v11, a24
v_accvgpr_read_b32 v12, a25
v_mfma_f32_16x16x32_bf16 v[184:187], v[16:19], a[180:183], 0
v_max3_f32 v25, v25, v11, v12
v_accvgpr_read_b32 v13, a26
v_mfma_f32_16x16x32_bf16 v[184:187], v[68:71], a[220:223], v[184:187]
v_accvgpr_read_b32 v14, a27
v_max3_f32 v25, v25, v13, v14
v_mfma_f32_16x16x32_bf16 v[200:203], v[16:19], a[184:187], 0
v_accvgpr_read_b32 v11, a28
v_accvgpr_read_b32 v12, a29
v_mfma_f32_16x16x32_bf16 v[200:203], v[68:71], a[224:227], v[200:203]
v_max3_f32 v25, v25, v11, v12
v_accvgpr_read_b32 v13, a30
v_mfma_f32_16x16x32_bf16 v[216:219], v[16:19], a[188:191], 0
v_accvgpr_read_b32 v14, a31
v_max3_f32 v25, v25, v13, v14
v_mfma_f32_16x16x32_bf16 v[216:219], v[68:71], a[228:231], v[216:219]
v_accvgpr_read_b32 v11, a32
v_accvgpr_read_b32 v12, a33
v_mfma_f32_16x16x32_bf16 v[232:235], v[16:19], a[192:195], 0
buffer_load_dwordx4 v[52:55], v4, s[36:39], s74 offen offset:768 nt
v_mfma_f32_16x16x32_bf16 v[232:235], v[68:71], a[232:235], v[232:235]
v_max_f32 v26, v11, v12
v_accvgpr_read_b32 v13, a34
v_mfma_f32_16x16x32_bf16 v[248:251], v[16:19], a[196:199], 0
v_accvgpr_read_b32 v14, a35
v_max3_f32 v26, v26, v13, v14
v_mfma_f32_16x16x32_bf16 v[248:251], v[68:71], a[236:239], v[248:251]
v_accvgpr_read_b32 v11, a36
v_accvgpr_read_b32 v12, a37
v_mfma_f32_16x16x32_bf16 v[108:111], v[72:75], a[160:163], 0
v_cndmask_b32_dpp v16, v84, v80, vcc quad_perm:[1,0,3,2] row_mask:0xf bank_mask:0xf
v_cndmask_b32_dpp v17, v85, v81, vcc quad_perm:[1,0,3,2] row_mask:0xf bank_mask:0xf
v_mfma_f32_16x16x32_bf16 v[108:111], v[20:23], a[200:203], v[108:111]
v_cndmask_b32_dpp v18, v86, v82, vcc quad_perm:[1,0,3,2] row_mask:0xf bank_mask:0xf
v_cndmask_b32_dpp v19, v87, v83, vcc quad_perm:[1,0,3,2] row_mask:0xf bank_mask:0xf
v_mfma_f32_16x16x32_bf16 v[124:127], v[72:75], a[164:167], 0
ds_read_b128 a[160:163], v1 offset:10240
s_mov_b64 vcc, s[78:79]
v_cndmask_b32_dpp v84, v80, v84, vcc quad_perm:[1,0,3,2] row_mask:0xf bank_mask:0xf
v_mfma_f32_16x16x32_bf16 v[124:127], v[20:23], a[204:207], v[124:127]
ds_read_b128 a[200:203], v1 offset:15360
v_cndmask_b32_dpp v85, v81, v85, vcc quad_perm:[1,0,3,2] row_mask:0xf bank_mask:0xf
v_mfma_f32_16x16x32_bf16 v[140:143], v[72:75], a[168:171], 0
ds_read_b128 a[164:167], v1 offset:11264
v_cndmask_b32_dpp v86, v82, v86, vcc quad_perm:[1,0,3,2] row_mask:0xf bank_mask:0xf
v_mfma_f32_16x16x32_bf16 v[140:143], v[20:23], a[208:211], v[140:143]
ds_read_b128 a[204:207], v1 offset:16384
v_cndmask_b32_dpp v87, v83, v87, vcc quad_perm:[1,0,3,2] row_mask:0xf bank_mask:0xf
v_mfma_f32_16x16x32_bf16 v[156:159], v[72:75], a[172:175], 0
ds_read_b128 a[168:171], v1 offset:12288
v_max3_f32 v26, v26, v11, v12
v_mfma_f32_16x16x32_bf16 v[156:159], v[20:23], a[212:215], v[156:159]
ds_read_b128 a[208:211], v1 offset:17408
v_accvgpr_read_b32 v13, a38
v_mfma_f32_16x16x32_bf16 v[172:175], v[72:75], a[176:179], 0
ds_read_b128 a[172:175], v1 offset:13312
v_accvgpr_read_b32 v14, a39
v_mfma_f32_16x16x32_bf16 v[172:175], v[20:23], a[216:219], v[172:175]
ds_read_b128 a[212:215], v1 offset:18432
v_max3_f32 v26, v26, v13, v14
v_mfma_f32_16x16x32_bf16 v[188:191], v[72:75], a[180:183], 0
ds_read_b128 a[176:179], v1 offset:14336
v_accvgpr_read_b32 v11, a40
v_mfma_f32_16x16x32_bf16 v[188:191], v[20:23], a[220:223], v[188:191]
ds_read_b128 a[216:219], v1 offset:19456
v_accvgpr_read_b32 v12, a41
v_mfma_f32_16x16x32_bf16 v[204:207], v[72:75], a[184:187], 0
ds_read_b128 a[180:183], v1 offset:51200
v_max3_f32 v26, v26, v11, v12
v_mfma_f32_16x16x32_bf16 v[204:207], v[20:23], a[224:227], v[204:207]
ds_read_b128 a[220:223], v1 offset:56320
v_accvgpr_read_b32 v13, a42
v_mfma_f32_16x16x32_bf16 v[220:223], v[72:75], a[188:191], 0
ds_read_b128 a[184:187], v1 offset:52224
v_accvgpr_read_b32 v14, a43
v_mfma_f32_16x16x32_bf16 v[220:223], v[20:23], a[228:231], v[220:223]
ds_read_b128 a[224:227], v1 offset:57344
v_max3_f32 v26, v26, v13, v14
v_mfma_f32_16x16x32_bf16 v[236:239], v[72:75], a[192:195], 0
s_waitcnt lgkmcnt(10)
ds_read_b128 a[188:191], v1 offset:53248
v_accvgpr_read_b32 v11, a44
v_mfma_f32_16x16x32_bf16 v[236:239], v[20:23], a[232:235], v[236:239]
ds_read_b128 a[228:231], v1 offset:58368
v_accvgpr_read_b32 v12, a45
v_mfma_f32_16x16x32_bf16 v[252:255], v[72:75], a[196:199], 0
ds_read_b128 a[192:195], v1 offset:54272
v_max3_f32 v26, v26, v11, v12
v_mfma_f32_16x16x32_bf16 v[252:255], v[20:23], a[236:239], v[252:255]
ds_read_b128 a[232:235], v1 offset:59392
v_accvgpr_read_b32 v13, a46
v_mfma_f32_16x16x32_bf16 v[96:99], v[16:19], a[160:163], v[96:99]
s_waitcnt lgkmcnt(10)
ds_read_b128 a[196:199], v1 offset:55296
v_cndmask_b32_dpp v20, v88, v92, vcc quad_perm:[1,0,3,2] row_mask:0xf bank_mask:0xf
v_mfma_f32_16x16x32_bf16 v[96:99], v[84:87], a[200:203], v[96:99]
ds_read_b128 a[236:239], v1 offset:60416
v_cndmask_b32_dpp v21, v89, v93, vcc quad_perm:[1,0,3,2] row_mask:0xf bank_mask:0xf
v_mfma_f32_16x16x32_bf16 v[112:115], v[16:19], a[164:167], v[112:115]
v_cndmask_b32_dpp v22, v90, v94, vcc quad_perm:[1,0,3,2] row_mask:0xf bank_mask:0xf
v_cndmask_b32_dpp v23, v91, v95, vcc quad_perm:[1,0,3,2] row_mask:0xf bank_mask:0xf
v_mfma_f32_16x16x32_bf16 v[112:115], v[84:87], a[204:207], v[112:115]
s_mov_b64 vcc, s[76:77]
v_cndmask_b32_dpp v88, v92, v88, vcc quad_perm:[1,0,3,2] row_mask:0xf bank_mask:0xf
v_cndmask_b32_dpp v89, v93, v89, vcc quad_perm:[1,0,3,2] row_mask:0xf bank_mask:0xf
v_mfma_f32_16x16x32_bf16 v[128:131], v[16:19], a[168:171], v[128:131]
v_cndmask_b32_dpp v90, v94, v90, vcc quad_perm:[1,0,3,2] row_mask:0xf bank_mask:0xf
v_cndmask_b32_dpp v91, v95, v91, vcc quad_perm:[1,0,3,2] row_mask:0xf bank_mask:0xf
v_mfma_f32_16x16x32_bf16 v[128:131], v[84:87], a[208:211], v[128:131]
v_accvgpr_read_b32 v14, a47
v_max3_f32 v26, v26, v13, v14
v_mfma_f32_16x16x32_bf16 v[144:147], v[16:19], a[172:175], v[144:147]
v_accvgpr_read_b32 v11, a48
v_accvgpr_read_b32 v12, a49
v_mfma_f32_16x16x32_bf16 v[144:147], v[84:87], a[212:215], v[144:147]
s_waitcnt lgkmcnt(11)
v_max_f32 v27, v11, v12
v_accvgpr_read_b32 v13, a50
v_mfma_f32_16x16x32_bf16 v[160:163], v[16:19], a[176:179], v[160:163]
buffer_load_dwordx4 v[56:59], v4, s[36:39], s74 offen offset:2304 nt
s_waitcnt lgkmcnt(10)
v_mfma_f32_16x16x32_bf16 v[160:163], v[84:87], a[216:219], v[160:163]
s_waitcnt lgkmcnt(9)
v_accvgpr_read_b32 v14, a51
v_max3_f32 v27, v27, v13, v14
v_mfma_f32_16x16x32_bf16 v[176:179], v[16:19], a[180:183], v[176:179]
s_waitcnt lgkmcnt(8)
v_accvgpr_read_b32 v11, a52
v_accvgpr_read_b32 v12, a53
v_mfma_f32_16x16x32_bf16 v[176:179], v[84:87], a[220:223], v[176:179]
s_waitcnt lgkmcnt(7)
v_max3_f32 v27, v27, v11, v12
v_accvgpr_read_b32 v13, a54
v_mfma_f32_16x16x32_bf16 v[192:195], v[16:19], a[184:187], v[192:195]
s_waitcnt lgkmcnt(6)
v_accvgpr_read_b32 v14, a55
v_max3_f32 v27, v27, v13, v14
v_mfma_f32_16x16x32_bf16 v[192:195], v[84:87], a[224:227], v[192:195]
s_waitcnt lgkmcnt(5)
v_accvgpr_read_b32 v11, a56
v_accvgpr_read_b32 v12, a57
v_mfma_f32_16x16x32_bf16 v[208:211], v[16:19], a[188:191], v[208:211]
s_waitcnt lgkmcnt(4)
v_max3_f32 v27, v27, v11, v12
v_accvgpr_read_b32 v13, a58
v_mfma_f32_16x16x32_bf16 v[208:211], v[84:87], a[228:231], v[208:211]
s_waitcnt lgkmcnt(3)
v_accvgpr_read_b32 v14, a59
v_max3_f32 v27, v27, v13, v14
v_mfma_f32_16x16x32_bf16 v[224:227], v[16:19], a[192:195], v[224:227]
buffer_load_dwordx4 v[60:63], v4, s[36:39], s74 offen offset:2816 nt
s_waitcnt lgkmcnt(2)
v_mfma_f32_16x16x32_bf16 v[224:227], v[84:87], a[232:235], v[224:227]
s_waitcnt lgkmcnt(1)
v_accvgpr_read_b32 v11, a60
v_accvgpr_read_b32 v12, a61
v_mfma_f32_16x16x32_bf16 v[240:243], v[16:19], a[196:199], v[240:243]
s_waitcnt lgkmcnt(0)
v_max3_f32 v27, v27, v11, v12
v_accvgpr_read_b32 v13, a62
v_mfma_f32_16x16x32_bf16 v[240:243], v[84:87], a[236:239], v[240:243]
v_accvgpr_read_b32 v14, a63
v_max3_f32 v27, v27, v13, v14
v_mfma_f32_16x16x32_bf16 v[100:103], v[88:91], a[160:163], v[100:103]
s_waitcnt vmcnt(4)
v_cndmask_b32_dpp v16, v36, v32, vcc quad_perm:[1,0,3,2] row_mask:0xf bank_mask:0xf
)"
R"(v_cndmask_b32_dpp v17, v37, v33, vcc quad_perm:[1,0,3,2] row_mask:0xf bank_mask:0xf
v_mfma_f32_16x16x32_bf16 v[100:103], v[20:23], a[200:203], v[100:103]
v_cndmask_b32_dpp v18, v38, v34, vcc quad_perm:[1,0,3,2] row_mask:0xf bank_mask:0xf
v_cndmask_b32_dpp v19, v39, v35, vcc quad_perm:[1,0,3,2] row_mask:0xf bank_mask:0xf
v_mfma_f32_16x16x32_bf16 v[116:119], v[88:91], a[164:167], v[116:119]
s_mov_b64 vcc, s[78:79]
v_cndmask_b32_dpp v36, v32, v36, vcc quad_perm:[1,0,3,2] row_mask:0xf bank_mask:0xf
v_cndmask_b32_dpp v37, v33, v37, vcc quad_perm:[1,0,3,2] row_mask:0xf bank_mask:0xf
v_mfma_f32_16x16x32_bf16 v[116:119], v[20:23], a[204:207], v[116:119]
v_cndmask_b32_dpp v38, v34, v38, vcc quad_perm:[1,0,3,2] row_mask:0xf bank_mask:0xf
v_cndmask_b32_dpp v39, v35, v39, vcc quad_perm:[1,0,3,2] row_mask:0xf bank_mask:0xf
v_mfma_f32_16x16x32_bf16 v[132:135], v[88:91], a[168:171], v[132:135]
buffer_load_dwordx4 v[64:67], v4, s[36:39], s74 offen offset:384 nt
v_mfma_f32_16x16x32_bf16 v[132:135], v[20:23], a[208:211], v[132:135]
v_accvgpr_read_b32 v11, a64
v_accvgpr_read_b32 v12, a65
v_mfma_f32_16x16x32_bf16 v[148:151], v[88:91], a[172:175], v[148:151]
v_max_f32 v28, v11, v12
v_accvgpr_read_b32 v13, a66
v_mfma_f32_16x16x32_bf16 v[148:151], v[20:23], a[212:215], v[148:151]
v_accvgpr_read_b32 v14, a67
v_max3_f32 v28, v28, v13, v14
v_mfma_f32_16x16x32_bf16 v[164:167], v[88:91], a[176:179], v[164:167]
v_accvgpr_read_b32 v11, a68
v_accvgpr_read_b32 v12, a69
v_mfma_f32_16x16x32_bf16 v[164:167], v[20:23], a[216:219], v[164:167]
v_max3_f32 v28, v28, v11, v12
v_accvgpr_read_b32 v13, a70
v_mfma_f32_16x16x32_bf16 v[180:183], v[88:91], a[180:183], v[180:183]
v_accvgpr_read_b32 v14, a71
v_max3_f32 v28, v28, v13, v14
v_mfma_f32_16x16x32_bf16 v[180:183], v[20:23], a[220:223], v[180:183]
v_accvgpr_read_b32 v11, a72
v_accvgpr_read_b32 v12, a73
v_mfma_f32_16x16x32_bf16 v[196:199], v[88:91], a[184:187], v[196:199]
buffer_load_dwordx4 v[68:71], v4, s[36:39], s74 offen offset:896 nt
v_mfma_f32_16x16x32_bf16 v[196:199], v[20:23], a[224:227], v[196:199]
v_max3_f32 v28, v28, v11, v12
v_accvgpr_read_b32 v13, a74
v_mfma_f32_16x16x32_bf16 v[212:215], v[88:91], a[188:191], v[212:215]
v_accvgpr_read_b32 v14, a75
v_max3_f32 v28, v28, v13, v14
v_mfma_f32_16x16x32_bf16 v[212:215], v[20:23], a[228:231], v[212:215]
v_accvgpr_read_b32 v11, a76
v_accvgpr_read_b32 v12, a77
v_mfma_f32_16x16x32_bf16 v[228:231], v[88:91], a[192:195], v[228:231]
v_max3_f32 v28, v28, v11, v12
v_accvgpr_read_b32 v13, a78
v_mfma_f32_16x16x32_bf16 v[228:231], v[20:23], a[232:235], v[228:231]
v_accvgpr_read_b32 v14, a79
v_max3_f32 v28, v28, v13, v14
v_mfma_f32_16x16x32_bf16 v[244:247], v[88:91], a[196:199], v[244:247]
v_accvgpr_read_b32 v11, a80
v_accvgpr_read_b32 v12, a81
v_mfma_f32_16x16x32_bf16 v[244:247], v[20:23], a[236:239], v[244:247]
v_max_f32 v29, v11, v12
v_accvgpr_read_b32 v13, a82
v_mfma_f32_16x16x32_bf16 v[104:107], v[16:19], a[160:163], v[104:107]
v_cndmask_b32_dpp v20, v40, v44, vcc quad_perm:[1,0,3,2] row_mask:0xf bank_mask:0xf
v_cndmask_b32_dpp v21, v41, v45, vcc quad_perm:[1,0,3,2] row_mask:0xf bank_mask:0xf
v_mfma_f32_16x16x32_bf16 v[104:107], v[36:39], a[200:203], v[104:107]
v_cndmask_b32_dpp v22, v42, v46, vcc quad_perm:[1,0,3,2] row_mask:0xf bank_mask:0xf
v_cndmask_b32_dpp v23, v43, v47, vcc quad_perm:[1,0,3,2] row_mask:0xf bank_mask:0xf
v_mfma_f32_16x16x32_bf16 v[120:123], v[16:19], a[164:167], v[120:123]
s_mov_b64 vcc, s[76:77]
v_cndmask_b32_dpp v40, v44, v40, vcc quad_perm:[1,0,3,2] row_mask:0xf bank_mask:0xf
v_cndmask_b32_dpp v41, v45, v41, vcc quad_perm:[1,0,3,2] row_mask:0xf bank_mask:0xf
v_mfma_f32_16x16x32_bf16 v[120:123], v[36:39], a[204:207], v[120:123]
v_cndmask_b32_dpp v42, v46, v42, vcc quad_perm:[1,0,3,2] row_mask:0xf bank_mask:0xf
v_cndmask_b32_dpp v43, v47, v43, vcc quad_perm:[1,0,3,2] row_mask:0xf bank_mask:0xf
v_mfma_f32_16x16x32_bf16 v[136:139], v[16:19], a[168:171], v[136:139]
v_accvgpr_read_b32 v14, a83
v_max3_f32 v29, v29, v13, v14
v_mfma_f32_16x16x32_bf16 v[136:139], v[36:39], a[208:211], v[136:139]
v_accvgpr_read_b32 v11, a84
v_accvgpr_read_b32 v12, a85
v_mfma_f32_16x16x32_bf16 v[152:155], v[16:19], a[172:175], v[152:155]
v_max3_f32 v29, v29, v11, v12
v_accvgpr_read_b32 v13, a86
v_mfma_f32_16x16x32_bf16 v[152:155], v[36:39], a[212:215], v[152:155]
v_accvgpr_read_b32 v14, a87
v_max3_f32 v29, v29, v13, v14
v_mfma_f32_16x16x32_bf16 v[168:171], v[16:19], a[176:179], v[168:171]
buffer_load_dwordx4 v[72:75], v4, s[36:39], s74 offen offset:2432 nt
v_mfma_f32_16x16x32_bf16 v[168:171], v[36:39], a[216:219], v[168:171]
v_accvgpr_read_b32 v11, a88
v_accvgpr_read_b32 v12, a89
v_mfma_f32_16x16x32_bf16 v[184:187], v[16:19], a[180:183], v[184:187]
v_max3_f32 v29, v29, v11, v12
v_accvgpr_read_b32 v13, a90
v_mfma_f32_16x16x32_bf16 v[184:187], v[36:39], a[220:223], v[184:187]
v_accvgpr_read_b32 v14, a91
v_max3_f32 v29, v29, v13, v14
v_mfma_f32_16x16x32_bf16 v[200:203], v[16:19], a[184:187], v[200:203]
v_accvgpr_read_b32 v11, a92
v_accvgpr_read_b32 v12, a93
v_mfma_f32_16x16x32_bf16 v[200:203], v[36:39], a[224:227], v[200:203]
v_max3_f32 v29, v29, v11, v12
v_accvgpr_read_b32 v13, a94
v_mfma_f32_16x16x32_bf16 v[216:219], v[16:19], a[188:191], v[216:219]
v_accvgpr_read_b32 v14, a95
v_max3_f32 v29, v29, v13, v14
v_mfma_f32_16x16x32_bf16 v[216:219], v[36:39], a[228:231], v[216:219]
v_accvgpr_read_b32 v11, a96
v_accvgpr_read_b32 v12, a97
v_mfma_f32_16x16x32_bf16 v[232:235], v[16:19], a[192:195], v[232:235]
buffer_load_dwordx4 v[76:79], v4, s[36:39], s74 offen offset:2944 nt
v_mfma_f32_16x16x32_bf16 v[232:235], v[36:39], a[232:235], v[232:235]
v_max_f32 v30, v11, v12
v_accvgpr_read_b32 v13, a98
v_mfma_f32_16x16x32_bf16 v[248:251], v[16:19], a[196:199], v[248:251]
v_accvgpr_read_b32 v14, a99
v_max3_f32 v30, v30, v13, v14
v_mfma_f32_16x16x32_bf16 v[248:251], v[36:39], a[236:239], v[248:251]
v_accvgpr_read_b32 v11, a100
v_accvgpr_read_b32 v12, a101
v_mfma_f32_16x16x32_bf16 v[108:111], v[40:43], a[160:163], v[108:111]
buffer_load_dwordx4 v[80:83], v4, s[36:39], s75 offen offset:256 nt
buffer_load_dwordx4 v[84:87], v4, s[36:39], s75 offen offset:768 nt
buffer_load_dwordx4 v[88:91], v4, s[36:39], s75 offen offset:2304 nt
buffer_load_dwordx4 v[92:95], v4, s[36:39], s75 offen offset:2816 nt
s_waitcnt vmcnt(8)
v_cndmask_b32_dpp v16, v52, v48, vcc quad_perm:[1,0,3,2] row_mask:0xf bank_mask:0xf
v_cndmask_b32_dpp v17, v53, v49, vcc quad_perm:[1,0,3,2] row_mask:0xf bank_mask:0xf
v_mfma_f32_16x16x32_bf16 v[108:111], v[20:23], a[200:203], v[108:111]
v_cndmask_b32_dpp v18, v54, v50, vcc quad_perm:[1,0,3,2] row_mask:0xf bank_mask:0xf
v_cndmask_b32_dpp v19, v55, v51, vcc quad_perm:[1,0,3,2] row_mask:0xf bank_mask:0xf
v_mfma_f32_16x16x32_bf16 v[124:127], v[40:43], a[164:167], v[124:127]
ds_read_b128 a[160:163], v1 offset:20480
s_mov_b64 vcc, s[78:79]
v_cndmask_b32_dpp v52, v48, v52, vcc quad_perm:[1,0,3,2] row_mask:0xf bank_mask:0xf
v_mfma_f32_16x16x32_bf16 v[124:127], v[20:23], a[204:207], v[124:127]
ds_read_b128 a[200:203], v1 offset:25600
v_cndmask_b32_dpp v53, v49, v53, vcc quad_perm:[1,0,3,2] row_mask:0xf bank_mask:0xf
v_mfma_f32_16x16x32_bf16 v[140:143], v[40:43], a[168:171], v[140:143]
ds_read_b128 a[164:167], v1 offset:21504
v_cndmask_b32_dpp v54, v50, v54, vcc quad_perm:[1,0,3,2] row_mask:0xf bank_mask:0xf
v_mfma_f32_16x16x32_bf16 v[140:143], v[20:23], a[208:211], v[140:143]
ds_read_b128 a[204:207], v1 offset:26624
v_cndmask_b32_dpp v55, v51, v55, vcc quad_perm:[1,0,3,2] row_mask:0xf bank_mask:0xf
v_mfma_f32_16x16x32_bf16 v[156:159], v[40:43], a[172:175], v[156:159]
ds_read_b128 a[168:171], v1 offset:22528
v_max3_f32 v30, v30, v11, v12
v_mfma_f32_16x16x32_bf16 v[156:159], v[20:23], a[212:215], v[156:159]
ds_read_b128 a[208:211], v1 offset:27648
v_accvgpr_read_b32 v13, a102
v_mfma_f32_16x16x32_bf16 v[172:175], v[40:43], a[176:179], v[172:175]
ds_read_b128 a[172:175], v1 offset:23552
v_accvgpr_read_b32 v14, a103
v_mfma_f32_16x16x32_bf16 v[172:175], v[20:23], a[216:219], v[172:175]
ds_read_b128 a[212:215], v1 offset:28672
v_max3_f32 v30, v30, v13, v14
v_mfma_f32_16x16x32_bf16 v[188:191], v[40:43], a[180:183], v[188:191]
ds_read_b128 a[176:179], v1 offset:24576
v_accvgpr_read_b32 v11, a104
v_mfma_f32_16x16x32_bf16 v[188:191], v[20:23], a[220:223], v[188:191]
ds_read_b128 a[216:219], v1 offset:29696
v_accvgpr_read_b32 v12, a105
v_mfma_f32_16x16x32_bf16 v[204:207], v[40:43], a[184:187], v[204:207]
ds_read_b128 a[180:183], v1 offset:61440
v_max3_f32 v30, v30, v11, v12
v_mfma_f32_16x16x32_bf16 v[204:207], v[20:23], a[224:227], v[204:207]
ds_read_b128 a[220:223], v2 offset:1024
v_accvgpr_read_b32 v13, a106
v_mfma_f32_16x16x32_bf16 v[220:223], v[40:43], a[188:191], v[220:223]
ds_read_b128 a[184:187], v1 offset:62464
v_accvgpr_read_b32 v14, a107
v_mfma_f32_16x16x32_bf16 v[220:223], v[20:23], a[228:231], v[220:223]
ds_read_b128 a[224:227], v2 offset:2048
v_max3_f32 v30, v30, v13, v14
v_mfma_f32_16x16x32_bf16 v[236:239], v[40:43], a[192:195], v[236:239]
s_waitcnt lgkmcnt(10)
ds_read_b128 a[188:191], v1 offset:63488
v_accvgpr_read_b32 v11, a108
v_mfma_f32_16x16x32_bf16 v[236:239], v[20:23], a[232:235], v[236:239]
ds_read_b128 a[228:231], v2 offset:3072
v_accvgpr_read_b32 v12, a109
v_mfma_f32_16x16x32_bf16 v[252:255], v[40:43], a[196:199], v[252:255]
ds_read_b128 a[192:195], v1 offset:64512
v_max3_f32 v30, v30, v11, v12
v_mfma_f32_16x16x32_bf16 v[252:255], v[20:23], a[236:239], v[252:255]
ds_read_b128 a[232:235], v2 offset:4096
v_accvgpr_read_b32 v13, a110
v_mfma_f32_16x16x32_bf16 v[96:99], v[16:19], a[160:163], v[96:99]
s_waitcnt lgkmcnt(10)
ds_read_b128 a[196:199], v2 offset:0
v_cndmask_b32_dpp v20, v56, v60, vcc quad_perm:[1,0,3,2] row_mask:0xf bank_mask:0xf
v_mfma_f32_16x16x32_bf16 v[96:99], v[52:55], a[200:203], v[96:99]
ds_read_b128 a[236:239], v2 offset:5120
v_cndmask_b32_dpp v21, v57, v61, vcc quad_perm:[1,0,3,2] row_mask:0xf bank_mask:0xf
v_mfma_f32_16x16x32_bf16 v[112:115], v[16:19], a[164:167], v[112:115]
v_cndmask_b32_dpp v22, v58, v62, vcc quad_perm:[1,0,3,2] row_mask:0xf bank_mask:0xf
v_cndmask_b32_dpp v23, v59, v63, vcc quad_perm:[1,0,3,2] row_mask:0xf bank_mask:0xf
v_mfma_f32_16x16x32_bf16 v[112:115], v[52:55], a[204:207], v[112:115]
s_mov_b64 vcc, s[76:77]
v_cndmask_b32_dpp v56, v60, v56, vcc quad_perm:[1,0,3,2] row_mask:0xf bank_mask:0xf
v_cndmask_b32_dpp v57, v61, v57, vcc quad_perm:[1,0,3,2] row_mask:0xf bank_mask:0xf
v_mfma_f32_16x16x32_bf16 v[128:131], v[16:19], a[168:171], v[128:131]
v_cndmask_b32_dpp v58, v62, v58, vcc quad_perm:[1,0,3,2] row_mask:0xf bank_mask:0xf
v_cndmask_b32_dpp v59, v63, v59, vcc quad_perm:[1,0,3,2] row_mask:0xf bank_mask:0xf
)"
R"(v_mfma_f32_16x16x32_bf16 v[128:131], v[52:55], a[208:211], v[128:131]
v_accvgpr_read_b32 v14, a111
v_max3_f32 v30, v30, v13, v14
v_mfma_f32_16x16x32_bf16 v[144:147], v[16:19], a[172:175], v[144:147]
v_accvgpr_read_b32 v11, a112
v_accvgpr_read_b32 v12, a113
v_mfma_f32_16x16x32_bf16 v[144:147], v[52:55], a[212:215], v[144:147]
s_waitcnt lgkmcnt(11)
v_max_f32 v31, v11, v12
v_accvgpr_read_b32 v13, a114
v_mfma_f32_16x16x32_bf16 v[160:163], v[16:19], a[176:179], v[160:163]
buffer_load_dwordx4 v[32:35], v4, s[36:39], s75 offen offset:384 nt
s_waitcnt lgkmcnt(10)
v_mfma_f32_16x16x32_bf16 v[160:163], v[52:55], a[216:219], v[160:163]
s_waitcnt lgkmcnt(9)
v_accvgpr_read_b32 v14, a115
v_max3_f32 v31, v31, v13, v14
v_mfma_f32_16x16x32_bf16 v[176:179], v[16:19], a[180:183], v[176:179]
s_waitcnt lgkmcnt(8)
v_accvgpr_read_b32 v11, a116
v_accvgpr_read_b32 v12, a117
v_mfma_f32_16x16x32_bf16 v[176:179], v[52:55], a[220:223], v[176:179]
s_waitcnt lgkmcnt(7)
v_max3_f32 v31, v31, v11, v12
v_accvgpr_read_b32 v13, a118
v_mfma_f32_16x16x32_bf16 v[192:195], v[16:19], a[184:187], v[192:195]
s_waitcnt lgkmcnt(6)
v_accvgpr_read_b32 v14, a119
v_max3_f32 v31, v31, v13, v14
v_mfma_f32_16x16x32_bf16 v[192:195], v[52:55], a[224:227], v[192:195]
s_waitcnt lgkmcnt(5)
v_accvgpr_read_b32 v11, a120
v_accvgpr_read_b32 v12, a121
v_mfma_f32_16x16x32_bf16 v[208:211], v[16:19], a[188:191], v[208:211]
s_waitcnt lgkmcnt(4)
v_max3_f32 v31, v31, v11, v12
v_accvgpr_read_b32 v13, a122
v_mfma_f32_16x16x32_bf16 v[208:211], v[52:55], a[228:231], v[208:211]
s_waitcnt lgkmcnt(3)
v_accvgpr_read_b32 v14, a123
v_max3_f32 v31, v31, v13, v14
v_mfma_f32_16x16x32_bf16 v[224:227], v[16:19], a[192:195], v[224:227]
buffer_load_dwordx4 v[36:39], v4, s[36:39], s75 offen offset:896 nt
s_waitcnt lgkmcnt(2)
v_mfma_f32_16x16x32_bf16 v[224:227], v[52:55], a[232:235], v[224:227]
s_waitcnt lgkmcnt(1)
v_accvgpr_read_b32 v11, a124
v_accvgpr_read_b32 v12, a125
v_mfma_f32_16x16x32_bf16 v[240:243], v[16:19], a[196:199], v[240:243]
s_waitcnt lgkmcnt(0)
v_max3_f32 v31, v31, v11, v12
v_accvgpr_read_b32 v13, a126
v_mfma_f32_16x16x32_bf16 v[240:243], v[52:55], a[236:239], v[240:243]
v_accvgpr_read_b32 v14, a127
v_max3_f32 v31, v31, v13, v14
v_mfma_f32_16x16x32_bf16 v[100:103], v[56:59], a[160:163], v[100:103]
buffer_load_dwordx4 v[40:43], v4, s[36:39], s75 offen offset:2432 nt
buffer_load_dwordx4 v[44:47], v4, s[36:39], s75 offen offset:2944 nt
s_waitcnt vmcnt(4)
v_cndmask_b32_dpp v16, v84, v80, vcc quad_perm:[1,0,3,2] row_mask:0xf bank_mask:0xf
v_cndmask_b32_dpp v17, v85, v81, vcc quad_perm:[1,0,3,2] row_mask:0xf bank_mask:0xf
v_mfma_f32_16x16x32_bf16 v[100:103], v[20:23], a[200:203], v[100:103]
v_cndmask_b32_dpp v18, v86, v82, vcc quad_perm:[1,0,3,2] row_mask:0xf bank_mask:0xf
v_cndmask_b32_dpp v19, v87, v83, vcc quad_perm:[1,0,3,2] row_mask:0xf bank_mask:0xf
v_mfma_f32_16x16x32_bf16 v[116:119], v[56:59], a[164:167], v[116:119]
s_mov_b64 vcc, s[78:79]
v_cndmask_b32_dpp v84, v80, v84, vcc quad_perm:[1,0,3,2] row_mask:0xf bank_mask:0xf
v_cndmask_b32_dpp v85, v81, v85, vcc quad_perm:[1,0,3,2] row_mask:0xf bank_mask:0xf
v_mfma_f32_16x16x32_bf16 v[116:119], v[20:23], a[204:207], v[116:119]
v_cndmask_b32_dpp v86, v82, v86, vcc quad_perm:[1,0,3,2] row_mask:0xf bank_mask:0xf
v_cndmask_b32_dpp v87, v83, v87, vcc quad_perm:[1,0,3,2] row_mask:0xf bank_mask:0xf
v_mfma_f32_16x16x32_bf16 v[132:135], v[56:59], a[168:171], v[132:135]
v_mfma_f32_16x16x32_bf16 v[132:135], v[20:23], a[208:211], v[132:135]
v_accvgpr_read_b32 v11, a128
v_accvgpr_read_b32 v12, a129
v_mfma_f32_16x16x32_bf16 v[148:151], v[56:59], a[172:175], v[148:151]
v_max_f32 v9, v11, v12
v_accvgpr_read_b32 v13, a130
v_mfma_f32_16x16x32_bf16 v[148:151], v[20:23], a[212:215], v[148:151]
v_accvgpr_read_b32 v14, a131
v_max3_f32 v9, v9, v13, v14
v_mfma_f32_16x16x32_bf16 v[164:167], v[56:59], a[176:179], v[164:167]
v_accvgpr_read_b32 v11, a132
v_accvgpr_read_b32 v12, a133
v_mfma_f32_16x16x32_bf16 v[164:167], v[20:23], a[216:219], v[164:167]
v_max3_f32 v9, v9, v11, v12
v_accvgpr_read_b32 v13, a134
v_mfma_f32_16x16x32_bf16 v[180:183], v[56:59], a[180:183], v[180:183]
v_accvgpr_read_b32 v14, a135
v_max3_f32 v9, v9, v13, v14
v_mfma_f32_16x16x32_bf16 v[180:183], v[20:23], a[220:223], v[180:183]
v_accvgpr_read_b32 v11, a136
v_accvgpr_read_b32 v12, a137
v_mfma_f32_16x16x32_bf16 v[196:199], v[56:59], a[184:187], v[196:199]
v_mfma_f32_16x16x32_bf16 v[196:199], v[20:23], a[224:227], v[196:199]
v_max3_f32 v9, v9, v11, v12
v_accvgpr_read_b32 v13, a138
v_mfma_f32_16x16x32_bf16 v[212:215], v[56:59], a[188:191], v[212:215]
v_accvgpr_read_b32 v14, a139
v_max3_f32 v9, v9, v13, v14
v_mfma_f32_16x16x32_bf16 v[212:215], v[20:23], a[228:231], v[212:215]
v_accvgpr_read_b32 v11, a140
v_accvgpr_read_b32 v12, a141
v_mfma_f32_16x16x32_bf16 v[228:231], v[56:59], a[192:195], v[228:231]
v_max3_f32 v9, v9, v11, v12
v_accvgpr_read_b32 v13, a142
v_mfma_f32_16x16x32_bf16 v[228:231], v[20:23], a[232:235], v[228:231]
v_accvgpr_read_b32 v14, a143
v_max3_f32 v9, v9, v13, v14
v_mfma_f32_16x16x32_bf16 v[244:247], v[56:59], a[196:199], v[244:247]
v_accvgpr_read_b32 v11, a144
v_accvgpr_read_b32 v12, a145
v_mfma_f32_16x16x32_bf16 v[244:247], v[20:23], a[236:239], v[244:247]
v_max_f32 v10, v11, v12
v_accvgpr_read_b32 v13, a146
v_mfma_f32_16x16x32_bf16 v[104:107], v[16:19], a[160:163], v[104:107]
v_cndmask_b32_dpp v20, v88, v92, vcc quad_perm:[1,0,3,2] row_mask:0xf bank_mask:0xf
v_cndmask_b32_dpp v21, v89, v93, vcc quad_perm:[1,0,3,2] row_mask:0xf bank_mask:0xf
v_mfma_f32_16x16x32_bf16 v[104:107], v[84:87], a[200:203], v[104:107]
v_cndmask_b32_dpp v22, v90, v94, vcc quad_perm:[1,0,3,2] row_mask:0xf bank_mask:0xf
v_cndmask_b32_dpp v23, v91, v95, vcc quad_perm:[1,0,3,2] row_mask:0xf bank_mask:0xf
v_mfma_f32_16x16x32_bf16 v[120:123], v[16:19], a[164:167], v[120:123]
s_mov_b64 vcc, s[76:77]
v_cndmask_b32_dpp v88, v92, v88, vcc quad_perm:[1,0,3,2] row_mask:0xf bank_mask:0xf
v_cndmask_b32_dpp v89, v93, v89, vcc quad_perm:[1,0,3,2] row_mask:0xf bank_mask:0xf
v_mfma_f32_16x16x32_bf16 v[120:123], v[84:87], a[204:207], v[120:123]
v_cndmask_b32_dpp v90, v94, v90, vcc quad_perm:[1,0,3,2] row_mask:0xf bank_mask:0xf
v_cndmask_b32_dpp v91, v95, v91, vcc quad_perm:[1,0,3,2] row_mask:0xf bank_mask:0xf
v_mfma_f32_16x16x32_bf16 v[136:139], v[16:19], a[168:171], v[136:139]
v_accvgpr_read_b32 v14, a147
v_max3_f32 v10, v10, v13, v14
v_mfma_f32_16x16x32_bf16 v[136:139], v[84:87], a[208:211], v[136:139]
v_accvgpr_read_b32 v11, a148
v_accvgpr_read_b32 v12, a149
v_mfma_f32_16x16x32_bf16 v[152:155], v[16:19], a[172:175], v[152:155]
v_max3_f32 v10, v10, v11, v12
v_accvgpr_read_b32 v13, a150
v_mfma_f32_16x16x32_bf16 v[152:155], v[84:87], a[212:215], v[152:155]
v_accvgpr_read_b32 v14, a151
v_max3_f32 v10, v10, v13, v14
v_mfma_f32_16x16x32_bf16 v[168:171], v[16:19], a[176:179], v[168:171]
v_mfma_f32_16x16x32_bf16 v[168:171], v[84:87], a[216:219], v[168:171]
v_accvgpr_read_b32 v11, a152
v_accvgpr_read_b32 v12, a153
v_mfma_f32_16x16x32_bf16 v[184:187], v[16:19], a[180:183], v[184:187]
v_max3_f32 v10, v10, v11, v12
v_accvgpr_read_b32 v13, a154
v_mfma_f32_16x16x32_bf16 v[184:187], v[84:87], a[220:223], v[184:187]
v_accvgpr_read_b32 v14, a155
v_max3_f32 v10, v10, v13, v14
v_mfma_f32_16x16x32_bf16 v[200:203], v[16:19], a[184:187], v[200:203]
v_accvgpr_read_b32 v11, a156
v_accvgpr_read_b32 v12, a157
v_mfma_f32_16x16x32_bf16 v[200:203], v[84:87], a[224:227], v[200:203]
v_max3_f32 v10, v10, v11, v12
v_accvgpr_read_b32 v13, a158
v_mfma_f32_16x16x32_bf16 v[216:219], v[16:19], a[188:191], v[216:219]
v_accvgpr_read_b32 v14, a159
v_max3_f32 v10, v10, v13, v14
v_mfma_f32_16x16x32_bf16 v[216:219], v[84:87], a[228:231], v[216:219]
v_mfma_f32_16x16x32_bf16 v[232:235], v[16:19], a[192:195], v[232:235]
v_mfma_f32_16x16x32_bf16 v[232:235], v[84:87], a[232:235], v[232:235]
v_mfma_f32_16x16x32_bf16 v[248:251], v[16:19], a[196:199], v[248:251]
v_mfma_f32_16x16x32_bf16 v[248:251], v[84:87], a[236:239], v[248:251]
v_mfma_f32_16x16x32_bf16 v[108:111], v[88:91], a[160:163], v[108:111]
v_cndmask_b32_dpp v16, v68, v64, vcc quad_perm:[1,0,3,2] row_mask:0xf bank_mask:0xf
v_cndmask_b32_dpp v17, v69, v65, vcc quad_perm:[1,0,3,2] row_mask:0xf bank_mask:0xf
v_mfma_f32_16x16x32_bf16 v[108:111], v[20:23], a[200:203], v[108:111]
v_cndmask_b32_dpp v18, v70, v66, vcc quad_perm:[1,0,3,2] row_mask:0xf bank_mask:0xf
v_cndmask_b32_dpp v19, v71, v67, vcc quad_perm:[1,0,3,2] row_mask:0xf bank_mask:0xf
v_mfma_f32_16x16x32_bf16 v[124:127], v[88:91], a[164:167], v[124:127]
ds_read_b128 a[160:163], v1 offset:30720
s_mov_b64 vcc, s[78:79]
v_cndmask_b32_dpp v68, v64, v68, vcc quad_perm:[1,0,3,2] row_mask:0xf bank_mask:0xf
v_mfma_f32_16x16x32_bf16 v[124:127], v[20:23], a[204:207], v[124:127]
ds_read_b128 a[200:203], v1 offset:35840
v_cndmask_b32_dpp v69, v65, v69, vcc quad_perm:[1,0,3,2] row_mask:0xf bank_mask:0xf
v_mfma_f32_16x16x32_bf16 v[140:143], v[88:91], a[168:171], v[140:143]
ds_read_b128 a[164:167], v1 offset:31744
v_cndmask_b32_dpp v70, v66, v70, vcc quad_perm:[1,0,3,2] row_mask:0xf bank_mask:0xf
v_mfma_f32_16x16x32_bf16 v[140:143], v[20:23], a[208:211], v[140:143]
ds_read_b128 a[204:207], v1 offset:36864
v_cndmask_b32_dpp v71, v67, v71, vcc quad_perm:[1,0,3,2] row_mask:0xf bank_mask:0xf
v_mfma_f32_16x16x32_bf16 v[156:159], v[88:91], a[172:175], v[156:159]
ds_read_b128 a[168:171], v1 offset:32768
v_mfma_f32_16x16x32_bf16 v[156:159], v[20:23], a[212:215], v[156:159]
ds_read_b128 a[208:211], v1 offset:37888
v_mfma_f32_16x16x32_bf16 v[172:175], v[88:91], a[176:179], v[172:175]
ds_read_b128 a[172:175], v1 offset:33792
v_mfma_f32_16x16x32_bf16 v[172:175], v[20:23], a[216:219], v[172:175]
ds_read_b128 a[212:215], v1 offset:38912
v_mfma_f32_16x16x32_bf16 v[188:191], v[88:91], a[180:183], v[188:191]
ds_read_b128 a[176:179], v1 offset:34816
v_mfma_f32_16x16x32_bf16 v[188:191], v[20:23], a[220:223], v[188:191]
ds_read_b128 a[216:219], v1 offset:39936
v_mfma_f32_16x16x32_bf16 v[204:207], v[88:91], a[184:187], v[204:207]
ds_read_b128 a[180:183], v2 offset:6144
v_mfma_f32_16x16x32_bf16 v[204:207], v[20:23], a[224:227], v[204:207]
ds_read_b128 a[220:223], v2 offset:11264
v_mfma_f32_16x16x32_bf16 v[220:223], v[88:91], a[188:191], v[220:223]
)"
R"(ds_read_b128 a[184:187], v2 offset:7168
v_mfma_f32_16x16x32_bf16 v[220:223], v[20:23], a[228:231], v[220:223]
ds_read_b128 a[224:227], v2 offset:12288
v_mfma_f32_16x16x32_bf16 v[236:239], v[88:91], a[192:195], v[236:239]
s_waitcnt lgkmcnt(10)
ds_read_b128 a[188:191], v2 offset:8192
v_mfma_f32_16x16x32_bf16 v[236:239], v[20:23], a[232:235], v[236:239]
ds_read_b128 a[228:231], v2 offset:13312
v_mfma_f32_16x16x32_bf16 v[252:255], v[88:91], a[196:199], v[252:255]
ds_read_b128 a[192:195], v2 offset:9216
v_mfma_f32_16x16x32_bf16 v[252:255], v[20:23], a[236:239], v[252:255]
ds_read_b128 a[232:235], v2 offset:14336
v_mfma_f32_16x16x32_bf16 v[96:99], v[16:19], a[160:163], v[96:99]
s_waitcnt lgkmcnt(10)
ds_read_b128 a[196:199], v2 offset:10240
v_cndmask_b32_dpp v20, v72, v76, vcc quad_perm:[1,0,3,2] row_mask:0xf bank_mask:0xf
v_mfma_f32_16x16x32_bf16 v[96:99], v[68:71], a[200:203], v[96:99]
ds_read_b128 a[236:239], v2 offset:15360
v_cndmask_b32_dpp v21, v73, v77, vcc quad_perm:[1,0,3,2] row_mask:0xf bank_mask:0xf
v_mfma_f32_16x16x32_bf16 v[112:115], v[16:19], a[164:167], v[112:115]
v_cndmask_b32_dpp v22, v74, v78, vcc quad_perm:[1,0,3,2] row_mask:0xf bank_mask:0xf
v_cndmask_b32_dpp v23, v75, v79, vcc quad_perm:[1,0,3,2] row_mask:0xf bank_mask:0xf
v_mfma_f32_16x16x32_bf16 v[112:115], v[68:71], a[204:207], v[112:115]
s_mov_b64 vcc, s[76:77]
v_cndmask_b32_dpp v72, v76, v72, vcc quad_perm:[1,0,3,2] row_mask:0xf bank_mask:0xf
v_cndmask_b32_dpp v73, v77, v73, vcc quad_perm:[1,0,3,2] row_mask:0xf bank_mask:0xf
v_mfma_f32_16x16x32_bf16 v[128:131], v[16:19], a[168:171], v[128:131]
v_cndmask_b32_dpp v74, v78, v74, vcc quad_perm:[1,0,3,2] row_mask:0xf bank_mask:0xf
v_cndmask_b32_dpp v75, v79, v75, vcc quad_perm:[1,0,3,2] row_mask:0xf bank_mask:0xf
v_mfma_f32_16x16x32_bf16 v[128:131], v[68:71], a[208:211], v[128:131]
v_mfma_f32_16x16x32_bf16 v[144:147], v[16:19], a[172:175], v[144:147]
v_mfma_f32_16x16x32_bf16 v[144:147], v[68:71], a[212:215], v[144:147]
s_waitcnt lgkmcnt(11)
v_mfma_f32_16x16x32_bf16 v[160:163], v[16:19], a[176:179], v[160:163]
s_waitcnt lgkmcnt(10)
v_mfma_f32_16x16x32_bf16 v[160:163], v[68:71], a[216:219], v[160:163]
s_waitcnt lgkmcnt(9)
v_mfma_f32_16x16x32_bf16 v[176:179], v[16:19], a[180:183], v[176:179]
s_waitcnt lgkmcnt(8)
v_mfma_f32_16x16x32_bf16 v[176:179], v[68:71], a[220:223], v[176:179]
s_waitcnt lgkmcnt(7)
v_mfma_f32_16x16x32_bf16 v[192:195], v[16:19], a[184:187], v[192:195]
s_waitcnt lgkmcnt(6)
v_mfma_f32_16x16x32_bf16 v[192:195], v[68:71], a[224:227], v[192:195]
s_waitcnt lgkmcnt(5)
v_mfma_f32_16x16x32_bf16 v[208:211], v[16:19], a[188:191], v[208:211]
s_waitcnt lgkmcnt(4)
v_mfma_f32_16x16x32_bf16 v[208:211], v[68:71], a[228:231], v[208:211]
s_waitcnt lgkmcnt(3)
v_mfma_f32_16x16x32_bf16 v[224:227], v[16:19], a[192:195], v[224:227]
s_waitcnt lgkmcnt(2)
v_mfma_f32_16x16x32_bf16 v[224:227], v[68:71], a[232:235], v[224:227]
s_waitcnt lgkmcnt(1)
v_mfma_f32_16x16x32_bf16 v[240:243], v[16:19], a[196:199], v[240:243]
s_waitcnt lgkmcnt(0)
v_mfma_f32_16x16x32_bf16 v[240:243], v[68:71], a[236:239], v[240:243]
v_mfma_f32_16x16x32_bf16 v[100:103], v[72:75], a[160:163], v[100:103]
s_waitcnt vmcnt(0)
v_cndmask_b32_dpp v16, v36, v32, vcc quad_perm:[1,0,3,2] row_mask:0xf bank_mask:0xf
v_cndmask_b32_dpp v17, v37, v33, vcc quad_perm:[1,0,3,2] row_mask:0xf bank_mask:0xf
v_mfma_f32_16x16x32_bf16 v[100:103], v[20:23], a[200:203], v[100:103]
v_cndmask_b32_dpp v18, v38, v34, vcc quad_perm:[1,0,3,2] row_mask:0xf bank_mask:0xf
v_cndmask_b32_dpp v19, v39, v35, vcc quad_perm:[1,0,3,2] row_mask:0xf bank_mask:0xf
v_mfma_f32_16x16x32_bf16 v[116:119], v[72:75], a[164:167], v[116:119]
s_mov_b64 vcc, s[78:79]
v_cndmask_b32_dpp v36, v32, v36, vcc quad_perm:[1,0,3,2] row_mask:0xf bank_mask:0xf
v_cndmask_b32_dpp v37, v33, v37, vcc quad_perm:[1,0,3,2] row_mask:0xf bank_mask:0xf
v_mfma_f32_16x16x32_bf16 v[116:119], v[20:23], a[204:207], v[116:119]
v_cndmask_b32_dpp v38, v34, v38, vcc quad_perm:[1,0,3,2] row_mask:0xf bank_mask:0xf
v_cndmask_b32_dpp v39, v35, v39, vcc quad_perm:[1,0,3,2] row_mask:0xf bank_mask:0xf
v_mfma_f32_16x16x32_bf16 v[132:135], v[72:75], a[168:171], v[132:135]
v_mfma_f32_16x16x32_bf16 v[132:135], v[20:23], a[208:211], v[132:135]
v_mfma_f32_16x16x32_bf16 v[148:151], v[72:75], a[172:175], v[148:151]
v_mfma_f32_16x16x32_bf16 v[148:151], v[20:23], a[212:215], v[148:151]
v_mfma_f32_16x16x32_bf16 v[164:167], v[72:75], a[176:179], v[164:167]
v_mfma_f32_16x16x32_bf16 v[164:167], v[20:23], a[216:219], v[164:167]
v_mfma_f32_16x16x32_bf16 v[180:183], v[72:75], a[180:183], v[180:183]
v_mfma_f32_16x16x32_bf16 v[180:183], v[20:23], a[220:223], v[180:183]
v_mfma_f32_16x16x32_bf16 v[196:199], v[72:75], a[184:187], v[196:199]
v_mfma_f32_16x16x32_bf16 v[196:199], v[20:23], a[224:227], v[196:199]
v_mfma_f32_16x16x32_bf16 v[212:215], v[72:75], a[188:191], v[212:215]
v_mfma_f32_16x16x32_bf16 v[212:215], v[20:23], a[228:231], v[212:215]
v_mfma_f32_16x16x32_bf16 v[228:231], v[72:75], a[192:195], v[228:231]
v_mfma_f32_16x16x32_bf16 v[228:231], v[20:23], a[232:235], v[228:231]
v_mfma_f32_16x16x32_bf16 v[244:247], v[72:75], a[196:199], v[244:247]
v_mfma_f32_16x16x32_bf16 v[244:247], v[20:23], a[236:239], v[244:247]
v_mfma_f32_16x16x32_bf16 v[104:107], v[16:19], a[160:163], v[104:107]
v_cndmask_b32_dpp v20, v40, v44, vcc quad_perm:[1,0,3,2] row_mask:0xf bank_mask:0xf
v_cndmask_b32_dpp v21, v41, v45, vcc quad_perm:[1,0,3,2] row_mask:0xf bank_mask:0xf
v_mfma_f32_16x16x32_bf16 v[104:107], v[36:39], a[200:203], v[104:107]
v_cndmask_b32_dpp v22, v42, v46, vcc quad_perm:[1,0,3,2] row_mask:0xf bank_mask:0xf
v_cndmask_b32_dpp v23, v43, v47, vcc quad_perm:[1,0,3,2] row_mask:0xf bank_mask:0xf
v_mfma_f32_16x16x32_bf16 v[120:123], v[16:19], a[164:167], v[120:123]
s_mov_b64 vcc, s[76:77]
v_cndmask_b32_dpp v40, v44, v40, vcc quad_perm:[1,0,3,2] row_mask:0xf bank_mask:0xf
v_cndmask_b32_dpp v41, v45, v41, vcc quad_perm:[1,0,3,2] row_mask:0xf bank_mask:0xf
v_mfma_f32_16x16x32_bf16 v[120:123], v[36:39], a[204:207], v[120:123]
v_cndmask_b32_dpp v42, v46, v42, vcc quad_perm:[1,0,3,2] row_mask:0xf bank_mask:0xf
v_cndmask_b32_dpp v43, v47, v43, vcc quad_perm:[1,0,3,2] row_mask:0xf bank_mask:0xf
v_mfma_f32_16x16x32_bf16 v[136:139], v[16:19], a[168:171], v[136:139]
v_mfma_f32_16x16x32_bf16 v[136:139], v[36:39], a[208:211], v[136:139]
v_mfma_f32_16x16x32_bf16 v[152:155], v[16:19], a[172:175], v[152:155]
v_mfma_f32_16x16x32_bf16 v[152:155], v[36:39], a[212:215], v[152:155]
v_mfma_f32_16x16x32_bf16 v[168:171], v[16:19], a[176:179], v[168:171]
v_mfma_f32_16x16x32_bf16 v[168:171], v[36:39], a[216:219], v[168:171]
v_mfma_f32_16x16x32_bf16 v[184:187], v[16:19], a[180:183], v[184:187]
v_mfma_f32_16x16x32_bf16 v[184:187], v[36:39], a[220:223], v[184:187]
v_mfma_f32_16x16x32_bf16 v[200:203], v[16:19], a[184:187], v[200:203]
v_mfma_f32_16x16x32_bf16 v[200:203], v[36:39], a[224:227], v[200:203]
v_mfma_f32_16x16x32_bf16 v[216:219], v[16:19], a[188:191], v[216:219]
v_mfma_f32_16x16x32_bf16 v[216:219], v[36:39], a[228:231], v[216:219]
v_mfma_f32_16x16x32_bf16 v[232:235], v[16:19], a[192:195], v[232:235]
v_mfma_f32_16x16x32_bf16 v[232:235], v[36:39], a[232:235], v[232:235]
v_mfma_f32_16x16x32_bf16 v[248:251], v[16:19], a[196:199], v[248:251]
v_mfma_f32_16x16x32_bf16 v[248:251], v[36:39], a[236:239], v[248:251]
v_mfma_f32_16x16x32_bf16 v[108:111], v[40:43], a[160:163], v[108:111]
v_mfma_f32_16x16x32_bf16 v[108:111], v[20:23], a[200:203], v[108:111]
v_mfma_f32_16x16x32_bf16 v[124:127], v[40:43], a[164:167], v[124:127]
v_mfma_f32_16x16x32_bf16 v[124:127], v[20:23], a[204:207], v[124:127]
v_mfma_f32_16x16x32_bf16 v[140:143], v[40:43], a[168:171], v[140:143]
v_mfma_f32_16x16x32_bf16 v[140:143], v[20:23], a[208:211], v[140:143]
v_mfma_f32_16x16x32_bf16 v[156:159], v[40:43], a[172:175], v[156:159]
v_mfma_f32_16x16x32_bf16 v[156:159], v[20:23], a[212:215], v[156:159]
v_mfma_f32_16x16x32_bf16 v[172:175], v[40:43], a[176:179], v[172:175]
v_mfma_f32_16x16x32_bf16 v[172:175], v[20:23], a[216:219], v[172:175]
v_mfma_f32_16x16x32_bf16 v[188:191], v[40:43], a[180:183], v[188:191]
v_mfma_f32_16x16x32_bf16 v[188:191], v[20:23], a[220:223], v[188:191]
v_mfma_f32_16x16x32_bf16 v[204:207], v[40:43], a[184:187], v[204:207]
v_mfma_f32_16x16x32_bf16 v[204:207], v[20:23], a[224:227], v[204:207]
v_mfma_f32_16x16x32_bf16 v[220:223], v[40:43], a[188:191], v[220:223]
v_mfma_f32_16x16x32_bf16 v[220:223], v[20:23], a[228:231], v[220:223]
v_mfma_f32_16x16x32_bf16 v[236:239], v[40:43], a[192:195], v[236:239]
v_mfma_f32_16x16x32_bf16 v[236:239], v[20:23], a[232:235], v[236:239]
v_mfma_f32_16x16x32_bf16 v[252:255], v[40:43], a[196:199], v[252:255]
s_mov_b32 s80, 32768
v_mfma_f32_16x16x32_bf16 v[252:255], v[20:23], a[236:239], v[252:255]
buffer_load_dwordx2 v[32:33], v5, s[40:43], s80 offen nt
buffer_load_dwordx2 v[34:35], v5, s[40:43], s80 offen offset:512 nt
buffer_load_dwordx2 v[36:37], v5, s[40:43], s80 offen offset:1024 nt
buffer_load_dwordx2 v[38:39], v5, s[40:43], s80 offen offset:1536 nt
v_max3_f32 v80, v24, v96, v97
buffer_load_dwordx2 v[40:41], v5, s[40:43], s80 offen offset:2048 nt
buffer_load_dwordx2 v[42:43], v5, s[40:43], s80 offen offset:2560 nt
buffer_load_dwordx2 v[44:45], v5, s[40:43], s80 offen offset:3072 nt
buffer_load_dwordx2 v[46:47], v5, s[40:43], s80 offen offset:3584 nt
v_max3_f32 v81, v25, v112, v113
v_max3_f32 v82, v26, v128, v129
v_max3_f32 v83, v27, v144, v145
v_max3_f32 v84, v28, v160, v161
v_max3_f32 v80, v80, v98, v99
v_max3_f32 v81, v81, v114, v115
v_max3_f32 v82, v82, v130, v131
v_max3_f32 v83, v83, v146, v147
v_max3_f32 v84, v84, v162, v163
v_max3_f32 v80, v80, v100, v101
v_max3_f32 v81, v81, v116, v117
v_max3_f32 v82, v82, v132, v133
v_max3_f32 v83, v83, v148, v149
v_max3_f32 v84, v84, v164, v165
v_max3_f32 v80, v80, v102, v103
v_max3_f32 v81, v81, v118, v119
v_max3_f32 v82, v82, v134, v135
v_max3_f32 v83, v83, v150, v151
v_max3_f32 v84, v84, v166, v167
v_max3_f32 v80, v80, v104, v105
v_max3_f32 v81, v81, v120, v121
v_max3_f32 v82, v82, v136, v137
v_max3_f32 v83, v83, v152, v153
v_max3_f32 v84, v84, v168, v169
v_max3_f32 v80, v80, v106, v107
v_max3_f32 v81, v81, v122, v123
v_max3_f32 v82, v82, v138, v139
v_max3_f32 v83, v83, v154, v155
v_max3_f32 v84, v84, v170, v171
v_max3_f32 v80, v80, v108, v109
v_max3_f32 v81, v81, v124, v125
v_max3_f32 v82, v82, v140, v141
v_max3_f32 v83, v83, v156, v157
v_max3_f32 v84, v84, v172, v173
v_max3_f32 v80, v80, v110, v111
v_max3_f32 v81, v81, v126, v127
v_max3_f32 v82, v82, v142, v143
v_max3_f32 v83, v83, v158, v159
v_max3_f32 v84, v84, v174, v175
v_max3_f32 v85, v29, v176, v177
v_max3_f32 v86, v30, v192, v193
v_max3_f32 v87, v31, v208, v209
v_max3_f32 v88, v9, v224, v225
v_max3_f32 v89, v10, v240, v241
v_max3_f32 v85, v85, v178, v179
v_max3_f32 v86, v86, v194, v195
v_max3_f32 v87, v87, v210, v211
v_max3_f32 v88, v88, v226, v227
v_max3_f32 v89, v89, v242, v243
v_max3_f32 v85, v85, v180, v181
v_max3_f32 v86, v86, v196, v197
v_max3_f32 v87, v87, v212, v213
v_max3_f32 v88, v88, v228, v229
v_max3_f32 v89, v89, v244, v245
v_max3_f32 v85, v85, v182, v183
)"
R"(v_max3_f32 v86, v86, v198, v199
v_max3_f32 v87, v87, v214, v215
v_max3_f32 v88, v88, v230, v231
v_max3_f32 v89, v89, v246, v247
v_max3_f32 v85, v85, v184, v185
v_max3_f32 v86, v86, v200, v201
v_max3_f32 v87, v87, v216, v217
v_max3_f32 v88, v88, v232, v233
v_max3_f32 v89, v89, v248, v249
v_max3_f32 v85, v85, v186, v187
v_max3_f32 v86, v86, v202, v203
v_max3_f32 v87, v87, v218, v219
v_max3_f32 v88, v88, v234, v235
v_max3_f32 v89, v89, v250, v251
v_max3_f32 v85, v85, v188, v189
v_max3_f32 v86, v86, v204, v205
v_max3_f32 v87, v87, v220, v221
v_max3_f32 v88, v88, v236, v237
v_max3_f32 v89, v89, v252, v253
v_max3_f32 v85, v85, v190, v191
v_max3_f32 v86, v86, v206, v207
v_max3_f32 v87, v87, v222, v223
v_max3_f32 v88, v88, v238, v239
v_max3_f32 v89, v89, v254, v255
v_mov_b32 v21, 0x3f803f80
v_accvgpr_write_b32 a160, v21
v_accvgpr_write_b32 a161, v21
v_accvgpr_write_b32 a162, v21
v_accvgpr_write_b32 a163, v21
v_mov_b32 v16, v80
v_mov_b32 v17, v81
v_mov_b32 v18, v82
v_mov_b32 v19, v83
v_mov_b32 v20, v84
v_permlane32_swap_b32 v16, v80
v_permlane32_swap_b32 v17, v81
v_permlane32_swap_b32 v18, v82
v_permlane32_swap_b32 v19, v83
v_permlane32_swap_b32 v20, v84
v_max_f32 v80, v16, v80
v_max_f32 v81, v17, v81
v_max_f32 v82, v18, v82
v_max_f32 v83, v19, v83
v_max_f32 v84, v20, v84
v_mov_b32 v16, v80
v_mov_b32 v17, v81
v_mov_b32 v18, v82
v_mov_b32 v19, v83
v_mov_b32 v20, v84
v_permlane16_swap_b32 v16, v80
v_permlane16_swap_b32 v17, v81
v_permlane16_swap_b32 v18, v82
v_permlane16_swap_b32 v19, v83
v_permlane16_swap_b32 v20, v84
v_max_f32 v80, v16, v80
v_max_f32 v81, v17, v81
v_max_f32 v82, v18, v82
v_max_f32 v83, v19, v83
v_max_f32 v84, v20, v84
v_mov_b32 v16, v85
v_mov_b32 v17, v86
v_mov_b32 v18, v87
v_mov_b32 v19, v88
v_mov_b32 v20, v89
v_permlane32_swap_b32 v16, v85
v_permlane32_swap_b32 v17, v86
v_permlane32_swap_b32 v18, v87
v_permlane32_swap_b32 v19, v88
v_permlane32_swap_b32 v20, v89
v_max_f32 v85, v16, v85
v_max_f32 v86, v17, v86
v_max_f32 v87, v18, v87
v_max_f32 v88, v19, v88
v_max_f32 v89, v20, v89
v_mov_b32 v16, v85
v_mov_b32 v17, v86
v_mov_b32 v18, v87
v_mov_b32 v19, v88
v_mov_b32 v20, v89
v_permlane16_swap_b32 v16, v85
v_permlane16_swap_b32 v17, v86
v_permlane16_swap_b32 v18, v87
v_permlane16_swap_b32 v19, v88
v_permlane16_swap_b32 v20, v89
v_max_f32 v85, v16, v85
v_max_f32 v86, v17, v86
v_max_f32 v87, v18, v87
v_max_f32 v88, v19, v88
v_max_f32 v89, v20, v89
v_accvgpr_read_b32 v9, a248
v_accvgpr_read_b32 v0, a247
s_barrier
ds_write_b32 v9, v80 offset:0
ds_write_b32 v9, v81 offset:256
ds_write_b32 v9, v82 offset:512
ds_write_b32 v9, v83 offset:768
ds_write_b32 v9, v84 offset:1024
ds_write_b32 v9, v85 offset:1280
ds_write_b32 v9, v86 offset:1536
ds_write_b32 v9, v87 offset:1792
ds_write_b32 v9, v88 offset:2048
ds_write_b32 v9, v89 offset:2304
v_and_b32 v10, 0xf0, v1
v_add_u32 v12, 0xfffec000, v0
s_waitcnt lgkmcnt(0)
s_barrier
s_add_u32 s91, s90, -1
s_or_b32 s91, s91, s92
s_cmp_lg_u32 s91, 0
s_cbranch_scc1 LpubA_%=
s_mov_b64 exec, 1
v_accvgpr_read_b32 v16, a252
v_accvgpr_read_b32 v17, a253
s_xor_b32 s91, s61, 0
v_add_u32 v16, 1, v16
v_add_u32 v17, 1, v17
v_mov_b32 v18, s91
buffer_store_dwordx2 v[16:17], v18, s[56:59], 0 offen sc1
s_mov_b64 exec, -1
LpubA_%=:
s_nop 1
ds_read_b128 v[16:19], v10 offset:0
ds_read_b128 v[20:23], v10 offset:256
ds_read_b128 v[24:27], v10 offset:512
ds_read_b128 v[28:31], v10 offset:768
s_waitcnt lgkmcnt(3)
v_max3_f32 v16, v16, v17, v18
v_max_f32 v16, v16, v19
v_mul_f32 v80, s86, v16
ds_read_b128 v[16:19], v10 offset:1024
s_waitcnt lgkmcnt(3)
v_max3_f32 v20, v20, v21, v22
v_max_f32 v20, v20, v23
v_mul_f32 v81, s86, v20
ds_read_b128 v[20:23], v10 offset:1280
s_waitcnt lgkmcnt(3)
v_max3_f32 v24, v24, v25, v26
v_max_f32 v24, v24, v27
v_mul_f32 v82, s86, v24
ds_read_b128 v[24:27], v10 offset:1536
s_waitcnt lgkmcnt(3)
v_max3_f32 v28, v28, v29, v30
v_max_f32 v28, v28, v31
v_mul_f32 v83, s86, v28
ds_read_b128 v[28:31], v10 offset:1792
s_waitcnt lgkmcnt(3)
v_max3_f32 v16, v16, v17, v18
v_max_f32 v16, v16, v19
v_mul_f32 v84, s86, v16
ds_read_b128 v[16:19], v10 offset:2048
s_waitcnt lgkmcnt(3)
v_max3_f32 v20, v20, v21, v22
v_max_f32 v20, v20, v23
v_mul_f32 v85, s86, v20
ds_read_b128 v[20:23], v10 offset:2304
s_waitcnt lgkmcnt(3)
v_max3_f32 v24, v24, v25, v26
v_max_f32 v24, v24, v27
v_mul_f32 v86, s86, v24
s_waitcnt lgkmcnt(2)
v_max3_f32 v28, v28, v29, v30
v_max_f32 v28, v28, v31
v_mul_f32 v87, s86, v28
s_waitcnt lgkmcnt(1)
v_max3_f32 v16, v16, v17, v18
v_max_f32 v16, v16, v19
v_mul_f32 v88, s86, v16
s_waitcnt lgkmcnt(0)
v_max3_f32 v20, v20, v21, v22
v_max_f32 v20, v20, v23
v_mul_f32 v89, s86, v20
s_barrier
v_mov_b32 v90, v80
v_pk_fma_f32 v[96:97], v[96:97], s[86:87], v[90:91] op_sel_hi:[1,1,0] neg_lo:[0,0,1] neg_hi:[0,0,1]
v_pk_fma_f32 v[98:99], v[98:99], s[86:87], v[90:91] op_sel_hi:[1,1,0] neg_lo:[0,0,1] neg_hi:[0,0,1]
v_pk_fma_f32 v[100:101], v[100:101], s[86:87], v[90:91] op_sel_hi:[1,1,0] neg_lo:[0,0,1] neg_hi:[0,0,1]
v_pk_fma_f32 v[102:103], v[102:103], s[86:87], v[90:91] op_sel_hi:[1,1,0] neg_lo:[0,0,1] neg_hi:[0,0,1]
v_pk_fma_f32 v[104:105], v[104:105], s[86:87], v[90:91] op_sel_hi:[1,1,0] neg_lo:[0,0,1] neg_hi:[0,0,1]
v_pk_fma_f32 v[106:107], v[106:107], s[86:87], v[90:91] op_sel_hi:[1,1,0] neg_lo:[0,0,1] neg_hi:[0,0,1]
v_pk_fma_f32 v[108:109], v[108:109], s[86:87], v[90:91] op_sel_hi:[1,1,0] neg_lo:[0,0,1] neg_hi:[0,0,1]
v_pk_fma_f32 v[110:111], v[110:111], s[86:87], v[90:91] op_sel_hi:[1,1,0] neg_lo:[0,0,1] neg_hi:[0,0,1]
v_exp_f32 v96, v96
v_exp_f32 v97, v97
v_exp_f32 v98, v98
v_exp_f32 v99, v99
v_exp_f32 v100, v100
v_exp_f32 v101, v101
v_exp_f32 v102, v102
v_exp_f32 v103, v103
v_exp_f32 v104, v104
v_exp_f32 v105, v105
v_exp_f32 v106, v106
v_exp_f32 v107, v107
v_exp_f32 v108, v108
v_exp_f32 v109, v109
v_exp_f32 v110, v110
v_exp_f32 v111, v111
v_cvt_pk_bf16_f32 v96, v96, v97
v_cvt_pk_bf16_f32 v97, v98, v99
v_cvt_pk_bf16_f32 v98, v100, v101
)"
R"(v_cvt_pk_bf16_f32 v99, v102, v103
v_cvt_pk_bf16_f32 v104, v104, v105
v_cvt_pk_bf16_f32 v105, v106, v107
v_cvt_pk_bf16_f32 v106, v108, v109
v_cvt_pk_bf16_f32 v107, v110, v111
v_mfma_f32_16x16x32_bf16 a[164:167], a[160:163], v[96:99], 0
ds_write_b128 v0, v[96:99] offset:0
v_mfma_f32_16x16x32_bf16 a[164:167], a[160:163], v[104:107], a[164:167]
ds_write_b128 v0, v[104:107] offset:10240
s_add_u32 s80, s80, 16384
s_waitcnt lgkmcnt(1)
buffer_load_dwordx2 v[96:97], v5, s[40:43], s80 offen nt
buffer_load_dwordx2 v[98:99], v5, s[40:43], s80 offen offset:512 nt
buffer_load_dwordx2 v[100:101], v5, s[40:43], s80 offen offset:1024 nt
v_mov_b32 v90, v81
buffer_load_dwordx2 v[102:103], v5, s[40:43], s80 offen offset:1536 nt
s_waitcnt lgkmcnt(0)
buffer_load_dwordx2 v[104:105], v5, s[40:43], s80 offen offset:2048 nt
buffer_load_dwordx2 v[106:107], v5, s[40:43], s80 offen offset:2560 nt
buffer_load_dwordx2 v[108:109], v5, s[40:43], s80 offen offset:3072 nt
v_pk_fma_f32 v[112:113], v[112:113], s[86:87], v[90:91] op_sel_hi:[1,1,0] neg_lo:[0,0,1] neg_hi:[0,0,1]
buffer_load_dwordx2 v[110:111], v5, s[40:43], s80 offen offset:3584 nt
v_pk_fma_f32 v[114:115], v[114:115], s[86:87], v[90:91] op_sel_hi:[1,1,0] neg_lo:[0,0,1] neg_hi:[0,0,1]
v_pk_fma_f32 v[116:117], v[116:117], s[86:87], v[90:91] op_sel_hi:[1,1,0] neg_lo:[0,0,1] neg_hi:[0,0,1]
v_pk_fma_f32 v[118:119], v[118:119], s[86:87], v[90:91] op_sel_hi:[1,1,0] neg_lo:[0,0,1] neg_hi:[0,0,1]
v_pk_fma_f32 v[120:121], v[120:121], s[86:87], v[90:91] op_sel_hi:[1,1,0] neg_lo:[0,0,1] neg_hi:[0,0,1]
v_pk_fma_f32 v[122:123], v[122:123], s[86:87], v[90:91] op_sel_hi:[1,1,0] neg_lo:[0,0,1] neg_hi:[0,0,1]
v_pk_fma_f32 v[124:125], v[124:125], s[86:87], v[90:91] op_sel_hi:[1,1,0] neg_lo:[0,0,1] neg_hi:[0,0,1]
v_pk_fma_f32 v[126:127], v[126:127], s[86:87], v[90:91] op_sel_hi:[1,1,0] neg_lo:[0,0,1] neg_hi:[0,0,1]
v_exp_f32 v112, v112
v_exp_f32 v113, v113
v_exp_f32 v114, v114
v_exp_f32 v115, v115
v_exp_f32 v116, v116
v_exp_f32 v117, v117
v_exp_f32 v118, v118
v_exp_f32 v119, v119
v_exp_f32 v120, v120
v_exp_f32 v121, v121
v_exp_f32 v122, v122
v_exp_f32 v123, v123
v_exp_f32 v124, v124
v_exp_f32 v125, v125
v_exp_f32 v126, v126
v_exp_f32 v127, v127
v_cvt_pk_bf16_f32 v112, v112, v113
v_cvt_pk_bf16_f32 v113, v114, v115
v_cvt_pk_bf16_f32 v114, v116, v117
v_cvt_pk_bf16_f32 v115, v118, v119
v_cvt_pk_bf16_f32 v120, v120, v121
v_cvt_pk_bf16_f32 v121, v122, v123
v_cvt_pk_bf16_f32 v122, v124, v125
v_cvt_pk_bf16_f32 v123, v126, v127
v_mfma_f32_16x16x32_bf16 a[168:171], a[160:163], v[112:115], 0
ds_write_b128 v0, v[112:115] offset:1024
v_mfma_f32_16x16x32_bf16 a[168:171], a[160:163], v[120:123], a[168:171]
ds_write_b128 v0, v[120:123] offset:11264
s_mov_b32 s80, 98304
s_waitcnt lgkmcnt(1)
buffer_load_dwordx2 v[112:113], v5, s[40:43], s80 offen nt
buffer_load_dwordx2 v[114:115], v5, s[40:43], s80 offen offset:512 nt
buffer_load_dwordx2 v[116:117], v5, s[40:43], s80 offen offset:1024 nt
v_mov_b32 v90, v82
buffer_load_dwordx2 v[118:119], v5, s[40:43], s80 offen offset:1536 nt
s_waitcnt lgkmcnt(0)
buffer_load_dwordx2 v[120:121], v5, s[40:43], s80 offen offset:2048 nt
buffer_load_dwordx2 v[122:123], v5, s[40:43], s80 offen offset:2560 nt
buffer_load_dwordx2 v[124:125], v5, s[40:43], s80 offen offset:3072 nt
v_pk_fma_f32 v[128:129], v[128:129], s[86:87], v[90:91] op_sel_hi:[1,1,0] neg_lo:[0,0,1] neg_hi:[0,0,1]
buffer_load_dwordx2 v[126:127], v5, s[40:43], s80 offen offset:3584 nt
v_pk_fma_f32 v[130:131], v[130:131], s[86:87], v[90:91] op_sel_hi:[1,1,0] neg_lo:[0,0,1] neg_hi:[0,0,1]
v_pk_fma_f32 v[132:133], v[132:133], s[86:87], v[90:91] op_sel_hi:[1,1,0] neg_lo:[0,0,1] neg_hi:[0,0,1]
v_pk_fma_f32 v[134:135], v[134:135], s[86:87], v[90:91] op_sel_hi:[1,1,0] neg_lo:[0,0,1] neg_hi:[0,0,1]
v_pk_fma_f32 v[136:137], v[136:137], s[86:87], v[90:91] op_sel_hi:[1,1,0] neg_lo:[0,0,1] neg_hi:[0,0,1]
v_pk_fma_f32 v[138:139], v[138:139], s[86:87], v[90:91] op_sel_hi:[1,1,0] neg_lo:[0,0,1] neg_hi:[0,0,1]
v_pk_fma_f32 v[140:141], v[140:141], s[86:87], v[90:91] op_sel_hi:[1,1,0] neg_lo:[0,0,1] neg_hi:[0,0,1]
v_pk_fma_f32 v[142:143], v[142:143], s[86:87], v[90:91] op_sel_hi:[1,1,0] neg_lo:[0,0,1] neg_hi:[0,0,1]
v_exp_f32 v128, v128
v_exp_f32 v129, v129
v_exp_f32 v130, v130
v_exp_f32 v131, v131
v_exp_f32 v132, v132
v_exp_f32 v133, v133
v_exp_f32 v134, v134
v_exp_f32 v135, v135
v_exp_f32 v136, v136
v_exp_f32 v137, v137
v_exp_f32 v138, v138
v_exp_f32 v139, v139
v_exp_f32 v140, v140
v_exp_f32 v141, v141
v_exp_f32 v142, v142
v_exp_f32 v143, v143
v_cvt_pk_bf16_f32 v128, v128, v129
v_cvt_pk_bf16_f32 v129, v130, v131
v_cvt_pk_bf16_f32 v130, v132, v133
v_cvt_pk_bf16_f32 v131, v134, v135
v_cvt_pk_bf16_f32 v136, v136, v137
v_cvt_pk_bf16_f32 v137, v138, v139
v_cvt_pk_bf16_f32 v138, v140, v141
v_cvt_pk_bf16_f32 v139, v142, v143
v_mfma_f32_16x16x32_bf16 a[172:175], a[160:163], v[128:131], 0
ds_write_b128 v0, v[128:131] offset:2048
v_mfma_f32_16x16x32_bf16 a[172:175], a[160:163], v[136:139], a[172:175]
ds_write_b128 v0, v[136:139] offset:12288
s_add_u32 s80, s80, 16384
s_waitcnt lgkmcnt(1)
buffer_load_dwordx2 v[128:129], v5, s[40:43], s80 offen nt
buffer_load_dwordx2 v[130:131], v5, s[40:43], s80 offen offset:512 nt
buffer_load_dwordx2 v[132:133], v5, s[40:43], s80 offen offset:1024 nt
v_mov_b32 v90, v83
v_pk_fma_f32 v[144:145], v[144:145], s[86:87], v[90:91] op_sel_hi:[1,1,0] neg_lo:[0,0,1] neg_hi:[0,0,1]
v_pk_fma_f32 v[146:147], v[146:147], s[86:87], v[90:91] op_sel_hi:[1,1,0] neg_lo:[0,0,1] neg_hi:[0,0,1]
v_pk_fma_f32 v[148:149], v[148:149], s[86:87], v[90:91] op_sel_hi:[1,1,0] neg_lo:[0,0,1] neg_hi:[0,0,1]
v_pk_fma_f32 v[150:151], v[150:151], s[86:87], v[90:91] op_sel_hi:[1,1,0] neg_lo:[0,0,1] neg_hi:[0,0,1]
v_pk_fma_f32 v[152:153], v[152:153], s[86:87], v[90:91] op_sel_hi:[1,1,0] neg_lo:[0,0,1] neg_hi:[0,0,1]
v_pk_fma_f32 v[154:155], v[154:155], s[86:87], v[90:91] op_sel_hi:[1,1,0] neg_lo:[0,0,1] neg_hi:[0,0,1]
v_pk_fma_f32 v[156:157], v[156:157], s[86:87], v[90:91] op_sel_hi:[1,1,0] neg_lo:[0,0,1] neg_hi:[0,0,1]
v_pk_fma_f32 v[158:159], v[158:159], s[86:87], v[90:91] op_sel_hi:[1,1,0] neg_lo:[0,0,1] neg_hi:[0,0,1]
v_exp_f32 v144, v144
v_exp_f32 v145, v145
v_exp_f32 v146, v146
v_exp_f32 v147, v147
v_exp_f32 v148, v148
v_exp_f32 v149, v149
v_exp_f32 v150, v150
v_exp_f32 v151, v151
v_exp_f32 v152, v152
v_exp_f32 v153, v153
v_exp_f32 v154, v154
v_exp_f32 v155, v155
v_exp_f32 v156, v156
v_exp_f32 v157, v157
v_exp_f32 v158, v158
v_exp_f32 v159, v159
v_cvt_pk_bf16_f32 v144, v144, v145
v_cvt_pk_bf16_f32 v145, v146, v147
v_cvt_pk_bf16_f32 v146, v148, v149
v_cvt_pk_bf16_f32 v147, v150, v151
v_cvt_pk_bf16_f32 v152, v152, v153
v_cvt_pk_bf16_f32 v153, v154, v155
v_cvt_pk_bf16_f32 v154, v156, v157
v_cvt_pk_bf16_f32 v155, v158, v159
v_mfma_f32_16x16x32_bf16 a[176:179], a[160:163], v[144:147], 0
ds_write_b128 v0, v[144:147] offset:3072
v_mfma_f32_16x16x32_bf16 a[176:179], a[160:163], v[152:155], a[176:179]
ds_write_b128 v0, v[152:155] offset:13312
v_mov_b32 v90, v84
v_pk_fma_f32 v[160:161], v[160:161], s[86:87], v[90:91] op_sel_hi:[1,1,0] neg_lo:[0,0,1] neg_hi:[0,0,1]
v_pk_fma_f32 v[162:163], v[162:163], s[86:87], v[90:91] op_sel_hi:[1,1,0] neg_lo:[0,0,1] neg_hi:[0,0,1]
v_pk_fma_f32 v[164:165], v[164:165], s[86:87], v[90:91] op_sel_hi:[1,1,0] neg_lo:[0,0,1] neg_hi:[0,0,1]
v_pk_fma_f32 v[166:167], v[166:167], s[86:87], v[90:91] op_sel_hi:[1,1,0] neg_lo:[0,0,1] neg_hi:[0,0,1]
v_pk_fma_f32 v[168:169], v[168:169], s[86:87], v[90:91] op_sel_hi:[1,1,0] neg_lo:[0,0,1] neg_hi:[0,0,1]
v_pk_fma_f32 v[170:171], v[170:171], s[86:87], v[90:91] op_sel_hi:[1,1,0] neg_lo:[0,0,1] neg_hi:[0,0,1]
v_pk_fma_f32 v[172:173], v[172:173], s[86:87], v[90:91] op_sel_hi:[1,1,0] neg_lo:[0,0,1] neg_hi:[0,0,1]
v_pk_fma_f32 v[174:175], v[174:175], s[86:87], v[90:91] op_sel_hi:[1,1,0] neg_lo:[0,0,1] neg_hi:[0,0,1]
v_exp_f32 v160, v160
v_exp_f32 v161, v161
v_exp_f32 v162, v162
v_exp_f32 v163, v163
v_exp_f32 v164, v164
v_exp_f32 v165, v165
v_exp_f32 v166, v166
v_exp_f32 v167, v167
v_exp_f32 v168, v168
v_exp_f32 v169, v169
v_exp_f32 v170, v170
v_exp_f32 v171, v171
v_exp_f32 v172, v172
v_exp_f32 v173, v173
v_exp_f32 v174, v174
v_exp_f32 v175, v175
v_cvt_pk_bf16_f32 v160, v160, v161
v_cvt_pk_bf16_f32 v161, v162, v163
v_cvt_pk_bf16_f32 v162, v164, v165
v_cvt_pk_bf16_f32 v163, v166, v167
v_cvt_pk_bf16_f32 v168, v168, v169
v_cvt_pk_bf16_f32 v169, v170, v171
v_cvt_pk_bf16_f32 v170, v172, v173
v_cvt_pk_bf16_f32 v171, v174, v175
v_mfma_f32_16x16x32_bf16 a[180:183], a[160:163], v[160:163], 0
ds_write_b128 v0, v[160:163] offset:4096
v_mfma_f32_16x16x32_bf16 a[180:183], a[160:163], v[168:171], a[180:183]
ds_write_b128 v0, v[168:171] offset:14336
v_mov_b32 v90, v85
v_pk_fma_f32 v[176:177], v[176:177], s[86:87], v[90:91] op_sel_hi:[1,1,0] neg_lo:[0,0,1] neg_hi:[0,0,1]
v_pk_fma_f32 v[178:179], v[178:179], s[86:87], v[90:91] op_sel_hi:[1,1,0] neg_lo:[0,0,1] neg_hi:[0,0,1]
v_pk_fma_f32 v[180:181], v[180:181], s[86:87], v[90:91] op_sel_hi:[1,1,0] neg_lo:[0,0,1] neg_hi:[0,0,1]
v_pk_fma_f32 v[182:183], v[182:183], s[86:87], v[90:91] op_sel_hi:[1,1,0] neg_lo:[0,0,1] neg_hi:[0,0,1]
v_pk_fma_f32 v[184:185], v[184:185], s[86:87], v[90:91] op_sel_hi:[1,1,0] neg_lo:[0,0,1] neg_hi:[0,0,1]
v_pk_fma_f32 v[186:187], v[186:187], s[86:87], v[90:91] op_sel_hi:[1,1,0] neg_lo:[0,0,1] neg_hi:[0,0,1]
v_pk_fma_f32 v[188:189], v[188:189], s[86:87], v[90:91] op_sel_hi:[1,1,0] neg_lo:[0,0,1] neg_hi:[0,0,1]
v_pk_fma_f32 v[190:191], v[190:191], s[86:87], v[90:91] op_sel_hi:[1,1,0] neg_lo:[0,0,1] neg_hi:[0,0,1]
v_exp_f32 v176, v176
v_exp_f32 v177, v177
v_exp_f32 v178, v178
v_exp_f32 v179, v179
v_exp_f32 v180, v180
v_exp_f32 v181, v181
v_exp_f32 v182, v182
)"
R"(v_exp_f32 v183, v183
buffer_load_dwordx2 v[134:135], v5, s[40:43], s80 offen offset:1536 nt
s_waitcnt lgkmcnt(4)
buffer_load_dwordx2 v[136:137], v5, s[40:43], s80 offen offset:2048 nt
buffer_load_dwordx2 v[138:139], v5, s[40:43], s80 offen offset:2560 nt
v_exp_f32 v184, v184
v_exp_f32 v185, v185
v_exp_f32 v186, v186
v_exp_f32 v187, v187
v_exp_f32 v188, v188
v_exp_f32 v189, v189
v_exp_f32 v190, v190
v_exp_f32 v191, v191
v_cvt_pk_bf16_f32 v176, v176, v177
v_cvt_pk_bf16_f32 v177, v178, v179
v_cvt_pk_bf16_f32 v178, v180, v181
v_cvt_pk_bf16_f32 v179, v182, v183
v_cvt_pk_bf16_f32 v184, v184, v185
v_cvt_pk_bf16_f32 v185, v186, v187
v_cvt_pk_bf16_f32 v186, v188, v189
v_cvt_pk_bf16_f32 v187, v190, v191
v_mfma_f32_16x16x32_bf16 a[184:187], a[160:163], v[176:179], 0
ds_write_b128 v0, v[176:179] offset:5120
v_mfma_f32_16x16x32_bf16 a[184:187], a[160:163], v[184:187], a[184:187]
ds_write_b128 v0, v[184:187] offset:15360
v_mov_b32 v90, v86
v_pk_fma_f32 v[192:193], v[192:193], s[86:87], v[90:91] op_sel_hi:[1,1,0] neg_lo:[0,0,1] neg_hi:[0,0,1]
v_pk_fma_f32 v[194:195], v[194:195], s[86:87], v[90:91] op_sel_hi:[1,1,0] neg_lo:[0,0,1] neg_hi:[0,0,1]
v_pk_fma_f32 v[196:197], v[196:197], s[86:87], v[90:91] op_sel_hi:[1,1,0] neg_lo:[0,0,1] neg_hi:[0,0,1]
v_pk_fma_f32 v[198:199], v[198:199], s[86:87], v[90:91] op_sel_hi:[1,1,0] neg_lo:[0,0,1] neg_hi:[0,0,1]
buffer_load_dwordx2 v[140:141], v5, s[40:43], s80 offen offset:3072 nt
buffer_load_dwordx2 v[142:143], v5, s[40:43], s80 offen offset:3584 nt
s_mov_b32 s80, 163840
s_waitcnt lgkmcnt(5)
buffer_load_dwordx2 v[144:145], v5, s[40:43], s80 offen nt
v_pk_fma_f32 v[200:201], v[200:201], s[86:87], v[90:91] op_sel_hi:[1,1,0] neg_lo:[0,0,1] neg_hi:[0,0,1]
v_pk_fma_f32 v[202:203], v[202:203], s[86:87], v[90:91] op_sel_hi:[1,1,0] neg_lo:[0,0,1] neg_hi:[0,0,1]
v_pk_fma_f32 v[204:205], v[204:205], s[86:87], v[90:91] op_sel_hi:[1,1,0] neg_lo:[0,0,1] neg_hi:[0,0,1]
v_pk_fma_f32 v[206:207], v[206:207], s[86:87], v[90:91] op_sel_hi:[1,1,0] neg_lo:[0,0,1] neg_hi:[0,0,1]
v_exp_f32 v192, v192
v_exp_f32 v193, v193
v_exp_f32 v194, v194
v_exp_f32 v195, v195
v_exp_f32 v196, v196
v_exp_f32 v197, v197
v_exp_f32 v198, v198
v_exp_f32 v199, v199
v_exp_f32 v200, v200
v_exp_f32 v201, v201
v_exp_f32 v202, v202
v_exp_f32 v203, v203
v_exp_f32 v204, v204
v_exp_f32 v205, v205
v_exp_f32 v206, v206
v_exp_f32 v207, v207
v_cvt_pk_bf16_f32 v192, v192, v193
v_cvt_pk_bf16_f32 v193, v194, v195
v_cvt_pk_bf16_f32 v194, v196, v197
v_cvt_pk_bf16_f32 v195, v198, v199
v_cvt_pk_bf16_f32 v200, v200, v201
v_cvt_pk_bf16_f32 v201, v202, v203
v_cvt_pk_bf16_f32 v202, v204, v205
v_cvt_pk_bf16_f32 v203, v206, v207
v_mfma_f32_16x16x32_bf16 a[188:191], a[160:163], v[192:195], 0
ds_write_b128 v0, v[192:195] offset:6144
v_mfma_f32_16x16x32_bf16 a[188:191], a[160:163], v[200:203], a[188:191]
buffer_load_dwordx2 v[146:147], v5, s[40:43], s80 offen offset:512 nt
buffer_load_dwordx2 v[148:149], v5, s[40:43], s80 offen offset:1024 nt
buffer_load_dwordx2 v[150:151], v5, s[40:43], s80 offen offset:1536 nt
ds_write_b128 v0, v[200:203] offset:16384
v_mov_b32 v90, v87
v_pk_fma_f32 v[208:209], v[208:209], s[86:87], v[90:91] op_sel_hi:[1,1,0] neg_lo:[0,0,1] neg_hi:[0,0,1]
v_pk_fma_f32 v[210:211], v[210:211], s[86:87], v[90:91] op_sel_hi:[1,1,0] neg_lo:[0,0,1] neg_hi:[0,0,1]
v_pk_fma_f32 v[212:213], v[212:213], s[86:87], v[90:91] op_sel_hi:[1,1,0] neg_lo:[0,0,1] neg_hi:[0,0,1]
v_pk_fma_f32 v[214:215], v[214:215], s[86:87], v[90:91] op_sel_hi:[1,1,0] neg_lo:[0,0,1] neg_hi:[0,0,1]
v_pk_fma_f32 v[216:217], v[216:217], s[86:87], v[90:91] op_sel_hi:[1,1,0] neg_lo:[0,0,1] neg_hi:[0,0,1]
v_pk_fma_f32 v[218:219], v[218:219], s[86:87], v[90:91] op_sel_hi:[1,1,0] neg_lo:[0,0,1] neg_hi:[0,0,1]
v_pk_fma_f32 v[220:221], v[220:221], s[86:87], v[90:91] op_sel_hi:[1,1,0] neg_lo:[0,0,1] neg_hi:[0,0,1]
v_pk_fma_f32 v[222:223], v[222:223], s[86:87], v[90:91] op_sel_hi:[1,1,0] neg_lo:[0,0,1] neg_hi:[0,0,1]
v_exp_f32 v208, v208
v_exp_f32 v209, v209
v_exp_f32 v210, v210
v_exp_f32 v211, v211
v_exp_f32 v212, v212
v_exp_f32 v213, v213
v_exp_f32 v214, v214
v_exp_f32 v215, v215
v_exp_f32 v216, v216
v_exp_f32 v217, v217
v_exp_f32 v218, v218
v_exp_f32 v219, v219
s_waitcnt lgkmcnt(6)
buffer_load_dwordx2 v[152:153], v5, s[40:43], s80 offen offset:2048 nt
buffer_load_dwordx2 v[154:155], v5, s[40:43], s80 offen offset:2560 nt
buffer_load_dwordx2 v[156:157], v5, s[40:43], s80 offen offset:3072 nt
v_exp_f32 v220, v220
v_exp_f32 v221, v221
v_exp_f32 v222, v222
v_exp_f32 v223, v223
v_cvt_pk_bf16_f32 v208, v208, v209
v_cvt_pk_bf16_f32 v209, v210, v211
v_cvt_pk_bf16_f32 v210, v212, v213
v_cvt_pk_bf16_f32 v211, v214, v215
v_cvt_pk_bf16_f32 v216, v216, v217
v_cvt_pk_bf16_f32 v217, v218, v219
v_cvt_pk_bf16_f32 v218, v220, v221
v_cvt_pk_bf16_f32 v219, v222, v223
v_mfma_f32_16x16x32_bf16 a[192:195], a[160:163], v[208:211], 0
ds_write_b128 v0, v[208:211] offset:7168
v_mfma_f32_16x16x32_bf16 a[192:195], a[160:163], v[216:219], a[192:195]
ds_write_b128 v0, v[216:219] offset:17408
v_mov_b32 v90, v88
v_pk_fma_f32 v[224:225], v[224:225], s[86:87], v[90:91] op_sel_hi:[1,1,0] neg_lo:[0,0,1] neg_hi:[0,0,1]
v_pk_fma_f32 v[226:227], v[226:227], s[86:87], v[90:91] op_sel_hi:[1,1,0] neg_lo:[0,0,1] neg_hi:[0,0,1]
v_pk_fma_f32 v[228:229], v[228:229], s[86:87], v[90:91] op_sel_hi:[1,1,0] neg_lo:[0,0,1] neg_hi:[0,0,1]
v_pk_fma_f32 v[230:231], v[230:231], s[86:87], v[90:91] op_sel_hi:[1,1,0] neg_lo:[0,0,1] neg_hi:[0,0,1]
v_pk_fma_f32 v[232:233], v[232:233], s[86:87], v[90:91] op_sel_hi:[1,1,0] neg_lo:[0,0,1] neg_hi:[0,0,1]
v_pk_fma_f32 v[234:235], v[234:235], s[86:87], v[90:91] op_sel_hi:[1,1,0] neg_lo:[0,0,1] neg_hi:[0,0,1]
v_pk_fma_f32 v[236:237], v[236:237], s[86:87], v[90:91] op_sel_hi:[1,1,0] neg_lo:[0,0,1] neg_hi:[0,0,1]
v_pk_fma_f32 v[238:239], v[238:239], s[86:87], v[90:91] op_sel_hi:[1,1,0] neg_lo:[0,0,1] neg_hi:[0,0,1]
v_exp_f32 v224, v224
v_exp_f32 v225, v225
v_exp_f32 v226, v226
buffer_load_dwordx2 v[158:159], v5, s[40:43], s80 offen offset:3584 nt
s_add_u32 s80, s80, 16384
s_waitcnt lgkmcnt(7)
buffer_load_dwordx2 v[160:161], v5, s[40:43], s80 offen nt
buffer_load_dwordx2 v[162:163], v5, s[40:43], s80 offen offset:512 nt
v_exp_f32 v227, v227
v_exp_f32 v228, v228
v_exp_f32 v229, v229
v_exp_f32 v230, v230
v_exp_f32 v231, v231
v_exp_f32 v232, v232
v_exp_f32 v233, v233
v_exp_f32 v234, v234
v_exp_f32 v235, v235
v_exp_f32 v236, v236
v_exp_f32 v237, v237
v_exp_f32 v238, v238
v_exp_f32 v239, v239
v_cvt_pk_bf16_f32 v224, v224, v225
v_cvt_pk_bf16_f32 v225, v226, v227
v_cvt_pk_bf16_f32 v226, v228, v229
v_cvt_pk_bf16_f32 v227, v230, v231
v_cvt_pk_bf16_f32 v232, v232, v233
v_cvt_pk_bf16_f32 v233, v234, v235
v_cvt_pk_bf16_f32 v234, v236, v237
v_cvt_pk_bf16_f32 v235, v238, v239
v_mfma_f32_16x16x32_bf16 a[196:199], a[160:163], v[224:227], 0
ds_write_b128 v0, v[224:227] offset:8192
v_mfma_f32_16x16x32_bf16 a[196:199], a[160:163], v[232:235], a[196:199]
ds_write_b128 v0, v[232:235] offset:18432
buffer_load_dwordx2 v[164:165], v5, s[40:43], s80 offen offset:1024 nt
buffer_load_dwordx2 v[166:167], v5, s[40:43], s80 offen offset:1536 nt
s_waitcnt lgkmcnt(8)
buffer_load_dwordx2 v[168:169], v5, s[40:43], s80 offen offset:2048 nt
v_mov_b32 v90, v89
v_pk_fma_f32 v[240:241], v[240:241], s[86:87], v[90:91] op_sel_hi:[1,1,0] neg_lo:[0,0,1] neg_hi:[0,0,1]
v_pk_fma_f32 v[242:243], v[242:243], s[86:87], v[90:91] op_sel_hi:[1,1,0] neg_lo:[0,0,1] neg_hi:[0,0,1]
v_pk_fma_f32 v[244:245], v[244:245], s[86:87], v[90:91] op_sel_hi:[1,1,0] neg_lo:[0,0,1] neg_hi:[0,0,1]
v_pk_fma_f32 v[246:247], v[246:247], s[86:87], v[90:91] op_sel_hi:[1,1,0] neg_lo:[0,0,1] neg_hi:[0,0,1]
v_pk_fma_f32 v[248:249], v[248:249], s[86:87], v[90:91] op_sel_hi:[1,1,0] neg_lo:[0,0,1] neg_hi:[0,0,1]
v_pk_fma_f32 v[250:251], v[250:251], s[86:87], v[90:91] op_sel_hi:[1,1,0] neg_lo:[0,0,1] neg_hi:[0,0,1]
v_pk_fma_f32 v[252:253], v[252:253], s[86:87], v[90:91] op_sel_hi:[1,1,0] neg_lo:[0,0,1] neg_hi:[0,0,1]
v_pk_fma_f32 v[254:255], v[254:255], s[86:87], v[90:91] op_sel_hi:[1,1,0] neg_lo:[0,0,1] neg_hi:[0,0,1]
v_exp_f32 v240, v240
v_exp_f32 v241, v241
v_exp_f32 v242, v242
v_exp_f32 v243, v243
v_exp_f32 v244, v244
v_exp_f32 v245, v245
v_exp_f32 v246, v246
v_exp_f32 v247, v247
v_exp_f32 v248, v248
v_exp_f32 v249, v249
v_exp_f32 v250, v250
v_exp_f32 v251, v251
v_exp_f32 v252, v252
v_exp_f32 v253, v253
v_exp_f32 v254, v254
v_exp_f32 v255, v255
v_cvt_pk_bf16_f32 v240, v240, v241
buffer_load_dwordx2 v[170:171], v5, s[40:43], s80 offen offset:2560 nt
buffer_load_dwordx2 v[172:173], v5, s[40:43], s80 offen offset:3072 nt
buffer_load_dwordx2 v[174:175], v5, s[40:43], s80 offen offset:3584 nt
v_cvt_pk_bf16_f32 v241, v242, v243
v_cvt_pk_bf16_f32 v242, v244, v245
v_cvt_pk_bf16_f32 v243, v246, v247
v_cvt_pk_bf16_f32 v248, v248, v249
v_cvt_pk_bf16_f32 v249, v250, v251
v_cvt_pk_bf16_f32 v250, v252, v253
v_cvt_pk_bf16_f32 v251, v254, v255
v_mfma_f32_16x16x32_bf16 a[200:203], a[160:163], v[240:243], 0
ds_write_b128 v0, v[240:243] offset:9216
v_mfma_f32_16x16x32_bf16 a[200:203], a[160:163], v[248:251], a[200:203]
ds_write_b128 v0, v[248:251] offset:19456
v_mov_b32 v90, v85
v_accvgpr_read_b32 v64, a80
v_accvgpr_read_b32 v65, a81
)"
R"(v_accvgpr_read_b32 v66, a82
v_accvgpr_read_b32 v67, a83
v_accvgpr_read_b32 v68, a84
v_accvgpr_read_b32 v69, a85
v_accvgpr_read_b32 v70, a86
v_accvgpr_read_b32 v71, a87
v_accvgpr_read_b32 v72, a88
v_accvgpr_read_b32 v73, a89
v_accvgpr_read_b32 v74, a90
v_accvgpr_read_b32 v75, a91
v_accvgpr_read_b32 v76, a92
v_accvgpr_read_b32 v77, a93
v_accvgpr_read_b32 v78, a94
v_accvgpr_read_b32 v79, a95
v_pk_fma_f32 v[64:65], v[64:65], s[86:87], v[90:91] op_sel_hi:[1,1,0] neg_lo:[0,0,1] neg_hi:[0,0,1]
v_pk_fma_f32 v[66:67], v[66:67], s[86:87], v[90:91] op_sel_hi:[1,1,0] neg_lo:[0,0,1] neg_hi:[0,0,1]
v_pk_fma_f32 v[68:69], v[68:69], s[86:87], v[90:91] op_sel_hi:[1,1,0] neg_lo:[0,0,1] neg_hi:[0,0,1]
v_pk_fma_f32 v[70:71], v[70:71], s[86:87], v[90:91] op_sel_hi:[1,1,0] neg_lo:[0,0,1] neg_hi:[0,0,1]
v_pk_fma_f32 v[72:73], v[72:73], s[86:87], v[90:91] op_sel_hi:[1,1,0] neg_lo:[0,0,1] neg_hi:[0,0,1]
v_pk_fma_f32 v[74:75], v[74:75], s[86:87], v[90:91] op_sel_hi:[1,1,0] neg_lo:[0,0,1] neg_hi:[0,0,1]
v_pk_fma_f32 v[76:77], v[76:77], s[86:87], v[90:91] op_sel_hi:[1,1,0] neg_lo:[0,0,1] neg_hi:[0,0,1]
v_pk_fma_f32 v[78:79], v[78:79], s[86:87], v[90:91] op_sel_hi:[1,1,0] neg_lo:[0,0,1] neg_hi:[0,0,1]
v_exp_f32 v64, v64
v_exp_f32 v65, v65
v_exp_f32 v66, v66
v_exp_f32 v67, v67
v_exp_f32 v68, v68
v_exp_f32 v69, v69
v_exp_f32 v70, v70
v_exp_f32 v71, v71
v_exp_f32 v72, v72
v_exp_f32 v73, v73
v_exp_f32 v74, v74
v_exp_f32 v75, v75
v_exp_f32 v76, v76
v_exp_f32 v77, v77
v_exp_f32 v78, v78
v_exp_f32 v79, v79
v_cvt_pk_bf16_f32 v64, v64, v65
v_cvt_pk_bf16_f32 v65, v66, v67
v_cvt_pk_bf16_f32 v66, v68, v69
v_cvt_pk_bf16_f32 v67, v70, v71
v_cvt_pk_bf16_f32 v72, v72, v73
v_cvt_pk_bf16_f32 v73, v74, v75
v_cvt_pk_bf16_f32 v74, v76, v77
v_cvt_pk_bf16_f32 v75, v78, v79
v_mfma_f32_16x16x32_bf16 a[184:187], a[160:163], v[64:67], a[184:187]
ds_write_b128 v12, v[64:67] offset:5120
v_mfma_f32_16x16x32_bf16 a[184:187], a[160:163], v[72:75], a[184:187]
ds_write_b128 v12, v[72:75] offset:15360
v_mov_b32 v90, v86
v_accvgpr_read_b32 v64, a96
v_accvgpr_read_b32 v65, a97
v_accvgpr_read_b32 v66, a98
v_accvgpr_read_b32 v67, a99
v_accvgpr_read_b32 v68, a100
v_accvgpr_read_b32 v69, a101
v_accvgpr_read_b32 v70, a102
v_accvgpr_read_b32 v71, a103
v_accvgpr_read_b32 v72, a104
v_accvgpr_read_b32 v73, a105
v_accvgpr_read_b32 v74, a106
v_accvgpr_read_b32 v75, a107
v_accvgpr_read_b32 v76, a108
v_accvgpr_read_b32 v77, a109
v_accvgpr_read_b32 v78, a110
v_accvgpr_read_b32 v79, a111
v_pk_fma_f32 v[64:65], v[64:65], s[86:87], v[90:91] op_sel_hi:[1,1,0] neg_lo:[0,0,1] neg_hi:[0,0,1]
v_pk_fma_f32 v[66:67], v[66:67], s[86:87], v[90:91] op_sel_hi:[1,1,0] neg_lo:[0,0,1] neg_hi:[0,0,1]
v_pk_fma_f32 v[68:69], v[68:69], s[86:87], v[90:91] op_sel_hi:[1,1,0] neg_lo:[0,0,1] neg_hi:[0,0,1]
v_pk_fma_f32 v[70:71], v[70:71], s[86:87], v[90:91] op_sel_hi:[1,1,0] neg_lo:[0,0,1] neg_hi:[0,0,1]
v_pk_fma_f32 v[72:73], v[72:73], s[86:87], v[90:91] op_sel_hi:[1,1,0] neg_lo:[0,0,1] neg_hi:[0,0,1]
v_pk_fma_f32 v[74:75], v[74:75], s[86:87], v[90:91] op_sel_hi:[1,1,0] neg_lo:[0,0,1] neg_hi:[0,0,1]
v_pk_fma_f32 v[76:77], v[76:77], s[86:87], v[90:91] op_sel_hi:[1,1,0] neg_lo:[0,0,1] neg_hi:[0,0,1]
v_pk_fma_f32 v[78:79], v[78:79], s[86:87], v[90:91] op_sel_hi:[1,1,0] neg_lo:[0,0,1] neg_hi:[0,0,1]
v_exp_f32 v64, v64
v_exp_f32 v65, v65
v_exp_f32 v66, v66
v_exp_f32 v67, v67
v_exp_f32 v68, v68
v_exp_f32 v69, v69
v_exp_f32 v70, v70
v_exp_f32 v71, v71
v_exp_f32 v72, v72
v_exp_f32 v73, v73
v_exp_f32 v74, v74
v_exp_f32 v75, v75
v_exp_f32 v76, v76
v_exp_f32 v77, v77
v_exp_f32 v78, v78
v_exp_f32 v79, v79
v_cvt_pk_bf16_f32 v64, v64, v65
v_cvt_pk_bf16_f32 v65, v66, v67
v_cvt_pk_bf16_f32 v66, v68, v69
v_cvt_pk_bf16_f32 v67, v70, v71
v_cvt_pk_bf16_f32 v72, v72, v73
v_cvt_pk_bf16_f32 v73, v74, v75
v_cvt_pk_bf16_f32 v74, v76, v77
v_cvt_pk_bf16_f32 v75, v78, v79
v_mfma_f32_16x16x32_bf16 a[188:191], a[160:163], v[64:67], a[188:191]
ds_write_b128 v12, v[64:67] offset:6144
v_mfma_f32_16x16x32_bf16 a[188:191], a[160:163], v[72:75], a[188:191]
ds_write_b128 v12, v[72:75] offset:16384
v_mov_b32 v90, v87
v_accvgpr_read_b32 v64, a112
v_accvgpr_read_b32 v65, a113
v_accvgpr_read_b32 v66, a114
v_accvgpr_read_b32 v67, a115
v_accvgpr_read_b32 v68, a116
v_accvgpr_read_b32 v69, a117
v_accvgpr_read_b32 v70, a118
v_accvgpr_read_b32 v71, a119
v_accvgpr_read_b32 v72, a120
v_accvgpr_read_b32 v73, a121
v_accvgpr_read_b32 v74, a122
v_accvgpr_read_b32 v75, a123
v_accvgpr_read_b32 v76, a124
v_accvgpr_read_b32 v77, a125
v_accvgpr_read_b32 v78, a126
v_accvgpr_read_b32 v79, a127
v_pk_fma_f32 v[64:65], v[64:65], s[86:87], v[90:91] op_sel_hi:[1,1,0] neg_lo:[0,0,1] neg_hi:[0,0,1]
v_pk_fma_f32 v[66:67], v[66:67], s[86:87], v[90:91] op_sel_hi:[1,1,0] neg_lo:[0,0,1] neg_hi:[0,0,1]
v_pk_fma_f32 v[68:69], v[68:69], s[86:87], v[90:91] op_sel_hi:[1,1,0] neg_lo:[0,0,1] neg_hi:[0,0,1]
v_pk_fma_f32 v[70:71], v[70:71], s[86:87], v[90:91] op_sel_hi:[1,1,0] neg_lo:[0,0,1] neg_hi:[0,0,1]
v_pk_fma_f32 v[72:73], v[72:73], s[86:87], v[90:91] op_sel_hi:[1,1,0] neg_lo:[0,0,1] neg_hi:[0,0,1]
v_pk_fma_f32 v[74:75], v[74:75], s[86:87], v[90:91] op_sel_hi:[1,1,0] neg_lo:[0,0,1] neg_hi:[0,0,1]
v_pk_fma_f32 v[76:77], v[76:77], s[86:87], v[90:91] op_sel_hi:[1,1,0] neg_lo:[0,0,1] neg_hi:[0,0,1]
v_pk_fma_f32 v[78:79], v[78:79], s[86:87], v[90:91] op_sel_hi:[1,1,0] neg_lo:[0,0,1] neg_hi:[0,0,1]
v_exp_f32 v64, v64
v_exp_f32 v65, v65
v_exp_f32 v66, v66
v_exp_f32 v67, v67
v_exp_f32 v68, v68
v_exp_f32 v69, v69
v_exp_f32 v70, v70
v_exp_f32 v71, v71
v_exp_f32 v72, v72
v_exp_f32 v73, v73
v_exp_f32 v74, v74
v_exp_f32 v75, v75
v_exp_f32 v76, v76
v_exp_f32 v77, v77
v_exp_f32 v78, v78
v_exp_f32 v79, v79
v_cvt_pk_bf16_f32 v64, v64, v65
v_cvt_pk_bf16_f32 v65, v66, v67
v_cvt_pk_bf16_f32 v66, v68, v69
v_cvt_pk_bf16_f32 v67, v70, v71
v_cvt_pk_bf16_f32 v72, v72, v73
v_cvt_pk_bf16_f32 v73, v74, v75
v_cvt_pk_bf16_f32 v74, v76, v77
v_cvt_pk_bf16_f32 v75, v78, v79
v_mfma_f32_16x16x32_bf16 a[192:195], a[160:163], v[64:67], a[192:195]
s_waitcnt lgkmcnt(10)
ds_write_b128 v12, v[64:67] offset:7168
v_mfma_f32_16x16x32_bf16 a[192:195], a[160:163], v[72:75], a[192:195]
ds_write_b128 v12, v[72:75] offset:17408
v_mov_b32 v90, v88
v_accvgpr_read_b32 v64, a128
v_accvgpr_read_b32 v65, a129
v_accvgpr_read_b32 v66, a130
v_accvgpr_read_b32 v67, a131
v_accvgpr_read_b32 v68, a132
v_accvgpr_read_b32 v69, a133
v_accvgpr_read_b32 v70, a134
v_accvgpr_read_b32 v71, a135
v_accvgpr_read_b32 v72, a136
v_accvgpr_read_b32 v73, a137
v_accvgpr_read_b32 v74, a138
v_accvgpr_read_b32 v75, a139
v_accvgpr_read_b32 v76, a140
v_accvgpr_read_b32 v77, a141
v_accvgpr_read_b32 v78, a142
v_accvgpr_read_b32 v79, a143
v_pk_fma_f32 v[64:65], v[64:65], s[86:87], v[90:91] op_sel_hi:[1,1,0] neg_lo:[0,0,1] neg_hi:[0,0,1]
v_pk_fma_f32 v[66:67], v[66:67], s[86:87], v[90:91] op_sel_hi:[1,1,0] neg_lo:[0,0,1] neg_hi:[0,0,1]
v_pk_fma_f32 v[68:69], v[68:69], s[86:87], v[90:91] op_sel_hi:[1,1,0] neg_lo:[0,0,1] neg_hi:[0,0,1]
v_pk_fma_f32 v[70:71], v[70:71], s[86:87], v[90:91] op_sel_hi:[1,1,0] neg_lo:[0,0,1] neg_hi:[0,0,1]
v_pk_fma_f32 v[72:73], v[72:73], s[86:87], v[90:91] op_sel_hi:[1,1,0] neg_lo:[0,0,1] neg_hi:[0,0,1]
v_pk_fma_f32 v[74:75], v[74:75], s[86:87], v[90:91] op_sel_hi:[1,1,0] neg_lo:[0,0,1] neg_hi:[0,0,1]
v_pk_fma_f32 v[76:77], v[76:77], s[86:87], v[90:91] op_sel_hi:[1,1,0] neg_lo:[0,0,1] neg_hi:[0,0,1]
v_pk_fma_f32 v[78:79], v[78:79], s[86:87], v[90:91] op_sel_hi:[1,1,0] neg_lo:[0,0,1] neg_hi:[0,0,1]
v_exp_f32 v64, v64
v_exp_f32 v65, v65
v_exp_f32 v66, v66
v_exp_f32 v67, v67
v_exp_f32 v68, v68
v_exp_f32 v69, v69
v_exp_f32 v70, v70
v_exp_f32 v71, v71
v_exp_f32 v72, v72
v_exp_f32 v73, v73
v_exp_f32 v74, v74
v_exp_f32 v75, v75
v_exp_f32 v76, v76
v_exp_f32 v77, v77
v_exp_f32 v78, v78
v_exp_f32 v79, v79
v_cvt_pk_bf16_f32 v64, v64, v65
v_cvt_pk_bf16_f32 v65, v66, v67
)"
R"(v_cvt_pk_bf16_f32 v66, v68, v69
v_cvt_pk_bf16_f32 v67, v70, v71
v_cvt_pk_bf16_f32 v72, v72, v73
v_cvt_pk_bf16_f32 v73, v74, v75
v_cvt_pk_bf16_f32 v74, v76, v77
v_cvt_pk_bf16_f32 v75, v78, v79
v_mfma_f32_16x16x32_bf16 a[196:199], a[160:163], v[64:67], a[196:199]
ds_write_b128 v12, v[64:67] offset:8192
v_mfma_f32_16x16x32_bf16 a[196:199], a[160:163], v[72:75], a[196:199]
ds_write_b128 v12, v[72:75] offset:18432
v_mov_b32 v90, v89
v_accvgpr_read_b32 v64, a144
v_accvgpr_read_b32 v65, a145
v_accvgpr_read_b32 v66, a146
v_accvgpr_read_b32 v67, a147
v_accvgpr_read_b32 v68, a148
v_accvgpr_read_b32 v69, a149
v_accvgpr_read_b32 v70, a150
v_accvgpr_read_b32 v71, a151
v_accvgpr_read_b32 v72, a152
v_accvgpr_read_b32 v73, a153
v_accvgpr_read_b32 v74, a154
v_accvgpr_read_b32 v75, a155
v_accvgpr_read_b32 v76, a156
v_accvgpr_read_b32 v77, a157
v_accvgpr_read_b32 v78, a158
v_accvgpr_read_b32 v79, a159
v_pk_fma_f32 v[64:65], v[64:65], s[86:87], v[90:91] op_sel_hi:[1,1,0] neg_lo:[0,0,1] neg_hi:[0,0,1]
v_pk_fma_f32 v[66:67], v[66:67], s[86:87], v[90:91] op_sel_hi:[1,1,0] neg_lo:[0,0,1] neg_hi:[0,0,1]
v_pk_fma_f32 v[68:69], v[68:69], s[86:87], v[90:91] op_sel_hi:[1,1,0] neg_lo:[0,0,1] neg_hi:[0,0,1]
v_pk_fma_f32 v[70:71], v[70:71], s[86:87], v[90:91] op_sel_hi:[1,1,0] neg_lo:[0,0,1] neg_hi:[0,0,1]
v_pk_fma_f32 v[72:73], v[72:73], s[86:87], v[90:91] op_sel_hi:[1,1,0] neg_lo:[0,0,1] neg_hi:[0,0,1]
v_pk_fma_f32 v[74:75], v[74:75], s[86:87], v[90:91] op_sel_hi:[1,1,0] neg_lo:[0,0,1] neg_hi:[0,0,1]
v_pk_fma_f32 v[76:77], v[76:77], s[86:87], v[90:91] op_sel_hi:[1,1,0] neg_lo:[0,0,1] neg_hi:[0,0,1]
v_pk_fma_f32 v[78:79], v[78:79], s[86:87], v[90:91] op_sel_hi:[1,1,0] neg_lo:[0,0,1] neg_hi:[0,0,1]
v_exp_f32 v64, v64
v_exp_f32 v65, v65
v_exp_f32 v66, v66
v_exp_f32 v67, v67
v_exp_f32 v68, v68
v_exp_f32 v69, v69
v_exp_f32 v70, v70
v_exp_f32 v71, v71
v_exp_f32 v72, v72
v_exp_f32 v73, v73
v_exp_f32 v74, v74
v_exp_f32 v75, v75
v_exp_f32 v76, v76
v_exp_f32 v77, v77
v_exp_f32 v78, v78
v_exp_f32 v79, v79
v_cvt_pk_bf16_f32 v64, v64, v65
v_cvt_pk_bf16_f32 v65, v66, v67
v_cvt_pk_bf16_f32 v66, v68, v69
v_cvt_pk_bf16_f32 v67, v70, v71
v_cvt_pk_bf16_f32 v72, v72, v73
v_cvt_pk_bf16_f32 v73, v74, v75
v_cvt_pk_bf16_f32 v74, v76, v77
v_cvt_pk_bf16_f32 v75, v78, v79
v_mfma_f32_16x16x32_bf16 a[200:203], a[160:163], v[64:67], a[200:203]
s_waitcnt lgkmcnt(10)
ds_write_b128 v12, v[64:67] offset:9216
v_mfma_f32_16x16x32_bf16 a[200:203], a[160:163], v[72:75], a[200:203]
ds_write_b128 v12, v[72:75] offset:19456
v_accvgpr_read_b32 v48, a164
v_accvgpr_read_b32 v49, a168
v_accvgpr_read_b32 v50, a172
v_accvgpr_read_b32 v51, a176
v_accvgpr_read_b32 v52, a180
v_accvgpr_read_b32 v53, a184
v_accvgpr_read_b32 v54, a188
v_accvgpr_read_b32 v55, a192
v_accvgpr_read_b32 v56, a196
v_accvgpr_read_b32 v57, a200
v_mov_b32 v21, 0x3f803f80
v_mov_b32 v60, v21
v_mov_b32 v61, v21
v_mov_b32 v62, v21
v_mov_b32 v63, v21
s_mov_b32 vcc_lo, 0x00ff00ff
s_mov_b32 vcc_hi, 0x00ff00ff
s_waitcnt vmcnt(32)
v_perm_b32 v16, v34, v32, s84
v_perm_b32 v17, v38, v36, s84
v_perm_b32 v18, v42, v40, s84
v_perm_b32 v19, v46, v44, s84
v_perm_b32 v20, v98, v96, s84
v_perm_b32 v21, v102, v100, s84
v_perm_b32 v22, v106, v104, s84
v_perm_b32 v23, v110, v108, s84
s_waitcnt lgkmcnt(0)
s_barrier
ds_read_b128 a[160:163], v2 offset:16384
ds_read_b128 a[164:167], v2 offset:17408
ds_read_b128 a[168:171], v2 offset:18432
ds_read_b128 a[172:175], v2 offset:19456
ds_read_b128 a[176:179], v2 offset:20480
ds_read_b128 a[180:183], v2 offset:21504
ds_read_b128 a[184:187], v2 offset:22528
ds_read_b128 a[188:191], v2 offset:23552
ds_read_b128 a[192:195], v2 offset:24576
ds_read_b128 a[196:199], v2 offset:25600
ds_read_b128 a[200:203], v2 offset:26624
ds_read_b128 a[204:207], v2 offset:27648
ds_read_b128 a[208:211], v2 offset:28672
ds_read_b128 a[212:215], v2 offset:29696
s_waitcnt lgkmcnt(10)
ds_read_b128 a[216:219], v2 offset:30720
ds_read_b128 a[220:223], v2 offset:31744
ds_read_b128 a[224:227], v2 offset:32768
ds_read_b128 a[228:231], v2 offset:33792
s_waitcnt lgkmcnt(10)
ds_read_b128 a[232:235], v2 offset:34816
ds_read_b128 a[236:239], v2 offset:35840
v_mfma_f32_16x16x32_bf16 v[176:179], v[16:19], a[160:163], 0
s_waitcnt lgkmcnt(0)
v_perm_b32 v24, v34, v32, s85
v_perm_b32 v25, v38, v36, s85
v_mfma_f32_16x16x32_bf16 v[176:179], v[20:23], a[200:203], v[176:179]
v_perm_b32 v26, v42, v40, s85
v_perm_b32 v27, v46, v44, s85
v_mfma_f32_16x16x32_bf16 v[192:195], v[16:19], a[164:167], 0
v_perm_b32 v28, v98, v96, s85
v_perm_b32 v29, v102, v100, s85
v_mfma_f32_16x16x32_bf16 v[192:195], v[20:23], a[204:207], v[192:195]
v_perm_b32 v30, v106, v104, s85
v_perm_b32 v31, v110, v108, s85
v_mfma_f32_16x16x32_bf16 v[208:211], v[16:19], a[168:171], 0
v_accvgpr_read_b32 v64, a0
v_accvgpr_read_b32 v65, a1
v_mfma_f32_16x16x32_bf16 v[208:211], v[20:23], a[208:211], v[208:211]
v_mfma_f32_16x16x32_bf16 v[224:227], v[16:19], a[172:175], 0
v_accvgpr_read_b32 v66, a2
v_accvgpr_read_b32 v67, a3
v_mfma_f32_16x16x32_bf16 v[224:227], v[20:23], a[212:215], v[224:227]
v_accvgpr_read_b32 v68, a4
v_accvgpr_read_b32 v69, a5
v_mfma_f32_16x16x32_bf16 v[240:243], v[16:19], a[176:179], 0
v_accvgpr_read_b32 v70, a6
v_accvgpr_read_b32 v71, a7
v_mfma_f32_16x16x32_bf16 v[240:243], v[20:23], a[216:219], v[240:243]
v_accvgpr_read_b32 v72, a8
v_accvgpr_read_b32 v73, a9
v_mfma_f32_16x16x32_bf16 a[80:83], v[16:19], a[180:183], 0
v_mfma_f32_16x16x32_bf16 a[80:83], v[20:23], a[220:223], a[80:83]
v_accvgpr_read_b32 v74, a10
v_accvgpr_read_b32 v75, a11
v_mfma_f32_16x16x32_bf16 a[96:99], v[16:19], a[184:187], 0
v_accvgpr_read_b32 v76, a12
v_accvgpr_read_b32 v77, a13
v_mfma_f32_16x16x32_bf16 a[96:99], v[20:23], a[224:227], a[96:99]
v_accvgpr_read_b32 v78, a14
v_accvgpr_read_b32 v79, a15
v_mfma_f32_16x16x32_bf16 a[112:115], v[16:19], a[188:191], 0
v_fma_f32 v64, v64, s86, -v80
v_fma_f32 v65, v65, s86, -v80
v_mfma_f32_16x16x32_bf16 a[112:115], v[20:23], a[228:231], a[112:115]
v_mfma_f32_16x16x32_bf16 a[128:131], v[16:19], a[192:195], 0
v_fma_f32 v66, v66, s86, -v80
v_fma_f32 v67, v67, s86, -v80
v_mfma_f32_16x16x32_bf16 a[128:131], v[20:23], a[232:235], a[128:131]
v_fma_f32 v68, v68, s86, -v80
v_fma_f32 v69, v69, s86, -v80
v_mfma_f32_16x16x32_bf16 a[144:147], v[16:19], a[196:199], 0
v_fma_f32 v70, v70, s86, -v80
v_fma_f32 v71, v71, s86, -v80
v_mfma_f32_16x16x32_bf16 a[144:147], v[20:23], a[236:239], a[144:147]
v_fma_f32 v72, v72, s86, -v80
v_fma_f32 v73, v73, s86, -v80
v_mfma_f32_16x16x32_bf16 v[180:183], v[24:27], a[160:163], 0
v_perm_b32 v16, v35, v33, s84
v_perm_b32 v17, v39, v37, s84
v_mfma_f32_16x16x32_bf16 v[180:183], v[28:31], a[200:203], v[180:183]
v_perm_b32 v18, v43, v41, s84
v_perm_b32 v19, v47, v45, s84
v_mfma_f32_16x16x32_bf16 v[196:199], v[24:27], a[164:167], 0
v_perm_b32 v20, v99, v97, s84
v_perm_b32 v21, v103, v101, s84
v_mfma_f32_16x16x32_bf16 v[196:199], v[28:31], a[204:207], v[196:199]
v_perm_b32 v22, v107, v105, s84
v_perm_b32 v23, v111, v109, s84
v_mfma_f32_16x16x32_bf16 v[212:215], v[24:27], a[168:171], 0
v_fma_f32 v74, v74, s86, -v80
v_fma_f32 v75, v75, s86, -v80
v_mfma_f32_16x16x32_bf16 v[212:215], v[28:31], a[208:211], v[212:215]
v_mfma_f32_16x16x32_bf16 v[228:231], v[24:27], a[172:175], 0
v_fma_f32 v76, v76, s86, -v80
v_fma_f32 v77, v77, s86, -v80
v_mfma_f32_16x16x32_bf16 v[228:231], v[28:31], a[212:215], v[228:231]
v_fma_f32 v78, v78, s86, -v80
v_fma_f32 v79, v79, s86, -v80
v_mfma_f32_16x16x32_bf16 v[244:247], v[24:27], a[176:179], 0
v_exp_f32 v64, v64
v_mfma_f32_16x16x32_bf16 v[244:247], v[28:31], a[216:219], v[244:247]
v_exp_f32 v65, v65
v_mfma_f32_16x16x32_bf16 a[84:87], v[24:27], a[180:183], 0
v_mfma_f32_16x16x32_bf16 a[84:87], v[28:31], a[220:223], a[84:87]
v_exp_f32 v66, v66
v_mfma_f32_16x16x32_bf16 a[100:103], v[24:27], a[184:187], 0
v_exp_f32 v67, v67
)"
R"(v_mfma_f32_16x16x32_bf16 a[100:103], v[28:31], a[224:227], a[100:103]
v_exp_f32 v68, v68
v_mfma_f32_16x16x32_bf16 a[116:119], v[24:27], a[188:191], 0
v_exp_f32 v69, v69
v_mfma_f32_16x16x32_bf16 a[116:119], v[28:31], a[228:231], a[116:119]
v_mfma_f32_16x16x32_bf16 a[132:135], v[24:27], a[192:195], 0
v_exp_f32 v70, v70
v_mfma_f32_16x16x32_bf16 a[132:135], v[28:31], a[232:235], a[132:135]
v_exp_f32 v71, v71
v_mfma_f32_16x16x32_bf16 a[148:151], v[24:27], a[196:199], 0
v_exp_f32 v72, v72
v_mfma_f32_16x16x32_bf16 a[148:151], v[28:31], a[236:239], a[148:151]
v_exp_f32 v73, v73
v_mfma_f32_16x16x32_bf16 v[184:187], v[16:19], a[160:163], 0
v_perm_b32 v24, v35, v33, s85
v_perm_b32 v25, v39, v37, s85
v_mfma_f32_16x16x32_bf16 v[184:187], v[20:23], a[200:203], v[184:187]
v_perm_b32 v26, v43, v41, s85
v_perm_b32 v27, v47, v45, s85
v_mfma_f32_16x16x32_bf16 v[200:203], v[16:19], a[164:167], 0
s_mov_b32 s80, 229376
v_perm_b32 v28, v99, v97, s85
v_perm_b32 v29, v103, v101, s85
v_mfma_f32_16x16x32_bf16 v[200:203], v[20:23], a[204:207], v[200:203]
v_perm_b32 v30, v107, v105, s85
v_perm_b32 v31, v111, v109, s85
v_mfma_f32_16x16x32_bf16 v[216:219], v[16:19], a[168:171], 0
v_exp_f32 v74, v74
v_mfma_f32_16x16x32_bf16 v[216:219], v[20:23], a[208:211], v[216:219]
buffer_load_dwordx2 v[32:33], v5, s[40:43], s80 offen nt
v_mfma_f32_16x16x32_bf16 v[232:235], v[16:19], a[172:175], 0
v_exp_f32 v75, v75
v_mfma_f32_16x16x32_bf16 v[232:235], v[20:23], a[212:215], v[232:235]
v_exp_f32 v76, v76
v_mfma_f32_16x16x32_bf16 v[248:251], v[16:19], a[176:179], 0
v_exp_f32 v77, v77
v_mfma_f32_16x16x32_bf16 v[248:251], v[20:23], a[216:219], v[248:251]
v_exp_f32 v78, v78
v_mfma_f32_16x16x32_bf16 a[88:91], v[16:19], a[180:183], 0
buffer_load_dwordx2 v[34:35], v5, s[40:43], s80 offen offset:512 nt
v_mfma_f32_16x16x32_bf16 a[88:91], v[20:23], a[220:223], a[88:91]
v_exp_f32 v79, v79
v_mfma_f32_16x16x32_bf16 a[104:107], v[16:19], a[184:187], 0
v_cvt_pk_bf16_f32 v64, v64, v65
v_cvt_pk_bf16_f32 v65, v66, v67
v_mfma_f32_16x16x32_bf16 a[104:107], v[20:23], a[224:227], a[104:107]
v_cvt_pk_bf16_f32 v66, v68, v69
v_cvt_pk_bf16_f32 v67, v70, v71
v_mfma_f32_16x16x32_bf16 a[120:123], v[16:19], a[188:191], 0
v_cvt_pk_bf16_f32 v72, v72, v73
v_cvt_pk_bf16_f32 v73, v74, v75
v_mfma_f32_16x16x32_bf16 a[120:123], v[20:23], a[228:231], a[120:123]
buffer_load_dwordx2 v[36:37], v5, s[40:43], s80 offen offset:1024 nt
v_mfma_f32_16x16x32_bf16 a[136:139], v[16:19], a[192:195], 0
v_cvt_pk_bf16_f32 v74, v76, v77
v_cvt_pk_bf16_f32 v75, v78, v79
v_mfma_f32_16x16x32_bf16 a[136:139], v[20:23], a[232:235], a[136:139]
buffer_load_dwordx2 v[38:39], v5, s[40:43], s80 offen offset:1536 nt
v_mfma_f32_16x16x32_bf16 v[92:95], v[60:63], v[64:67], 0
ds_write_b128 v12, v[64:67] offset:0
v_mfma_f32_16x16x32_bf16 v[92:95], v[60:63], v[72:75], v[92:95]
ds_write_b128 v12, v[72:75] offset:10240
v_mfma_f32_16x16x32_bf16 a[152:155], v[16:19], a[196:199], 0
v_accvgpr_read_b32 v64, a16
v_accvgpr_read_b32 v65, a17
v_mfma_f32_16x16x32_bf16 a[152:155], v[20:23], a[236:239], a[152:155]
v_accvgpr_read_b32 v66, a18
v_accvgpr_read_b32 v67, a19
v_mfma_f32_16x16x32_bf16 v[188:191], v[24:27], a[160:163], 0
s_waitcnt vmcnt(20)
v_perm_b32 v16, v114, v112, s84
v_perm_b32 v17, v118, v116, s84
v_mfma_f32_16x16x32_bf16 v[188:191], v[28:31], a[200:203], v[188:191]
v_perm_b32 v18, v122, v120, s84
v_perm_b32 v19, v126, v124, s84
v_mfma_f32_16x16x32_bf16 v[204:207], v[24:27], a[164:167], 0
ds_read_b128 a[160:163], v2 offset:36864
v_perm_b32 v20, v130, v128, s84
v_mfma_f32_16x16x32_bf16 v[204:207], v[28:31], a[204:207], v[204:207]
ds_read_b128 a[200:203], v2 offset:47104
v_perm_b32 v21, v134, v132, s84
v_mfma_f32_16x16x32_bf16 v[220:223], v[24:27], a[168:171], 0
ds_read_b128 a[164:167], v2 offset:37888
v_perm_b32 v22, v138, v136, s84
v_mfma_f32_16x16x32_bf16 v[220:223], v[28:31], a[208:211], v[220:223]
ds_read_b128 a[204:207], v2 offset:48128
v_perm_b32 v23, v142, v140, s84
v_mfma_f32_16x16x32_bf16 v[236:239], v[24:27], a[172:175], 0
ds_read_b128 a[168:171], v2 offset:38912
v_accvgpr_read_b32 v68, a20
v_mfma_f32_16x16x32_bf16 v[236:239], v[28:31], a[212:215], v[236:239]
ds_read_b128 a[208:211], v2 offset:49152
v_accvgpr_read_b32 v69, a21
v_mfma_f32_16x16x32_bf16 v[252:255], v[24:27], a[176:179], 0
ds_read_b128 a[172:175], v2 offset:39936
v_add_f32 v48, v48, v92
v_mfma_f32_16x16x32_bf16 v[252:255], v[28:31], a[216:219], v[252:255]
ds_read_b128 a[212:215], v2 offset:50176
v_accvgpr_read_b32 v70, a22
v_mfma_f32_16x16x32_bf16 a[92:95], v[24:27], a[180:183], 0
ds_read_b128 a[176:179], v2 offset:40960
v_accvgpr_read_b32 v71, a23
v_mfma_f32_16x16x32_bf16 a[92:95], v[28:31], a[220:223], a[92:95]
ds_read_b128 a[216:219], v2 offset:51200
v_accvgpr_read_b32 v72, a24
v_mfma_f32_16x16x32_bf16 a[108:111], v[24:27], a[184:187], 0
ds_read_b128 a[180:183], v2 offset:41984
v_accvgpr_read_b32 v73, a25
v_mfma_f32_16x16x32_bf16 a[108:111], v[28:31], a[224:227], a[108:111]
ds_read_b128 a[220:223], v2 offset:52224
v_accvgpr_read_b32 v74, a26
v_mfma_f32_16x16x32_bf16 a[124:127], v[24:27], a[188:191], 0
s_waitcnt lgkmcnt(10)
ds_read_b128 a[184:187], v2 offset:43008
v_accvgpr_read_b32 v75, a27
v_mfma_f32_16x16x32_bf16 a[124:127], v[28:31], a[228:231], a[124:127]
ds_read_b128 a[224:227], v2 offset:53248
v_accvgpr_read_b32 v76, a28
v_mfma_f32_16x16x32_bf16 a[140:143], v[24:27], a[192:195], 0
ds_read_b128 a[188:191], v2 offset:44032
v_accvgpr_read_b32 v77, a29
v_mfma_f32_16x16x32_bf16 a[140:143], v[28:31], a[232:235], a[140:143]
ds_read_b128 a[228:231], v2 offset:54272
v_accvgpr_read_b32 v78, a30
v_mfma_f32_16x16x32_bf16 a[156:159], v[24:27], a[196:199], 0
s_waitcnt lgkmcnt(10)
ds_read_b128 a[192:195], v2 offset:45056
v_accvgpr_read_b32 v79, a31
v_mfma_f32_16x16x32_bf16 a[156:159], v[28:31], a[236:239], a[156:159]
ds_read_b128 a[232:235], v2 offset:55296
v_fma_f32 v64, v64, s86, -v81
v_mfma_f32_16x16x32_bf16 v[176:179], v[16:19], a[160:163], v[176:179]
ds_read_b128 a[196:199], v2 offset:46080
v_perm_b32 v24, v114, v112, s85
v_mfma_f32_16x16x32_bf16 v[176:179], v[20:23], a[200:203], v[176:179]
ds_read_b128 a[236:239], v2 offset:56320
v_perm_b32 v25, v118, v116, s85
v_mfma_f32_16x16x32_bf16 v[192:195], v[16:19], a[164:167], v[192:195]
v_perm_b32 v26, v122, v120, s85
v_perm_b32 v27, v126, v124, s85
v_mfma_f32_16x16x32_bf16 v[192:195], v[20:23], a[204:207], v[192:195]
v_perm_b32 v28, v130, v128, s85
v_perm_b32 v29, v134, v132, s85
v_mfma_f32_16x16x32_bf16 v[208:211], v[16:19], a[168:171], v[208:211]
v_perm_b32 v30, v138, v136, s85
v_perm_b32 v31, v142, v140, s85
v_mfma_f32_16x16x32_bf16 v[208:211], v[20:23], a[208:211], v[208:211]
buffer_load_dwordx2 v[40:41], v5, s[40:43], s80 offen offset:2048 nt
s_waitcnt lgkmcnt(13)
v_mfma_f32_16x16x32_bf16 v[224:227], v[16:19], a[172:175], v[224:227]
s_waitcnt lgkmcnt(12)
v_fma_f32 v65, v65, s86, -v81
v_fma_f32 v66, v66, s86, -v81
v_mfma_f32_16x16x32_bf16 v[224:227], v[20:23], a[212:215], v[224:227]
s_waitcnt lgkmcnt(11)
v_fma_f32 v67, v67, s86, -v81
v_fma_f32 v68, v68, s86, -v81
v_mfma_f32_16x16x32_bf16 v[240:243], v[16:19], a[176:179], v[240:243]
s_waitcnt lgkmcnt(10)
v_fma_f32 v69, v69, s86, -v81
v_fma_f32 v70, v70, s86, -v81
v_mfma_f32_16x16x32_bf16 v[240:243], v[20:23], a[216:219], v[240:243]
s_waitcnt lgkmcnt(9)
v_fma_f32 v71, v71, s86, -v81
v_fma_f32 v72, v72, s86, -v81
v_mfma_f32_16x16x32_bf16 a[80:83], v[16:19], a[180:183], a[80:83]
buffer_load_dwordx2 v[42:43], v5, s[40:43], s80 offen offset:2560 nt
s_waitcnt lgkmcnt(8)
v_mfma_f32_16x16x32_bf16 a[80:83], v[20:23], a[220:223], a[80:83]
s_waitcnt lgkmcnt(7)
v_fma_f32 v73, v73, s86, -v81
v_fma_f32 v74, v74, s86, -v81
v_mfma_f32_16x16x32_bf16 a[96:99], v[16:19], a[184:187], a[96:99]
s_waitcnt lgkmcnt(6)
v_fma_f32 v75, v75, s86, -v81
v_fma_f32 v76, v76, s86, -v81
v_mfma_f32_16x16x32_bf16 a[96:99], v[20:23], a[224:227], a[96:99]
s_waitcnt lgkmcnt(5)
v_fma_f32 v77, v77, s86, -v81
v_fma_f32 v78, v78, s86, -v81
v_mfma_f32_16x16x32_bf16 a[112:115], v[16:19], a[188:191], a[112:115]
buffer_load_dwordx2 v[44:45], v5, s[40:43], s80 offen offset:3072 nt
v_fma_f32 v79, v79, s86, -v81
s_waitcnt lgkmcnt(4)
v_mfma_f32_16x16x32_bf16 a[112:115], v[20:23], a[228:231], a[112:115]
buffer_load_dwordx2 v[46:47], v5, s[40:43], s80 offen offset:3584 nt
s_waitcnt lgkmcnt(3)
v_mfma_f32_16x16x32_bf16 a[128:131], v[16:19], a[192:195], a[128:131]
s_add_u32 s80, s80, 16384
v_exp_f32 v64, v64
s_waitcnt lgkmcnt(2)
v_mfma_f32_16x16x32_bf16 a[128:131], v[20:23], a[232:235], a[128:131]
v_exp_f32 v65, v65
s_waitcnt lgkmcnt(1)
v_mfma_f32_16x16x32_bf16 a[144:147], v[16:19], a[196:199], a[144:147]
v_exp_f32 v66, v66
s_waitcnt lgkmcnt(0)
v_mfma_f32_16x16x32_bf16 a[144:147], v[20:23], a[236:239], a[144:147]
v_exp_f32 v67, v67
v_mfma_f32_16x16x32_bf16 v[180:183], v[24:27], a[160:163], v[180:183]
)"
R"(v_perm_b32 v16, v115, v113, s84
v_perm_b32 v17, v119, v117, s84
v_mfma_f32_16x16x32_bf16 v[180:183], v[28:31], a[200:203], v[180:183]
v_perm_b32 v18, v123, v121, s84
v_perm_b32 v19, v127, v125, s84
v_mfma_f32_16x16x32_bf16 v[196:199], v[24:27], a[164:167], v[196:199]
v_perm_b32 v20, v131, v129, s84
v_perm_b32 v21, v135, v133, s84
v_mfma_f32_16x16x32_bf16 v[196:199], v[28:31], a[204:207], v[196:199]
v_perm_b32 v22, v139, v137, s84
v_perm_b32 v23, v143, v141, s84
v_mfma_f32_16x16x32_bf16 v[212:215], v[24:27], a[168:171], v[212:215]
v_exp_f32 v68, v68
v_mfma_f32_16x16x32_bf16 v[212:215], v[28:31], a[208:211], v[212:215]
buffer_load_dwordx2 v[96:97], v5, s[40:43], s80 offen nt
v_mfma_f32_16x16x32_bf16 v[228:231], v[24:27], a[172:175], v[228:231]
v_exp_f32 v69, v69
v_mfma_f32_16x16x32_bf16 v[228:231], v[28:31], a[212:215], v[228:231]
v_exp_f32 v70, v70
v_mfma_f32_16x16x32_bf16 v[244:247], v[24:27], a[176:179], v[244:247]
v_exp_f32 v71, v71
v_mfma_f32_16x16x32_bf16 v[244:247], v[28:31], a[216:219], v[244:247]
v_exp_f32 v72, v72
v_mfma_f32_16x16x32_bf16 a[84:87], v[24:27], a[180:183], a[84:87]
buffer_load_dwordx2 v[98:99], v5, s[40:43], s80 offen offset:512 nt
v_mfma_f32_16x16x32_bf16 a[84:87], v[28:31], a[220:223], a[84:87]
v_exp_f32 v73, v73
v_mfma_f32_16x16x32_bf16 a[100:103], v[24:27], a[184:187], a[100:103]
v_exp_f32 v74, v74
v_mfma_f32_16x16x32_bf16 a[100:103], v[28:31], a[224:227], a[100:103]
v_exp_f32 v75, v75
v_mfma_f32_16x16x32_bf16 a[116:119], v[24:27], a[188:191], a[116:119]
v_exp_f32 v76, v76
v_mfma_f32_16x16x32_bf16 a[116:119], v[28:31], a[228:231], a[116:119]
buffer_load_dwordx2 v[100:101], v5, s[40:43], s80 offen offset:1024 nt
v_mfma_f32_16x16x32_bf16 a[132:135], v[24:27], a[192:195], a[132:135]
v_exp_f32 v77, v77
v_mfma_f32_16x16x32_bf16 a[132:135], v[28:31], a[232:235], a[132:135]
v_exp_f32 v78, v78
v_mfma_f32_16x16x32_bf16 a[148:151], v[24:27], a[196:199], a[148:151]
v_exp_f32 v79, v79
v_mfma_f32_16x16x32_bf16 a[148:151], v[28:31], a[236:239], a[148:151]
v_cvt_pk_bf16_f32 v64, v64, v65
v_cvt_pk_bf16_f32 v65, v66, v67
v_mfma_f32_16x16x32_bf16 v[184:187], v[16:19], a[160:163], v[184:187]
v_perm_b32 v24, v115, v113, s85
v_perm_b32 v25, v119, v117, s85
v_mfma_f32_16x16x32_bf16 v[184:187], v[20:23], a[200:203], v[184:187]
v_perm_b32 v26, v123, v121, s85
v_perm_b32 v27, v127, v125, s85
v_mfma_f32_16x16x32_bf16 v[200:203], v[16:19], a[164:167], v[200:203]
v_perm_b32 v28, v131, v129, s85
v_perm_b32 v29, v135, v133, s85
v_mfma_f32_16x16x32_bf16 v[200:203], v[20:23], a[204:207], v[200:203]
v_perm_b32 v30, v139, v137, s85
v_perm_b32 v31, v143, v141, s85
v_mfma_f32_16x16x32_bf16 v[216:219], v[16:19], a[168:171], v[216:219]
v_cvt_pk_bf16_f32 v66, v68, v69
v_cvt_pk_bf16_f32 v67, v70, v71
v_mfma_f32_16x16x32_bf16 v[216:219], v[20:23], a[208:211], v[216:219]
buffer_load_dwordx2 v[102:103], v5, s[40:43], s80 offen offset:1536 nt
v_mfma_f32_16x16x32_bf16 v[232:235], v[16:19], a[172:175], v[232:235]
v_cvt_pk_bf16_f32 v72, v72, v73
v_cvt_pk_bf16_f32 v73, v74, v75
v_mfma_f32_16x16x32_bf16 v[232:235], v[20:23], a[212:215], v[232:235]
v_cvt_pk_bf16_f32 v74, v76, v77
v_cvt_pk_bf16_f32 v75, v78, v79
v_mfma_f32_16x16x32_bf16 v[92:95], v[60:63], v[64:67], 0
ds_write_b128 v12, v[64:67] offset:1024
v_mfma_f32_16x16x32_bf16 v[92:95], v[60:63], v[72:75], v[92:95]
ds_write_b128 v12, v[72:75] offset:11264
v_mfma_f32_16x16x32_bf16 v[248:251], v[16:19], a[176:179], v[248:251]
v_accvgpr_read_b32 v64, a32
v_accvgpr_read_b32 v65, a33
v_mfma_f32_16x16x32_bf16 v[248:251], v[20:23], a[216:219], v[248:251]
v_accvgpr_read_b32 v66, a34
v_accvgpr_read_b32 v67, a35
v_mfma_f32_16x16x32_bf16 a[88:91], v[16:19], a[180:183], a[88:91]
buffer_load_dwordx2 v[104:105], v5, s[40:43], s80 offen offset:2048 nt
v_mfma_f32_16x16x32_bf16 a[88:91], v[20:23], a[220:223], a[88:91]
v_accvgpr_read_b32 v68, a36
v_accvgpr_read_b32 v69, a37
v_mfma_f32_16x16x32_bf16 a[104:107], v[16:19], a[184:187], a[104:107]
v_add_f32 v49, v49, v92
v_accvgpr_read_b32 v70, a38
v_mfma_f32_16x16x32_bf16 a[104:107], v[20:23], a[224:227], a[104:107]
v_accvgpr_read_b32 v71, a39
v_accvgpr_read_b32 v72, a40
v_mfma_f32_16x16x32_bf16 a[120:123], v[16:19], a[188:191], a[120:123]
v_accvgpr_read_b32 v73, a41
v_accvgpr_read_b32 v74, a42
v_mfma_f32_16x16x32_bf16 a[120:123], v[20:23], a[228:231], a[120:123]
buffer_load_dwordx2 v[106:107], v5, s[40:43], s80 offen offset:2560 nt
v_mfma_f32_16x16x32_bf16 a[136:139], v[16:19], a[192:195], a[136:139]
v_accvgpr_read_b32 v75, a43
v_accvgpr_read_b32 v76, a44
v_mfma_f32_16x16x32_bf16 a[136:139], v[20:23], a[232:235], a[136:139]
v_accvgpr_read_b32 v77, a45
v_accvgpr_read_b32 v78, a46
v_mfma_f32_16x16x32_bf16 a[152:155], v[16:19], a[196:199], a[152:155]
v_accvgpr_read_b32 v79, a47
v_fma_f32 v64, v64, s86, -v82
v_mfma_f32_16x16x32_bf16 a[152:155], v[20:23], a[236:239], a[152:155]
v_fma_f32 v65, v65, s86, -v82
v_fma_f32 v66, v66, s86, -v82
v_mfma_f32_16x16x32_bf16 v[188:191], v[24:27], a[160:163], v[188:191]
s_waitcnt vmcnt(14)
v_perm_b32 v16, v146, v144, s84
v_perm_b32 v17, v150, v148, s84
v_mfma_f32_16x16x32_bf16 v[188:191], v[28:31], a[200:203], v[188:191]
v_perm_b32 v18, v154, v152, s84
v_perm_b32 v19, v158, v156, s84
v_mfma_f32_16x16x32_bf16 v[204:207], v[24:27], a[164:167], v[204:207]
ds_read_b128 a[160:163], v2 offset:57344
v_perm_b32 v20, v162, v160, s84
v_mfma_f32_16x16x32_bf16 v[204:207], v[28:31], a[204:207], v[204:207]
ds_read_b128 a[200:203], v3 offset:2048
v_perm_b32 v21, v166, v164, s84
v_mfma_f32_16x16x32_bf16 v[220:223], v[24:27], a[168:171], v[220:223]
ds_read_b128 a[164:167], v2 offset:58368
v_perm_b32 v22, v170, v168, s84
v_mfma_f32_16x16x32_bf16 v[220:223], v[28:31], a[208:211], v[220:223]
ds_read_b128 a[204:207], v3 offset:3072
v_perm_b32 v23, v174, v172, s84
v_mfma_f32_16x16x32_bf16 v[236:239], v[24:27], a[172:175], v[236:239]
ds_read_b128 a[168:171], v2 offset:59392
v_fma_f32 v67, v67, s86, -v82
v_mfma_f32_16x16x32_bf16 v[236:239], v[28:31], a[212:215], v[236:239]
ds_read_b128 a[208:211], v3 offset:4096
v_fma_f32 v68, v68, s86, -v82
v_mfma_f32_16x16x32_bf16 v[252:255], v[24:27], a[176:179], v[252:255]
ds_read_b128 a[172:175], v2 offset:60416
v_fma_f32 v69, v69, s86, -v82
v_mfma_f32_16x16x32_bf16 v[252:255], v[28:31], a[216:219], v[252:255]
ds_read_b128 a[212:215], v3 offset:5120
v_fma_f32 v70, v70, s86, -v82
v_mfma_f32_16x16x32_bf16 a[92:95], v[24:27], a[180:183], a[92:95]
ds_read_b128 a[176:179], v2 offset:61440
v_fma_f32 v71, v71, s86, -v82
v_mfma_f32_16x16x32_bf16 a[92:95], v[28:31], a[220:223], a[92:95]
ds_read_b128 a[216:219], v3 offset:6144
v_fma_f32 v72, v72, s86, -v82
v_mfma_f32_16x16x32_bf16 a[108:111], v[24:27], a[184:187], a[108:111]
ds_read_b128 a[180:183], v2 offset:62464
v_fma_f32 v73, v73, s86, -v82
v_mfma_f32_16x16x32_bf16 a[108:111], v[28:31], a[224:227], a[108:111]
ds_read_b128 a[220:223], v3 offset:7168
v_fma_f32 v74, v74, s86, -v82
v_mfma_f32_16x16x32_bf16 a[124:127], v[24:27], a[188:191], a[124:127]
s_waitcnt lgkmcnt(10)
ds_read_b128 a[184:187], v2 offset:63488
v_fma_f32 v75, v75, s86, -v82
v_mfma_f32_16x16x32_bf16 a[124:127], v[28:31], a[228:231], a[124:127]
ds_read_b128 a[224:227], v3 offset:8192
v_fma_f32 v76, v76, s86, -v82
v_mfma_f32_16x16x32_bf16 a[140:143], v[24:27], a[192:195], a[140:143]
ds_read_b128 a[188:191], v2 offset:64512
v_fma_f32 v77, v77, s86, -v82
v_mfma_f32_16x16x32_bf16 a[140:143], v[28:31], a[232:235], a[140:143]
ds_read_b128 a[228:231], v3 offset:9216
v_fma_f32 v78, v78, s86, -v82
v_mfma_f32_16x16x32_bf16 a[156:159], v[24:27], a[196:199], a[156:159]
s_waitcnt lgkmcnt(10)
ds_read_b128 a[192:195], v3 offset:0
v_fma_f32 v79, v79, s86, -v82
v_mfma_f32_16x16x32_bf16 a[156:159], v[28:31], a[236:239], a[156:159]
buffer_load_dwordx2 v[108:109], v5, s[40:43], s80 offen offset:3072 nt
ds_read_b128 a[232:235], v3 offset:10240
v_mfma_f32_16x16x32_bf16 v[176:179], v[16:19], a[160:163], v[176:179]
ds_read_b128 a[196:199], v3 offset:1024
v_perm_b32 v24, v146, v144, s85
v_mfma_f32_16x16x32_bf16 v[176:179], v[20:23], a[200:203], v[176:179]
ds_read_b128 a[236:239], v3 offset:11264
v_perm_b32 v25, v150, v148, s85
v_mfma_f32_16x16x32_bf16 v[192:195], v[16:19], a[164:167], v[192:195]
v_perm_b32 v26, v154, v152, s85
v_perm_b32 v27, v158, v156, s85
v_mfma_f32_16x16x32_bf16 v[192:195], v[20:23], a[204:207], v[192:195]
v_perm_b32 v28, v162, v160, s85
v_perm_b32 v29, v166, v164, s85
v_mfma_f32_16x16x32_bf16 v[208:211], v[16:19], a[168:171], v[208:211]
v_perm_b32 v30, v170, v168, s85
v_perm_b32 v31, v174, v172, s85
v_mfma_f32_16x16x32_bf16 v[208:211], v[20:23], a[208:211], v[208:211]
buffer_load_dwordx2 v[110:111], v5, s[40:43], s80 offen offset:3584 nt
s_waitcnt lgkmcnt(13)
v_mfma_f32_16x16x32_bf16 v[224:227], v[16:19], a[172:175], v[224:227]
s_mov_b32 s80, 0
v_exp_f32 v64, v64
s_waitcnt lgkmcnt(12)
v_mfma_f32_16x16x32_bf16 v[224:227], v[20:23], a[212:215], v[224:227]
v_exp_f32 v65, v65
s_waitcnt lgkmcnt(11)
v_mfma_f32_16x16x32_bf16 v[240:243], v[16:19], a[176:179], v[240:243]
v_exp_f32 v66, v66
s_waitcnt lgkmcnt(10)
v_mfma_f32_16x16x32_bf16 v[240:243], v[20:23], a[216:219], v[240:243]
v_exp_f32 v67, v67
s_waitcnt lgkmcnt(9)
v_mfma_f32_16x16x32_bf16 a[80:83], v[16:19], a[180:183], a[80:83]
)"
R"(buffer_load_dwordx2 v[112:113], v5, s[40:43], s80 offen nt
s_waitcnt lgkmcnt(8)
v_mfma_f32_16x16x32_bf16 a[80:83], v[20:23], a[220:223], a[80:83]
v_exp_f32 v68, v68
s_waitcnt lgkmcnt(7)
v_mfma_f32_16x16x32_bf16 a[96:99], v[16:19], a[184:187], a[96:99]
v_exp_f32 v69, v69
s_waitcnt lgkmcnt(6)
v_mfma_f32_16x16x32_bf16 a[96:99], v[20:23], a[224:227], a[96:99]
v_exp_f32 v70, v70
s_waitcnt lgkmcnt(5)
v_mfma_f32_16x16x32_bf16 a[112:115], v[16:19], a[188:191], a[112:115]
v_exp_f32 v71, v71
s_waitcnt lgkmcnt(4)
v_mfma_f32_16x16x32_bf16 a[112:115], v[20:23], a[228:231], a[112:115]
buffer_load_dwordx2 v[114:115], v5, s[40:43], s80 offen offset:512 nt
s_waitcnt lgkmcnt(3)
v_mfma_f32_16x16x32_bf16 a[128:131], v[16:19], a[192:195], a[128:131]
v_exp_f32 v72, v72
s_waitcnt lgkmcnt(2)
v_mfma_f32_16x16x32_bf16 a[128:131], v[20:23], a[232:235], a[128:131]
v_exp_f32 v73, v73
s_waitcnt lgkmcnt(1)
v_mfma_f32_16x16x32_bf16 a[144:147], v[16:19], a[196:199], a[144:147]
v_exp_f32 v74, v74
s_waitcnt lgkmcnt(0)
v_mfma_f32_16x16x32_bf16 a[144:147], v[20:23], a[236:239], a[144:147]
v_exp_f32 v75, v75
v_mfma_f32_16x16x32_bf16 v[180:183], v[24:27], a[160:163], v[180:183]
v_perm_b32 v16, v147, v145, s84
v_perm_b32 v17, v151, v149, s84
v_mfma_f32_16x16x32_bf16 v[180:183], v[28:31], a[200:203], v[180:183]
v_perm_b32 v18, v155, v153, s84
v_perm_b32 v19, v159, v157, s84
v_mfma_f32_16x16x32_bf16 v[196:199], v[24:27], a[164:167], v[196:199]
v_perm_b32 v20, v163, v161, s84
v_perm_b32 v21, v167, v165, s84
v_mfma_f32_16x16x32_bf16 v[196:199], v[28:31], a[204:207], v[196:199]
v_perm_b32 v22, v171, v169, s84
v_perm_b32 v23, v175, v173, s84
v_mfma_f32_16x16x32_bf16 v[212:215], v[24:27], a[168:171], v[212:215]
v_exp_f32 v76, v76
v_mfma_f32_16x16x32_bf16 v[212:215], v[28:31], a[208:211], v[212:215]
buffer_load_dwordx2 v[116:117], v5, s[40:43], s80 offen offset:1024 nt
v_mfma_f32_16x16x32_bf16 v[228:231], v[24:27], a[172:175], v[228:231]
v_exp_f32 v77, v77
v_mfma_f32_16x16x32_bf16 v[228:231], v[28:31], a[212:215], v[228:231]
v_exp_f32 v78, v78
v_mfma_f32_16x16x32_bf16 v[244:247], v[24:27], a[176:179], v[244:247]
v_exp_f32 v79, v79
v_mfma_f32_16x16x32_bf16 v[244:247], v[28:31], a[216:219], v[244:247]
v_cvt_pk_bf16_f32 v64, v64, v65
v_cvt_pk_bf16_f32 v65, v66, v67
v_mfma_f32_16x16x32_bf16 a[84:87], v[24:27], a[180:183], a[84:87]
buffer_load_dwordx2 v[118:119], v5, s[40:43], s80 offen offset:1536 nt
v_mfma_f32_16x16x32_bf16 a[84:87], v[28:31], a[220:223], a[84:87]
v_cvt_pk_bf16_f32 v66, v68, v69
v_cvt_pk_bf16_f32 v67, v70, v71
v_mfma_f32_16x16x32_bf16 a[100:103], v[24:27], a[184:187], a[100:103]
v_cvt_pk_bf16_f32 v72, v72, v73
v_cvt_pk_bf16_f32 v73, v74, v75
v_mfma_f32_16x16x32_bf16 a[100:103], v[28:31], a[224:227], a[100:103]
v_cvt_pk_bf16_f32 v74, v76, v77
v_cvt_pk_bf16_f32 v75, v78, v79
v_mfma_f32_16x16x32_bf16 v[92:95], v[60:63], v[64:67], 0
ds_write_b128 v12, v[64:67] offset:2048
v_mfma_f32_16x16x32_bf16 v[92:95], v[60:63], v[72:75], v[92:95]
ds_write_b128 v12, v[72:75] offset:12288
v_mfma_f32_16x16x32_bf16 a[116:119], v[24:27], a[188:191], a[116:119]
v_accvgpr_read_b32 v64, a48
v_accvgpr_read_b32 v65, a49
v_mfma_f32_16x16x32_bf16 a[116:119], v[28:31], a[228:231], a[116:119]
buffer_load_dwordx2 v[120:121], v5, s[40:43], s80 offen offset:2048 nt
v_mfma_f32_16x16x32_bf16 a[132:135], v[24:27], a[192:195], a[132:135]
v_accvgpr_read_b32 v66, a50
v_accvgpr_read_b32 v67, a51
v_mfma_f32_16x16x32_bf16 a[132:135], v[28:31], a[232:235], a[132:135]
v_accvgpr_read_b32 v68, a52
v_accvgpr_read_b32 v69, a53
v_mfma_f32_16x16x32_bf16 a[148:151], v[24:27], a[196:199], a[148:151]
v_add_f32 v50, v50, v92
v_accvgpr_read_b32 v70, a54
v_mfma_f32_16x16x32_bf16 a[148:151], v[28:31], a[236:239], a[148:151]
v_accvgpr_read_b32 v71, a55
v_accvgpr_read_b32 v72, a56
v_mfma_f32_16x16x32_bf16 v[184:187], v[16:19], a[160:163], v[184:187]
v_perm_b32 v24, v147, v145, s85
v_perm_b32 v25, v151, v149, s85
v_mfma_f32_16x16x32_bf16 v[184:187], v[20:23], a[200:203], v[184:187]
v_perm_b32 v26, v155, v153, s85
v_perm_b32 v27, v159, v157, s85
v_mfma_f32_16x16x32_bf16 v[200:203], v[16:19], a[164:167], v[200:203]
v_perm_b32 v28, v163, v161, s85
v_perm_b32 v29, v167, v165, s85
v_mfma_f32_16x16x32_bf16 v[200:203], v[20:23], a[204:207], v[200:203]
v_perm_b32 v30, v171, v169, s85
v_perm_b32 v31, v175, v173, s85
v_mfma_f32_16x16x32_bf16 v[216:219], v[16:19], a[168:171], v[216:219]
v_accvgpr_read_b32 v73, a57
v_accvgpr_read_b32 v74, a58
v_mfma_f32_16x16x32_bf16 v[216:219], v[20:23], a[208:211], v[216:219]
buffer_load_dwordx2 v[122:123], v5, s[40:43], s80 offen offset:2560 nt
v_mfma_f32_16x16x32_bf16 v[232:235], v[16:19], a[172:175], v[232:235]
v_accvgpr_read_b32 v75, a59
v_accvgpr_read_b32 v76, a60
v_mfma_f32_16x16x32_bf16 v[232:235], v[20:23], a[212:215], v[232:235]
v_accvgpr_read_b32 v77, a61
v_accvgpr_read_b32 v78, a62
v_mfma_f32_16x16x32_bf16 v[248:251], v[16:19], a[176:179], v[248:251]
v_accvgpr_read_b32 v79, a63
v_fma_f32 v64, v64, s86, -v83
v_mfma_f32_16x16x32_bf16 v[248:251], v[20:23], a[216:219], v[248:251]
v_fma_f32 v65, v65, s86, -v83
v_fma_f32 v66, v66, s86, -v83
v_mfma_f32_16x16x32_bf16 a[88:91], v[16:19], a[180:183], a[88:91]
buffer_load_dwordx2 v[124:125], v5, s[40:43], s80 offen offset:3072 nt
v_mfma_f32_16x16x32_bf16 a[88:91], v[20:23], a[220:223], a[88:91]
v_fma_f32 v67, v67, s86, -v83
v_fma_f32 v68, v68, s86, -v83
v_mfma_f32_16x16x32_bf16 a[104:107], v[16:19], a[184:187], a[104:107]
v_fma_f32 v69, v69, s86, -v83
v_fma_f32 v70, v70, s86, -v83
v_mfma_f32_16x16x32_bf16 a[104:107], v[20:23], a[224:227], a[104:107]
v_fma_f32 v71, v71, s86, -v83
v_fma_f32 v72, v72, s86, -v83
v_mfma_f32_16x16x32_bf16 a[120:123], v[16:19], a[188:191], a[120:123]
v_fma_f32 v73, v73, s86, -v83
v_fma_f32 v74, v74, s86, -v83
v_mfma_f32_16x16x32_bf16 a[120:123], v[20:23], a[228:231], a[120:123]
buffer_load_dwordx2 v[126:127], v5, s[40:43], s80 offen offset:3584 nt
v_mfma_f32_16x16x32_bf16 a[136:139], v[16:19], a[192:195], a[136:139]
s_add_u32 s80, s80, 16384
v_fma_f32 v75, v75, s86, -v83
v_fma_f32 v76, v76, s86, -v83
v_mfma_f32_16x16x32_bf16 a[136:139], v[20:23], a[232:235], a[136:139]
v_fma_f32 v77, v77, s86, -v83
v_fma_f32 v78, v78, s86, -v83
v_mfma_f32_16x16x32_bf16 a[152:155], v[16:19], a[196:199], a[152:155]
buffer_load_dwordx2 v[128:129], v5, s[40:43], s80 offen nt
v_fma_f32 v79, v79, s86, -v83
v_mfma_f32_16x16x32_bf16 a[152:155], v[20:23], a[236:239], a[152:155]
v_exp_f32 v64, v64
v_mfma_f32_16x16x32_bf16 v[188:191], v[24:27], a[160:163], v[188:191]
buffer_load_dwordx2 v[130:131], v5, s[40:43], s80 offen offset:512 nt
buffer_load_dwordx2 v[132:133], v5, s[40:43], s80 offen offset:1024 nt
buffer_load_dwordx2 v[134:135], v5, s[40:43], s80 offen offset:1536 nt
buffer_load_dwordx2 v[136:137], v5, s[40:43], s80 offen offset:2048 nt
buffer_load_dwordx2 v[138:139], v5, s[40:43], s80 offen offset:2560 nt
buffer_load_dwordx2 v[140:141], v5, s[40:43], s80 offen offset:3072 nt
buffer_load_dwordx2 v[142:143], v5, s[40:43], s80 offen offset:3584 nt
s_mov_b32 s80, 65536
buffer_load_dwordx2 v[144:145], v5, s[40:43], s80 offen nt
buffer_load_dwordx2 v[146:147], v5, s[40:43], s80 offen offset:512 nt
buffer_load_dwordx2 v[148:149], v5, s[40:43], s80 offen offset:1024 nt
s_waitcnt vmcnt(19)
v_perm_b32 v16, v34, v32, s84
v_perm_b32 v17, v38, v36, s84
v_mfma_f32_16x16x32_bf16 v[188:191], v[28:31], a[200:203], v[188:191]
v_perm_b32 v18, v42, v40, s84
v_perm_b32 v19, v46, v44, s84
v_mfma_f32_16x16x32_bf16 v[204:207], v[24:27], a[164:167], v[204:207]
ds_read_b128 a[160:163], v3 offset:12288
v_perm_b32 v20, v98, v96, s84
v_mfma_f32_16x16x32_bf16 v[204:207], v[28:31], a[204:207], v[204:207]
ds_read_b128 a[200:203], v3 offset:22528
v_perm_b32 v21, v102, v100, s84
v_mfma_f32_16x16x32_bf16 v[220:223], v[24:27], a[168:171], v[220:223]
ds_read_b128 a[164:167], v3 offset:13312
v_perm_b32 v22, v106, v104, s84
v_mfma_f32_16x16x32_bf16 v[220:223], v[28:31], a[208:211], v[220:223]
ds_read_b128 a[204:207], v3 offset:23552
v_perm_b32 v23, v110, v108, s84
v_mfma_f32_16x16x32_bf16 v[236:239], v[24:27], a[172:175], v[236:239]
ds_read_b128 a[168:171], v3 offset:14336
v_mfma_f32_16x16x32_bf16 v[236:239], v[28:31], a[212:215], v[236:239]
ds_read_b128 a[208:211], v3 offset:24576
v_mfma_f32_16x16x32_bf16 v[252:255], v[24:27], a[176:179], v[252:255]
ds_read_b128 a[172:175], v3 offset:15360
v_mfma_f32_16x16x32_bf16 v[252:255], v[28:31], a[216:219], v[252:255]
ds_read_b128 a[212:215], v3 offset:25600
v_mfma_f32_16x16x32_bf16 a[92:95], v[24:27], a[180:183], a[92:95]
ds_read_b128 a[176:179], v3 offset:16384
v_mfma_f32_16x16x32_bf16 a[92:95], v[28:31], a[220:223], a[92:95]
ds_read_b128 a[216:219], v3 offset:26624
v_mfma_f32_16x16x32_bf16 a[108:111], v[24:27], a[184:187], a[108:111]
ds_read_b128 a[180:183], v3 offset:17408
v_mfma_f32_16x16x32_bf16 a[108:111], v[28:31], a[224:227], a[108:111]
ds_read_b128 a[220:223], v3 offset:27648
v_mfma_f32_16x16x32_bf16 a[124:127], v[24:27], a[188:191], a[124:127]
s_waitcnt lgkmcnt(10)
ds_read_b128 a[184:187], v3 offset:18432
v_mfma_f32_16x16x32_bf16 a[124:127], v[28:31], a[228:231], a[124:127]
ds_read_b128 a[224:227], v3 offset:28672
v_mfma_f32_16x16x32_bf16 a[140:143], v[24:27], a[192:195], a[140:143]
ds_read_b128 a[188:191], v3 offset:19456
v_mfma_f32_16x16x32_bf16 a[140:143], v[28:31], a[232:235], a[140:143]
ds_read_b128 a[228:231], v3 offset:29696
v_mfma_f32_16x16x32_bf16 a[156:159], v[24:27], a[196:199], a[156:159]
s_waitcnt lgkmcnt(10)
ds_read_b128 a[192:195], v3 offset:20480
)"
R"(v_mfma_f32_16x16x32_bf16 a[156:159], v[28:31], a[236:239], a[156:159]
ds_read_b128 a[232:235], v3 offset:30720
v_mfma_f32_16x16x32_bf16 v[176:179], v[16:19], a[160:163], v[176:179]
ds_read_b128 a[196:199], v3 offset:21504
v_perm_b32 v24, v34, v32, s85
v_mfma_f32_16x16x32_bf16 v[176:179], v[20:23], a[200:203], v[176:179]
ds_read_b128 a[236:239], v3 offset:31744
v_perm_b32 v25, v38, v36, s85
v_mfma_f32_16x16x32_bf16 v[192:195], v[16:19], a[164:167], v[192:195]
v_perm_b32 v26, v42, v40, s85
v_perm_b32 v27, v46, v44, s85
v_mfma_f32_16x16x32_bf16 v[192:195], v[20:23], a[204:207], v[192:195]
v_perm_b32 v28, v98, v96, s85
v_perm_b32 v29, v102, v100, s85
v_mfma_f32_16x16x32_bf16 v[208:211], v[16:19], a[168:171], v[208:211]
v_perm_b32 v30, v106, v104, s85
v_perm_b32 v31, v110, v108, s85
v_mfma_f32_16x16x32_bf16 v[208:211], v[20:23], a[208:211], v[208:211]
buffer_load_dwordx2 v[150:151], v5, s[40:43], s80 offen offset:1536 nt
s_waitcnt lgkmcnt(13)
v_mfma_f32_16x16x32_bf16 v[224:227], v[16:19], a[172:175], v[224:227]
v_exp_f32 v65, v65
s_waitcnt lgkmcnt(12)
v_mfma_f32_16x16x32_bf16 v[224:227], v[20:23], a[212:215], v[224:227]
v_exp_f32 v66, v66
s_waitcnt lgkmcnt(11)
v_mfma_f32_16x16x32_bf16 v[240:243], v[16:19], a[176:179], v[240:243]
v_exp_f32 v67, v67
s_waitcnt lgkmcnt(10)
v_mfma_f32_16x16x32_bf16 v[240:243], v[20:23], a[216:219], v[240:243]
v_exp_f32 v68, v68
s_waitcnt lgkmcnt(9)
v_mfma_f32_16x16x32_bf16 a[80:83], v[16:19], a[180:183], a[80:83]
buffer_load_dwordx2 v[152:153], v5, s[40:43], s80 offen offset:2048 nt
s_waitcnt lgkmcnt(8)
v_mfma_f32_16x16x32_bf16 a[80:83], v[20:23], a[220:223], a[80:83]
v_exp_f32 v69, v69
s_waitcnt lgkmcnt(7)
v_mfma_f32_16x16x32_bf16 a[96:99], v[16:19], a[184:187], a[96:99]
v_exp_f32 v70, v70
s_waitcnt lgkmcnt(6)
v_mfma_f32_16x16x32_bf16 a[96:99], v[20:23], a[224:227], a[96:99]
v_exp_f32 v71, v71
s_waitcnt lgkmcnt(5)
v_mfma_f32_16x16x32_bf16 a[112:115], v[16:19], a[188:191], a[112:115]
v_exp_f32 v72, v72
s_waitcnt lgkmcnt(4)
v_mfma_f32_16x16x32_bf16 a[112:115], v[20:23], a[228:231], a[112:115]
buffer_load_dwordx2 v[154:155], v5, s[40:43], s80 offen offset:2560 nt
s_waitcnt lgkmcnt(3)
v_mfma_f32_16x16x32_bf16 a[128:131], v[16:19], a[192:195], a[128:131]
v_exp_f32 v73, v73
s_waitcnt lgkmcnt(2)
v_mfma_f32_16x16x32_bf16 a[128:131], v[20:23], a[232:235], a[128:131]
v_exp_f32 v74, v74
s_waitcnt lgkmcnt(1)
v_mfma_f32_16x16x32_bf16 a[144:147], v[16:19], a[196:199], a[144:147]
v_exp_f32 v75, v75
s_waitcnt lgkmcnt(0)
v_mfma_f32_16x16x32_bf16 a[144:147], v[20:23], a[236:239], a[144:147]
v_exp_f32 v76, v76
v_mfma_f32_16x16x32_bf16 v[180:183], v[24:27], a[160:163], v[180:183]
v_perm_b32 v16, v35, v33, s84
v_perm_b32 v17, v39, v37, s84
v_mfma_f32_16x16x32_bf16 v[180:183], v[28:31], a[200:203], v[180:183]
v_perm_b32 v18, v43, v41, s84
v_perm_b32 v19, v47, v45, s84
v_mfma_f32_16x16x32_bf16 v[196:199], v[24:27], a[164:167], v[196:199]
v_perm_b32 v20, v99, v97, s84
v_perm_b32 v21, v103, v101, s84
v_mfma_f32_16x16x32_bf16 v[196:199], v[28:31], a[204:207], v[196:199]
v_perm_b32 v22, v107, v105, s84
v_perm_b32 v23, v111, v109, s84
v_mfma_f32_16x16x32_bf16 v[212:215], v[24:27], a[168:171], v[212:215]
v_exp_f32 v77, v77
v_mfma_f32_16x16x32_bf16 v[212:215], v[28:31], a[208:211], v[212:215]
buffer_load_dwordx2 v[156:157], v5, s[40:43], s80 offen offset:3072 nt
v_mfma_f32_16x16x32_bf16 v[228:231], v[24:27], a[172:175], v[228:231]
v_exp_f32 v78, v78
v_mfma_f32_16x16x32_bf16 v[228:231], v[28:31], a[212:215], v[228:231]
v_exp_f32 v79, v79
v_mfma_f32_16x16x32_bf16 v[244:247], v[24:27], a[176:179], v[244:247]
v_cvt_pk_bf16_f32 v64, v64, v65
v_cvt_pk_bf16_f32 v65, v66, v67
v_mfma_f32_16x16x32_bf16 v[244:247], v[28:31], a[216:219], v[244:247]
v_cvt_pk_bf16_f32 v66, v68, v69
v_cvt_pk_bf16_f32 v67, v70, v71
v_mfma_f32_16x16x32_bf16 a[84:87], v[24:27], a[180:183], a[84:87]
buffer_load_dwordx2 v[158:159], v5, s[40:43], s80 offen offset:3584 nt
v_mfma_f32_16x16x32_bf16 a[84:87], v[28:31], a[220:223], a[84:87]
s_add_u32 s80, s80, 16384
v_cvt_pk_bf16_f32 v72, v72, v73
v_cvt_pk_bf16_f32 v73, v74, v75
v_mfma_f32_16x16x32_bf16 a[100:103], v[24:27], a[184:187], a[100:103]
v_cvt_pk_bf16_f32 v74, v76, v77
v_cvt_pk_bf16_f32 v75, v78, v79
v_mfma_f32_16x16x32_bf16 a[100:103], v[28:31], a[224:227], a[100:103]
buffer_load_dwordx2 v[160:161], v5, s[40:43], s80 offen nt
v_mfma_f32_16x16x32_bf16 v[92:95], v[60:63], v[64:67], 0
ds_write_b128 v12, v[64:67] offset:3072
v_mfma_f32_16x16x32_bf16 v[92:95], v[60:63], v[72:75], v[92:95]
ds_write_b128 v12, v[72:75] offset:13312
v_mfma_f32_16x16x32_bf16 a[116:119], v[24:27], a[188:191], a[116:119]
v_accvgpr_read_b32 v64, a64
v_accvgpr_read_b32 v65, a65
v_mfma_f32_16x16x32_bf16 a[116:119], v[28:31], a[228:231], a[116:119]
buffer_load_dwordx2 v[162:163], v5, s[40:43], s80 offen offset:512 nt
v_mfma_f32_16x16x32_bf16 a[132:135], v[24:27], a[192:195], a[132:135]
v_accvgpr_read_b32 v66, a66
v_accvgpr_read_b32 v67, a67
v_mfma_f32_16x16x32_bf16 a[132:135], v[28:31], a[232:235], a[132:135]
v_accvgpr_read_b32 v68, a68
v_accvgpr_read_b32 v69, a69
v_mfma_f32_16x16x32_bf16 a[148:151], v[24:27], a[196:199], a[148:151]
v_add_f32 v51, v51, v92
v_accvgpr_read_b32 v70, a70
v_mfma_f32_16x16x32_bf16 a[148:151], v[28:31], a[236:239], a[148:151]
v_accvgpr_read_b32 v71, a71
v_accvgpr_read_b32 v72, a72
v_mfma_f32_16x16x32_bf16 v[184:187], v[16:19], a[160:163], v[184:187]
v_perm_b32 v24, v35, v33, s85
v_perm_b32 v25, v39, v37, s85
v_mfma_f32_16x16x32_bf16 v[184:187], v[20:23], a[200:203], v[184:187]
v_perm_b32 v26, v43, v41, s85
v_perm_b32 v27, v47, v45, s85
v_mfma_f32_16x16x32_bf16 v[200:203], v[16:19], a[164:167], v[200:203]
v_perm_b32 v28, v99, v97, s85
v_perm_b32 v29, v103, v101, s85
v_mfma_f32_16x16x32_bf16 v[200:203], v[20:23], a[204:207], v[200:203]
v_perm_b32 v30, v107, v105, s85
v_perm_b32 v31, v111, v109, s85
v_mfma_f32_16x16x32_bf16 v[216:219], v[16:19], a[168:171], v[216:219]
v_accvgpr_read_b32 v73, a73
v_accvgpr_read_b32 v74, a74
v_mfma_f32_16x16x32_bf16 v[216:219], v[20:23], a[208:211], v[216:219]
buffer_load_dwordx2 v[164:165], v5, s[40:43], s80 offen offset:1024 nt
v_mfma_f32_16x16x32_bf16 v[232:235], v[16:19], a[172:175], v[232:235]
v_accvgpr_read_b32 v75, a75
v_accvgpr_read_b32 v76, a76
v_mfma_f32_16x16x32_bf16 v[232:235], v[20:23], a[212:215], v[232:235]
v_accvgpr_read_b32 v77, a77
v_accvgpr_read_b32 v78, a78
v_mfma_f32_16x16x32_bf16 v[248:251], v[16:19], a[176:179], v[248:251]
v_accvgpr_read_b32 v79, a79
v_fma_f32 v64, v64, s86, -v84
v_mfma_f32_16x16x32_bf16 v[248:251], v[20:23], a[216:219], v[248:251]
v_fma_f32 v65, v65, s86, -v84
v_fma_f32 v66, v66, s86, -v84
v_mfma_f32_16x16x32_bf16 a[88:91], v[16:19], a[180:183], a[88:91]
buffer_load_dwordx2 v[166:167], v5, s[40:43], s80 offen offset:1536 nt
v_mfma_f32_16x16x32_bf16 a[88:91], v[20:23], a[220:223], a[88:91]
v_fma_f32 v67, v67, s86, -v84
v_fma_f32 v68, v68, s86, -v84
v_mfma_f32_16x16x32_bf16 a[104:107], v[16:19], a[184:187], a[104:107]
v_fma_f32 v69, v69, s86, -v84
v_fma_f32 v70, v70, s86, -v84
v_mfma_f32_16x16x32_bf16 a[104:107], v[20:23], a[224:227], a[104:107]
v_fma_f32 v71, v71, s86, -v84
v_fma_f32 v72, v72, s86, -v84
v_mfma_f32_16x16x32_bf16 a[120:123], v[16:19], a[188:191], a[120:123]
v_fma_f32 v73, v73, s86, -v84
v_fma_f32 v74, v74, s86, -v84
v_mfma_f32_16x16x32_bf16 a[120:123], v[20:23], a[228:231], a[120:123]
buffer_load_dwordx2 v[168:169], v5, s[40:43], s80 offen offset:2048 nt
v_mfma_f32_16x16x32_bf16 a[136:139], v[16:19], a[192:195], a[136:139]
v_fma_f32 v75, v75, s86, -v84
v_fma_f32 v76, v76, s86, -v84
v_mfma_f32_16x16x32_bf16 a[136:139], v[20:23], a[232:235], a[136:139]
v_fma_f32 v77, v77, s86, -v84
v_fma_f32 v78, v78, s86, -v84
v_mfma_f32_16x16x32_bf16 a[152:155], v[16:19], a[196:199], a[152:155]
v_fma_f32 v79, v79, s86, -v84
v_mfma_f32_16x16x32_bf16 a[152:155], v[20:23], a[236:239], a[152:155]
v_exp_f32 v64, v64
v_mfma_f32_16x16x32_bf16 v[188:191], v[24:27], a[160:163], v[188:191]
buffer_load_dwordx2 v[170:171], v5, s[40:43], s80 offen offset:2560 nt
v_mfma_f32_16x16x32_bf16 v[188:191], v[28:31], a[200:203], v[188:191]
v_exp_f32 v65, v65
v_mfma_f32_16x16x32_bf16 v[204:207], v[24:27], a[164:167], v[204:207]
v_exp_f32 v66, v66
v_mfma_f32_16x16x32_bf16 v[204:207], v[28:31], a[204:207], v[204:207]
v_exp_f32 v67, v67
v_mfma_f32_16x16x32_bf16 v[220:223], v[24:27], a[168:171], v[220:223]
v_exp_f32 v68, v68
v_mfma_f32_16x16x32_bf16 v[220:223], v[28:31], a[208:211], v[220:223]
buffer_load_dwordx2 v[172:173], v5, s[40:43], s80 offen offset:3072 nt
v_mfma_f32_16x16x32_bf16 v[236:239], v[24:27], a[172:175], v[236:239]
v_exp_f32 v69, v69
v_mfma_f32_16x16x32_bf16 v[236:239], v[28:31], a[212:215], v[236:239]
v_exp_f32 v70, v70
v_mfma_f32_16x16x32_bf16 v[252:255], v[24:27], a[176:179], v[252:255]
v_exp_f32 v71, v71
v_mfma_f32_16x16x32_bf16 v[252:255], v[28:31], a[216:219], v[252:255]
v_exp_f32 v72, v72
v_mfma_f32_16x16x32_bf16 a[92:95], v[24:27], a[180:183], a[92:95]
buffer_load_dwordx2 v[174:175], v5, s[40:43], s80 offen offset:3584 nt
v_mfma_f32_16x16x32_bf16 a[92:95], v[28:31], a[220:223], a[92:95]
v_exp_f32 v73, v73
v_mfma_f32_16x16x32_bf16 a[108:111], v[24:27], a[184:187], a[108:111]
v_exp_f32 v74, v74
)"
R"(v_mfma_f32_16x16x32_bf16 a[108:111], v[28:31], a[224:227], a[108:111]
v_exp_f32 v75, v75
v_mfma_f32_16x16x32_bf16 a[124:127], v[24:27], a[188:191], a[124:127]
v_exp_f32 v76, v76
v_mfma_f32_16x16x32_bf16 a[124:127], v[28:31], a[228:231], a[124:127]
v_mfma_f32_16x16x32_bf16 a[140:143], v[24:27], a[192:195], a[140:143]
v_exp_f32 v77, v77
v_mfma_f32_16x16x32_bf16 a[140:143], v[28:31], a[232:235], a[140:143]
v_exp_f32 v78, v78
v_mfma_f32_16x16x32_bf16 a[156:159], v[24:27], a[196:199], a[156:159]
v_exp_f32 v79, v79
v_mfma_f32_16x16x32_bf16 a[156:159], v[28:31], a[236:239], a[156:159]
v_cvt_pk_bf16_f32 v64, v64, v65
v_cvt_pk_bf16_f32 v65, v66, v67
v_cvt_pk_bf16_f32 v66, v68, v69
v_cvt_pk_bf16_f32 v67, v70, v71
v_cvt_pk_bf16_f32 v72, v72, v73
v_cvt_pk_bf16_f32 v73, v74, v75
v_cvt_pk_bf16_f32 v74, v76, v77
v_cvt_pk_bf16_f32 v75, v78, v79
v_mfma_f32_16x16x32_bf16 v[92:95], v[60:63], v[64:67], 0
ds_write_b128 v12, v[64:67] offset:4096
v_mfma_f32_16x16x32_bf16 v[92:95], v[60:63], v[72:75], v[92:95]
ds_write_b128 v12, v[72:75] offset:14336
s_nop 6
v_add_f32 v52, v52, v92
s_cmp_eq_u32 s90, 0
s_cselect_b32 s54, 71680, 0
s_cselect_b32 s46, 3662848, 0
s_mov_b64 s[44:45], %[wq1]
s_and_b32 s45, s45, 0xffff
s_mov_b64 s[52:53], %[x1]
v_add_u32 v9, 0x27600, v9
s_and_b32 s53, s53, 0xffff
v_accvgpr_read_b32 v6, a241
s_mov_b32 s94, s93
buffer_load_dwordx4 a[0:3], v6, s[52:55], s94 offen
s_waitcnt vmcnt(17)
v_perm_b32 v16, v114, v112, s84
s_add_u32 s94, s94, 128
s_cmp_eq_u32 s94, 896
s_cselect_b32 s94, 0, s94
buffer_load_dwordx4 a[4:7], v6, s[52:55], s94 offen
s_add_u32 s94, s94, 128
s_cmp_eq_u32 s94, 896
s_cselect_b32 s94, 0, s94
buffer_load_dwordx4 a[8:11], v6, s[52:55], s94 offen
v_perm_b32 v17, v118, v116, s84
s_add_u32 s94, s94, 128
s_cmp_eq_u32 s94, 896
s_cselect_b32 s94, 0, s94
buffer_load_dwordx4 a[12:15], v6, s[52:55], s94 offen
s_add_u32 s94, s94, 128
s_cmp_eq_u32 s94, 896
s_cselect_b32 s94, 0, s94
buffer_load_dwordx4 a[16:19], v6, s[52:55], s94 offen
v_perm_b32 v18, v122, v120, s84
s_add_u32 s94, s94, 128
s_cmp_eq_u32 s94, 896
s_cselect_b32 s94, 0, s94
v_perm_b32 v19, v126, v124, s84
v_perm_b32 v20, v130, v128, s84
v_perm_b32 v21, v134, v132, s84
v_perm_b32 v22, v138, v136, s84
v_perm_b32 v23, v142, v140, s84
s_waitcnt lgkmcnt(0)
s_barrier
ds_write_b32 v9, v48 offset:0
ds_write_b32 v9, v49 offset:256
ds_write_b32 v9, v50 offset:512
ds_write_b32 v9, v51 offset:768
ds_write_b32 v9, v52 offset:1024
ds_write_b32 v9, v53 offset:1280
ds_write_b32 v9, v54 offset:1536
buffer_load_dwordx4 a[20:23], v6, s[52:55], s94 offen
s_add_u32 s94, s94, 128
s_cmp_eq_u32 s94, 896
s_cselect_b32 s94, 0, s94
buffer_load_dwordx4 a[24:27], v6, s[52:55], s94 offen
s_add_u32 s94, s94, 128
s_cmp_eq_u32 s94, 896
s_cselect_b32 s94, 0, s94
ds_write_b32 v9, v55 offset:1792
v_add_u32 v6, 0x7000, v6
ds_write_b32 v9, v56 offset:2048
ds_write_b32 v9, v57 offset:2304
ds_read_b128 a[160:163], v1 offset:0
ds_read_b128 a[164:167], v1 offset:1024
ds_read_b128 a[168:171], v1 offset:2048
ds_read_b128 a[172:175], v1 offset:3072
s_waitcnt lgkmcnt(10)
ds_read_b128 a[176:179], v1 offset:4096
ds_read_b128 a[180:183], v1 offset:5120
ds_read_b128 a[184:187], v1 offset:6144
ds_read_b128 a[188:191], v1 offset:7168
s_waitcnt lgkmcnt(10)
ds_read_b128 a[192:195], v1 offset:8192
ds_read_b128 a[196:199], v1 offset:9216
ds_read_b128 a[200:203], v1 offset:10240
ds_read_b128 a[204:207], v1 offset:11264
s_waitcnt lgkmcnt(10)
ds_read_b128 a[208:211], v1 offset:12288
ds_read_b128 a[212:215], v1 offset:13312
ds_read_b128 a[216:219], v1 offset:14336
ds_read_b128 a[220:223], v1 offset:15360
s_waitcnt lgkmcnt(10)
ds_read_b128 a[224:227], v1 offset:16384
ds_read_b128 a[228:231], v1 offset:17408
ds_read_b128 a[232:235], v1 offset:18432
ds_read_b128 a[236:239], v1 offset:19456
v_mfma_f32_16x16x32_bf16 v[176:179], v[16:19], a[160:163], v[176:179]
s_waitcnt lgkmcnt(0)
v_perm_b32 v24, v114, v112, s85
v_perm_b32 v25, v118, v116, s85
v_mfma_f32_16x16x32_bf16 v[176:179], v[20:23], a[200:203], v[176:179]
v_perm_b32 v26, v122, v120, s85
v_perm_b32 v27, v126, v124, s85
v_mfma_f32_16x16x32_bf16 v[192:195], v[16:19], a[164:167], v[192:195]
v_perm_b32 v28, v130, v128, s85
v_perm_b32 v29, v134, v132, s85
v_mfma_f32_16x16x32_bf16 v[192:195], v[20:23], a[204:207], v[192:195]
v_perm_b32 v30, v138, v136, s85
v_perm_b32 v31, v142, v140, s85
v_mfma_f32_16x16x32_bf16 v[208:211], v[16:19], a[168:171], v[208:211]
buffer_load_dwordx4 a[32:35], v6, s[52:55], s94 offen
v_mfma_f32_16x16x32_bf16 v[208:211], v[20:23], a[208:211], v[208:211]
s_add_u32 s94, s94, 128
s_cmp_eq_u32 s94, 896
s_cselect_b32 s94, 0, s94
v_mfma_f32_16x16x32_bf16 v[224:227], v[16:19], a[172:175], v[224:227]
buffer_load_dwordx4 a[36:39], v6, s[52:55], s94 offen
v_mfma_f32_16x16x32_bf16 v[224:227], v[20:23], a[212:215], v[224:227]
s_add_u32 s94, s94, 128
s_cmp_eq_u32 s94, 896
s_cselect_b32 s94, 0, s94
v_mfma_f32_16x16x32_bf16 v[240:243], v[16:19], a[176:179], v[240:243]
v_mfma_f32_16x16x32_bf16 v[240:243], v[20:23], a[216:219], v[240:243]
v_mfma_f32_16x16x32_bf16 a[80:83], v[16:19], a[180:183], a[80:83]
v_mfma_f32_16x16x32_bf16 a[80:83], v[20:23], a[220:223], a[80:83]
buffer_load_dwordx4 a[40:43], v6, s[52:55], s94 offen
v_mfma_f32_16x16x32_bf16 a[96:99], v[16:19], a[184:187], a[96:99]
s_add_u32 s94, s94, 128
s_cmp_eq_u32 s94, 896
s_cselect_b32 s94, 0, s94
v_mfma_f32_16x16x32_bf16 a[96:99], v[20:23], a[224:227], a[96:99]
v_mfma_f32_16x16x32_bf16 a[112:115], v[16:19], a[188:191], a[112:115]
v_mfma_f32_16x16x32_bf16 a[112:115], v[20:23], a[228:231], a[112:115]
v_mfma_f32_16x16x32_bf16 a[128:131], v[16:19], a[192:195], a[128:131]
v_mfma_f32_16x16x32_bf16 a[128:131], v[20:23], a[232:235], a[128:131]
v_mfma_f32_16x16x32_bf16 a[144:147], v[16:19], a[196:199], a[144:147]
v_mfma_f32_16x16x32_bf16 a[144:147], v[20:23], a[236:239], a[144:147]
v_mfma_f32_16x16x32_bf16 v[180:183], v[24:27], a[160:163], v[180:183]
v_perm_b32 v16, v115, v113, s84
v_perm_b32 v17, v119, v117, s84
v_mfma_f32_16x16x32_bf16 v[180:183], v[28:31], a[200:203], v[180:183]
v_perm_b32 v18, v123, v121, s84
v_perm_b32 v19, v127, v125, s84
v_mfma_f32_16x16x32_bf16 v[196:199], v[24:27], a[164:167], v[196:199]
v_perm_b32 v20, v131, v129, s84
v_perm_b32 v21, v135, v133, s84
v_mfma_f32_16x16x32_bf16 v[196:199], v[28:31], a[204:207], v[196:199]
v_perm_b32 v22, v139, v137, s84
v_perm_b32 v23, v143, v141, s84
v_mfma_f32_16x16x32_bf16 v[212:215], v[24:27], a[168:171], v[212:215]
buffer_load_dwordx4 a[44:47], v6, s[52:55], s94 offen
v_mfma_f32_16x16x32_bf16 v[212:215], v[28:31], a[208:211], v[212:215]
s_add_u32 s94, s94, 128
s_cmp_eq_u32 s94, 896
s_cselect_b32 s94, 0, s94
v_mfma_f32_16x16x32_bf16 v[228:231], v[24:27], a[172:175], v[228:231]
buffer_load_dwordx4 a[48:51], v6, s[52:55], s94 offen
v_mfma_f32_16x16x32_bf16 v[228:231], v[28:31], a[212:215], v[228:231]
s_add_u32 s94, s94, 128
s_cmp_eq_u32 s94, 896
s_cselect_b32 s94, 0, s94
v_mfma_f32_16x16x32_bf16 v[244:247], v[24:27], a[176:179], v[244:247]
v_mfma_f32_16x16x32_bf16 v[244:247], v[28:31], a[216:219], v[244:247]
v_mfma_f32_16x16x32_bf16 a[84:87], v[24:27], a[180:183], a[84:87]
v_mfma_f32_16x16x32_bf16 a[84:87], v[28:31], a[220:223], a[84:87]
v_mfma_f32_16x16x32_bf16 a[100:103], v[24:27], a[184:187], a[100:103]
v_mfma_f32_16x16x32_bf16 a[100:103], v[28:31], a[224:227], a[100:103]
v_mfma_f32_16x16x32_bf16 a[116:119], v[24:27], a[188:191], a[116:119]
v_mfma_f32_16x16x32_bf16 a[116:119], v[28:31], a[228:231], a[116:119]
v_mfma_f32_16x16x32_bf16 a[132:135], v[24:27], a[192:195], a[132:135]
v_mfma_f32_16x16x32_bf16 a[132:135], v[28:31], a[232:235], a[132:135]
v_mfma_f32_16x16x32_bf16 a[148:151], v[24:27], a[196:199], a[148:151]
v_mfma_f32_16x16x32_bf16 a[148:151], v[28:31], a[236:239], a[148:151]
v_mfma_f32_16x16x32_bf16 v[184:187], v[16:19], a[160:163], v[184:187]
v_perm_b32 v24, v115, v113, s85
v_perm_b32 v25, v119, v117, s85
v_mfma_f32_16x16x32_bf16 v[184:187], v[20:23], a[200:203], v[184:187]
v_perm_b32 v26, v123, v121, s85
v_perm_b32 v27, v127, v125, s85
v_mfma_f32_16x16x32_bf16 v[200:203], v[16:19], a[164:167], v[200:203]
v_perm_b32 v28, v131, v129, s85
v_perm_b32 v29, v135, v133, s85
v_mfma_f32_16x16x32_bf16 v[200:203], v[20:23], a[204:207], v[200:203]
v_perm_b32 v30, v139, v137, s85
v_perm_b32 v31, v143, v141, s85
v_mfma_f32_16x16x32_bf16 v[216:219], v[16:19], a[168:171], v[216:219]
)"
R"(buffer_load_dwordx4 a[52:55], v6, s[52:55], s94 offen
v_mfma_f32_16x16x32_bf16 v[216:219], v[20:23], a[208:211], v[216:219]
s_add_u32 s94, s94, 128
s_cmp_eq_u32 s94, 896
s_cselect_b32 s94, 0, s94
v_mfma_f32_16x16x32_bf16 v[232:235], v[16:19], a[172:175], v[232:235]
v_mfma_f32_16x16x32_bf16 v[232:235], v[20:23], a[212:215], v[232:235]
v_mfma_f32_16x16x32_bf16 v[248:251], v[16:19], a[176:179], v[248:251]
v_mfma_f32_16x16x32_bf16 v[248:251], v[20:23], a[216:219], v[248:251]
v_mfma_f32_16x16x32_bf16 a[88:91], v[16:19], a[180:183], a[88:91]
v_mfma_f32_16x16x32_bf16 a[88:91], v[20:23], a[220:223], a[88:91]
v_mfma_f32_16x16x32_bf16 a[104:107], v[16:19], a[184:187], a[104:107]
buffer_load_dwordx4 a[56:59], v6, s[52:55], s94 offen
v_mfma_f32_16x16x32_bf16 a[104:107], v[20:23], a[224:227], a[104:107]
s_add_u32 s94, s94, 128
s_cmp_eq_u32 s94, 896
s_cselect_b32 s94, 0, s94
v_mfma_f32_16x16x32_bf16 a[120:123], v[16:19], a[188:191], a[120:123]
s_mov_b32 s80, 131072
v_mfma_f32_16x16x32_bf16 a[120:123], v[20:23], a[228:231], a[120:123]
v_mfma_f32_16x16x32_bf16 a[136:139], v[16:19], a[192:195], a[136:139]
v_mfma_f32_16x16x32_bf16 a[136:139], v[20:23], a[232:235], a[136:139]
v_mfma_f32_16x16x32_bf16 a[152:155], v[16:19], a[196:199], a[152:155]
v_mfma_f32_16x16x32_bf16 a[152:155], v[20:23], a[236:239], a[152:155]
v_mfma_f32_16x16x32_bf16 v[188:191], v[24:27], a[160:163], v[188:191]
s_waitcnt vmcnt(14)
v_perm_b32 v16, v146, v144, s84
v_perm_b32 v17, v150, v148, s84
v_mfma_f32_16x16x32_bf16 v[188:191], v[28:31], a[200:203], v[188:191]
v_perm_b32 v18, v154, v152, s84
v_perm_b32 v19, v158, v156, s84
v_mfma_f32_16x16x32_bf16 v[204:207], v[24:27], a[164:167], v[204:207]
ds_read_b128 a[160:163], v1 offset:20480
v_perm_b32 v20, v162, v160, s84
v_mfma_f32_16x16x32_bf16 v[204:207], v[28:31], a[204:207], v[204:207]
ds_read_b128 a[200:203], v1 offset:30720
v_perm_b32 v21, v166, v164, s84
v_mfma_f32_16x16x32_bf16 v[220:223], v[24:27], a[168:171], v[220:223]
ds_read_b128 a[164:167], v1 offset:21504
v_perm_b32 v22, v170, v168, s84
v_mfma_f32_16x16x32_bf16 v[220:223], v[28:31], a[208:211], v[220:223]
ds_read_b128 a[204:207], v1 offset:31744
v_perm_b32 v23, v174, v172, s84
v_mfma_f32_16x16x32_bf16 v[236:239], v[24:27], a[172:175], v[236:239]
ds_read_b128 a[168:171], v1 offset:22528
v_mfma_f32_16x16x32_bf16 v[236:239], v[28:31], a[212:215], v[236:239]
ds_read_b128 a[208:211], v1 offset:32768
v_mfma_f32_16x16x32_bf16 v[252:255], v[24:27], a[176:179], v[252:255]
ds_read_b128 a[172:175], v1 offset:23552
v_mfma_f32_16x16x32_bf16 v[252:255], v[28:31], a[216:219], v[252:255]
ds_read_b128 a[212:215], v1 offset:33792
v_mfma_f32_16x16x32_bf16 a[92:95], v[24:27], a[180:183], a[92:95]
ds_read_b128 a[176:179], v1 offset:24576
v_mfma_f32_16x16x32_bf16 a[92:95], v[28:31], a[220:223], a[92:95]
ds_read_b128 a[216:219], v1 offset:34816
v_mfma_f32_16x16x32_bf16 a[108:111], v[24:27], a[184:187], a[108:111]
ds_read_b128 a[180:183], v1 offset:25600
v_mfma_f32_16x16x32_bf16 a[108:111], v[28:31], a[224:227], a[108:111]
ds_read_b128 a[220:223], v1 offset:35840
v_mfma_f32_16x16x32_bf16 a[124:127], v[24:27], a[188:191], a[124:127]
ds_read_b128 a[184:187], v1 offset:26624
v_mfma_f32_16x16x32_bf16 a[124:127], v[28:31], a[228:231], a[124:127]
ds_read_b128 a[224:227], v1 offset:36864
v_mfma_f32_16x16x32_bf16 a[140:143], v[24:27], a[192:195], a[140:143]
s_waitcnt lgkmcnt(10)
ds_read_b128 a[188:191], v1 offset:27648
v_mfma_f32_16x16x32_bf16 a[140:143], v[28:31], a[232:235], a[140:143]
ds_read_b128 a[228:231], v1 offset:37888
v_mfma_f32_16x16x32_bf16 a[156:159], v[24:27], a[196:199], a[156:159]
ds_read_b128 a[192:195], v1 offset:28672
v_mfma_f32_16x16x32_bf16 a[156:159], v[28:31], a[236:239], a[156:159]
ds_read_b128 a[232:235], v1 offset:38912
v_mfma_f32_16x16x32_bf16 v[176:179], v[16:19], a[160:163], v[176:179]
s_waitcnt lgkmcnt(10)
ds_read_b128 a[196:199], v1 offset:29696
v_perm_b32 v24, v146, v144, s85
v_mfma_f32_16x16x32_bf16 v[176:179], v[20:23], a[200:203], v[176:179]
ds_read_b128 a[236:239], v1 offset:39936
v_perm_b32 v25, v150, v148, s85
v_mfma_f32_16x16x32_bf16 v[192:195], v[16:19], a[164:167], v[192:195]
v_perm_b32 v26, v154, v152, s85
v_perm_b32 v27, v158, v156, s85
v_mfma_f32_16x16x32_bf16 v[192:195], v[20:23], a[204:207], v[192:195]
v_perm_b32 v28, v162, v160, s85
v_perm_b32 v29, v166, v164, s85
v_mfma_f32_16x16x32_bf16 v[208:211], v[16:19], a[168:171], v[208:211]
v_perm_b32 v30, v170, v168, s85
v_perm_b32 v31, v174, v172, s85
v_mfma_f32_16x16x32_bf16 v[208:211], v[20:23], a[208:211], v[208:211]
buffer_load_dwordx2 v[64:65], v5, s[40:43], s80 offen nt
v_mfma_f32_16x16x32_bf16 v[224:227], v[16:19], a[172:175], v[224:227]
buffer_load_dwordx2 v[66:67], v5, s[40:43], s80 offen offset:512 nt
v_mfma_f32_16x16x32_bf16 v[224:227], v[20:23], a[212:215], v[224:227]
buffer_load_dwordx2 v[68:69], v5, s[40:43], s80 offen offset:1024 nt
s_waitcnt lgkmcnt(11)
v_mfma_f32_16x16x32_bf16 v[240:243], v[16:19], a[176:179], v[240:243]
buffer_load_dwordx2 v[70:71], v5, s[40:43], s80 offen offset:1536 nt
s_waitcnt lgkmcnt(10)
v_mfma_f32_16x16x32_bf16 v[240:243], v[20:23], a[216:219], v[240:243]
buffer_load_dwordx2 v[72:73], v5, s[40:43], s80 offen offset:2048 nt
s_waitcnt lgkmcnt(9)
v_mfma_f32_16x16x32_bf16 a[80:83], v[16:19], a[180:183], a[80:83]
buffer_load_dwordx2 v[74:75], v5, s[40:43], s80 offen offset:2560 nt
s_waitcnt lgkmcnt(8)
v_mfma_f32_16x16x32_bf16 a[80:83], v[20:23], a[220:223], a[80:83]
s_waitcnt lgkmcnt(7)
v_mfma_f32_16x16x32_bf16 a[96:99], v[16:19], a[184:187], a[96:99]
buffer_load_dwordx2 v[76:77], v5, s[40:43], s80 offen offset:3072 nt
s_waitcnt lgkmcnt(6)
v_mfma_f32_16x16x32_bf16 a[96:99], v[20:23], a[224:227], a[96:99]
buffer_load_dwordx2 v[78:79], v5, s[40:43], s80 offen offset:3584 nt
s_waitcnt lgkmcnt(5)
v_mfma_f32_16x16x32_bf16 a[112:115], v[16:19], a[188:191], a[112:115]
s_add_u32 s80, s80, 16384
s_waitcnt lgkmcnt(4)
v_mfma_f32_16x16x32_bf16 a[112:115], v[20:23], a[228:231], a[112:115]
s_waitcnt lgkmcnt(3)
v_mfma_f32_16x16x32_bf16 a[128:131], v[16:19], a[192:195], a[128:131]
s_waitcnt lgkmcnt(2)
v_mfma_f32_16x16x32_bf16 a[128:131], v[20:23], a[232:235], a[128:131]
s_waitcnt lgkmcnt(1)
v_mfma_f32_16x16x32_bf16 a[144:147], v[16:19], a[196:199], a[144:147]
s_waitcnt lgkmcnt(0)
v_mfma_f32_16x16x32_bf16 a[144:147], v[20:23], a[236:239], a[144:147]
v_mfma_f32_16x16x32_bf16 v[180:183], v[24:27], a[160:163], v[180:183]
v_perm_b32 v16, v147, v145, s84
v_perm_b32 v17, v151, v149, s84
v_mfma_f32_16x16x32_bf16 v[180:183], v[28:31], a[200:203], v[180:183]
v_perm_b32 v18, v155, v153, s84
v_perm_b32 v19, v159, v157, s84
v_mfma_f32_16x16x32_bf16 v[196:199], v[24:27], a[164:167], v[196:199]
v_perm_b32 v20, v163, v161, s84
v_perm_b32 v21, v167, v165, s84
v_mfma_f32_16x16x32_bf16 v[196:199], v[28:31], a[204:207], v[196:199]
v_perm_b32 v22, v171, v169, s84
v_perm_b32 v23, v175, v173, s84
v_mfma_f32_16x16x32_bf16 v[212:215], v[24:27], a[168:171], v[212:215]
buffer_load_dwordx2 v[80:81], v5, s[40:43], s80 offen nt
v_mfma_f32_16x16x32_bf16 v[212:215], v[28:31], a[208:211], v[212:215]
buffer_load_dwordx2 v[82:83], v5, s[40:43], s80 offen offset:512 nt
v_mfma_f32_16x16x32_bf16 v[228:231], v[24:27], a[172:175], v[228:231]
v_mfma_f32_16x16x32_bf16 v[228:231], v[28:31], a[212:215], v[228:231]
v_mfma_f32_16x16x32_bf16 v[244:247], v[24:27], a[176:179], v[244:247]
buffer_load_dwordx2 v[84:85], v5, s[40:43], s80 offen offset:1024 nt
v_mfma_f32_16x16x32_bf16 v[244:247], v[28:31], a[216:219], v[244:247]
v_mfma_f32_16x16x32_bf16 a[84:87], v[24:27], a[180:183], a[84:87]
v_mfma_f32_16x16x32_bf16 a[84:87], v[28:31], a[220:223], a[84:87]
v_mfma_f32_16x16x32_bf16 a[100:103], v[24:27], a[184:187], a[100:103]
v_mfma_f32_16x16x32_bf16 a[100:103], v[28:31], a[224:227], a[100:103]
v_mfma_f32_16x16x32_bf16 a[116:119], v[24:27], a[188:191], a[116:119]
v_mfma_f32_16x16x32_bf16 a[116:119], v[28:31], a[228:231], a[116:119]
v_mfma_f32_16x16x32_bf16 a[132:135], v[24:27], a[192:195], a[132:135]
buffer_load_dwordx2 v[86:87], v5, s[40:43], s80 offen offset:1536 nt
v_mfma_f32_16x16x32_bf16 a[132:135], v[28:31], a[232:235], a[132:135]
buffer_load_dwordx2 v[88:89], v5, s[40:43], s80 offen offset:2048 nt
v_mfma_f32_16x16x32_bf16 a[148:151], v[24:27], a[196:199], a[148:151]
v_mfma_f32_16x16x32_bf16 a[148:151], v[28:31], a[236:239], a[148:151]
v_mfma_f32_16x16x32_bf16 v[184:187], v[16:19], a[160:163], v[184:187]
v_perm_b32 v24, v147, v145, s85
v_perm_b32 v25, v151, v149, s85
v_mfma_f32_16x16x32_bf16 v[184:187], v[20:23], a[200:203], v[184:187]
v_perm_b32 v26, v155, v153, s85
v_perm_b32 v27, v159, v157, s85
v_mfma_f32_16x16x32_bf16 v[200:203], v[16:19], a[164:167], v[200:203]
v_perm_b32 v28, v163, v161, s85
v_perm_b32 v29, v167, v165, s85
v_mfma_f32_16x16x32_bf16 v[200:203], v[20:23], a[204:207], v[200:203]
v_perm_b32 v30, v171, v169, s85
v_perm_b32 v31, v175, v173, s85
v_mfma_f32_16x16x32_bf16 v[216:219], v[16:19], a[168:171], v[216:219]
buffer_load_dwordx2 v[90:91], v5, s[40:43], s80 offen offset:2560 nt
v_mfma_f32_16x16x32_bf16 v[216:219], v[20:23], a[208:211], v[216:219]
buffer_load_dwordx2 v[92:93], v5, s[40:43], s80 offen offset:3072 nt
v_mfma_f32_16x16x32_bf16 v[232:235], v[16:19], a[172:175], v[232:235]
v_mfma_f32_16x16x32_bf16 v[232:235], v[20:23], a[212:215], v[232:235]
v_mfma_f32_16x16x32_bf16 v[248:251], v[16:19], a[176:179], v[248:251]
v_mfma_f32_16x16x32_bf16 v[248:251], v[20:23], a[216:219], v[248:251]
v_mfma_f32_16x16x32_bf16 a[88:91], v[16:19], a[180:183], a[88:91]
v_mfma_f32_16x16x32_bf16 a[88:91], v[20:23], a[220:223], a[88:91]
v_mfma_f32_16x16x32_bf16 a[104:107], v[16:19], a[184:187], a[104:107]
buffer_load_dwordx2 v[94:95], v5, s[40:43], s80 offen offset:3584 nt
v_mfma_f32_16x16x32_bf16 a[104:107], v[20:23], a[224:227], a[104:107]
s_mov_b32 s80, 196608
v_mfma_f32_16x16x32_bf16 a[120:123], v[16:19], a[188:191], a[120:123]
buffer_load_dwordx2 v[96:97], v5, s[40:43], s80 offen nt
v_mfma_f32_16x16x32_bf16 a[120:123], v[20:23], a[228:231], a[120:123]
v_mfma_f32_16x16x32_bf16 a[136:139], v[16:19], a[192:195], a[136:139]
v_mfma_f32_16x16x32_bf16 a[136:139], v[20:23], a[232:235], a[136:139]
v_mfma_f32_16x16x32_bf16 a[152:155], v[16:19], a[196:199], a[152:155]
v_mfma_f32_16x16x32_bf16 a[152:155], v[20:23], a[236:239], a[152:155]
v_mfma_f32_16x16x32_bf16 v[188:191], v[24:27], a[160:163], v[188:191]
buffer_load_dwordx2 v[98:99], v5, s[40:43], s80 offen offset:512 nt
buffer_load_dwordx2 v[100:101], v5, s[40:43], s80 offen offset:1024 nt
buffer_load_dwordx2 v[102:103], v5, s[40:43], s80 offen offset:1536 nt
buffer_load_dwordx2 v[104:105], v5, s[40:43], s80 offen offset:2048 nt
buffer_load_dwordx2 v[106:107], v5, s[40:43], s80 offen offset:2560 nt
buffer_load_dwordx2 v[108:109], v5, s[40:43], s80 offen offset:3072 nt
buffer_load_dwordx2 v[110:111], v5, s[40:43], s80 offen offset:3584 nt
s_add_u32 s80, s80, 16384
buffer_load_dwordx2 v[112:113], v5, s[40:43], s80 offen nt
)"
R"(buffer_load_dwordx2 v[114:115], v5, s[40:43], s80 offen offset:512 nt
buffer_load_dwordx2 v[116:117], v5, s[40:43], s80 offen offset:1024 nt
buffer_load_dwordx2 v[118:119], v5, s[40:43], s80 offen offset:1536 nt
buffer_load_dwordx2 v[120:121], v5, s[40:43], s80 offen offset:2048 nt
buffer_load_dwordx2 v[122:123], v5, s[40:43], s80 offen offset:2560 nt
buffer_load_dwordx2 v[124:125], v5, s[40:43], s80 offen offset:3072 nt
buffer_load_dwordx2 v[126:127], v5, s[40:43], s80 offen offset:3584 nt
v_add_u32 v6, 0x7000, v6
buffer_load_dwordx4 v[32:35], v6, s[52:55], s94 offen
s_add_u32 s94, s94, 128
s_cmp_eq_u32 s94, 896
s_cselect_b32 s94, 0, s94
buffer_load_dwordx4 v[36:39], v6, s[52:55], s94 offen
s_add_u32 s94, s94, 128
s_cmp_eq_u32 s94, 896
s_cselect_b32 s94, 0, s94
s_waitcnt vmcnt(18)
v_perm_b32 v16, v66, v64, s84
v_perm_b32 v17, v70, v68, s84
v_mfma_f32_16x16x32_bf16 v[188:191], v[28:31], a[200:203], v[188:191]
v_perm_b32 v18, v74, v72, s84
v_perm_b32 v19, v78, v76, s84
v_mfma_f32_16x16x32_bf16 v[204:207], v[24:27], a[164:167], v[204:207]
ds_read_b128 a[160:163], v1 offset:40960
v_perm_b32 v20, v82, v80, s84
v_mfma_f32_16x16x32_bf16 v[204:207], v[28:31], a[204:207], v[204:207]
ds_read_b128 a[200:203], v1 offset:51200
v_perm_b32 v21, v86, v84, s84
v_mfma_f32_16x16x32_bf16 v[220:223], v[24:27], a[168:171], v[220:223]
ds_read_b128 a[164:167], v1 offset:41984
v_perm_b32 v22, v90, v88, s84
v_mfma_f32_16x16x32_bf16 v[220:223], v[28:31], a[208:211], v[220:223]
ds_read_b128 a[204:207], v1 offset:52224
v_perm_b32 v23, v94, v92, s84
v_mfma_f32_16x16x32_bf16 v[236:239], v[24:27], a[172:175], v[236:239]
ds_read_b128 a[168:171], v1 offset:43008
v_mfma_f32_16x16x32_bf16 v[236:239], v[28:31], a[212:215], v[236:239]
ds_read_b128 a[208:211], v1 offset:53248
v_mfma_f32_16x16x32_bf16 v[252:255], v[24:27], a[176:179], v[252:255]
ds_read_b128 a[172:175], v1 offset:44032
v_mfma_f32_16x16x32_bf16 v[252:255], v[28:31], a[216:219], v[252:255]
ds_read_b128 a[212:215], v1 offset:54272
v_mfma_f32_16x16x32_bf16 a[92:95], v[24:27], a[180:183], a[92:95]
ds_read_b128 a[176:179], v1 offset:45056
v_mfma_f32_16x16x32_bf16 a[92:95], v[28:31], a[220:223], a[92:95]
ds_read_b128 a[216:219], v1 offset:55296
v_mfma_f32_16x16x32_bf16 a[108:111], v[24:27], a[184:187], a[108:111]
ds_read_b128 a[180:183], v1 offset:46080
v_mfma_f32_16x16x32_bf16 a[108:111], v[28:31], a[224:227], a[108:111]
ds_read_b128 a[220:223], v1 offset:56320
v_mfma_f32_16x16x32_bf16 a[124:127], v[24:27], a[188:191], a[124:127]
ds_read_b128 a[184:187], v1 offset:47104
v_mfma_f32_16x16x32_bf16 a[124:127], v[28:31], a[228:231], a[124:127]
ds_read_b128 a[224:227], v1 offset:57344
v_mfma_f32_16x16x32_bf16 a[140:143], v[24:27], a[192:195], a[140:143]
s_waitcnt lgkmcnt(10)
ds_read_b128 a[188:191], v1 offset:48128
v_mfma_f32_16x16x32_bf16 a[140:143], v[28:31], a[232:235], a[140:143]
ds_read_b128 a[228:231], v1 offset:58368
v_mfma_f32_16x16x32_bf16 a[156:159], v[24:27], a[196:199], a[156:159]
ds_read_b128 a[192:195], v1 offset:49152
v_mfma_f32_16x16x32_bf16 a[156:159], v[28:31], a[236:239], a[156:159]
ds_read_b128 a[232:235], v1 offset:59392
v_mfma_f32_16x16x32_bf16 v[176:179], v[16:19], a[160:163], v[176:179]
s_waitcnt lgkmcnt(10)
ds_read_b128 a[196:199], v1 offset:50176
v_perm_b32 v24, v66, v64, s85
v_mfma_f32_16x16x32_bf16 v[176:179], v[20:23], a[200:203], v[176:179]
ds_read_b128 a[236:239], v1 offset:60416
v_perm_b32 v25, v70, v68, s85
v_mfma_f32_16x16x32_bf16 v[192:195], v[16:19], a[164:167], v[192:195]
v_perm_b32 v26, v74, v72, s85
v_perm_b32 v27, v78, v76, s85
v_mfma_f32_16x16x32_bf16 v[192:195], v[20:23], a[204:207], v[192:195]
v_perm_b32 v28, v82, v80, s85
v_perm_b32 v29, v86, v84, s85
v_mfma_f32_16x16x32_bf16 v[208:211], v[16:19], a[168:171], v[208:211]
v_perm_b32 v30, v90, v88, s85
v_perm_b32 v31, v94, v92, s85
v_mfma_f32_16x16x32_bf16 v[208:211], v[20:23], a[208:211], v[208:211]
buffer_load_dwordx4 v[40:43], v6, s[52:55], s94 offen
v_mfma_f32_16x16x32_bf16 v[224:227], v[16:19], a[172:175], v[224:227]
s_add_u32 s94, s94, 128
s_cmp_eq_u32 s94, 896
s_cselect_b32 s94, 0, s94
v_mfma_f32_16x16x32_bf16 v[224:227], v[20:23], a[212:215], v[224:227]
buffer_load_dwordx4 v[44:47], v6, s[52:55], s94 offen
s_waitcnt lgkmcnt(11)
v_mfma_f32_16x16x32_bf16 v[240:243], v[16:19], a[176:179], v[240:243]
s_add_u32 s94, s94, 128
s_cmp_eq_u32 s94, 896
s_cselect_b32 s94, 0, s94
s_waitcnt lgkmcnt(10)
v_mfma_f32_16x16x32_bf16 v[240:243], v[20:23], a[216:219], v[240:243]
buffer_load_dwordx4 v[48:51], v6, s[52:55], s94 offen
s_waitcnt lgkmcnt(9)
v_mfma_f32_16x16x32_bf16 a[80:83], v[16:19], a[180:183], a[80:83]
s_add_u32 s94, s94, 128
s_cmp_eq_u32 s94, 896
s_cselect_b32 s94, 0, s94
s_waitcnt lgkmcnt(8)
v_mfma_f32_16x16x32_bf16 a[80:83], v[20:23], a[220:223], a[80:83]
buffer_load_dwordx4 v[52:55], v6, s[52:55], s94 offen
s_waitcnt lgkmcnt(7)
v_mfma_f32_16x16x32_bf16 a[96:99], v[16:19], a[184:187], a[96:99]
s_add_u32 s94, s94, 128
s_cmp_eq_u32 s94, 896
s_cselect_b32 s94, 0, s94
s_waitcnt lgkmcnt(6)
v_mfma_f32_16x16x32_bf16 a[96:99], v[20:23], a[224:227], a[96:99]
s_waitcnt lgkmcnt(5)
v_mfma_f32_16x16x32_bf16 a[112:115], v[16:19], a[188:191], a[112:115]
s_waitcnt lgkmcnt(4)
v_mfma_f32_16x16x32_bf16 a[112:115], v[20:23], a[228:231], a[112:115]
s_waitcnt lgkmcnt(3)
v_mfma_f32_16x16x32_bf16 a[128:131], v[16:19], a[192:195], a[128:131]
s_waitcnt lgkmcnt(2)
v_mfma_f32_16x16x32_bf16 a[128:131], v[20:23], a[232:235], a[128:131]
s_waitcnt lgkmcnt(1)
v_mfma_f32_16x16x32_bf16 a[144:147], v[16:19], a[196:199], a[144:147]
buffer_load_dwordx4 v[56:59], v6, s[52:55], s94 offen
s_waitcnt lgkmcnt(0)
v_mfma_f32_16x16x32_bf16 a[144:147], v[20:23], a[236:239], a[144:147]
s_add_u32 s94, s94, 128
s_cmp_eq_u32 s94, 896
s_cselect_b32 s94, 0, s94
v_mfma_f32_16x16x32_bf16 v[180:183], v[24:27], a[160:163], v[180:183]
s_add_u32 s82, s95, 0
s_and_b32 s82, s82, 7
s_mul_i32 s82, s82, 28672
v_perm_b32 v16, v67, v65, s84
v_perm_b32 v17, v71, v69, s84
v_mfma_f32_16x16x32_bf16 v[180:183], v[28:31], a[200:203], v[180:183]
v_perm_b32 v18, v75, v73, s84
v_perm_b32 v19, v79, v77, s84
v_mfma_f32_16x16x32_bf16 v[196:199], v[24:27], a[164:167], v[196:199]
v_perm_b32 v20, v83, v81, s84
v_perm_b32 v21, v87, v85, s84
v_mfma_f32_16x16x32_bf16 v[196:199], v[28:31], a[204:207], v[196:199]
v_perm_b32 v22, v91, v89, s84
v_perm_b32 v23, v95, v93, s84
v_mfma_f32_16x16x32_bf16 v[212:215], v[24:27], a[168:171], v[212:215]
v_mfma_f32_16x16x32_bf16 v[212:215], v[28:31], a[208:211], v[212:215]
v_mfma_f32_16x16x32_bf16 v[228:231], v[24:27], a[172:175], v[228:231]
v_mfma_f32_16x16x32_bf16 v[228:231], v[28:31], a[212:215], v[228:231]
buffer_load_dwordx4 v[128:131], v7, s[48:51], s82 offen
v_mfma_f32_16x16x32_bf16 v[244:247], v[24:27], a[176:179], v[244:247]
v_mfma_f32_16x16x32_bf16 v[244:247], v[28:31], a[216:219], v[244:247]
v_mfma_f32_16x16x32_bf16 a[84:87], v[24:27], a[180:183], a[84:87]
v_mfma_f32_16x16x32_bf16 a[84:87], v[28:31], a[220:223], a[84:87]
v_mfma_f32_16x16x32_bf16 a[100:103], v[24:27], a[184:187], a[100:103]
buffer_load_dwordx4 v[132:135], v7, s[48:51], s82 offen offset:896
v_mfma_f32_16x16x32_bf16 a[100:103], v[28:31], a[224:227], a[100:103]
v_mfma_f32_16x16x32_bf16 a[116:119], v[24:27], a[188:191], a[116:119]
v_mfma_f32_16x16x32_bf16 a[116:119], v[28:31], a[228:231], a[116:119]
v_mfma_f32_16x16x32_bf16 a[132:135], v[24:27], a[192:195], a[132:135]
v_mfma_f32_16x16x32_bf16 a[132:135], v[28:31], a[232:235], a[132:135]
v_mfma_f32_16x16x32_bf16 a[148:151], v[24:27], a[196:199], a[148:151]
v_mfma_f32_16x16x32_bf16 a[148:151], v[28:31], a[236:239], a[148:151]
v_mfma_f32_16x16x32_bf16 v[184:187], v[16:19], a[160:163], v[184:187]
v_perm_b32 v24, v67, v65, s85
v_perm_b32 v25, v71, v69, s85
v_mfma_f32_16x16x32_bf16 v[184:187], v[20:23], a[200:203], v[184:187]
v_perm_b32 v26, v75, v73, s85
v_perm_b32 v27, v79, v77, s85
v_mfma_f32_16x16x32_bf16 v[200:203], v[16:19], a[164:167], v[200:203]
v_perm_b32 v28, v83, v81, s85
v_perm_b32 v29, v87, v85, s85
v_mfma_f32_16x16x32_bf16 v[200:203], v[20:23], a[204:207], v[200:203]
v_perm_b32 v30, v91, v89, s85
v_perm_b32 v31, v95, v93, s85
v_mfma_f32_16x16x32_bf16 v[216:219], v[16:19], a[168:171], v[216:219]
buffer_load_dwordx4 v[136:139], v7, s[48:51], s82 offen offset:1792
v_mfma_f32_16x16x32_bf16 v[216:219], v[20:23], a[208:211], v[216:219]
v_mfma_f32_16x16x32_bf16 v[232:235], v[16:19], a[172:175], v[232:235]
v_mfma_f32_16x16x32_bf16 v[232:235], v[20:23], a[212:215], v[232:235]
v_mfma_f32_16x16x32_bf16 v[248:251], v[16:19], a[176:179], v[248:251]
v_mfma_f32_16x16x32_bf16 v[248:251], v[20:23], a[216:219], v[248:251]
v_mfma_f32_16x16x32_bf16 a[88:91], v[16:19], a[180:183], a[88:91]
v_mfma_f32_16x16x32_bf16 a[88:91], v[20:23], a[220:223], a[88:91]
buffer_load_dwordx4 v[140:143], v7, s[48:51], s82 offen offset:2688
v_mfma_f32_16x16x32_bf16 a[104:107], v[16:19], a[184:187], a[104:107]
s_add_u32 s83, s82, 3584
v_mfma_f32_16x16x32_bf16 a[104:107], v[20:23], a[224:227], a[104:107]
v_mfma_f32_16x16x32_bf16 a[120:123], v[16:19], a[188:191], a[120:123]
v_mfma_f32_16x16x32_bf16 a[120:123], v[20:23], a[228:231], a[120:123]
buffer_load_dwordx4 v[144:147], v7, s[48:51], s83 offen
v_mfma_f32_16x16x32_bf16 a[136:139], v[16:19], a[192:195], a[136:139]
v_mfma_f32_16x16x32_bf16 a[136:139], v[20:23], a[232:235], a[136:139]
v_mfma_f32_16x16x32_bf16 a[152:155], v[16:19], a[196:199], a[152:155]
v_mfma_f32_16x16x32_bf16 a[152:155], v[20:23], a[236:239], a[152:155]
v_mfma_f32_16x16x32_bf16 v[188:191], v[24:27], a[160:163], v[188:191]
s_waitcnt vmcnt(12)
v_perm_b32 v16, v98, v96, s84
v_perm_b32 v17, v102, v100, s84
v_mfma_f32_16x16x32_bf16 v[188:191], v[28:31], a[200:203], v[188:191]
v_perm_b32 v18, v106, v104, s84
v_perm_b32 v19, v110, v108, s84
v_mfma_f32_16x16x32_bf16 v[204:207], v[24:27], a[164:167], v[204:207]
ds_read_b128 a[160:163], v1 offset:61440
)"
R"(v_perm_b32 v20, v114, v112, s84
v_mfma_f32_16x16x32_bf16 v[204:207], v[28:31], a[204:207], v[204:207]
ds_read_b128 a[200:203], v2 offset:6144
v_perm_b32 v21, v118, v116, s84
v_mfma_f32_16x16x32_bf16 v[220:223], v[24:27], a[168:171], v[220:223]
ds_read_b128 a[164:167], v1 offset:62464
v_perm_b32 v22, v122, v120, s84
v_mfma_f32_16x16x32_bf16 v[220:223], v[28:31], a[208:211], v[220:223]
ds_read_b128 a[204:207], v2 offset:7168
v_perm_b32 v23, v126, v124, s84
v_mfma_f32_16x16x32_bf16 v[236:239], v[24:27], a[172:175], v[236:239]
ds_read_b128 a[168:171], v1 offset:63488
v_mfma_f32_16x16x32_bf16 v[236:239], v[28:31], a[212:215], v[236:239]
ds_read_b128 a[208:211], v2 offset:8192
v_mfma_f32_16x16x32_bf16 v[252:255], v[24:27], a[176:179], v[252:255]
ds_read_b128 a[172:175], v1 offset:64512
v_mfma_f32_16x16x32_bf16 v[252:255], v[28:31], a[216:219], v[252:255]
ds_read_b128 a[212:215], v2 offset:9216
v_mfma_f32_16x16x32_bf16 a[92:95], v[24:27], a[180:183], a[92:95]
ds_read_b128 a[176:179], v2 offset:0
v_mfma_f32_16x16x32_bf16 a[92:95], v[28:31], a[220:223], a[92:95]
ds_read_b128 a[216:219], v2 offset:10240
v_mfma_f32_16x16x32_bf16 a[108:111], v[24:27], a[184:187], a[108:111]
ds_read_b128 a[180:183], v2 offset:1024
v_mfma_f32_16x16x32_bf16 a[108:111], v[28:31], a[224:227], a[108:111]
ds_read_b128 a[220:223], v2 offset:11264
v_mfma_f32_16x16x32_bf16 a[124:127], v[24:27], a[188:191], a[124:127]
ds_read_b128 a[184:187], v2 offset:2048
v_mfma_f32_16x16x32_bf16 a[124:127], v[28:31], a[228:231], a[124:127]
ds_read_b128 a[224:227], v2 offset:12288
v_mfma_f32_16x16x32_bf16 a[140:143], v[24:27], a[192:195], a[140:143]
s_waitcnt lgkmcnt(10)
ds_read_b128 a[188:191], v2 offset:3072
v_mfma_f32_16x16x32_bf16 a[140:143], v[28:31], a[232:235], a[140:143]
ds_read_b128 a[228:231], v2 offset:13312
v_mfma_f32_16x16x32_bf16 a[156:159], v[24:27], a[196:199], a[156:159]
ds_read_b128 a[192:195], v2 offset:4096
v_mfma_f32_16x16x32_bf16 a[156:159], v[28:31], a[236:239], a[156:159]
ds_read_b128 a[232:235], v2 offset:14336
v_mfma_f32_16x16x32_bf16 v[176:179], v[16:19], a[160:163], v[176:179]
s_waitcnt lgkmcnt(10)
ds_read_b128 a[196:199], v2 offset:5120
v_perm_b32 v24, v98, v96, s85
v_mfma_f32_16x16x32_bf16 v[176:179], v[20:23], a[200:203], v[176:179]
ds_read_b128 a[236:239], v2 offset:15360
v_perm_b32 v25, v102, v100, s85
v_mfma_f32_16x16x32_bf16 v[192:195], v[16:19], a[164:167], v[192:195]
v_perm_b32 v26, v106, v104, s85
v_perm_b32 v27, v110, v108, s85
v_mfma_f32_16x16x32_bf16 v[192:195], v[20:23], a[204:207], v[192:195]
v_perm_b32 v28, v114, v112, s85
v_perm_b32 v29, v118, v116, s85
v_mfma_f32_16x16x32_bf16 v[208:211], v[16:19], a[168:171], v[208:211]
v_perm_b32 v30, v122, v120, s85
v_perm_b32 v31, v126, v124, s85
v_mfma_f32_16x16x32_bf16 v[208:211], v[20:23], a[208:211], v[208:211]
buffer_load_dwordx4 v[148:151], v7, s[48:51], s83 offen offset:896
v_mfma_f32_16x16x32_bf16 v[224:227], v[16:19], a[172:175], v[224:227]
buffer_load_dwordx4 v[152:155], v7, s[48:51], s83 offen offset:1792
v_mfma_f32_16x16x32_bf16 v[224:227], v[20:23], a[212:215], v[224:227]
buffer_load_dwordx4 v[156:159], v7, s[48:51], s83 offen offset:2688
s_waitcnt lgkmcnt(11)
v_mfma_f32_16x16x32_bf16 v[240:243], v[16:19], a[176:179], v[240:243]
s_add_u32 s82, s95, 1
s_and_b32 s82, s82, 7
s_mul_i32 s82, s82, 28672
s_waitcnt lgkmcnt(10)
v_mfma_f32_16x16x32_bf16 v[240:243], v[20:23], a[216:219], v[240:243]
s_waitcnt lgkmcnt(9)
v_mfma_f32_16x16x32_bf16 a[80:83], v[16:19], a[180:183], a[80:83]
s_waitcnt lgkmcnt(8)
v_mfma_f32_16x16x32_bf16 a[80:83], v[20:23], a[220:223], a[80:83]
s_waitcnt lgkmcnt(7)
v_mfma_f32_16x16x32_bf16 a[96:99], v[16:19], a[184:187], a[96:99]
s_waitcnt lgkmcnt(6)
v_mfma_f32_16x16x32_bf16 a[96:99], v[20:23], a[224:227], a[96:99]
s_waitcnt lgkmcnt(5)
v_mfma_f32_16x16x32_bf16 a[112:115], v[16:19], a[188:191], a[112:115]
s_waitcnt lgkmcnt(4)
v_mfma_f32_16x16x32_bf16 a[112:115], v[20:23], a[228:231], a[112:115]
buffer_load_dwordx4 v[160:163], v7, s[48:51], s82 offen
s_waitcnt lgkmcnt(3)
v_mfma_f32_16x16x32_bf16 a[128:131], v[16:19], a[192:195], a[128:131]
s_waitcnt lgkmcnt(2)
v_mfma_f32_16x16x32_bf16 a[128:131], v[20:23], a[232:235], a[128:131]
s_waitcnt lgkmcnt(1)
v_mfma_f32_16x16x32_bf16 a[144:147], v[16:19], a[196:199], a[144:147]
s_waitcnt lgkmcnt(0)
v_mfma_f32_16x16x32_bf16 a[144:147], v[20:23], a[236:239], a[144:147]
v_mfma_f32_16x16x32_bf16 v[180:183], v[24:27], a[160:163], v[180:183]
v_perm_b32 v16, v99, v97, s84
v_perm_b32 v17, v103, v101, s84
v_mfma_f32_16x16x32_bf16 v[180:183], v[28:31], a[200:203], v[180:183]
v_perm_b32 v18, v107, v105, s84
v_perm_b32 v19, v111, v109, s84
v_mfma_f32_16x16x32_bf16 v[196:199], v[24:27], a[164:167], v[196:199]
v_perm_b32 v20, v115, v113, s84
v_perm_b32 v21, v119, v117, s84
v_mfma_f32_16x16x32_bf16 v[196:199], v[28:31], a[204:207], v[196:199]
v_perm_b32 v22, v123, v121, s84
v_perm_b32 v23, v127, v125, s84
v_mfma_f32_16x16x32_bf16 v[212:215], v[24:27], a[168:171], v[212:215]
v_accvgpr_read_b32 v13, a242
v_add_u32 v14, 0xe000, v13
v_mfma_f32_16x16x32_bf16 v[212:215], v[28:31], a[208:211], v[212:215]
buffer_load_dwordx4 v[164:167], v7, s[48:51], s82 offen offset:896
v_mfma_f32_16x16x32_bf16 v[228:231], v[24:27], a[172:175], v[228:231]
ds_write_b128 v13, a[0:3] offset:0
v_mfma_f32_16x16x32_bf16 v[228:231], v[28:31], a[212:215], v[228:231]
ds_write_b128 v13, a[4:7] offset:2048
v_mfma_f32_16x16x32_bf16 v[244:247], v[24:27], a[176:179], v[244:247]
ds_write_b128 v13, a[8:11] offset:4096
v_mfma_f32_16x16x32_bf16 v[244:247], v[28:31], a[216:219], v[244:247]
ds_write_b128 v13, a[12:15] offset:6144
v_mfma_f32_16x16x32_bf16 a[84:87], v[24:27], a[180:183], a[84:87]
buffer_load_dwordx4 v[168:171], v7, s[48:51], s82 offen offset:1792
v_mfma_f32_16x16x32_bf16 a[84:87], v[28:31], a[220:223], a[84:87]
ds_write_b128 v13, a[16:19] offset:8192
v_mfma_f32_16x16x32_bf16 a[100:103], v[24:27], a[184:187], a[100:103]
ds_write_b128 v13, a[20:23] offset:10240
v_mfma_f32_16x16x32_bf16 a[100:103], v[28:31], a[224:227], a[100:103]
ds_write_b128 v13, a[24:27] offset:12288
v_mfma_f32_16x16x32_bf16 a[116:119], v[24:27], a[188:191], a[116:119]
ds_write_b128 v13, a[32:35] offset:28672
v_mfma_f32_16x16x32_bf16 a[116:119], v[28:31], a[228:231], a[116:119]
buffer_load_dwordx4 v[172:175], v7, s[48:51], s82 offen offset:2688
v_mfma_f32_16x16x32_bf16 a[132:135], v[24:27], a[192:195], a[132:135]
ds_write_b128 v13, a[36:39] offset:30720
v_mfma_f32_16x16x32_bf16 a[132:135], v[28:31], a[232:235], a[132:135]
ds_write_b128 v13, a[40:43] offset:32768
v_mfma_f32_16x16x32_bf16 a[148:151], v[24:27], a[196:199], a[148:151]
ds_write_b128 v13, a[44:47] offset:34816
v_mfma_f32_16x16x32_bf16 a[148:151], v[28:31], a[236:239], a[148:151]
ds_write_b128 v13, a[48:51] offset:36864
v_mfma_f32_16x16x32_bf16 v[184:187], v[16:19], a[160:163], v[184:187]
v_perm_b32 v24, v99, v97, s85
v_perm_b32 v25, v103, v101, s85
v_mfma_f32_16x16x32_bf16 v[184:187], v[20:23], a[200:203], v[184:187]
v_perm_b32 v26, v107, v105, s85
v_perm_b32 v27, v111, v109, s85
v_mfma_f32_16x16x32_bf16 v[200:203], v[16:19], a[164:167], v[200:203]
s_add_u32 s83, s82, 3584
v_perm_b32 v28, v115, v113, s85
v_perm_b32 v29, v119, v117, s85
v_mfma_f32_16x16x32_bf16 v[200:203], v[20:23], a[204:207], v[200:203]
v_perm_b32 v30, v123, v121, s85
v_perm_b32 v31, v127, v125, s85
v_mfma_f32_16x16x32_bf16 v[216:219], v[16:19], a[168:171], v[216:219]
ds_write_b128 v13, a[52:55] offset:38912
v_mfma_f32_16x16x32_bf16 v[216:219], v[20:23], a[208:211], v[216:219]
buffer_load_dwordx4 v[96:99], v7, s[48:51], s83 offen
v_mfma_f32_16x16x32_bf16 v[232:235], v[16:19], a[172:175], v[232:235]
ds_write_b128 v13, a[56:59] offset:40960
v_mfma_f32_16x16x32_bf16 v[232:235], v[20:23], a[212:215], v[232:235]
buffer_load_dwordx4 v[100:103], v7, s[48:51], s83 offen offset:896
v_mfma_f32_16x16x32_bf16 v[248:251], v[16:19], a[176:179], v[248:251]
buffer_load_dwordx4 v[104:107], v7, s[48:51], s83 offen offset:1792
v_mfma_f32_16x16x32_bf16 v[248:251], v[20:23], a[216:219], v[248:251]
buffer_load_dwordx4 v[108:111], v7, s[48:51], s83 offen offset:2688
v_mfma_f32_16x16x32_bf16 a[88:91], v[16:19], a[180:183], a[88:91]
s_add_u32 s82, s95, 2
s_and_b32 s82, s82, 7
s_mul_i32 s82, s82, 28672
v_mfma_f32_16x16x32_bf16 a[88:91], v[20:23], a[220:223], a[88:91]
buffer_load_dwordx4 v[112:115], v7, s[48:51], s82 offen
v_mfma_f32_16x16x32_bf16 a[104:107], v[16:19], a[184:187], a[104:107]
buffer_load_dwordx4 v[116:119], v7, s[48:51], s82 offen offset:896
v_mfma_f32_16x16x32_bf16 a[104:107], v[20:23], a[224:227], a[104:107]
buffer_load_dwordx4 v[120:123], v7, s[48:51], s82 offen offset:1792
v_mfma_f32_16x16x32_bf16 a[120:123], v[16:19], a[188:191], a[120:123]
buffer_load_dwordx4 v[124:127], v7, s[48:51], s82 offen offset:2688
v_mfma_f32_16x16x32_bf16 a[120:123], v[20:23], a[228:231], a[120:123]
v_mfma_f32_16x16x32_bf16 a[136:139], v[16:19], a[192:195], a[136:139]
v_mfma_f32_16x16x32_bf16 a[136:139], v[20:23], a[232:235], a[136:139]
v_mfma_f32_16x16x32_bf16 a[152:155], v[16:19], a[196:199], a[152:155]
v_mfma_f32_16x16x32_bf16 a[152:155], v[20:23], a[236:239], a[152:155]
v_mfma_f32_16x16x32_bf16 v[188:191], v[24:27], a[160:163], v[188:191]
v_mfma_f32_16x16x32_bf16 v[188:191], v[28:31], a[200:203], v[188:191]
v_mfma_f32_16x16x32_bf16 v[204:207], v[24:27], a[164:167], v[204:207]
v_mfma_f32_16x16x32_bf16 v[204:207], v[28:31], a[204:207], v[204:207]
v_mfma_f32_16x16x32_bf16 v[220:223], v[24:27], a[168:171], v[220:223]
v_mfma_f32_16x16x32_bf16 v[220:223], v[28:31], a[208:211], v[220:223]
v_mfma_f32_16x16x32_bf16 v[236:239], v[24:27], a[172:175], v[236:239]
v_mfma_f32_16x16x32_bf16 v[236:239], v[28:31], a[212:215], v[236:239]
v_mfma_f32_16x16x32_bf16 v[252:255], v[24:27], a[176:179], v[252:255]
v_mfma_f32_16x16x32_bf16 v[252:255], v[28:31], a[216:219], v[252:255]
v_mfma_f32_16x16x32_bf16 a[92:95], v[24:27], a[180:183], a[92:95]
v_mfma_f32_16x16x32_bf16 a[92:95], v[28:31], a[220:223], a[92:95]
v_mfma_f32_16x16x32_bf16 a[108:111], v[24:27], a[184:187], a[108:111]
v_mfma_f32_16x16x32_bf16 a[108:111], v[28:31], a[224:227], a[108:111]
v_mfma_f32_16x16x32_bf16 a[124:127], v[24:27], a[188:191], a[124:127]
v_mfma_f32_16x16x32_bf16 a[124:127], v[28:31], a[228:231], a[124:127]
v_mfma_f32_16x16x32_bf16 a[140:143], v[24:27], a[192:195], a[140:143]
v_mfma_f32_16x16x32_bf16 a[140:143], v[28:31], a[232:235], a[140:143]
v_mfma_f32_16x16x32_bf16 a[156:159], v[24:27], a[196:199], a[156:159]
v_mfma_f32_16x16x32_bf16 a[156:159], v[28:31], a[236:239], a[156:159]
s_mov_b64 vcc, s[76:77]
v_add_u32 v10, 0x27600, v10
v_accvgpr_read_b32 v9, a246
s_waitcnt lgkmcnt(0)
)"
R"(s_barrier
ds_read_b128 v[16:19], v10 offset:0
ds_read_b128 v[20:23], v10 offset:256
ds_read_b128 v[24:27], v10 offset:512
ds_read_b128 v[28:31], v10 offset:768
s_waitcnt lgkmcnt(3)
v_add_f32 v16, v16, v17
v_add_f32 v18, v18, v19
v_add_f32 v16, v16, v18
v_rcp_f32 v80, v16
ds_read_b128 v[16:19], v10 offset:1024
s_waitcnt lgkmcnt(3)
v_add_f32 v20, v20, v21
v_add_f32 v22, v22, v23
v_add_f32 v20, v20, v22
v_rcp_f32 v81, v20
ds_read_b128 v[20:23], v10 offset:1280
s_waitcnt lgkmcnt(3)
v_add_f32 v24, v24, v25
v_add_f32 v26, v26, v27
v_add_f32 v24, v24, v26
v_rcp_f32 v82, v24
ds_read_b128 v[24:27], v10 offset:1536
s_waitcnt lgkmcnt(3)
v_add_f32 v28, v28, v29
v_add_f32 v30, v30, v31
v_add_f32 v28, v28, v30
v_rcp_f32 v83, v28
ds_read_b128 v[28:31], v10 offset:1792
s_waitcnt lgkmcnt(3)
v_add_f32 v16, v16, v17
v_add_f32 v18, v18, v19
v_add_f32 v16, v16, v18
v_rcp_f32 v84, v16
ds_read_b128 v[16:19], v10 offset:2048
s_waitcnt lgkmcnt(3)
v_add_f32 v20, v20, v21
v_add_f32 v22, v22, v23
v_add_f32 v20, v20, v22
v_rcp_f32 v85, v20
ds_read_b128 v[20:23], v10 offset:2304
s_waitcnt lgkmcnt(3)
v_add_f32 v24, v24, v25
v_add_f32 v26, v26, v27
v_add_f32 v24, v24, v26
v_rcp_f32 v86, v24
s_waitcnt lgkmcnt(2)
v_add_f32 v28, v28, v29
v_add_f32 v30, v30, v31
v_add_f32 v28, v28, v30
v_rcp_f32 v87, v28
s_waitcnt lgkmcnt(1)
v_add_f32 v16, v16, v17
v_add_f32 v18, v18, v19
v_add_f32 v16, v16, v18
v_rcp_f32 v88, v16
s_waitcnt lgkmcnt(0)
v_add_f32 v20, v20, v21
v_add_f32 v22, v22, v23
v_add_f32 v20, v20, v22
v_rcp_f32 v89, v20
v_mov_b32 v24, v80
v_pk_mul_f32 v[176:177], v[176:177], v[24:25] op_sel_hi:[1,0]
v_pk_mul_f32 v[178:179], v[178:179], v[24:25] op_sel_hi:[1,0]
v_pk_mul_f32 v[180:181], v[180:181], v[24:25] op_sel_hi:[1,0]
v_pk_mul_f32 v[182:183], v[182:183], v[24:25] op_sel_hi:[1,0]
v_pk_mul_f32 v[184:185], v[184:185], v[24:25] op_sel_hi:[1,0]
v_pk_mul_f32 v[186:187], v[186:187], v[24:25] op_sel_hi:[1,0]
v_pk_mul_f32 v[188:189], v[188:189], v[24:25] op_sel_hi:[1,0]
v_pk_mul_f32 v[190:191], v[190:191], v[24:25] op_sel_hi:[1,0]
v_cvt_pk_bf16_f32 v16, v176, v180
v_cvt_pk_bf16_f32 v17, v184, v188
v_cvt_pk_bf16_f32 v18, v177, v181
v_cvt_pk_bf16_f32 v19, v185, v189
v_cvt_pk_bf16_f32 v20, v178, v182
v_cvt_pk_bf16_f32 v21, v186, v190
v_cvt_pk_bf16_f32 v22, v179, v183
v_cvt_pk_bf16_f32 v23, v187, v191
ds_write_b128 v9, v[16:19] offset:0
ds_write_b128 v9, v[20:23] offset:256
s_add_u32 s83, s82, 3584
buffer_load_dwordx4 v[176:179], v7, s[48:51], s83 offen
buffer_load_dwordx4 v[180:183], v7, s[48:51], s83 offen offset:896
buffer_load_dwordx4 v[184:187], v7, s[48:51], s83 offen offset:1792
v_mov_b32 v24, v81
buffer_load_dwordx4 v[188:191], v7, s[48:51], s83 offen offset:2688
v_pk_mul_f32 v[192:193], v[192:193], v[24:25] op_sel_hi:[1,0]
v_pk_mul_f32 v[194:195], v[194:195], v[24:25] op_sel_hi:[1,0]
v_pk_mul_f32 v[196:197], v[196:197], v[24:25] op_sel_hi:[1,0]
v_pk_mul_f32 v[198:199], v[198:199], v[24:25] op_sel_hi:[1,0]
v_pk_mul_f32 v[200:201], v[200:201], v[24:25] op_sel_hi:[1,0]
v_pk_mul_f32 v[202:203], v[202:203], v[24:25] op_sel_hi:[1,0]
v_pk_mul_f32 v[204:205], v[204:205], v[24:25] op_sel_hi:[1,0]
v_pk_mul_f32 v[206:207], v[206:207], v[24:25] op_sel_hi:[1,0]
v_cvt_pk_bf16_f32 v16, v192, v196
v_cvt_pk_bf16_f32 v17, v200, v204
v_cvt_pk_bf16_f32 v18, v193, v197
v_cvt_pk_bf16_f32 v19, v201, v205
v_cvt_pk_bf16_f32 v20, v194, v198
v_cvt_pk_bf16_f32 v21, v202, v206
v_cvt_pk_bf16_f32 v22, v195, v199
v_cvt_pk_bf16_f32 v23, v203, v207
ds_write_b128 v9, v[16:19] offset:1024
ds_write_b128 v9, v[20:23] offset:1280
s_add_u32 s82, s95, 3
s_and_b32 s82, s82, 7
s_mul_i32 s82, s82, 28672
buffer_load_dwordx4 v[192:195], v7, s[48:51], s82 offen
buffer_load_dwordx4 v[196:199], v7, s[48:51], s82 offen offset:896
buffer_load_dwordx4 v[200:203], v7, s[48:51], s82 offen offset:1792
v_mov_b32 v24, v82
buffer_load_dwordx4 v[204:207], v7, s[48:51], s82 offen offset:2688
v_pk_mul_f32 v[208:209], v[208:209], v[24:25] op_sel_hi:[1,0]
v_pk_mul_f32 v[210:211], v[210:211], v[24:25] op_sel_hi:[1,0]
v_pk_mul_f32 v[212:213], v[212:213], v[24:25] op_sel_hi:[1,0]
v_pk_mul_f32 v[214:215], v[214:215], v[24:25] op_sel_hi:[1,0]
v_pk_mul_f32 v[216:217], v[216:217], v[24:25] op_sel_hi:[1,0]
v_pk_mul_f32 v[218:219], v[218:219], v[24:25] op_sel_hi:[1,0]
v_pk_mul_f32 v[220:221], v[220:221], v[24:25] op_sel_hi:[1,0]
v_pk_mul_f32 v[222:223], v[222:223], v[24:25] op_sel_hi:[1,0]
v_cvt_pk_bf16_f32 v16, v208, v212
v_cvt_pk_bf16_f32 v17, v216, v220
v_cvt_pk_bf16_f32 v18, v209, v213
v_cvt_pk_bf16_f32 v19, v217, v221
v_cvt_pk_bf16_f32 v20, v210, v214
v_cvt_pk_bf16_f32 v21, v218, v222
v_cvt_pk_bf16_f32 v22, v211, v215
v_cvt_pk_bf16_f32 v23, v219, v223
ds_write_b128 v9, v[16:19] offset:2048
ds_write_b128 v9, v[20:23] offset:2304
s_add_u32 s83, s82, 3584
buffer_load_dwordx4 v[208:211], v7, s[48:51], s83 offen
buffer_load_dwordx4 v[212:215], v7, s[48:51], s83 offen offset:896
buffer_load_dwordx4 v[216:219], v7, s[48:51], s83 offen offset:1792
v_mov_b32 v24, v83
v_pk_mul_f32 v[224:225], v[224:225], v[24:25] op_sel_hi:[1,0]
v_pk_mul_f32 v[226:227], v[226:227], v[24:25] op_sel_hi:[1,0]
v_pk_mul_f32 v[228:229], v[228:229], v[24:25] op_sel_hi:[1,0]
v_pk_mul_f32 v[230:231], v[230:231], v[24:25] op_sel_hi:[1,0]
v_pk_mul_f32 v[232:233], v[232:233], v[24:25] op_sel_hi:[1,0]
v_pk_mul_f32 v[234:235], v[234:235], v[24:25] op_sel_hi:[1,0]
v_pk_mul_f32 v[236:237], v[236:237], v[24:25] op_sel_hi:[1,0]
v_pk_mul_f32 v[238:239], v[238:239], v[24:25] op_sel_hi:[1,0]
v_cvt_pk_bf16_f32 v16, v224, v228
v_cvt_pk_bf16_f32 v17, v232, v236
v_cvt_pk_bf16_f32 v18, v225, v229
v_cvt_pk_bf16_f32 v19, v233, v237
v_cvt_pk_bf16_f32 v20, v226, v230
v_cvt_pk_bf16_f32 v21, v234, v238
v_cvt_pk_bf16_f32 v22, v227, v231
v_cvt_pk_bf16_f32 v23, v235, v239
ds_write_b128 v9, v[16:19] offset:3072
buffer_load_dwordx4 v[220:223], v7, s[48:51], s83 offen offset:2688
ds_write_b128 v9, v[20:23] offset:3328
s_add_u32 s82, s95, 4
s_and_b32 s82, s82, 7
s_mul_i32 s82, s82, 28672
buffer_load_dwordx4 v[224:227], v7, s[48:51], s82 offen
buffer_load_dwordx4 v[228:231], v7, s[48:51], s82 offen offset:896
v_mov_b32 v24, v84
v_pk_mul_f32 v[240:241], v[240:241], v[24:25] op_sel_hi:[1,0]
v_pk_mul_f32 v[242:243], v[242:243], v[24:25] op_sel_hi:[1,0]
v_pk_mul_f32 v[244:245], v[244:245], v[24:25] op_sel_hi:[1,0]
v_pk_mul_f32 v[246:247], v[246:247], v[24:25] op_sel_hi:[1,0]
v_pk_mul_f32 v[248:249], v[248:249], v[24:25] op_sel_hi:[1,0]
v_pk_mul_f32 v[250:251], v[250:251], v[24:25] op_sel_hi:[1,0]
v_pk_mul_f32 v[252:253], v[252:253], v[24:25] op_sel_hi:[1,0]
v_pk_mul_f32 v[254:255], v[254:255], v[24:25] op_sel_hi:[1,0]
v_cvt_pk_bf16_f32 v16, v240, v244
v_cvt_pk_bf16_f32 v17, v248, v252
v_cvt_pk_bf16_f32 v18, v241, v245
v_cvt_pk_bf16_f32 v19, v249, v253
v_cvt_pk_bf16_f32 v20, v242, v246
v_cvt_pk_bf16_f32 v21, v250, v254
v_cvt_pk_bf16_f32 v22, v243, v247
v_cvt_pk_bf16_f32 v23, v251, v255
ds_write_b128 v9, v[16:19] offset:4096
buffer_load_dwordx4 v[232:235], v7, s[48:51], s82 offen offset:1792
buffer_load_dwordx4 v[236:239], v7, s[48:51], s82 offen offset:2688
ds_write_b128 v9, v[20:23] offset:4352
s_add_u32 s83, s82, 3584
buffer_load_dwordx4 v[240:243], v7, s[48:51], s83 offen
v_accvgpr_read_b32 v64, a80
v_accvgpr_read_b32 v65, a81
v_accvgpr_read_b32 v66, a82
v_accvgpr_read_b32 v67, a83
v_accvgpr_read_b32 v68, a84
v_accvgpr_read_b32 v69, a85
v_accvgpr_read_b32 v70, a86
v_accvgpr_read_b32 v71, a87
v_accvgpr_read_b32 v72, a88
v_accvgpr_read_b32 v73, a89
v_accvgpr_read_b32 v74, a90
v_accvgpr_read_b32 v75, a91
v_accvgpr_read_b32 v76, a92
v_accvgpr_read_b32 v77, a93
v_accvgpr_read_b32 v78, a94
v_accvgpr_read_b32 v79, a95
buffer_load_dwordx4 v[244:247], v7, s[48:51], s83 offen offset:896
buffer_load_dwordx4 v[248:251], v7, s[48:51], s83 offen offset:1792
)"
R"(v_mov_b32 v24, v85
v_pk_mul_f32 v[64:65], v[64:65], v[24:25] op_sel_hi:[1,0]
v_pk_mul_f32 v[66:67], v[66:67], v[24:25] op_sel_hi:[1,0]
v_pk_mul_f32 v[68:69], v[68:69], v[24:25] op_sel_hi:[1,0]
v_pk_mul_f32 v[70:71], v[70:71], v[24:25] op_sel_hi:[1,0]
v_pk_mul_f32 v[72:73], v[72:73], v[24:25] op_sel_hi:[1,0]
v_pk_mul_f32 v[74:75], v[74:75], v[24:25] op_sel_hi:[1,0]
v_pk_mul_f32 v[76:77], v[76:77], v[24:25] op_sel_hi:[1,0]
v_pk_mul_f32 v[78:79], v[78:79], v[24:25] op_sel_hi:[1,0]
v_cvt_pk_bf16_f32 v16, v64, v68
v_cvt_pk_bf16_f32 v17, v72, v76
v_cvt_pk_bf16_f32 v18, v65, v69
v_cvt_pk_bf16_f32 v19, v73, v77
v_cvt_pk_bf16_f32 v20, v66, v70
v_cvt_pk_bf16_f32 v21, v74, v78
v_cvt_pk_bf16_f32 v22, v67, v71
v_cvt_pk_bf16_f32 v23, v75, v79
ds_write_b128 v9, v[16:19] offset:40960
buffer_load_dwordx4 v[252:255], v7, s[48:51], s83 offen offset:2688
ds_write_b128 v9, v[20:23] offset:41216
v_accvgpr_read_b32 v64, a96
v_accvgpr_read_b32 v65, a97
v_accvgpr_read_b32 v66, a98
v_accvgpr_read_b32 v67, a99
v_accvgpr_read_b32 v68, a100
v_accvgpr_read_b32 v69, a101
v_accvgpr_read_b32 v70, a102
v_accvgpr_read_b32 v71, a103
v_accvgpr_read_b32 v72, a104
v_accvgpr_read_b32 v73, a105
v_accvgpr_read_b32 v74, a106
v_accvgpr_read_b32 v75, a107
v_accvgpr_read_b32 v76, a108
v_accvgpr_read_b32 v77, a109
v_accvgpr_read_b32 v78, a110
v_accvgpr_read_b32 v79, a111
v_mov_b32 v24, v86
v_pk_mul_f32 v[64:65], v[64:65], v[24:25] op_sel_hi:[1,0]
v_pk_mul_f32 v[66:67], v[66:67], v[24:25] op_sel_hi:[1,0]
v_pk_mul_f32 v[68:69], v[68:69], v[24:25] op_sel_hi:[1,0]
v_pk_mul_f32 v[70:71], v[70:71], v[24:25] op_sel_hi:[1,0]
v_pk_mul_f32 v[72:73], v[72:73], v[24:25] op_sel_hi:[1,0]
v_pk_mul_f32 v[74:75], v[74:75], v[24:25] op_sel_hi:[1,0]
v_pk_mul_f32 v[76:77], v[76:77], v[24:25] op_sel_hi:[1,0]
v_pk_mul_f32 v[78:79], v[78:79], v[24:25] op_sel_hi:[1,0]
v_cvt_pk_bf16_f32 v16, v64, v68
v_cvt_pk_bf16_f32 v17, v72, v76
v_cvt_pk_bf16_f32 v18, v65, v69
v_cvt_pk_bf16_f32 v19, v73, v77
v_cvt_pk_bf16_f32 v20, v66, v70
v_cvt_pk_bf16_f32 v21, v74, v78
v_cvt_pk_bf16_f32 v22, v67, v71
v_cvt_pk_bf16_f32 v23, v75, v79
ds_write_b128 v9, v[16:19] offset:41984
ds_write_b128 v9, v[20:23] offset:42240
v_accvgpr_read_b32 v64, a112
v_accvgpr_read_b32 v65, a113
v_accvgpr_read_b32 v66, a114
v_accvgpr_read_b32 v67, a115
v_accvgpr_read_b32 v68, a116
v_accvgpr_read_b32 v69, a117
v_accvgpr_read_b32 v70, a118
v_accvgpr_read_b32 v71, a119
v_accvgpr_read_b32 v72, a120
v_accvgpr_read_b32 v73, a121
v_accvgpr_read_b32 v74, a122
v_accvgpr_read_b32 v75, a123
v_accvgpr_read_b32 v76, a124
v_accvgpr_read_b32 v77, a125
v_accvgpr_read_b32 v78, a126
v_accvgpr_read_b32 v79, a127
v_mov_b32 v24, v87
v_pk_mul_f32 v[64:65], v[64:65], v[24:25] op_sel_hi:[1,0]
v_pk_mul_f32 v[66:67], v[66:67], v[24:25] op_sel_hi:[1,0]
v_pk_mul_f32 v[68:69], v[68:69], v[24:25] op_sel_hi:[1,0]
v_pk_mul_f32 v[70:71], v[70:71], v[24:25] op_sel_hi:[1,0]
v_pk_mul_f32 v[72:73], v[72:73], v[24:25] op_sel_hi:[1,0]
v_pk_mul_f32 v[74:75], v[74:75], v[24:25] op_sel_hi:[1,0]
v_pk_mul_f32 v[76:77], v[76:77], v[24:25] op_sel_hi:[1,0]
v_pk_mul_f32 v[78:79], v[78:79], v[24:25] op_sel_hi:[1,0]
v_cvt_pk_bf16_f32 v16, v64, v68
v_cvt_pk_bf16_f32 v17, v72, v76
v_cvt_pk_bf16_f32 v18, v65, v69
v_cvt_pk_bf16_f32 v19, v73, v77
v_cvt_pk_bf16_f32 v20, v66, v70
v_cvt_pk_bf16_f32 v21, v74, v78
v_cvt_pk_bf16_f32 v22, v67, v71
v_cvt_pk_bf16_f32 v23, v75, v79
s_waitcnt lgkmcnt(10)
ds_write_b128 v9, v[16:19] offset:43008
ds_write_b128 v9, v[20:23] offset:43264
v_accvgpr_read_b32 v64, a128
v_accvgpr_read_b32 v65, a129
v_accvgpr_read_b32 v66, a130
v_accvgpr_read_b32 v67, a131
v_accvgpr_read_b32 v68, a132
v_accvgpr_read_b32 v69, a133
v_accvgpr_read_b32 v70, a134
v_accvgpr_read_b32 v71, a135
v_accvgpr_read_b32 v72, a136
v_accvgpr_read_b32 v73, a137
v_accvgpr_read_b32 v74, a138
v_accvgpr_read_b32 v75, a139
v_accvgpr_read_b32 v76, a140
v_accvgpr_read_b32 v77, a141
v_accvgpr_read_b32 v78, a142
v_accvgpr_read_b32 v79, a143
v_mov_b32 v24, v88
v_pk_mul_f32 v[64:65], v[64:65], v[24:25] op_sel_hi:[1,0]
v_pk_mul_f32 v[66:67], v[66:67], v[24:25] op_sel_hi:[1,0]
v_pk_mul_f32 v[68:69], v[68:69], v[24:25] op_sel_hi:[1,0]
v_pk_mul_f32 v[70:71], v[70:71], v[24:25] op_sel_hi:[1,0]
v_pk_mul_f32 v[72:73], v[72:73], v[24:25] op_sel_hi:[1,0]
v_pk_mul_f32 v[74:75], v[74:75], v[24:25] op_sel_hi:[1,0]
v_pk_mul_f32 v[76:77], v[76:77], v[24:25] op_sel_hi:[1,0]
v_pk_mul_f32 v[78:79], v[78:79], v[24:25] op_sel_hi:[1,0]
v_cvt_pk_bf16_f32 v16, v64, v68
v_cvt_pk_bf16_f32 v17, v72, v76
v_cvt_pk_bf16_f32 v18, v65, v69
v_cvt_pk_bf16_f32 v19, v73, v77
v_cvt_pk_bf16_f32 v20, v66, v70
v_cvt_pk_bf16_f32 v21, v74, v78
v_cvt_pk_bf16_f32 v22, v67, v71
v_cvt_pk_bf16_f32 v23, v75, v79
ds_write_b128 v9, v[16:19] offset:44032
ds_write_b128 v9, v[20:23] offset:44288
v_accvgpr_read_b32 v64, a144
v_accvgpr_read_b32 v65, a145
v_accvgpr_read_b32 v66, a146
v_accvgpr_read_b32 v67, a147
v_accvgpr_read_b32 v68, a148
v_accvgpr_read_b32 v69, a149
v_accvgpr_read_b32 v70, a150
v_accvgpr_read_b32 v71, a151
v_accvgpr_read_b32 v72, a152
v_accvgpr_read_b32 v73, a153
v_accvgpr_read_b32 v74, a154
v_accvgpr_read_b32 v75, a155
v_accvgpr_read_b32 v76, a156
v_accvgpr_read_b32 v77, a157
v_accvgpr_read_b32 v78, a158
v_accvgpr_read_b32 v79, a159
v_mov_b32 v24, v89
v_pk_mul_f32 v[64:65], v[64:65], v[24:25] op_sel_hi:[1,0]
v_pk_mul_f32 v[66:67], v[66:67], v[24:25] op_sel_hi:[1,0]
v_pk_mul_f32 v[68:69], v[68:69], v[24:25] op_sel_hi:[1,0]
v_pk_mul_f32 v[70:71], v[70:71], v[24:25] op_sel_hi:[1,0]
v_pk_mul_f32 v[72:73], v[72:73], v[24:25] op_sel_hi:[1,0]
v_pk_mul_f32 v[74:75], v[74:75], v[24:25] op_sel_hi:[1,0]
v_pk_mul_f32 v[76:77], v[76:77], v[24:25] op_sel_hi:[1,0]
v_pk_mul_f32 v[78:79], v[78:79], v[24:25] op_sel_hi:[1,0]
v_cvt_pk_bf16_f32 v16, v64, v68
v_cvt_pk_bf16_f32 v17, v72, v76
v_cvt_pk_bf16_f32 v18, v65, v69
v_cvt_pk_bf16_f32 v19, v73, v77
v_cvt_pk_bf16_f32 v20, v66, v70
v_cvt_pk_bf16_f32 v21, v74, v78
v_cvt_pk_bf16_f32 v22, v67, v71
v_cvt_pk_bf16_f32 v23, v75, v79
s_waitcnt lgkmcnt(10)
ds_write_b128 v9, v[16:19] offset:45056
ds_write_b128 v9, v[20:23] offset:45312
s_add_u32 s82, s95, 5
s_and_b32 s82, s82, 7
s_mul_i32 s82, s82, 28672
buffer_load_dwordx4 v[64:67], v7, s[48:51], s82 offen
buffer_load_dwordx4 v[68:71], v7, s[48:51], s82 offen offset:896
buffer_load_dwordx4 v[72:75], v7, s[48:51], s82 offen offset:1792
s_waitcnt vmcnt(35)
v_perm_b32 v16, v132, v128, s84
buffer_load_dwordx4 v[76:79], v7, s[48:51], s82 offen offset:2688
s_add_u32 s83, s82, 3584
buffer_load_dwordx4 v[80:83], v7, s[48:51], s83 offen
buffer_load_dwordx4 v[84:87], v7, s[48:51], s83 offen offset:896
v_perm_b32 v17, v140, v136, s84
buffer_load_dwordx4 v[88:91], v7, s[48:51], s83 offen offset:1792
buffer_load_dwordx4 v[92:95], v7, s[48:51], s83 offen offset:2688
v_perm_b32 v18, v148, v144, s84
v_perm_b32 v19, v156, v152, s84
s_waitcnt vmcnt(32)
v_perm_b32 v20, v164, v160, s84
v_perm_b32 v21, v172, v168, s84
v_perm_b32 v22, v100, v96, s84
v_perm_b32 v23, v108, v104, s84
s_waitcnt lgkmcnt(0)
s_barrier
s_cmp_eq_u32 s90, 0
s_cbranch_scc1 LpkA_%=
s_and_b32 s91, s61, 0xfc00
v_and_b32 v9, 0x70, v1
v_lshlrev_b32 v9, 3, v9
v_add_u32 v9, s91, v9
buffer_load_dwordx2 v[14:15], v9, s[56:59], 0 offen sc1
LpkA_%=:
ds_read_b128 a[160:163], v1 offset:0
ds_read_b128 a[164:167], v1 offset:1024
ds_read_b128 a[168:171], v1 offset:2048
ds_read_b128 a[172:175], v1 offset:3072
ds_read_b128 a[176:179], v1 offset:4096
ds_read_b128 a[180:183], v1 offset:5120
)"
R"(ds_read_b128 a[184:187], v1 offset:6144
ds_read_b128 a[188:191], v1 offset:7168
ds_read_b128 a[192:195], v1 offset:8192
ds_read_b128 a[196:199], v1 offset:9216
s_waitcnt lgkmcnt(5)
v_mfma_f32_16x16x32_bf16 a[0:3], v[16:19], a[160:163], 0
s_waitcnt lgkmcnt(0)
v_perm_b32 v24, v132, v128, s85
v_perm_b32 v25, v140, v136, s85
v_mfma_f32_16x16x32_bf16 a[0:3], v[20:23], a[180:183], a[0:3]
v_perm_b32 v26, v148, v144, s85
v_perm_b32 v27, v156, v152, s85
v_mfma_f32_16x16x32_bf16 a[4:7], v[16:19], a[164:167], 0
v_perm_b32 v28, v164, v160, s85
v_perm_b32 v29, v172, v168, s85
v_mfma_f32_16x16x32_bf16 a[4:7], v[20:23], a[184:187], a[4:7]
v_perm_b32 v30, v100, v96, s85
v_perm_b32 v31, v108, v104, s85
v_mfma_f32_16x16x32_bf16 a[8:11], v[16:19], a[168:171], 0
v_accvgpr_read_b32 v10, a242
v_add_u32 v11, 0xe000, v10
v_mfma_f32_16x16x32_bf16 a[8:11], v[20:23], a[188:191], a[8:11]
v_mfma_f32_16x16x32_bf16 a[12:15], v[16:19], a[172:175], 0
v_mfma_f32_16x16x32_bf16 a[12:15], v[20:23], a[192:195], a[12:15]
ds_write_b128 v11, v[32:35] offset:0
v_mfma_f32_16x16x32_bf16 a[16:19], v[16:19], a[176:179], 0
ds_read_b128 a[200:203], v1 offset:10240
v_mfma_f32_16x16x32_bf16 a[16:19], v[20:23], a[196:199], a[16:19]
ds_read_b128 a[220:223], v1 offset:15360
v_mfma_f32_16x16x32_bf16 a[20:23], v[24:27], a[160:163], 0
v_perm_b32 v16, v133, v129, s84
v_perm_b32 v17, v141, v137, s84
v_mfma_f32_16x16x32_bf16 a[20:23], v[28:31], a[180:183], a[20:23]
v_perm_b32 v18, v149, v145, s84
v_perm_b32 v19, v157, v153, s84
v_mfma_f32_16x16x32_bf16 a[24:27], v[24:27], a[164:167], 0
v_perm_b32 v20, v165, v161, s84
v_perm_b32 v21, v173, v169, s84
v_mfma_f32_16x16x32_bf16 a[24:27], v[28:31], a[184:187], a[24:27]
v_perm_b32 v22, v101, v97, s84
v_perm_b32 v23, v109, v105, s84
v_mfma_f32_16x16x32_bf16 a[28:31], v[24:27], a[168:171], 0
ds_write_b128 v11, v[36:39] offset:2048
v_mfma_f32_16x16x32_bf16 a[28:31], v[28:31], a[188:191], a[28:31]
v_mfma_f32_16x16x32_bf16 a[32:35], v[24:27], a[172:175], 0
v_mfma_f32_16x16x32_bf16 a[32:35], v[28:31], a[192:195], a[32:35]
ds_write_b128 v11, v[40:43] offset:4096
v_mfma_f32_16x16x32_bf16 a[36:39], v[24:27], a[176:179], 0
ds_write_b128 v11, v[44:47] offset:6144
v_mfma_f32_16x16x32_bf16 a[36:39], v[28:31], a[196:199], a[36:39]
s_add_u32 s82, s95, 6
s_and_b32 s82, s82, 7
s_mul_i32 s82, s82, 28672
ds_write_b128 v11, v[48:51] offset:8192
v_mfma_f32_16x16x32_bf16 a[40:43], v[16:19], a[160:163], 0
v_perm_b32 v24, v133, v129, s85
v_perm_b32 v25, v141, v137, s85
v_mfma_f32_16x16x32_bf16 a[40:43], v[20:23], a[180:183], a[40:43]
v_perm_b32 v26, v149, v145, s85
v_perm_b32 v27, v157, v153, s85
v_mfma_f32_16x16x32_bf16 a[44:47], v[16:19], a[164:167], 0
v_perm_b32 v28, v165, v161, s85
v_perm_b32 v29, v173, v169, s85
v_mfma_f32_16x16x32_bf16 a[44:47], v[20:23], a[184:187], a[44:47]
v_perm_b32 v30, v101, v97, s85
v_perm_b32 v31, v109, v105, s85
v_mfma_f32_16x16x32_bf16 a[48:51], v[16:19], a[168:171], 0
s_waitcnt lgkmcnt(6)
buffer_load_dwordx4 v[32:35], v7, s[48:51], s82 offen
ds_read_b128 a[204:207], v1 offset:11264
v_mfma_f32_16x16x32_bf16 a[48:51], v[20:23], a[188:191], a[48:51]
s_waitcnt lgkmcnt(4)
buffer_load_dwordx4 v[36:39], v7, s[48:51], s82 offen offset:896
ds_read_b128 a[224:227], v1 offset:16384
v_mfma_f32_16x16x32_bf16 a[52:55], v[16:19], a[172:175], 0
s_waitcnt lgkmcnt(4)
buffer_load_dwordx4 v[40:43], v7, s[48:51], s82 offen offset:1792
v_mfma_f32_16x16x32_bf16 a[52:55], v[20:23], a[192:195], a[52:55]
ds_write_b128 v11, v[52:55] offset:10240
v_mfma_f32_16x16x32_bf16 a[56:59], v[16:19], a[176:179], 0
ds_write_b128 v11, v[56:59] offset:12288
v_mfma_f32_16x16x32_bf16 a[56:59], v[20:23], a[196:199], a[56:59]
s_waitcnt lgkmcnt(5)
buffer_load_dwordx4 v[44:47], v7, s[48:51], s82 offen offset:2688
v_mfma_f32_16x16x32_bf16 a[60:63], v[24:27], a[160:163], 0
s_add_u32 s83, s82, 3584
v_perm_b32 v16, v134, v130, s84
v_perm_b32 v17, v142, v138, s84
v_mfma_f32_16x16x32_bf16 a[60:63], v[28:31], a[180:183], a[60:63]
v_perm_b32 v18, v150, v146, s84
v_perm_b32 v19, v158, v154, s84
v_mfma_f32_16x16x32_bf16 a[64:67], v[24:27], a[164:167], 0
v_perm_b32 v20, v166, v162, s84
v_perm_b32 v21, v174, v170, s84
v_mfma_f32_16x16x32_bf16 a[64:67], v[28:31], a[184:187], a[64:67]
v_perm_b32 v22, v102, v98, s84
v_perm_b32 v23, v110, v106, s84
v_mfma_f32_16x16x32_bf16 a[68:71], v[24:27], a[168:171], 0
s_waitcnt lgkmcnt(4)
buffer_load_dwordx4 v[48:51], v7, s[48:51], s83 offen
v_mfma_f32_16x16x32_bf16 a[68:71], v[28:31], a[188:191], a[68:71]
s_waitcnt lgkmcnt(1)
buffer_load_dwordx4 v[52:55], v7, s[48:51], s83 offen offset:896
v_mfma_f32_16x16x32_bf16 a[72:75], v[24:27], a[172:175], 0
s_waitcnt lgkmcnt(0)
buffer_load_dwordx4 v[56:59], v7, s[48:51], s83 offen offset:1792
ds_read_b128 a[208:211], v1 offset:12288
v_mfma_f32_16x16x32_bf16 a[72:75], v[28:31], a[192:195], a[72:75]
ds_read_b128 a[228:231], v1 offset:17408
v_mfma_f32_16x16x32_bf16 a[76:79], v[24:27], a[176:179], 0
buffer_load_dwordx4 v[60:63], v7, s[48:51], s83 offen offset:2688
v_mfma_f32_16x16x32_bf16 a[76:79], v[28:31], a[196:199], a[76:79]
v_mfma_f32_16x16x32_bf16 a[80:83], v[16:19], a[160:163], 0
v_perm_b32 v24, v134, v130, s85
v_perm_b32 v25, v142, v138, s85
v_mfma_f32_16x16x32_bf16 a[80:83], v[20:23], a[180:183], a[80:83]
v_perm_b32 v26, v150, v146, s85
v_perm_b32 v27, v158, v154, s85
v_mfma_f32_16x16x32_bf16 a[84:87], v[16:19], a[164:167], 0
v_perm_b32 v28, v166, v162, s85
v_perm_b32 v29, v174, v170, s85
v_mfma_f32_16x16x32_bf16 a[84:87], v[20:23], a[184:187], a[84:87]
v_perm_b32 v30, v102, v98, s85
v_perm_b32 v31, v110, v106, s85
v_mfma_f32_16x16x32_bf16 a[88:91], v[16:19], a[168:171], 0
v_mfma_f32_16x16x32_bf16 a[88:91], v[20:23], a[188:191], a[88:91]
v_mfma_f32_16x16x32_bf16 a[92:95], v[16:19], a[172:175], 0
v_mfma_f32_16x16x32_bf16 a[92:95], v[20:23], a[192:195], a[92:95]
v_mfma_f32_16x16x32_bf16 a[96:99], v[16:19], a[176:179], 0
v_mfma_f32_16x16x32_bf16 a[96:99], v[20:23], a[196:199], a[96:99]
v_mfma_f32_16x16x32_bf16 a[100:103], v[24:27], a[160:163], 0
v_perm_b32 v16, v135, v131, s84
v_perm_b32 v17, v143, v139, s84
v_mfma_f32_16x16x32_bf16 a[100:103], v[28:31], a[180:183], a[100:103]
v_perm_b32 v18, v151, v147, s84
v_perm_b32 v19, v159, v155, s84
v_mfma_f32_16x16x32_bf16 a[104:107], v[24:27], a[164:167], 0
ds_read_b128 a[212:215], v1 offset:13312
v_perm_b32 v20, v167, v163, s84
v_mfma_f32_16x16x32_bf16 a[104:107], v[28:31], a[184:187], a[104:107]
ds_read_b128 a[232:235], v1 offset:18432
v_perm_b32 v21, v175, v171, s84
v_mfma_f32_16x16x32_bf16 a[108:111], v[24:27], a[168:171], 0
v_perm_b32 v22, v103, v99, s84
v_perm_b32 v23, v111, v107, s84
v_mfma_f32_16x16x32_bf16 a[108:111], v[28:31], a[188:191], a[108:111]
v_mfma_f32_16x16x32_bf16 a[112:115], v[24:27], a[172:175], 0
v_mfma_f32_16x16x32_bf16 a[112:115], v[28:31], a[192:195], a[112:115]
v_mfma_f32_16x16x32_bf16 a[116:119], v[24:27], a[176:179], 0
v_mfma_f32_16x16x32_bf16 a[116:119], v[28:31], a[196:199], a[116:119]
v_mfma_f32_16x16x32_bf16 a[120:123], v[16:19], a[160:163], 0
v_perm_b32 v24, v135, v131, s85
v_perm_b32 v25, v143, v139, s85
v_mfma_f32_16x16x32_bf16 a[120:123], v[20:23], a[180:183], a[120:123]
s_add_u32 s82, s95, 7
s_and_b32 s82, s82, 7
s_mul_i32 s82, s82, 28672
v_perm_b32 v26, v151, v147, s85
v_perm_b32 v27, v159, v155, s85
v_mfma_f32_16x16x32_bf16 a[124:127], v[16:19], a[164:167], 0
v_perm_b32 v28, v167, v163, s85
v_perm_b32 v29, v175, v171, s85
v_mfma_f32_16x16x32_bf16 a[124:127], v[20:23], a[184:187], a[124:127]
v_perm_b32 v30, v103, v99, s85
v_perm_b32 v31, v111, v107, s85
v_mfma_f32_16x16x32_bf16 a[128:131], v[16:19], a[168:171], 0
buffer_load_dwordx4 v[128:131], v7, s[48:51], s82 offen
ds_read_b128 a[216:219], v1 offset:14336
v_mfma_f32_16x16x32_bf16 a[128:131], v[20:23], a[188:191], a[128:131]
buffer_load_dwordx4 v[132:135], v7, s[48:51], s82 offen offset:896
ds_read_b128 a[236:239], v1 offset:19456
v_mfma_f32_16x16x32_bf16 a[132:135], v[16:19], a[172:175], 0
buffer_load_dwordx4 v[136:139], v7, s[48:51], s82 offen offset:1792
v_mfma_f32_16x16x32_bf16 a[132:135], v[20:23], a[192:195], a[132:135]
buffer_load_dwordx4 v[140:143], v7, s[48:51], s82 offen offset:2688
v_mfma_f32_16x16x32_bf16 a[136:139], v[16:19], a[176:179], 0
s_add_u32 s83, s82, 3584
v_mfma_f32_16x16x32_bf16 a[136:139], v[20:23], a[196:199], a[136:139]
buffer_load_dwordx4 v[96:99], v7, s[48:51], s83 offen
v_mfma_f32_16x16x32_bf16 a[140:143], v[24:27], a[160:163], 0
s_waitcnt vmcnt(37)
v_perm_b32 v16, v116, v112, s84
v_perm_b32 v17, v124, v120, s84
v_mfma_f32_16x16x32_bf16 a[140:143], v[28:31], a[180:183], a[140:143]
v_perm_b32 v18, v180, v176, s84
v_perm_b32 v19, v188, v184, s84
v_mfma_f32_16x16x32_bf16 a[144:147], v[24:27], a[164:167], 0
s_waitcnt vmcnt(29)
v_perm_b32 v20, v196, v192, s84
v_perm_b32 v21, v204, v200, s84
v_mfma_f32_16x16x32_bf16 a[144:147], v[28:31], a[184:187], a[144:147]
v_perm_b32 v22, v212, v208, s84
v_perm_b32 v23, v220, v216, s84
v_mfma_f32_16x16x32_bf16 a[148:151], v[24:27], a[168:171], 0
buffer_load_dwordx4 v[100:103], v7, s[48:51], s83 offen offset:896
v_mfma_f32_16x16x32_bf16 a[148:151], v[28:31], a[188:191], a[148:151]
buffer_load_dwordx4 v[104:107], v7, s[48:51], s83 offen offset:1792
v_mfma_f32_16x16x32_bf16 a[152:155], v[24:27], a[172:175], 0
buffer_load_dwordx4 v[108:111], v7, s[48:51], s83 offen offset:2688
v_mfma_f32_16x16x32_bf16 a[152:155], v[28:31], a[192:195], a[152:155]
)"
R"(s_add_u32 s82, s95, 0
s_and_b32 s82, s82, 7
s_mul_i32 s82, s82, 28672
s_add_u32 s82, s82, 229376
v_mfma_f32_16x16x32_bf16 a[156:159], v[24:27], a[176:179], 0
buffer_load_dwordx4 v[144:147], v7, s[48:51], s82 offen
v_mfma_f32_16x16x32_bf16 a[156:159], v[28:31], a[196:199], a[156:159]
buffer_load_dwordx4 v[148:151], v7, s[48:51], s82 offen offset:896
v_mfma_f32_16x16x32_bf16 a[0:3], v[16:19], a[200:203], a[0:3]
v_perm_b32 v24, v116, v112, s85
v_perm_b32 v25, v124, v120, s85
v_mfma_f32_16x16x32_bf16 a[0:3], v[20:23], a[220:223], a[0:3]
v_perm_b32 v26, v180, v176, s85
v_perm_b32 v27, v188, v184, s85
v_mfma_f32_16x16x32_bf16 a[4:7], v[16:19], a[204:207], a[4:7]
v_perm_b32 v28, v196, v192, s85
v_perm_b32 v29, v204, v200, s85
v_mfma_f32_16x16x32_bf16 a[4:7], v[20:23], a[224:227], a[4:7]
s_waitcnt lgkmcnt(1)
v_perm_b32 v30, v212, v208, s85
v_perm_b32 v31, v220, v216, s85
v_mfma_f32_16x16x32_bf16 a[8:11], v[16:19], a[208:211], a[8:11]
buffer_load_dwordx4 v[152:155], v7, s[48:51], s82 offen offset:1792
v_mfma_f32_16x16x32_bf16 a[8:11], v[20:23], a[228:231], a[8:11]
buffer_load_dwordx4 v[156:159], v7, s[48:51], s82 offen offset:2688
v_mfma_f32_16x16x32_bf16 a[12:15], v[16:19], a[212:215], a[12:15]
s_add_u32 s83, s82, 3584
ds_read_b128 a[160:163], v1 offset:20480
v_mfma_f32_16x16x32_bf16 a[12:15], v[20:23], a[232:235], a[12:15]
ds_read_b128 a[180:183], v1 offset:25600
v_mfma_f32_16x16x32_bf16 a[16:19], v[16:19], a[216:219], a[16:19]
s_waitcnt lgkmcnt(2)
v_mfma_f32_16x16x32_bf16 a[16:19], v[20:23], a[236:239], a[16:19]
v_mfma_f32_16x16x32_bf16 a[20:23], v[24:27], a[200:203], a[20:23]
v_perm_b32 v16, v117, v113, s84
v_perm_b32 v17, v125, v121, s84
v_mfma_f32_16x16x32_bf16 a[20:23], v[28:31], a[220:223], a[20:23]
v_perm_b32 v18, v181, v177, s84
v_perm_b32 v19, v189, v185, s84
v_mfma_f32_16x16x32_bf16 a[24:27], v[24:27], a[204:207], a[24:27]
v_perm_b32 v20, v197, v193, s84
v_perm_b32 v21, v205, v201, s84
v_mfma_f32_16x16x32_bf16 a[24:27], v[28:31], a[224:227], a[24:27]
v_perm_b32 v22, v213, v209, s84
v_perm_b32 v23, v221, v217, s84
v_mfma_f32_16x16x32_bf16 a[28:31], v[24:27], a[208:211], a[28:31]
buffer_load_dwordx4 v[160:163], v7, s[48:51], s83 offen
v_mfma_f32_16x16x32_bf16 a[28:31], v[28:31], a[228:231], a[28:31]
v_mfma_f32_16x16x32_bf16 a[32:35], v[24:27], a[212:215], a[32:35]
buffer_load_dwordx4 v[164:167], v7, s[48:51], s83 offen offset:896
v_mfma_f32_16x16x32_bf16 a[32:35], v[28:31], a[232:235], a[32:35]
v_mfma_f32_16x16x32_bf16 a[36:39], v[24:27], a[216:219], a[36:39]
v_mfma_f32_16x16x32_bf16 a[36:39], v[28:31], a[236:239], a[36:39]
buffer_load_dwordx4 v[168:171], v7, s[48:51], s83 offen offset:1792
v_mfma_f32_16x16x32_bf16 a[40:43], v[16:19], a[200:203], a[40:43]
v_perm_b32 v24, v117, v113, s85
v_perm_b32 v25, v125, v121, s85
v_mfma_f32_16x16x32_bf16 a[40:43], v[20:23], a[220:223], a[40:43]
v_perm_b32 v26, v181, v177, s85
v_perm_b32 v27, v189, v185, s85
v_mfma_f32_16x16x32_bf16 a[44:47], v[16:19], a[204:207], a[44:47]
ds_read_b128 a[164:167], v1 offset:21504
v_perm_b32 v28, v197, v193, s85
v_mfma_f32_16x16x32_bf16 a[44:47], v[20:23], a[224:227], a[44:47]
ds_read_b128 a[184:187], v1 offset:26624
v_perm_b32 v29, v205, v201, s85
v_mfma_f32_16x16x32_bf16 a[48:51], v[16:19], a[208:211], a[48:51]
v_perm_b32 v30, v213, v209, s85
v_perm_b32 v31, v221, v217, s85
v_mfma_f32_16x16x32_bf16 a[48:51], v[20:23], a[228:231], a[48:51]
buffer_load_dwordx4 v[172:175], v7, s[48:51], s83 offen offset:2688
v_mfma_f32_16x16x32_bf16 a[52:55], v[16:19], a[212:215], a[52:55]
v_mfma_f32_16x16x32_bf16 a[52:55], v[20:23], a[232:235], a[52:55]
v_mfma_f32_16x16x32_bf16 a[56:59], v[16:19], a[216:219], a[56:59]
v_mfma_f32_16x16x32_bf16 a[56:59], v[20:23], a[236:239], a[56:59]
v_mfma_f32_16x16x32_bf16 a[60:63], v[24:27], a[200:203], a[60:63]
v_perm_b32 v16, v118, v114, s84
v_perm_b32 v17, v126, v122, s84
v_mfma_f32_16x16x32_bf16 a[60:63], v[28:31], a[220:223], a[60:63]
v_perm_b32 v18, v182, v178, s84
v_perm_b32 v19, v190, v186, s84
v_mfma_f32_16x16x32_bf16 a[64:67], v[24:27], a[204:207], a[64:67]
v_perm_b32 v20, v198, v194, s84
v_perm_b32 v21, v206, v202, s84
v_mfma_f32_16x16x32_bf16 a[64:67], v[28:31], a[224:227], a[64:67]
v_perm_b32 v22, v214, v210, s84
v_perm_b32 v23, v222, v218, s84
v_mfma_f32_16x16x32_bf16 a[68:71], v[24:27], a[208:211], a[68:71]
v_mfma_f32_16x16x32_bf16 a[68:71], v[28:31], a[228:231], a[68:71]
ds_read_b128 a[168:171], v1 offset:22528
v_mfma_f32_16x16x32_bf16 a[72:75], v[24:27], a[212:215], a[72:75]
ds_read_b128 a[188:191], v1 offset:27648
v_mfma_f32_16x16x32_bf16 a[72:75], v[28:31], a[232:235], a[72:75]
v_mfma_f32_16x16x32_bf16 a[76:79], v[24:27], a[216:219], a[76:79]
v_mfma_f32_16x16x32_bf16 a[76:79], v[28:31], a[236:239], a[76:79]
v_mfma_f32_16x16x32_bf16 a[80:83], v[16:19], a[200:203], a[80:83]
v_perm_b32 v24, v118, v114, s85
v_perm_b32 v25, v126, v122, s85
v_mfma_f32_16x16x32_bf16 a[80:83], v[20:23], a[220:223], a[80:83]
v_perm_b32 v26, v182, v178, s85
v_perm_b32 v27, v190, v186, s85
v_mfma_f32_16x16x32_bf16 a[84:87], v[16:19], a[204:207], a[84:87]
v_perm_b32 v28, v198, v194, s85
v_perm_b32 v29, v206, v202, s85
v_mfma_f32_16x16x32_bf16 a[84:87], v[20:23], a[224:227], a[84:87]
v_perm_b32 v30, v214, v210, s85
v_perm_b32 v31, v222, v218, s85
v_mfma_f32_16x16x32_bf16 a[88:91], v[16:19], a[208:211], a[88:91]
v_mfma_f32_16x16x32_bf16 a[88:91], v[20:23], a[228:231], a[88:91]
v_mfma_f32_16x16x32_bf16 a[92:95], v[16:19], a[212:215], a[92:95]
v_mfma_f32_16x16x32_bf16 a[92:95], v[20:23], a[232:235], a[92:95]
v_mfma_f32_16x16x32_bf16 a[96:99], v[16:19], a[216:219], a[96:99]
v_mfma_f32_16x16x32_bf16 a[96:99], v[20:23], a[236:239], a[96:99]
v_mfma_f32_16x16x32_bf16 a[100:103], v[24:27], a[200:203], a[100:103]
v_perm_b32 v16, v119, v115, s84
v_perm_b32 v17, v127, v123, s84
v_mfma_f32_16x16x32_bf16 a[100:103], v[28:31], a[220:223], a[100:103]
v_perm_b32 v18, v183, v179, s84
v_perm_b32 v19, v191, v187, s84
v_mfma_f32_16x16x32_bf16 a[104:107], v[24:27], a[204:207], a[104:107]
ds_read_b128 a[172:175], v1 offset:23552
v_perm_b32 v20, v199, v195, s84
v_mfma_f32_16x16x32_bf16 a[104:107], v[28:31], a[224:227], a[104:107]
ds_read_b128 a[192:195], v1 offset:28672
v_perm_b32 v21, v207, v203, s84
v_mfma_f32_16x16x32_bf16 a[108:111], v[24:27], a[208:211], a[108:111]
v_perm_b32 v22, v215, v211, s84
v_perm_b32 v23, v223, v219, s84
v_mfma_f32_16x16x32_bf16 a[108:111], v[28:31], a[228:231], a[108:111]
v_mfma_f32_16x16x32_bf16 a[112:115], v[24:27], a[212:215], a[112:115]
v_mfma_f32_16x16x32_bf16 a[112:115], v[28:31], a[232:235], a[112:115]
v_mfma_f32_16x16x32_bf16 a[116:119], v[24:27], a[216:219], a[116:119]
v_mfma_f32_16x16x32_bf16 a[116:119], v[28:31], a[236:239], a[116:119]
v_mfma_f32_16x16x32_bf16 a[120:123], v[16:19], a[200:203], a[120:123]
v_perm_b32 v24, v119, v115, s85
v_perm_b32 v25, v127, v123, s85
v_mfma_f32_16x16x32_bf16 a[120:123], v[20:23], a[220:223], a[120:123]
s_add_u32 s82, s95, 1
s_and_b32 s82, s82, 7
s_mul_i32 s82, s82, 28672
s_add_u32 s82, s82, 229376
v_perm_b32 v26, v183, v179, s85
v_perm_b32 v27, v191, v187, s85
v_mfma_f32_16x16x32_bf16 a[124:127], v[16:19], a[204:207], a[124:127]
v_perm_b32 v28, v199, v195, s85
v_perm_b32 v29, v207, v203, s85
v_mfma_f32_16x16x32_bf16 a[124:127], v[20:23], a[224:227], a[124:127]
v_perm_b32 v30, v215, v211, s85
v_perm_b32 v31, v223, v219, s85
v_mfma_f32_16x16x32_bf16 a[128:131], v[16:19], a[208:211], a[128:131]
buffer_load_dwordx4 v[112:115], v7, s[48:51], s82 offen
ds_read_b128 a[176:179], v1 offset:24576
v_mfma_f32_16x16x32_bf16 a[128:131], v[20:23], a[228:231], a[128:131]
buffer_load_dwordx4 v[116:119], v7, s[48:51], s82 offen offset:896
ds_read_b128 a[196:199], v1 offset:29696
v_mfma_f32_16x16x32_bf16 a[132:135], v[16:19], a[212:215], a[132:135]
buffer_load_dwordx4 v[120:123], v7, s[48:51], s82 offen offset:1792
v_mfma_f32_16x16x32_bf16 a[132:135], v[20:23], a[232:235], a[132:135]
buffer_load_dwordx4 v[124:127], v7, s[48:51], s82 offen offset:2688
v_mfma_f32_16x16x32_bf16 a[136:139], v[16:19], a[216:219], a[136:139]
s_add_u32 s83, s82, 3584
v_mfma_f32_16x16x32_bf16 a[136:139], v[20:23], a[236:239], a[136:139]
buffer_load_dwordx4 v[176:179], v7, s[48:51], s83 offen
v_mfma_f32_16x16x32_bf16 a[140:143], v[24:27], a[200:203], a[140:143]
s_waitcnt vmcnt(37)
v_perm_b32 v16, v228, v224, s84
v_perm_b32 v17, v236, v232, s84
v_mfma_f32_16x16x32_bf16 a[140:143], v[28:31], a[220:223], a[140:143]
v_perm_b32 v18, v244, v240, s84
v_perm_b32 v19, v252, v248, s84
v_mfma_f32_16x16x32_bf16 a[144:147], v[24:27], a[204:207], a[144:147]
s_waitcnt vmcnt(29)
v_perm_b32 v20, v68, v64, s84
v_perm_b32 v21, v76, v72, s84
v_mfma_f32_16x16x32_bf16 a[144:147], v[28:31], a[224:227], a[144:147]
v_perm_b32 v22, v84, v80, s84
v_perm_b32 v23, v92, v88, s84
v_mfma_f32_16x16x32_bf16 a[148:151], v[24:27], a[208:211], a[148:151]
buffer_load_dwordx4 v[180:183], v7, s[48:51], s83 offen offset:896
v_mfma_f32_16x16x32_bf16 a[148:151], v[28:31], a[228:231], a[148:151]
buffer_load_dwordx4 v[184:187], v7, s[48:51], s83 offen offset:1792
v_mfma_f32_16x16x32_bf16 a[152:155], v[24:27], a[212:215], a[152:155]
buffer_load_dwordx4 v[188:191], v7, s[48:51], s83 offen offset:2688
v_mfma_f32_16x16x32_bf16 a[152:155], v[28:31], a[232:235], a[152:155]
s_add_u32 s82, s95, 2
s_and_b32 s82, s82, 7
s_mul_i32 s82, s82, 28672
s_add_u32 s82, s82, 229376
v_mfma_f32_16x16x32_bf16 a[156:159], v[24:27], a[216:219], a[156:159]
buffer_load_dwordx4 v[192:195], v7, s[48:51], s82 offen
v_mfma_f32_16x16x32_bf16 a[156:159], v[28:31], a[236:239], a[156:159]
buffer_load_dwordx4 v[196:199], v7, s[48:51], s82 offen offset:896
s_waitcnt lgkmcnt(1)
v_mfma_f32_16x16x32_bf16 a[0:3], v[16:19], a[160:163], a[0:3]
v_perm_b32 v24, v228, v224, s85
v_perm_b32 v25, v236, v232, s85
v_mfma_f32_16x16x32_bf16 a[0:3], v[20:23], a[180:183], a[0:3]
v_perm_b32 v26, v244, v240, s85
v_perm_b32 v27, v252, v248, s85
v_mfma_f32_16x16x32_bf16 a[4:7], v[16:19], a[164:167], a[4:7]
)"
R"(v_perm_b32 v28, v68, v64, s85
v_perm_b32 v29, v76, v72, s85
v_mfma_f32_16x16x32_bf16 a[4:7], v[20:23], a[184:187], a[4:7]
v_perm_b32 v30, v84, v80, s85
v_perm_b32 v31, v92, v88, s85
v_mfma_f32_16x16x32_bf16 a[8:11], v[16:19], a[168:171], a[8:11]
buffer_load_dwordx4 v[200:203], v7, s[48:51], s82 offen offset:1792
v_mfma_f32_16x16x32_bf16 a[8:11], v[20:23], a[188:191], a[8:11]
buffer_load_dwordx4 v[204:207], v7, s[48:51], s82 offen offset:2688
v_mfma_f32_16x16x32_bf16 a[12:15], v[16:19], a[172:175], a[12:15]
s_add_u32 s83, s82, 3584
ds_read_b128 a[200:203], v1 offset:30720
v_mfma_f32_16x16x32_bf16 a[12:15], v[20:23], a[192:195], a[12:15]
ds_read_b128 a[220:223], v1 offset:35840
v_mfma_f32_16x16x32_bf16 a[16:19], v[16:19], a[176:179], a[16:19]
s_waitcnt lgkmcnt(2)
v_mfma_f32_16x16x32_bf16 a[16:19], v[20:23], a[196:199], a[16:19]
v_mfma_f32_16x16x32_bf16 a[20:23], v[24:27], a[160:163], a[20:23]
v_perm_b32 v16, v229, v225, s84
v_perm_b32 v17, v237, v233, s84
v_mfma_f32_16x16x32_bf16 a[20:23], v[28:31], a[180:183], a[20:23]
v_perm_b32 v18, v245, v241, s84
v_perm_b32 v19, v253, v249, s84
v_mfma_f32_16x16x32_bf16 a[24:27], v[24:27], a[164:167], a[24:27]
v_perm_b32 v20, v69, v65, s84
v_perm_b32 v21, v77, v73, s84
v_mfma_f32_16x16x32_bf16 a[24:27], v[28:31], a[184:187], a[24:27]
v_perm_b32 v22, v85, v81, s84
v_perm_b32 v23, v93, v89, s84
v_mfma_f32_16x16x32_bf16 a[28:31], v[24:27], a[168:171], a[28:31]
buffer_load_dwordx4 v[208:211], v7, s[48:51], s83 offen
v_mfma_f32_16x16x32_bf16 a[28:31], v[28:31], a[188:191], a[28:31]
v_mfma_f32_16x16x32_bf16 a[32:35], v[24:27], a[172:175], a[32:35]
buffer_load_dwordx4 v[212:215], v7, s[48:51], s83 offen offset:896
v_mfma_f32_16x16x32_bf16 a[32:35], v[28:31], a[192:195], a[32:35]
v_mfma_f32_16x16x32_bf16 a[36:39], v[24:27], a[176:179], a[36:39]
v_mfma_f32_16x16x32_bf16 a[36:39], v[28:31], a[196:199], a[36:39]
buffer_load_dwordx4 v[216:219], v7, s[48:51], s83 offen offset:1792
v_mfma_f32_16x16x32_bf16 a[40:43], v[16:19], a[160:163], a[40:43]
v_perm_b32 v24, v229, v225, s85
v_perm_b32 v25, v237, v233, s85
v_mfma_f32_16x16x32_bf16 a[40:43], v[20:23], a[180:183], a[40:43]
v_perm_b32 v26, v245, v241, s85
v_perm_b32 v27, v253, v249, s85
v_mfma_f32_16x16x32_bf16 a[44:47], v[16:19], a[164:167], a[44:47]
ds_read_b128 a[204:207], v1 offset:31744
v_perm_b32 v28, v69, v65, s85
v_mfma_f32_16x16x32_bf16 a[44:47], v[20:23], a[184:187], a[44:47]
ds_read_b128 a[224:227], v1 offset:36864
v_perm_b32 v29, v77, v73, s85
v_mfma_f32_16x16x32_bf16 a[48:51], v[16:19], a[168:171], a[48:51]
v_perm_b32 v30, v85, v81, s85
v_perm_b32 v31, v93, v89, s85
v_mfma_f32_16x16x32_bf16 a[48:51], v[20:23], a[188:191], a[48:51]
buffer_load_dwordx4 v[220:223], v7, s[48:51], s83 offen offset:2688
v_mfma_f32_16x16x32_bf16 a[52:55], v[16:19], a[172:175], a[52:55]
v_mfma_f32_16x16x32_bf16 a[52:55], v[20:23], a[192:195], a[52:55]
v_mfma_f32_16x16x32_bf16 a[56:59], v[16:19], a[176:179], a[56:59]
v_mfma_f32_16x16x32_bf16 a[56:59], v[20:23], a[196:199], a[56:59]
v_mfma_f32_16x16x32_bf16 a[60:63], v[24:27], a[160:163], a[60:63]
v_perm_b32 v16, v230, v226, s84
v_perm_b32 v17, v238, v234, s84
v_mfma_f32_16x16x32_bf16 a[60:63], v[28:31], a[180:183], a[60:63]
v_perm_b32 v18, v246, v242, s84
v_perm_b32 v19, v254, v250, s84
v_mfma_f32_16x16x32_bf16 a[64:67], v[24:27], a[164:167], a[64:67]
v_perm_b32 v20, v70, v66, s84
v_perm_b32 v21, v78, v74, s84
v_mfma_f32_16x16x32_bf16 a[64:67], v[28:31], a[184:187], a[64:67]
v_perm_b32 v22, v86, v82, s84
v_perm_b32 v23, v94, v90, s84
v_mfma_f32_16x16x32_bf16 a[68:71], v[24:27], a[168:171], a[68:71]
v_mfma_f32_16x16x32_bf16 a[68:71], v[28:31], a[188:191], a[68:71]
ds_read_b128 a[208:211], v1 offset:32768
v_mfma_f32_16x16x32_bf16 a[72:75], v[24:27], a[172:175], a[72:75]
ds_read_b128 a[228:231], v1 offset:37888
v_mfma_f32_16x16x32_bf16 a[72:75], v[28:31], a[192:195], a[72:75]
v_mfma_f32_16x16x32_bf16 a[76:79], v[24:27], a[176:179], a[76:79]
v_mfma_f32_16x16x32_bf16 a[76:79], v[28:31], a[196:199], a[76:79]
v_mfma_f32_16x16x32_bf16 a[80:83], v[16:19], a[160:163], a[80:83]
v_perm_b32 v24, v230, v226, s85
v_perm_b32 v25, v238, v234, s85
v_mfma_f32_16x16x32_bf16 a[80:83], v[20:23], a[180:183], a[80:83]
v_perm_b32 v26, v246, v242, s85
v_perm_b32 v27, v254, v250, s85
v_mfma_f32_16x16x32_bf16 a[84:87], v[16:19], a[164:167], a[84:87]
v_perm_b32 v28, v70, v66, s85
v_perm_b32 v29, v78, v74, s85
v_mfma_f32_16x16x32_bf16 a[84:87], v[20:23], a[184:187], a[84:87]
v_perm_b32 v30, v86, v82, s85
v_perm_b32 v31, v94, v90, s85
v_mfma_f32_16x16x32_bf16 a[88:91], v[16:19], a[168:171], a[88:91]
v_mfma_f32_16x16x32_bf16 a[88:91], v[20:23], a[188:191], a[88:91]
v_mfma_f32_16x16x32_bf16 a[92:95], v[16:19], a[172:175], a[92:95]
v_mfma_f32_16x16x32_bf16 a[92:95], v[20:23], a[192:195], a[92:95]
v_mfma_f32_16x16x32_bf16 a[96:99], v[16:19], a[176:179], a[96:99]
v_mfma_f32_16x16x32_bf16 a[96:99], v[20:23], a[196:199], a[96:99]
v_mfma_f32_16x16x32_bf16 a[100:103], v[24:27], a[160:163], a[100:103]
v_perm_b32 v16, v231, v227, s84
v_perm_b32 v17, v239, v235, s84
v_mfma_f32_16x16x32_bf16 a[100:103], v[28:31], a[180:183], a[100:103]
v_perm_b32 v18, v247, v243, s84
v_perm_b32 v19, v255, v251, s84
v_mfma_f32_16x16x32_bf16 a[104:107], v[24:27], a[164:167], a[104:107]
ds_read_b128 a[212:215], v1 offset:33792
v_perm_b32 v20, v71, v67, s84
v_mfma_f32_16x16x32_bf16 a[104:107], v[28:31], a[184:187], a[104:107]
ds_read_b128 a[232:235], v1 offset:38912
v_perm_b32 v21, v79, v75, s84
v_mfma_f32_16x16x32_bf16 a[108:111], v[24:27], a[168:171], a[108:111]
v_perm_b32 v22, v87, v83, s84
v_perm_b32 v23, v95, v91, s84
v_mfma_f32_16x16x32_bf16 a[108:111], v[28:31], a[188:191], a[108:111]
v_mfma_f32_16x16x32_bf16 a[112:115], v[24:27], a[172:175], a[112:115]
v_mfma_f32_16x16x32_bf16 a[112:115], v[28:31], a[192:195], a[112:115]
v_mfma_f32_16x16x32_bf16 a[116:119], v[24:27], a[176:179], a[116:119]
v_mfma_f32_16x16x32_bf16 a[116:119], v[28:31], a[196:199], a[116:119]
v_mfma_f32_16x16x32_bf16 a[120:123], v[16:19], a[160:163], a[120:123]
v_perm_b32 v24, v231, v227, s85
v_perm_b32 v25, v239, v235, s85
v_mfma_f32_16x16x32_bf16 a[120:123], v[20:23], a[180:183], a[120:123]
s_add_u32 s82, s95, 3
s_and_b32 s82, s82, 7
s_mul_i32 s82, s82, 28672
s_add_u32 s82, s82, 229376
v_perm_b32 v26, v247, v243, s85
v_perm_b32 v27, v255, v251, s85
v_mfma_f32_16x16x32_bf16 a[124:127], v[16:19], a[164:167], a[124:127]
v_perm_b32 v28, v71, v67, s85
v_perm_b32 v29, v79, v75, s85
v_mfma_f32_16x16x32_bf16 a[124:127], v[20:23], a[184:187], a[124:127]
v_perm_b32 v30, v87, v83, s85
v_perm_b32 v31, v95, v91, s85
v_mfma_f32_16x16x32_bf16 a[128:131], v[16:19], a[168:171], a[128:131]
buffer_load_dwordx4 v[224:227], v7, s[48:51], s82 offen
ds_read_b128 a[216:219], v1 offset:34816
v_mfma_f32_16x16x32_bf16 a[128:131], v[20:23], a[188:191], a[128:131]
buffer_load_dwordx4 v[228:231], v7, s[48:51], s82 offen offset:896
ds_read_b128 a[236:239], v1 offset:39936
v_mfma_f32_16x16x32_bf16 a[132:135], v[16:19], a[172:175], a[132:135]
buffer_load_dwordx4 v[232:235], v7, s[48:51], s82 offen offset:1792
v_mfma_f32_16x16x32_bf16 a[132:135], v[20:23], a[192:195], a[132:135]
buffer_load_dwordx4 v[236:239], v7, s[48:51], s82 offen offset:2688
v_mfma_f32_16x16x32_bf16 a[136:139], v[16:19], a[176:179], a[136:139]
s_add_u32 s83, s82, 3584
v_mfma_f32_16x16x32_bf16 a[136:139], v[20:23], a[196:199], a[136:139]
buffer_load_dwordx4 v[64:67], v7, s[48:51], s83 offen
v_mfma_f32_16x16x32_bf16 a[140:143], v[24:27], a[160:163], a[140:143]
s_waitcnt vmcnt(37)
v_perm_b32 v16, v36, v32, s84
v_perm_b32 v17, v44, v40, s84
v_mfma_f32_16x16x32_bf16 a[140:143], v[28:31], a[180:183], a[140:143]
v_perm_b32 v18, v52, v48, s84
v_perm_b32 v19, v60, v56, s84
v_mfma_f32_16x16x32_bf16 a[144:147], v[24:27], a[164:167], a[144:147]
s_waitcnt vmcnt(29)
v_perm_b32 v20, v132, v128, s84
v_perm_b32 v21, v140, v136, s84
v_mfma_f32_16x16x32_bf16 a[144:147], v[28:31], a[184:187], a[144:147]
v_perm_b32 v22, v100, v96, s84
v_perm_b32 v23, v108, v104, s84
v_mfma_f32_16x16x32_bf16 a[148:151], v[24:27], a[168:171], a[148:151]
buffer_load_dwordx4 v[68:71], v7, s[48:51], s83 offen offset:896
v_mfma_f32_16x16x32_bf16 a[148:151], v[28:31], a[188:191], a[148:151]
buffer_load_dwordx4 v[72:75], v7, s[48:51], s83 offen offset:1792
v_mfma_f32_16x16x32_bf16 a[152:155], v[24:27], a[172:175], a[152:155]
buffer_load_dwordx4 v[76:79], v7, s[48:51], s83 offen offset:2688
v_mfma_f32_16x16x32_bf16 a[152:155], v[28:31], a[192:195], a[152:155]
s_add_u32 s82, s95, 4
s_and_b32 s82, s82, 7
s_mul_i32 s82, s82, 28672
s_add_u32 s82, s82, 229376
v_mfma_f32_16x16x32_bf16 a[156:159], v[24:27], a[176:179], a[156:159]
buffer_load_dwordx4 v[80:83], v7, s[48:51], s82 offen
v_mfma_f32_16x16x32_bf16 a[156:159], v[28:31], a[196:199], a[156:159]
buffer_load_dwordx4 v[84:87], v7, s[48:51], s82 offen offset:896
s_waitcnt lgkmcnt(1)
v_mfma_f32_16x16x32_bf16 a[0:3], v[16:19], a[200:203], a[0:3]
v_perm_b32 v24, v36, v32, s85
v_perm_b32 v25, v44, v40, s85
v_mfma_f32_16x16x32_bf16 a[0:3], v[20:23], a[220:223], a[0:3]
v_perm_b32 v26, v52, v48, s85
v_perm_b32 v27, v60, v56, s85
v_mfma_f32_16x16x32_bf16 a[4:7], v[16:19], a[204:207], a[4:7]
v_perm_b32 v28, v132, v128, s85
v_perm_b32 v29, v140, v136, s85
v_mfma_f32_16x16x32_bf16 a[4:7], v[20:23], a[224:227], a[4:7]
v_perm_b32 v30, v100, v96, s85
v_perm_b32 v31, v108, v104, s85
v_mfma_f32_16x16x32_bf16 a[8:11], v[16:19], a[208:211], a[8:11]
buffer_load_dwordx4 v[88:91], v7, s[48:51], s82 offen offset:1792
v_mfma_f32_16x16x32_bf16 a[8:11], v[20:23], a[228:231], a[8:11]
buffer_load_dwordx4 v[92:95], v7, s[48:51], s82 offen offset:2688
v_mfma_f32_16x16x32_bf16 a[12:15], v[16:19], a[212:215], a[12:15]
s_add_u32 s83, s82, 3584
ds_read_b128 a[160:163], v1 offset:40960
v_mfma_f32_16x16x32_bf16 a[12:15], v[20:23], a[232:235], a[12:15]
ds_read_b128 a[180:183], v1 offset:46080
v_mfma_f32_16x16x32_bf16 a[16:19], v[16:19], a[216:219], a[16:19]
s_waitcnt lgkmcnt(2)
)"
R"(v_mfma_f32_16x16x32_bf16 a[16:19], v[20:23], a[236:239], a[16:19]
v_mfma_f32_16x16x32_bf16 a[20:23], v[24:27], a[200:203], a[20:23]
v_perm_b32 v16, v37, v33, s84
v_perm_b32 v17, v45, v41, s84
v_mfma_f32_16x16x32_bf16 a[20:23], v[28:31], a[220:223], a[20:23]
v_perm_b32 v18, v53, v49, s84
v_perm_b32 v19, v61, v57, s84
v_mfma_f32_16x16x32_bf16 a[24:27], v[24:27], a[204:207], a[24:27]
v_perm_b32 v20, v133, v129, s84
v_perm_b32 v21, v141, v137, s84
v_mfma_f32_16x16x32_bf16 a[24:27], v[28:31], a[224:227], a[24:27]
v_perm_b32 v22, v101, v97, s84
v_perm_b32 v23, v109, v105, s84
v_mfma_f32_16x16x32_bf16 a[28:31], v[24:27], a[208:211], a[28:31]
buffer_load_dwordx4 v[240:243], v7, s[48:51], s83 offen
v_mfma_f32_16x16x32_bf16 a[28:31], v[28:31], a[228:231], a[28:31]
v_mfma_f32_16x16x32_bf16 a[32:35], v[24:27], a[212:215], a[32:35]
buffer_load_dwordx4 v[244:247], v7, s[48:51], s83 offen offset:896
v_mfma_f32_16x16x32_bf16 a[32:35], v[28:31], a[232:235], a[32:35]
v_mfma_f32_16x16x32_bf16 a[36:39], v[24:27], a[216:219], a[36:39]
v_mfma_f32_16x16x32_bf16 a[36:39], v[28:31], a[236:239], a[36:39]
buffer_load_dwordx4 v[248:251], v7, s[48:51], s83 offen offset:1792
v_mfma_f32_16x16x32_bf16 a[40:43], v[16:19], a[200:203], a[40:43]
v_perm_b32 v24, v37, v33, s85
v_perm_b32 v25, v45, v41, s85
v_mfma_f32_16x16x32_bf16 a[40:43], v[20:23], a[220:223], a[40:43]
v_perm_b32 v26, v53, v49, s85
v_perm_b32 v27, v61, v57, s85
v_mfma_f32_16x16x32_bf16 a[44:47], v[16:19], a[204:207], a[44:47]
ds_read_b128 a[164:167], v1 offset:41984
v_perm_b32 v28, v133, v129, s85
v_mfma_f32_16x16x32_bf16 a[44:47], v[20:23], a[224:227], a[44:47]
ds_read_b128 a[184:187], v1 offset:47104
v_perm_b32 v29, v141, v137, s85
v_mfma_f32_16x16x32_bf16 a[48:51], v[16:19], a[208:211], a[48:51]
v_perm_b32 v30, v101, v97, s85
v_perm_b32 v31, v109, v105, s85
v_mfma_f32_16x16x32_bf16 a[48:51], v[20:23], a[228:231], a[48:51]
buffer_load_dwordx4 v[252:255], v7, s[48:51], s83 offen offset:2688
v_mfma_f32_16x16x32_bf16 a[52:55], v[16:19], a[212:215], a[52:55]
v_mfma_f32_16x16x32_bf16 a[52:55], v[20:23], a[232:235], a[52:55]
v_mfma_f32_16x16x32_bf16 a[56:59], v[16:19], a[216:219], a[56:59]
v_mfma_f32_16x16x32_bf16 a[56:59], v[20:23], a[236:239], a[56:59]
v_mfma_f32_16x16x32_bf16 a[60:63], v[24:27], a[200:203], a[60:63]
v_perm_b32 v16, v38, v34, s84
v_perm_b32 v17, v46, v42, s84
v_mfma_f32_16x16x32_bf16 a[60:63], v[28:31], a[220:223], a[60:63]
v_perm_b32 v18, v54, v50, s84
v_perm_b32 v19, v62, v58, s84
v_mfma_f32_16x16x32_bf16 a[64:67], v[24:27], a[204:207], a[64:67]
v_perm_b32 v20, v134, v130, s84
v_perm_b32 v21, v142, v138, s84
v_mfma_f32_16x16x32_bf16 a[64:67], v[28:31], a[224:227], a[64:67]
v_perm_b32 v22, v102, v98, s84
v_perm_b32 v23, v110, v106, s84
v_mfma_f32_16x16x32_bf16 a[68:71], v[24:27], a[208:211], a[68:71]
v_mfma_f32_16x16x32_bf16 a[68:71], v[28:31], a[228:231], a[68:71]
ds_read_b128 a[168:171], v1 offset:43008
v_mfma_f32_16x16x32_bf16 a[72:75], v[24:27], a[212:215], a[72:75]
ds_read_b128 a[188:191], v1 offset:48128
v_mfma_f32_16x16x32_bf16 a[72:75], v[28:31], a[232:235], a[72:75]
v_mfma_f32_16x16x32_bf16 a[76:79], v[24:27], a[216:219], a[76:79]
v_mfma_f32_16x16x32_bf16 a[76:79], v[28:31], a[236:239], a[76:79]
v_mfma_f32_16x16x32_bf16 a[80:83], v[16:19], a[200:203], a[80:83]
v_perm_b32 v24, v38, v34, s85
v_perm_b32 v25, v46, v42, s85
v_mfma_f32_16x16x32_bf16 a[80:83], v[20:23], a[220:223], a[80:83]
v_perm_b32 v26, v54, v50, s85
v_perm_b32 v27, v62, v58, s85
v_mfma_f32_16x16x32_bf16 a[84:87], v[16:19], a[204:207], a[84:87]
v_perm_b32 v28, v134, v130, s85
v_perm_b32 v29, v142, v138, s85
v_mfma_f32_16x16x32_bf16 a[84:87], v[20:23], a[224:227], a[84:87]
v_perm_b32 v30, v102, v98, s85
v_perm_b32 v31, v110, v106, s85
v_mfma_f32_16x16x32_bf16 a[88:91], v[16:19], a[208:211], a[88:91]
v_mfma_f32_16x16x32_bf16 a[88:91], v[20:23], a[228:231], a[88:91]
v_mfma_f32_16x16x32_bf16 a[92:95], v[16:19], a[212:215], a[92:95]
v_mfma_f32_16x16x32_bf16 a[92:95], v[20:23], a[232:235], a[92:95]
v_mfma_f32_16x16x32_bf16 a[96:99], v[16:19], a[216:219], a[96:99]
v_mfma_f32_16x16x32_bf16 a[96:99], v[20:23], a[236:239], a[96:99]
v_mfma_f32_16x16x32_bf16 a[100:103], v[24:27], a[200:203], a[100:103]
v_perm_b32 v16, v39, v35, s84
v_perm_b32 v17, v47, v43, s84
v_mfma_f32_16x16x32_bf16 a[100:103], v[28:31], a[220:223], a[100:103]
v_perm_b32 v18, v55, v51, s84
v_perm_b32 v19, v63, v59, s84
v_mfma_f32_16x16x32_bf16 a[104:107], v[24:27], a[204:207], a[104:107]
ds_read_b128 a[172:175], v1 offset:44032
v_perm_b32 v20, v135, v131, s84
v_mfma_f32_16x16x32_bf16 a[104:107], v[28:31], a[224:227], a[104:107]
ds_read_b128 a[192:195], v1 offset:49152
v_perm_b32 v21, v143, v139, s84
v_mfma_f32_16x16x32_bf16 a[108:111], v[24:27], a[208:211], a[108:111]
v_perm_b32 v22, v103, v99, s84
v_perm_b32 v23, v111, v107, s84
v_mfma_f32_16x16x32_bf16 a[108:111], v[28:31], a[228:231], a[108:111]
v_mfma_f32_16x16x32_bf16 a[112:115], v[24:27], a[212:215], a[112:115]
v_mfma_f32_16x16x32_bf16 a[112:115], v[28:31], a[232:235], a[112:115]
v_mfma_f32_16x16x32_bf16 a[116:119], v[24:27], a[216:219], a[116:119]
v_mfma_f32_16x16x32_bf16 a[116:119], v[28:31], a[236:239], a[116:119]
v_mfma_f32_16x16x32_bf16 a[120:123], v[16:19], a[200:203], a[120:123]
v_perm_b32 v24, v39, v35, s85
v_perm_b32 v25, v47, v43, s85
v_mfma_f32_16x16x32_bf16 a[120:123], v[20:23], a[220:223], a[120:123]
s_add_u32 s82, s95, 5
s_and_b32 s82, s82, 7
s_mul_i32 s82, s82, 28672
s_add_u32 s82, s82, 229376
v_perm_b32 v26, v55, v51, s85
v_perm_b32 v27, v63, v59, s85
v_mfma_f32_16x16x32_bf16 a[124:127], v[16:19], a[204:207], a[124:127]
v_perm_b32 v28, v135, v131, s85
v_perm_b32 v29, v143, v139, s85
v_mfma_f32_16x16x32_bf16 a[124:127], v[20:23], a[224:227], a[124:127]
v_perm_b32 v30, v103, v99, s85
v_perm_b32 v31, v111, v107, s85
v_mfma_f32_16x16x32_bf16 a[128:131], v[16:19], a[208:211], a[128:131]
buffer_load_dwordx4 v[32:35], v7, s[48:51], s82 offen
ds_read_b128 a[176:179], v1 offset:45056
v_mfma_f32_16x16x32_bf16 a[128:131], v[20:23], a[228:231], a[128:131]
buffer_load_dwordx4 v[36:39], v7, s[48:51], s82 offen offset:896
ds_read_b128 a[196:199], v1 offset:50176
v_mfma_f32_16x16x32_bf16 a[132:135], v[16:19], a[212:215], a[132:135]
buffer_load_dwordx4 v[40:43], v7, s[48:51], s82 offen offset:1792
v_mfma_f32_16x16x32_bf16 a[132:135], v[20:23], a[232:235], a[132:135]
buffer_load_dwordx4 v[44:47], v7, s[48:51], s82 offen offset:2688
v_mfma_f32_16x16x32_bf16 a[136:139], v[16:19], a[216:219], a[136:139]
s_add_u32 s83, s82, 3584
v_mfma_f32_16x16x32_bf16 a[136:139], v[20:23], a[236:239], a[136:139]
buffer_load_dwordx4 v[96:99], v7, s[48:51], s83 offen
v_mfma_f32_16x16x32_bf16 a[140:143], v[24:27], a[200:203], a[140:143]
s_waitcnt vmcnt(37)
v_perm_b32 v16, v148, v144, s84
v_perm_b32 v17, v156, v152, s84
v_mfma_f32_16x16x32_bf16 a[140:143], v[28:31], a[220:223], a[140:143]
v_perm_b32 v18, v164, v160, s84
v_perm_b32 v19, v172, v168, s84
v_mfma_f32_16x16x32_bf16 a[144:147], v[24:27], a[204:207], a[144:147]
s_waitcnt vmcnt(29)
v_perm_b32 v20, v116, v112, s84
v_perm_b32 v21, v124, v120, s84
v_mfma_f32_16x16x32_bf16 a[144:147], v[28:31], a[224:227], a[144:147]
v_perm_b32 v22, v180, v176, s84
v_perm_b32 v23, v188, v184, s84
v_mfma_f32_16x16x32_bf16 a[148:151], v[24:27], a[208:211], a[148:151]
buffer_load_dwordx4 v[100:103], v7, s[48:51], s83 offen offset:896
v_mfma_f32_16x16x32_bf16 a[148:151], v[28:31], a[228:231], a[148:151]
buffer_load_dwordx4 v[104:107], v7, s[48:51], s83 offen offset:1792
v_mfma_f32_16x16x32_bf16 a[152:155], v[24:27], a[212:215], a[152:155]
buffer_load_dwordx4 v[108:111], v7, s[48:51], s83 offen offset:2688
v_mfma_f32_16x16x32_bf16 a[152:155], v[28:31], a[232:235], a[152:155]
s_add_u32 s82, s95, 6
s_and_b32 s82, s82, 7
s_mul_i32 s82, s82, 28672
s_add_u32 s82, s82, 229376
v_mfma_f32_16x16x32_bf16 a[156:159], v[24:27], a[216:219], a[156:159]
buffer_load_dwordx4 v[48:51], v7, s[48:51], s82 offen
v_mfma_f32_16x16x32_bf16 a[156:159], v[28:31], a[236:239], a[156:159]
buffer_load_dwordx4 v[52:55], v7, s[48:51], s82 offen offset:896
s_waitcnt lgkmcnt(1)
v_mfma_f32_16x16x32_bf16 a[0:3], v[16:19], a[160:163], a[0:3]
v_perm_b32 v24, v148, v144, s85
v_perm_b32 v25, v156, v152, s85
v_mfma_f32_16x16x32_bf16 a[0:3], v[20:23], a[180:183], a[0:3]
v_perm_b32 v26, v164, v160, s85
v_perm_b32 v27, v172, v168, s85
v_mfma_f32_16x16x32_bf16 a[4:7], v[16:19], a[164:167], a[4:7]
v_perm_b32 v28, v116, v112, s85
v_perm_b32 v29, v124, v120, s85
v_mfma_f32_16x16x32_bf16 a[4:7], v[20:23], a[184:187], a[4:7]
v_perm_b32 v30, v180, v176, s85
v_perm_b32 v31, v188, v184, s85
v_mfma_f32_16x16x32_bf16 a[8:11], v[16:19], a[168:171], a[8:11]
buffer_load_dwordx4 v[56:59], v7, s[48:51], s82 offen offset:1792
v_mfma_f32_16x16x32_bf16 a[8:11], v[20:23], a[188:191], a[8:11]
buffer_load_dwordx4 v[60:63], v7, s[48:51], s82 offen offset:2688
v_mfma_f32_16x16x32_bf16 a[12:15], v[16:19], a[172:175], a[12:15]
s_add_u32 s83, s82, 3584
ds_read_b128 a[200:203], v1 offset:51200
v_mfma_f32_16x16x32_bf16 a[12:15], v[20:23], a[192:195], a[12:15]
ds_read_b128 a[220:223], v1 offset:56320
v_mfma_f32_16x16x32_bf16 a[16:19], v[16:19], a[176:179], a[16:19]
s_waitcnt lgkmcnt(2)
v_mfma_f32_16x16x32_bf16 a[16:19], v[20:23], a[196:199], a[16:19]
v_mfma_f32_16x16x32_bf16 a[20:23], v[24:27], a[160:163], a[20:23]
v_perm_b32 v16, v149, v145, s84
v_perm_b32 v17, v157, v153, s84
v_mfma_f32_16x16x32_bf16 a[20:23], v[28:31], a[180:183], a[20:23]
v_perm_b32 v18, v165, v161, s84
v_perm_b32 v19, v173, v169, s84
v_mfma_f32_16x16x32_bf16 a[24:27], v[24:27], a[164:167], a[24:27]
v_perm_b32 v20, v117, v113, s84
v_perm_b32 v21, v125, v121, s84
v_mfma_f32_16x16x32_bf16 a[24:27], v[28:31], a[184:187], a[24:27]
v_perm_b32 v22, v181, v177, s84
v_perm_b32 v23, v189, v185, s84
v_mfma_f32_16x16x32_bf16 a[28:31], v[24:27], a[168:171], a[28:31]
buffer_load_dwordx4 v[128:131], v7, s[48:51], s83 offen
v_mfma_f32_16x16x32_bf16 a[28:31], v[28:31], a[188:191], a[28:31]
)"
R"(v_mfma_f32_16x16x32_bf16 a[32:35], v[24:27], a[172:175], a[32:35]
buffer_load_dwordx4 v[132:135], v7, s[48:51], s83 offen offset:896
v_mfma_f32_16x16x32_bf16 a[32:35], v[28:31], a[192:195], a[32:35]
v_mfma_f32_16x16x32_bf16 a[36:39], v[24:27], a[176:179], a[36:39]
v_mfma_f32_16x16x32_bf16 a[36:39], v[28:31], a[196:199], a[36:39]
buffer_load_dwordx4 v[136:139], v7, s[48:51], s83 offen offset:1792
v_mfma_f32_16x16x32_bf16 a[40:43], v[16:19], a[160:163], a[40:43]
v_perm_b32 v24, v149, v145, s85
v_perm_b32 v25, v157, v153, s85
v_mfma_f32_16x16x32_bf16 a[40:43], v[20:23], a[180:183], a[40:43]
v_perm_b32 v26, v165, v161, s85
v_perm_b32 v27, v173, v169, s85
v_mfma_f32_16x16x32_bf16 a[44:47], v[16:19], a[164:167], a[44:47]
ds_read_b128 a[204:207], v1 offset:52224
v_perm_b32 v28, v117, v113, s85
v_mfma_f32_16x16x32_bf16 a[44:47], v[20:23], a[184:187], a[44:47]
ds_read_b128 a[224:227], v1 offset:57344
v_perm_b32 v29, v125, v121, s85
v_mfma_f32_16x16x32_bf16 a[48:51], v[16:19], a[168:171], a[48:51]
v_perm_b32 v30, v181, v177, s85
v_perm_b32 v31, v189, v185, s85
v_mfma_f32_16x16x32_bf16 a[48:51], v[20:23], a[188:191], a[48:51]
buffer_load_dwordx4 v[140:143], v7, s[48:51], s83 offen offset:2688
v_mfma_f32_16x16x32_bf16 a[52:55], v[16:19], a[172:175], a[52:55]
v_mfma_f32_16x16x32_bf16 a[52:55], v[20:23], a[192:195], a[52:55]
v_mfma_f32_16x16x32_bf16 a[56:59], v[16:19], a[176:179], a[56:59]
v_mfma_f32_16x16x32_bf16 a[56:59], v[20:23], a[196:199], a[56:59]
v_mfma_f32_16x16x32_bf16 a[60:63], v[24:27], a[160:163], a[60:63]
v_perm_b32 v16, v150, v146, s84
v_perm_b32 v17, v158, v154, s84
v_mfma_f32_16x16x32_bf16 a[60:63], v[28:31], a[180:183], a[60:63]
v_perm_b32 v18, v166, v162, s84
v_perm_b32 v19, v174, v170, s84
v_mfma_f32_16x16x32_bf16 a[64:67], v[24:27], a[164:167], a[64:67]
v_perm_b32 v20, v118, v114, s84
v_perm_b32 v21, v126, v122, s84
v_mfma_f32_16x16x32_bf16 a[64:67], v[28:31], a[184:187], a[64:67]
v_perm_b32 v22, v182, v178, s84
v_perm_b32 v23, v190, v186, s84
v_mfma_f32_16x16x32_bf16 a[68:71], v[24:27], a[168:171], a[68:71]
v_mfma_f32_16x16x32_bf16 a[68:71], v[28:31], a[188:191], a[68:71]
ds_read_b128 a[208:211], v1 offset:53248
v_mfma_f32_16x16x32_bf16 a[72:75], v[24:27], a[172:175], a[72:75]
ds_read_b128 a[228:231], v1 offset:58368
v_mfma_f32_16x16x32_bf16 a[72:75], v[28:31], a[192:195], a[72:75]
v_mfma_f32_16x16x32_bf16 a[76:79], v[24:27], a[176:179], a[76:79]
v_mfma_f32_16x16x32_bf16 a[76:79], v[28:31], a[196:199], a[76:79]
v_mfma_f32_16x16x32_bf16 a[80:83], v[16:19], a[160:163], a[80:83]
v_perm_b32 v24, v150, v146, s85
v_perm_b32 v25, v158, v154, s85
v_mfma_f32_16x16x32_bf16 a[80:83], v[20:23], a[180:183], a[80:83]
v_perm_b32 v26, v166, v162, s85
v_perm_b32 v27, v174, v170, s85
v_mfma_f32_16x16x32_bf16 a[84:87], v[16:19], a[164:167], a[84:87]
v_perm_b32 v28, v118, v114, s85
v_perm_b32 v29, v126, v122, s85
v_mfma_f32_16x16x32_bf16 a[84:87], v[20:23], a[184:187], a[84:87]
v_perm_b32 v30, v182, v178, s85
v_perm_b32 v31, v190, v186, s85
v_mfma_f32_16x16x32_bf16 a[88:91], v[16:19], a[168:171], a[88:91]
v_mfma_f32_16x16x32_bf16 a[88:91], v[20:23], a[188:191], a[88:91]
v_mfma_f32_16x16x32_bf16 a[92:95], v[16:19], a[172:175], a[92:95]
v_mfma_f32_16x16x32_bf16 a[92:95], v[20:23], a[192:195], a[92:95]
v_mfma_f32_16x16x32_bf16 a[96:99], v[16:19], a[176:179], a[96:99]
v_mfma_f32_16x16x32_bf16 a[96:99], v[20:23], a[196:199], a[96:99]
v_mfma_f32_16x16x32_bf16 a[100:103], v[24:27], a[160:163], a[100:103]
v_perm_b32 v16, v151, v147, s84
v_perm_b32 v17, v159, v155, s84
v_mfma_f32_16x16x32_bf16 a[100:103], v[28:31], a[180:183], a[100:103]
v_perm_b32 v18, v167, v163, s84
v_perm_b32 v19, v175, v171, s84
v_mfma_f32_16x16x32_bf16 a[104:107], v[24:27], a[164:167], a[104:107]
ds_read_b128 a[212:215], v1 offset:54272
v_perm_b32 v20, v119, v115, s84
v_mfma_f32_16x16x32_bf16 a[104:107], v[28:31], a[184:187], a[104:107]
ds_read_b128 a[232:235], v1 offset:59392
v_perm_b32 v21, v127, v123, s84
v_mfma_f32_16x16x32_bf16 a[108:111], v[24:27], a[168:171], a[108:111]
v_perm_b32 v22, v183, v179, s84
v_perm_b32 v23, v191, v187, s84
v_mfma_f32_16x16x32_bf16 a[108:111], v[28:31], a[188:191], a[108:111]
v_mfma_f32_16x16x32_bf16 a[112:115], v[24:27], a[172:175], a[112:115]
v_mfma_f32_16x16x32_bf16 a[112:115], v[28:31], a[192:195], a[112:115]
v_mfma_f32_16x16x32_bf16 a[116:119], v[24:27], a[176:179], a[116:119]
v_mfma_f32_16x16x32_bf16 a[116:119], v[28:31], a[196:199], a[116:119]
v_mfma_f32_16x16x32_bf16 a[120:123], v[16:19], a[160:163], a[120:123]
v_perm_b32 v24, v151, v147, s85
v_perm_b32 v25, v159, v155, s85
v_mfma_f32_16x16x32_bf16 a[120:123], v[20:23], a[180:183], a[120:123]
s_add_u32 s82, s95, 7
s_and_b32 s82, s82, 7
s_mul_i32 s82, s82, 28672
s_add_u32 s82, s82, 229376
v_perm_b32 v26, v167, v163, s85
v_perm_b32 v27, v175, v171, s85
v_mfma_f32_16x16x32_bf16 a[124:127], v[16:19], a[164:167], a[124:127]
v_perm_b32 v28, v119, v115, s85
v_perm_b32 v29, v127, v123, s85
v_mfma_f32_16x16x32_bf16 a[124:127], v[20:23], a[184:187], a[124:127]
v_perm_b32 v30, v183, v179, s85
v_perm_b32 v31, v191, v187, s85
v_mfma_f32_16x16x32_bf16 a[128:131], v[16:19], a[168:171], a[128:131]
buffer_load_dwordx4 v[144:147], v7, s[48:51], s82 offen
ds_read_b128 a[216:219], v1 offset:55296
v_mfma_f32_16x16x32_bf16 a[128:131], v[20:23], a[188:191], a[128:131]
buffer_load_dwordx4 v[148:151], v7, s[48:51], s82 offen offset:896
ds_read_b128 a[236:239], v1 offset:60416
v_mfma_f32_16x16x32_bf16 a[132:135], v[16:19], a[172:175], a[132:135]
buffer_load_dwordx4 v[152:155], v7, s[48:51], s82 offen offset:1792
v_mfma_f32_16x16x32_bf16 a[132:135], v[20:23], a[192:195], a[132:135]
buffer_load_dwordx4 v[156:159], v7, s[48:51], s82 offen offset:2688
v_mfma_f32_16x16x32_bf16 a[136:139], v[16:19], a[176:179], a[136:139]
s_add_u32 s83, s82, 3584
v_mfma_f32_16x16x32_bf16 a[136:139], v[20:23], a[196:199], a[136:139]
buffer_load_dwordx4 v[112:115], v7, s[48:51], s83 offen
v_mfma_f32_16x16x32_bf16 a[140:143], v[24:27], a[160:163], a[140:143]
s_waitcnt vmcnt(37)
v_perm_b32 v16, v196, v192, s84
v_perm_b32 v17, v204, v200, s84
v_mfma_f32_16x16x32_bf16 a[140:143], v[28:31], a[180:183], a[140:143]
v_perm_b32 v18, v212, v208, s84
v_perm_b32 v19, v220, v216, s84
v_mfma_f32_16x16x32_bf16 a[144:147], v[24:27], a[164:167], a[144:147]
s_waitcnt vmcnt(29)
v_perm_b32 v20, v228, v224, s84
v_perm_b32 v21, v236, v232, s84
v_mfma_f32_16x16x32_bf16 a[144:147], v[28:31], a[184:187], a[144:147]
v_perm_b32 v22, v68, v64, s84
v_perm_b32 v23, v76, v72, s84
v_mfma_f32_16x16x32_bf16 a[148:151], v[24:27], a[168:171], a[148:151]
buffer_load_dwordx4 v[116:119], v7, s[48:51], s83 offen offset:896
v_mfma_f32_16x16x32_bf16 a[148:151], v[28:31], a[188:191], a[148:151]
buffer_load_dwordx4 v[120:123], v7, s[48:51], s83 offen offset:1792
v_mfma_f32_16x16x32_bf16 a[152:155], v[24:27], a[172:175], a[152:155]
buffer_load_dwordx4 v[124:127], v7, s[48:51], s83 offen offset:2688
v_mfma_f32_16x16x32_bf16 a[152:155], v[28:31], a[192:195], a[152:155]
v_mfma_f32_16x16x32_bf16 a[156:159], v[24:27], a[176:179], a[156:159]
v_mfma_f32_16x16x32_bf16 a[156:159], v[28:31], a[196:199], a[156:159]
s_waitcnt lgkmcnt(1)
v_mfma_f32_16x16x32_bf16 a[0:3], v[16:19], a[200:203], a[0:3]
v_perm_b32 v24, v196, v192, s85
v_perm_b32 v25, v204, v200, s85
v_mfma_f32_16x16x32_bf16 a[0:3], v[20:23], a[220:223], a[0:3]
v_perm_b32 v26, v212, v208, s85
v_perm_b32 v27, v220, v216, s85
v_mfma_f32_16x16x32_bf16 a[4:7], v[16:19], a[204:207], a[4:7]
v_perm_b32 v28, v228, v224, s85
v_perm_b32 v29, v236, v232, s85
v_mfma_f32_16x16x32_bf16 a[4:7], v[20:23], a[224:227], a[4:7]
v_perm_b32 v30, v68, v64, s85
v_perm_b32 v31, v76, v72, s85
v_mfma_f32_16x16x32_bf16 a[8:11], v[16:19], a[208:211], a[8:11]
v_mfma_f32_16x16x32_bf16 a[8:11], v[20:23], a[228:231], a[8:11]
v_mfma_f32_16x16x32_bf16 a[12:15], v[16:19], a[212:215], a[12:15]
ds_read_b128 a[160:163], v1 offset:61440
v_mfma_f32_16x16x32_bf16 a[12:15], v[20:23], a[232:235], a[12:15]
ds_read_b128 a[180:183], v2 offset:1024
v_mfma_f32_16x16x32_bf16 a[16:19], v[16:19], a[216:219], a[16:19]
s_waitcnt lgkmcnt(2)
v_mfma_f32_16x16x32_bf16 a[16:19], v[20:23], a[236:239], a[16:19]
v_mfma_f32_16x16x32_bf16 a[20:23], v[24:27], a[200:203], a[20:23]
v_perm_b32 v16, v197, v193, s84
v_perm_b32 v17, v205, v201, s84
v_mfma_f32_16x16x32_bf16 a[20:23], v[28:31], a[220:223], a[20:23]
v_perm_b32 v18, v213, v209, s84
v_perm_b32 v19, v221, v217, s84
v_mfma_f32_16x16x32_bf16 a[24:27], v[24:27], a[204:207], a[24:27]
v_perm_b32 v20, v229, v225, s84
v_perm_b32 v21, v237, v233, s84
v_mfma_f32_16x16x32_bf16 a[24:27], v[28:31], a[224:227], a[24:27]
v_perm_b32 v22, v69, v65, s84
v_perm_b32 v23, v77, v73, s84
v_mfma_f32_16x16x32_bf16 a[28:31], v[24:27], a[208:211], a[28:31]
v_mfma_f32_16x16x32_bf16 a[28:31], v[28:31], a[228:231], a[28:31]
v_mfma_f32_16x16x32_bf16 a[32:35], v[24:27], a[212:215], a[32:35]
v_mfma_f32_16x16x32_bf16 a[32:35], v[28:31], a[232:235], a[32:35]
v_mfma_f32_16x16x32_bf16 a[36:39], v[24:27], a[216:219], a[36:39]
v_mfma_f32_16x16x32_bf16 a[36:39], v[28:31], a[236:239], a[36:39]
v_mfma_f32_16x16x32_bf16 a[40:43], v[16:19], a[200:203], a[40:43]
v_perm_b32 v24, v197, v193, s85
v_perm_b32 v25, v205, v201, s85
v_mfma_f32_16x16x32_bf16 a[40:43], v[20:23], a[220:223], a[40:43]
v_perm_b32 v26, v213, v209, s85
v_perm_b32 v27, v221, v217, s85
v_mfma_f32_16x16x32_bf16 a[44:47], v[16:19], a[204:207], a[44:47]
ds_read_b128 a[164:167], v1 offset:62464
v_perm_b32 v28, v229, v225, s85
v_mfma_f32_16x16x32_bf16 a[44:47], v[20:23], a[224:227], a[44:47]
ds_read_b128 a[184:187], v2 offset:2048
v_perm_b32 v29, v237, v233, s85
v_mfma_f32_16x16x32_bf16 a[48:51], v[16:19], a[208:211], a[48:51]
v_perm_b32 v30, v69, v65, s85
v_perm_b32 v31, v77, v73, s85
v_mfma_f32_16x16x32_bf16 a[48:51], v[20:23], a[228:231], a[48:51]
v_mfma_f32_16x16x32_bf16 a[52:55], v[16:19], a[212:215], a[52:55]
v_mfma_f32_16x16x32_bf16 a[52:55], v[20:23], a[232:235], a[52:55]
v_mfma_f32_16x16x32_bf16 a[56:59], v[16:19], a[216:219], a[56:59]
v_mfma_f32_16x16x32_bf16 a[56:59], v[20:23], a[236:239], a[56:59]
v_mfma_f32_16x16x32_bf16 a[60:63], v[24:27], a[200:203], a[60:63]
v_perm_b32 v16, v198, v194, s84
)"
R"(v_perm_b32 v17, v206, v202, s84
v_mfma_f32_16x16x32_bf16 a[60:63], v[28:31], a[220:223], a[60:63]
v_perm_b32 v18, v214, v210, s84
v_perm_b32 v19, v222, v218, s84
v_mfma_f32_16x16x32_bf16 a[64:67], v[24:27], a[204:207], a[64:67]
v_perm_b32 v20, v230, v226, s84
v_perm_b32 v21, v238, v234, s84
v_mfma_f32_16x16x32_bf16 a[64:67], v[28:31], a[224:227], a[64:67]
v_perm_b32 v22, v70, v66, s84
v_perm_b32 v23, v78, v74, s84
v_mfma_f32_16x16x32_bf16 a[68:71], v[24:27], a[208:211], a[68:71]
v_mfma_f32_16x16x32_bf16 a[68:71], v[28:31], a[228:231], a[68:71]
ds_read_b128 a[168:171], v1 offset:63488
v_mfma_f32_16x16x32_bf16 a[72:75], v[24:27], a[212:215], a[72:75]
ds_read_b128 a[188:191], v2 offset:3072
v_mfma_f32_16x16x32_bf16 a[72:75], v[28:31], a[232:235], a[72:75]
v_mfma_f32_16x16x32_bf16 a[76:79], v[24:27], a[216:219], a[76:79]
v_mfma_f32_16x16x32_bf16 a[76:79], v[28:31], a[236:239], a[76:79]
v_mfma_f32_16x16x32_bf16 a[80:83], v[16:19], a[200:203], a[80:83]
v_perm_b32 v24, v198, v194, s85
v_perm_b32 v25, v206, v202, s85
v_mfma_f32_16x16x32_bf16 a[80:83], v[20:23], a[220:223], a[80:83]
v_perm_b32 v26, v214, v210, s85
v_perm_b32 v27, v222, v218, s85
v_mfma_f32_16x16x32_bf16 a[84:87], v[16:19], a[204:207], a[84:87]
v_perm_b32 v28, v230, v226, s85
v_perm_b32 v29, v238, v234, s85
v_mfma_f32_16x16x32_bf16 a[84:87], v[20:23], a[224:227], a[84:87]
v_perm_b32 v30, v70, v66, s85
v_perm_b32 v31, v78, v74, s85
v_mfma_f32_16x16x32_bf16 a[88:91], v[16:19], a[208:211], a[88:91]
v_mfma_f32_16x16x32_bf16 a[88:91], v[20:23], a[228:231], a[88:91]
v_mfma_f32_16x16x32_bf16 a[92:95], v[16:19], a[212:215], a[92:95]
v_mfma_f32_16x16x32_bf16 a[92:95], v[20:23], a[232:235], a[92:95]
v_mfma_f32_16x16x32_bf16 a[96:99], v[16:19], a[216:219], a[96:99]
v_mfma_f32_16x16x32_bf16 a[96:99], v[20:23], a[236:239], a[96:99]
v_mfma_f32_16x16x32_bf16 a[100:103], v[24:27], a[200:203], a[100:103]
v_perm_b32 v16, v199, v195, s84
v_perm_b32 v17, v207, v203, s84
v_mfma_f32_16x16x32_bf16 a[100:103], v[28:31], a[220:223], a[100:103]
v_perm_b32 v18, v215, v211, s84
v_perm_b32 v19, v223, v219, s84
v_mfma_f32_16x16x32_bf16 a[104:107], v[24:27], a[204:207], a[104:107]
ds_read_b128 a[172:175], v1 offset:64512
v_perm_b32 v20, v231, v227, s84
v_mfma_f32_16x16x32_bf16 a[104:107], v[28:31], a[224:227], a[104:107]
ds_read_b128 a[192:195], v2 offset:4096
v_perm_b32 v21, v239, v235, s84
v_mfma_f32_16x16x32_bf16 a[108:111], v[24:27], a[208:211], a[108:111]
v_perm_b32 v22, v71, v67, s84
v_perm_b32 v23, v79, v75, s84
v_mfma_f32_16x16x32_bf16 a[108:111], v[28:31], a[228:231], a[108:111]
v_mfma_f32_16x16x32_bf16 a[112:115], v[24:27], a[212:215], a[112:115]
v_mfma_f32_16x16x32_bf16 a[112:115], v[28:31], a[232:235], a[112:115]
v_mfma_f32_16x16x32_bf16 a[116:119], v[24:27], a[216:219], a[116:119]
v_mfma_f32_16x16x32_bf16 a[116:119], v[28:31], a[236:239], a[116:119]
v_mfma_f32_16x16x32_bf16 a[120:123], v[16:19], a[200:203], a[120:123]
v_perm_b32 v24, v199, v195, s85
v_perm_b32 v25, v207, v203, s85
v_mfma_f32_16x16x32_bf16 a[120:123], v[20:23], a[220:223], a[120:123]
v_perm_b32 v26, v215, v211, s85
v_perm_b32 v27, v223, v219, s85
v_mfma_f32_16x16x32_bf16 a[124:127], v[16:19], a[204:207], a[124:127]
v_perm_b32 v28, v231, v227, s85
v_perm_b32 v29, v239, v235, s85
v_mfma_f32_16x16x32_bf16 a[124:127], v[20:23], a[224:227], a[124:127]
v_perm_b32 v30, v71, v67, s85
v_perm_b32 v31, v79, v75, s85
v_mfma_f32_16x16x32_bf16 a[128:131], v[16:19], a[208:211], a[128:131]
ds_read_b128 a[176:179], v2 offset:0
v_mfma_f32_16x16x32_bf16 a[128:131], v[20:23], a[228:231], a[128:131]
ds_read_b128 a[196:199], v2 offset:5120
v_mfma_f32_16x16x32_bf16 a[132:135], v[16:19], a[212:215], a[132:135]
v_mfma_f32_16x16x32_bf16 a[132:135], v[20:23], a[232:235], a[132:135]
v_mfma_f32_16x16x32_bf16 a[136:139], v[16:19], a[216:219], a[136:139]
v_mfma_f32_16x16x32_bf16 a[136:139], v[20:23], a[236:239], a[136:139]
v_mfma_f32_16x16x32_bf16 a[140:143], v[24:27], a[200:203], a[140:143]
s_waitcnt vmcnt(24)
v_perm_b32 v16, v84, v80, s84
v_perm_b32 v17, v92, v88, s84
v_mfma_f32_16x16x32_bf16 a[140:143], v[28:31], a[220:223], a[140:143]
v_perm_b32 v18, v244, v240, s84
v_perm_b32 v19, v252, v248, s84
v_mfma_f32_16x16x32_bf16 a[144:147], v[24:27], a[204:207], a[144:147]
s_waitcnt vmcnt(16)
v_perm_b32 v20, v36, v32, s84
v_perm_b32 v21, v44, v40, s84
v_mfma_f32_16x16x32_bf16 a[144:147], v[28:31], a[224:227], a[144:147]
v_perm_b32 v22, v100, v96, s84
v_perm_b32 v23, v108, v104, s84
v_mfma_f32_16x16x32_bf16 a[148:151], v[24:27], a[208:211], a[148:151]
v_mfma_f32_16x16x32_bf16 a[148:151], v[28:31], a[228:231], a[148:151]
v_mfma_f32_16x16x32_bf16 a[152:155], v[24:27], a[212:215], a[152:155]
v_mfma_f32_16x16x32_bf16 a[152:155], v[28:31], a[232:235], a[152:155]
v_mfma_f32_16x16x32_bf16 a[156:159], v[24:27], a[216:219], a[156:159]
v_mfma_f32_16x16x32_bf16 a[156:159], v[28:31], a[236:239], a[156:159]
s_waitcnt lgkmcnt(1)
v_mfma_f32_16x16x32_bf16 a[0:3], v[16:19], a[160:163], a[0:3]
v_perm_b32 v24, v84, v80, s85
v_perm_b32 v25, v92, v88, s85
v_mfma_f32_16x16x32_bf16 a[0:3], v[20:23], a[180:183], a[0:3]
v_perm_b32 v26, v244, v240, s85
v_perm_b32 v27, v252, v248, s85
v_mfma_f32_16x16x32_bf16 a[4:7], v[16:19], a[164:167], a[4:7]
v_perm_b32 v28, v36, v32, s85
v_perm_b32 v29, v44, v40, s85
v_mfma_f32_16x16x32_bf16 a[4:7], v[20:23], a[184:187], a[4:7]
v_perm_b32 v30, v100, v96, s85
v_perm_b32 v31, v108, v104, s85
v_mfma_f32_16x16x32_bf16 a[8:11], v[16:19], a[168:171], a[8:11]
v_mfma_f32_16x16x32_bf16 a[8:11], v[20:23], a[188:191], a[8:11]
v_mfma_f32_16x16x32_bf16 a[12:15], v[16:19], a[172:175], a[12:15]
ds_read_b128 a[200:203], v2 offset:6144
v_mfma_f32_16x16x32_bf16 a[12:15], v[20:23], a[192:195], a[12:15]
ds_read_b128 a[220:223], v2 offset:11264
v_mfma_f32_16x16x32_bf16 a[16:19], v[16:19], a[176:179], a[16:19]
s_waitcnt lgkmcnt(2)
v_mfma_f32_16x16x32_bf16 a[16:19], v[20:23], a[196:199], a[16:19]
v_mfma_f32_16x16x32_bf16 a[20:23], v[24:27], a[160:163], a[20:23]
v_perm_b32 v16, v85, v81, s84
v_perm_b32 v17, v93, v89, s84
v_mfma_f32_16x16x32_bf16 a[20:23], v[28:31], a[180:183], a[20:23]
v_perm_b32 v18, v245, v241, s84
v_perm_b32 v19, v253, v249, s84
v_mfma_f32_16x16x32_bf16 a[24:27], v[24:27], a[164:167], a[24:27]
v_perm_b32 v20, v37, v33, s84
v_perm_b32 v21, v45, v41, s84
v_mfma_f32_16x16x32_bf16 a[24:27], v[28:31], a[184:187], a[24:27]
v_perm_b32 v22, v101, v97, s84
v_perm_b32 v23, v109, v105, s84
v_mfma_f32_16x16x32_bf16 a[28:31], v[24:27], a[168:171], a[28:31]
v_mfma_f32_16x16x32_bf16 a[28:31], v[28:31], a[188:191], a[28:31]
v_mfma_f32_16x16x32_bf16 a[32:35], v[24:27], a[172:175], a[32:35]
v_mfma_f32_16x16x32_bf16 a[32:35], v[28:31], a[192:195], a[32:35]
v_mfma_f32_16x16x32_bf16 a[36:39], v[24:27], a[176:179], a[36:39]
v_mfma_f32_16x16x32_bf16 a[36:39], v[28:31], a[196:199], a[36:39]
v_mfma_f32_16x16x32_bf16 a[40:43], v[16:19], a[160:163], a[40:43]
v_perm_b32 v24, v85, v81, s85
v_perm_b32 v25, v93, v89, s85
v_mfma_f32_16x16x32_bf16 a[40:43], v[20:23], a[180:183], a[40:43]
v_perm_b32 v26, v245, v241, s85
v_perm_b32 v27, v253, v249, s85
v_mfma_f32_16x16x32_bf16 a[44:47], v[16:19], a[164:167], a[44:47]
ds_read_b128 a[204:207], v2 offset:7168
v_perm_b32 v28, v37, v33, s85
v_mfma_f32_16x16x32_bf16 a[44:47], v[20:23], a[184:187], a[44:47]
ds_read_b128 a[224:227], v2 offset:12288
v_perm_b32 v29, v45, v41, s85
v_mfma_f32_16x16x32_bf16 a[48:51], v[16:19], a[168:171], a[48:51]
v_perm_b32 v30, v101, v97, s85
v_perm_b32 v31, v109, v105, s85
v_mfma_f32_16x16x32_bf16 a[48:51], v[20:23], a[188:191], a[48:51]
v_mfma_f32_16x16x32_bf16 a[52:55], v[16:19], a[172:175], a[52:55]
v_mfma_f32_16x16x32_bf16 a[52:55], v[20:23], a[192:195], a[52:55]
v_mfma_f32_16x16x32_bf16 a[56:59], v[16:19], a[176:179], a[56:59]
v_mfma_f32_16x16x32_bf16 a[56:59], v[20:23], a[196:199], a[56:59]
v_mfma_f32_16x16x32_bf16 a[60:63], v[24:27], a[160:163], a[60:63]
v_perm_b32 v16, v86, v82, s84
v_perm_b32 v17, v94, v90, s84
v_mfma_f32_16x16x32_bf16 a[60:63], v[28:31], a[180:183], a[60:63]
v_perm_b32 v18, v246, v242, s84
v_perm_b32 v19, v254, v250, s84
v_mfma_f32_16x16x32_bf16 a[64:67], v[24:27], a[164:167], a[64:67]
v_perm_b32 v20, v38, v34, s84
v_perm_b32 v21, v46, v42, s84
v_mfma_f32_16x16x32_bf16 a[64:67], v[28:31], a[184:187], a[64:67]
v_perm_b32 v22, v102, v98, s84
v_perm_b32 v23, v110, v106, s84
v_mfma_f32_16x16x32_bf16 a[68:71], v[24:27], a[168:171], a[68:71]
v_mfma_f32_16x16x32_bf16 a[68:71], v[28:31], a[188:191], a[68:71]
ds_read_b128 a[208:211], v2 offset:8192
v_mfma_f32_16x16x32_bf16 a[72:75], v[24:27], a[172:175], a[72:75]
ds_read_b128 a[228:231], v2 offset:13312
v_mfma_f32_16x16x32_bf16 a[72:75], v[28:31], a[192:195], a[72:75]
v_mfma_f32_16x16x32_bf16 a[76:79], v[24:27], a[176:179], a[76:79]
v_mfma_f32_16x16x32_bf16 a[76:79], v[28:31], a[196:199], a[76:79]
v_mfma_f32_16x16x32_bf16 a[80:83], v[16:19], a[160:163], a[80:83]
v_perm_b32 v24, v86, v82, s85
v_perm_b32 v25, v94, v90, s85
v_mfma_f32_16x16x32_bf16 a[80:83], v[20:23], a[180:183], a[80:83]
v_perm_b32 v26, v246, v242, s85
v_perm_b32 v27, v254, v250, s85
v_mfma_f32_16x16x32_bf16 a[84:87], v[16:19], a[164:167], a[84:87]
v_perm_b32 v28, v38, v34, s85
v_perm_b32 v29, v46, v42, s85
v_mfma_f32_16x16x32_bf16 a[84:87], v[20:23], a[184:187], a[84:87]
v_perm_b32 v30, v102, v98, s85
v_perm_b32 v31, v110, v106, s85
v_mfma_f32_16x16x32_bf16 a[88:91], v[16:19], a[168:171], a[88:91]
v_mfma_f32_16x16x32_bf16 a[88:91], v[20:23], a[188:191], a[88:91]
v_mfma_f32_16x16x32_bf16 a[92:95], v[16:19], a[172:175], a[92:95]
v_mfma_f32_16x16x32_bf16 a[92:95], v[20:23], a[192:195], a[92:95]
v_mfma_f32_16x16x32_bf16 a[96:99], v[16:19], a[176:179], a[96:99]
v_mfma_f32_16x16x32_bf16 a[96:99], v[20:23], a[196:199], a[96:99]
v_mfma_f32_16x16x32_bf16 a[100:103], v[24:27], a[160:163], a[100:103]
v_perm_b32 v16, v87, v83, s84
v_perm_b32 v17, v95, v91, s84
v_mfma_f32_16x16x32_bf16 a[100:103], v[28:31], a[180:183], a[100:103]
v_perm_b32 v18, v247, v243, s84
v_perm_b32 v19, v255, v251, s84
)"
R"(v_mfma_f32_16x16x32_bf16 a[104:107], v[24:27], a[164:167], a[104:107]
ds_read_b128 a[212:215], v2 offset:9216
v_perm_b32 v20, v39, v35, s84
v_mfma_f32_16x16x32_bf16 a[104:107], v[28:31], a[184:187], a[104:107]
ds_read_b128 a[232:235], v2 offset:14336
v_perm_b32 v21, v47, v43, s84
v_mfma_f32_16x16x32_bf16 a[108:111], v[24:27], a[168:171], a[108:111]
v_perm_b32 v22, v103, v99, s84
v_perm_b32 v23, v111, v107, s84
v_mfma_f32_16x16x32_bf16 a[108:111], v[28:31], a[188:191], a[108:111]
v_mfma_f32_16x16x32_bf16 a[112:115], v[24:27], a[172:175], a[112:115]
v_mfma_f32_16x16x32_bf16 a[112:115], v[28:31], a[192:195], a[112:115]
v_mfma_f32_16x16x32_bf16 a[116:119], v[24:27], a[176:179], a[116:119]
v_mfma_f32_16x16x32_bf16 a[116:119], v[28:31], a[196:199], a[116:119]
v_mfma_f32_16x16x32_bf16 a[120:123], v[16:19], a[160:163], a[120:123]
v_perm_b32 v24, v87, v83, s85
v_perm_b32 v25, v95, v91, s85
v_mfma_f32_16x16x32_bf16 a[120:123], v[20:23], a[180:183], a[120:123]
v_perm_b32 v26, v247, v243, s85
v_perm_b32 v27, v255, v251, s85
v_mfma_f32_16x16x32_bf16 a[124:127], v[16:19], a[164:167], a[124:127]
v_perm_b32 v28, v39, v35, s85
v_perm_b32 v29, v47, v43, s85
v_mfma_f32_16x16x32_bf16 a[124:127], v[20:23], a[184:187], a[124:127]
v_perm_b32 v30, v103, v99, s85
v_perm_b32 v31, v111, v107, s85
v_mfma_f32_16x16x32_bf16 a[128:131], v[16:19], a[168:171], a[128:131]
v_accvgpr_read_b32 v6, a240
ds_read_b128 a[216:219], v2 offset:10240
v_mfma_f32_16x16x32_bf16 a[128:131], v[20:23], a[188:191], a[128:131]
buffer_load_dwordx4 v[32:35], v6, s[44:47], s64 offen
ds_read_b128 a[236:239], v2 offset:15360
v_mfma_f32_16x16x32_bf16 a[132:135], v[16:19], a[172:175], a[132:135]
buffer_load_dwordx4 v[36:39], v6, s[44:47], s65 offen
v_mfma_f32_16x16x32_bf16 a[132:135], v[20:23], a[192:195], a[132:135]
buffer_load_dwordx4 v[40:43], v6, s[44:47], s66 offen
v_mfma_f32_16x16x32_bf16 a[136:139], v[16:19], a[176:179], a[136:139]
buffer_load_dwordx4 v[44:47], v6, s[44:47], s67 offen
v_mfma_f32_16x16x32_bf16 a[136:139], v[20:23], a[196:199], a[136:139]
v_mfma_f32_16x16x32_bf16 a[140:143], v[24:27], a[160:163], a[140:143]
s_waitcnt vmcnt(12)
v_perm_b32 v16, v52, v48, s84
v_perm_b32 v17, v60, v56, s84
v_mfma_f32_16x16x32_bf16 a[140:143], v[28:31], a[180:183], a[140:143]
v_perm_b32 v18, v132, v128, s84
v_perm_b32 v19, v140, v136, s84
v_mfma_f32_16x16x32_bf16 a[144:147], v[24:27], a[164:167], a[144:147]
s_waitcnt vmcnt(4)
v_perm_b32 v20, v148, v144, s84
v_perm_b32 v21, v156, v152, s84
v_mfma_f32_16x16x32_bf16 a[144:147], v[28:31], a[184:187], a[144:147]
v_perm_b32 v22, v116, v112, s84
v_perm_b32 v23, v124, v120, s84
v_mfma_f32_16x16x32_bf16 a[148:151], v[24:27], a[168:171], a[148:151]
v_mfma_f32_16x16x32_bf16 a[148:151], v[28:31], a[188:191], a[148:151]
v_mfma_f32_16x16x32_bf16 a[152:155], v[24:27], a[172:175], a[152:155]
v_mfma_f32_16x16x32_bf16 a[152:155], v[28:31], a[192:195], a[152:155]
v_mfma_f32_16x16x32_bf16 a[156:159], v[24:27], a[176:179], a[156:159]
v_mfma_f32_16x16x32_bf16 a[156:159], v[28:31], a[196:199], a[156:159]
s_waitcnt lgkmcnt(1)
v_mfma_f32_16x16x32_bf16 a[0:3], v[16:19], a[200:203], a[0:3]
v_perm_b32 v24, v52, v48, s85
v_perm_b32 v25, v60, v56, s85
v_mfma_f32_16x16x32_bf16 a[0:3], v[20:23], a[220:223], a[0:3]
v_perm_b32 v26, v132, v128, s85
v_perm_b32 v27, v140, v136, s85
v_mfma_f32_16x16x32_bf16 a[4:7], v[16:19], a[204:207], a[4:7]
v_perm_b32 v28, v148, v144, s85
v_perm_b32 v29, v156, v152, s85
v_mfma_f32_16x16x32_bf16 a[4:7], v[20:23], a[224:227], a[4:7]
v_perm_b32 v30, v116, v112, s85
v_perm_b32 v31, v124, v120, s85
v_mfma_f32_16x16x32_bf16 a[8:11], v[16:19], a[208:211], a[8:11]
v_mfma_f32_16x16x32_bf16 a[8:11], v[20:23], a[228:231], a[8:11]
v_mfma_f32_16x16x32_bf16 a[12:15], v[16:19], a[212:215], a[12:15]
v_mfma_f32_16x16x32_bf16 a[12:15], v[20:23], a[232:235], a[12:15]
v_mfma_f32_16x16x32_bf16 a[16:19], v[16:19], a[216:219], a[16:19]
s_waitcnt lgkmcnt(0)
v_mfma_f32_16x16x32_bf16 a[16:19], v[20:23], a[236:239], a[16:19]
v_mfma_f32_16x16x32_bf16 a[20:23], v[24:27], a[200:203], a[20:23]
v_perm_b32 v16, v53, v49, s84
v_perm_b32 v17, v61, v57, s84
v_mfma_f32_16x16x32_bf16 a[20:23], v[28:31], a[220:223], a[20:23]
v_perm_b32 v18, v133, v129, s84
v_perm_b32 v19, v141, v137, s84
v_mfma_f32_16x16x32_bf16 a[24:27], v[24:27], a[204:207], a[24:27]
v_perm_b32 v20, v149, v145, s84
v_perm_b32 v21, v157, v153, s84
v_mfma_f32_16x16x32_bf16 a[24:27], v[28:31], a[224:227], a[24:27]
v_perm_b32 v22, v117, v113, s84
v_perm_b32 v23, v125, v121, s84
v_mfma_f32_16x16x32_bf16 a[28:31], v[24:27], a[208:211], a[28:31]
v_mfma_f32_16x16x32_bf16 a[28:31], v[28:31], a[228:231], a[28:31]
v_mfma_f32_16x16x32_bf16 a[32:35], v[24:27], a[212:215], a[32:35]
v_mfma_f32_16x16x32_bf16 a[32:35], v[28:31], a[232:235], a[32:35]
v_mfma_f32_16x16x32_bf16 a[36:39], v[24:27], a[216:219], a[36:39]
v_mfma_f32_16x16x32_bf16 a[36:39], v[28:31], a[236:239], a[36:39]
v_mfma_f32_16x16x32_bf16 a[40:43], v[16:19], a[200:203], a[40:43]
v_perm_b32 v24, v53, v49, s85
v_perm_b32 v25, v61, v57, s85
v_mfma_f32_16x16x32_bf16 a[40:43], v[20:23], a[220:223], a[40:43]
v_perm_b32 v26, v133, v129, s85
v_perm_b32 v27, v141, v137, s85
v_mfma_f32_16x16x32_bf16 a[44:47], v[16:19], a[204:207], a[44:47]
v_perm_b32 v28, v149, v145, s85
v_perm_b32 v29, v157, v153, s85
v_mfma_f32_16x16x32_bf16 a[44:47], v[20:23], a[224:227], a[44:47]
v_perm_b32 v30, v117, v113, s85
v_perm_b32 v31, v125, v121, s85
v_mfma_f32_16x16x32_bf16 a[48:51], v[16:19], a[208:211], a[48:51]
v_mfma_f32_16x16x32_bf16 a[48:51], v[20:23], a[228:231], a[48:51]
v_mfma_f32_16x16x32_bf16 a[52:55], v[16:19], a[212:215], a[52:55]
v_mfma_f32_16x16x32_bf16 a[52:55], v[20:23], a[232:235], a[52:55]
v_mfma_f32_16x16x32_bf16 a[56:59], v[16:19], a[216:219], a[56:59]
v_mfma_f32_16x16x32_bf16 a[56:59], v[20:23], a[236:239], a[56:59]
v_mfma_f32_16x16x32_bf16 a[60:63], v[24:27], a[200:203], a[60:63]
v_perm_b32 v16, v54, v50, s84
v_perm_b32 v17, v62, v58, s84
v_mfma_f32_16x16x32_bf16 a[60:63], v[28:31], a[220:223], a[60:63]
v_perm_b32 v18, v134, v130, s84
v_perm_b32 v19, v142, v138, s84
v_mfma_f32_16x16x32_bf16 a[64:67], v[24:27], a[204:207], a[64:67]
v_perm_b32 v20, v150, v146, s84
v_perm_b32 v21, v158, v154, s84
v_mfma_f32_16x16x32_bf16 a[64:67], v[28:31], a[224:227], a[64:67]
v_perm_b32 v22, v118, v114, s84
v_perm_b32 v23, v126, v122, s84
v_mfma_f32_16x16x32_bf16 a[68:71], v[24:27], a[208:211], a[68:71]
v_mfma_f32_16x16x32_bf16 a[68:71], v[28:31], a[228:231], a[68:71]
v_mfma_f32_16x16x32_bf16 a[72:75], v[24:27], a[212:215], a[72:75]
v_mfma_f32_16x16x32_bf16 a[72:75], v[28:31], a[232:235], a[72:75]
v_mfma_f32_16x16x32_bf16 a[76:79], v[24:27], a[216:219], a[76:79]
v_mfma_f32_16x16x32_bf16 a[76:79], v[28:31], a[236:239], a[76:79]
v_mfma_f32_16x16x32_bf16 a[80:83], v[16:19], a[200:203], a[80:83]
v_perm_b32 v24, v54, v50, s85
v_perm_b32 v25, v62, v58, s85
v_mfma_f32_16x16x32_bf16 a[80:83], v[20:23], a[220:223], a[80:83]
v_perm_b32 v26, v134, v130, s85
v_perm_b32 v27, v142, v138, s85
v_mfma_f32_16x16x32_bf16 a[84:87], v[16:19], a[204:207], a[84:87]
v_perm_b32 v28, v150, v146, s85
v_perm_b32 v29, v158, v154, s85
v_mfma_f32_16x16x32_bf16 a[84:87], v[20:23], a[224:227], a[84:87]
v_perm_b32 v30, v118, v114, s85
v_perm_b32 v31, v126, v122, s85
v_mfma_f32_16x16x32_bf16 a[88:91], v[16:19], a[208:211], a[88:91]
v_mfma_f32_16x16x32_bf16 a[88:91], v[20:23], a[228:231], a[88:91]
v_mfma_f32_16x16x32_bf16 a[92:95], v[16:19], a[212:215], a[92:95]
v_mfma_f32_16x16x32_bf16 a[92:95], v[20:23], a[232:235], a[92:95]
v_mfma_f32_16x16x32_bf16 a[96:99], v[16:19], a[216:219], a[96:99]
v_mfma_f32_16x16x32_bf16 a[96:99], v[20:23], a[236:239], a[96:99]
v_mfma_f32_16x16x32_bf16 a[100:103], v[24:27], a[200:203], a[100:103]
v_perm_b32 v16, v55, v51, s84
v_perm_b32 v17, v63, v59, s84
v_mfma_f32_16x16x32_bf16 a[100:103], v[28:31], a[220:223], a[100:103]
v_perm_b32 v18, v135, v131, s84
v_perm_b32 v19, v143, v139, s84
v_mfma_f32_16x16x32_bf16 a[104:107], v[24:27], a[204:207], a[104:107]
v_perm_b32 v20, v151, v147, s84
v_perm_b32 v21, v159, v155, s84
v_mfma_f32_16x16x32_bf16 a[104:107], v[28:31], a[224:227], a[104:107]
v_perm_b32 v22, v119, v115, s84
v_perm_b32 v23, v127, v123, s84
v_mfma_f32_16x16x32_bf16 a[108:111], v[24:27], a[208:211], a[108:111]
v_mfma_f32_16x16x32_bf16 a[108:111], v[28:31], a[228:231], a[108:111]
v_mfma_f32_16x16x32_bf16 a[112:115], v[24:27], a[212:215], a[112:115]
v_mfma_f32_16x16x32_bf16 a[112:115], v[28:31], a[232:235], a[112:115]
v_mfma_f32_16x16x32_bf16 a[116:119], v[24:27], a[216:219], a[116:119]
v_mfma_f32_16x16x32_bf16 a[116:119], v[28:31], a[236:239], a[116:119]
v_mfma_f32_16x16x32_bf16 a[120:123], v[16:19], a[200:203], a[120:123]
v_perm_b32 v24, v55, v51, s85
v_perm_b32 v25, v63, v59, s85
v_mfma_f32_16x16x32_bf16 a[120:123], v[20:23], a[220:223], a[120:123]
v_perm_b32 v26, v135, v131, s85
v_perm_b32 v27, v143, v139, s85
v_mfma_f32_16x16x32_bf16 a[124:127], v[16:19], a[204:207], a[124:127]
v_perm_b32 v28, v151, v147, s85
v_perm_b32 v29, v159, v155, s85
v_mfma_f32_16x16x32_bf16 a[124:127], v[20:23], a[224:227], a[124:127]
v_perm_b32 v30, v119, v115, s85
v_perm_b32 v31, v127, v123, s85
v_mfma_f32_16x16x32_bf16 a[128:131], v[16:19], a[208:211], a[128:131]
buffer_load_dwordx4 v[48:51], v6, s[44:47], s68 offen
v_mfma_f32_16x16x32_bf16 a[128:131], v[20:23], a[228:231], a[128:131]
buffer_load_dwordx4 v[52:55], v6, s[44:47], s69 offen
v_mfma_f32_16x16x32_bf16 a[132:135], v[16:19], a[212:215], a[132:135]
buffer_load_dwordx4 v[56:59], v6, s[44:47], s70 offen
v_mfma_f32_16x16x32_bf16 a[132:135], v[20:23], a[232:235], a[132:135]
buffer_load_dwordx4 v[60:63], v6, s[44:47], s71 offen
v_mfma_f32_16x16x32_bf16 a[136:139], v[16:19], a[216:219], a[136:139]
v_add_u32 v6, 0x40000, v6
buffer_load_dwordx4 v[64:67], v6, s[44:47], s64 offen
v_mfma_f32_16x16x32_bf16 a[136:139], v[20:23], a[236:239], a[136:139]
buffer_load_dwordx4 v[68:71], v6, s[44:47], s65 offen
v_mfma_f32_16x16x32_bf16 a[140:143], v[24:27], a[200:203], a[140:143]
buffer_load_dwordx4 v[72:75], v6, s[44:47], s66 offen
v_mfma_f32_16x16x32_bf16 a[140:143], v[28:31], a[220:223], a[140:143]
buffer_load_dwordx4 v[76:79], v6, s[44:47], s67 offen
v_mfma_f32_16x16x32_bf16 a[144:147], v[24:27], a[204:207], a[144:147]
buffer_load_dwordx4 v[80:83], v6, s[44:47], s68 offen
)"
R"(v_mfma_f32_16x16x32_bf16 a[144:147], v[28:31], a[224:227], a[144:147]
buffer_load_dwordx4 v[84:87], v6, s[44:47], s69 offen
v_mfma_f32_16x16x32_bf16 a[148:151], v[24:27], a[208:211], a[148:151]
buffer_load_dwordx4 v[88:91], v6, s[44:47], s70 offen
v_mfma_f32_16x16x32_bf16 a[148:151], v[28:31], a[228:231], a[148:151]
buffer_load_dwordx4 v[92:95], v6, s[44:47], s71 offen
v_mfma_f32_16x16x32_bf16 a[152:155], v[24:27], a[212:215], a[152:155]
s_mov_b64 s[36:37], %[k1]
v_mfma_f32_16x16x32_bf16 a[152:155], v[28:31], a[232:235], a[152:155]
s_and_b32 s37, s37, 0xffff
v_mfma_f32_16x16x32_bf16 a[156:159], v[24:27], a[216:219], a[156:159]
s_lshr_b32 s38, s61, 15
s_xor_b32 s38, s38, 1
s_lshl_b32 s38, s38, 18
s_add_u32 s38, s38, 262144
s_cmp_eq_u32 s90, 0
s_cselect_b32 s38, s38, 0
v_mfma_f32_16x16x32_bf16 a[156:159], v[28:31], a[236:239], a[156:159]
s_branch Ltail_%=
LbodyB_%=:
ds_read_b128 a[160:163], v1 offset:0
ds_read_b128 a[164:167], v1 offset:1024
ds_read_b128 a[168:171], v1 offset:2048
ds_read_b128 a[172:175], v1 offset:3072
ds_read_b128 a[176:179], v1 offset:4096
ds_read_b128 a[180:183], v1 offset:5120
ds_read_b128 a[184:187], v1 offset:6144
ds_read_b128 a[188:191], v1 offset:7168
ds_read_b128 a[192:195], v1 offset:8192
ds_read_b128 a[196:199], v1 offset:9216
s_waitcnt lgkmcnt(0)
v_mfma_f32_16x16x32_bf16 v[96:99], v[16:19], a[160:163], 0
v_cndmask_b32_dpp v20, v56, v60, vcc quad_perm:[1,0,3,2] row_mask:0xf bank_mask:0xf
v_cndmask_b32_dpp v21, v57, v61, vcc quad_perm:[1,0,3,2] row_mask:0xf bank_mask:0xf
v_mfma_f32_16x16x32_bf16 v[96:99], v[52:55], a[180:183], v[96:99]
v_cndmask_b32_dpp v22, v58, v62, vcc quad_perm:[1,0,3,2] row_mask:0xf bank_mask:0xf
v_cndmask_b32_dpp v23, v59, v63, vcc quad_perm:[1,0,3,2] row_mask:0xf bank_mask:0xf
v_mfma_f32_16x16x32_bf16 v[112:115], v[16:19], a[164:167], 0
s_mov_b64 vcc, s[76:77]
v_cndmask_b32_dpp v56, v60, v56, vcc quad_perm:[1,0,3,2] row_mask:0xf bank_mask:0xf
v_cndmask_b32_dpp v57, v61, v57, vcc quad_perm:[1,0,3,2] row_mask:0xf bank_mask:0xf
v_mfma_f32_16x16x32_bf16 v[112:115], v[52:55], a[184:187], v[112:115]
ds_read_b128 a[200:203], v1 offset:10240
v_cndmask_b32_dpp v58, v62, v58, vcc quad_perm:[1,0,3,2] row_mask:0xf bank_mask:0xf
v_mfma_f32_16x16x32_bf16 v[128:131], v[16:19], a[168:171], 0
ds_read_b128 a[204:207], v1 offset:11264
v_cndmask_b32_dpp v59, v63, v59, vcc quad_perm:[1,0,3,2] row_mask:0xf bank_mask:0xf
v_mfma_f32_16x16x32_bf16 v[128:131], v[52:55], a[188:191], v[128:131]
ds_read_b128 a[208:211], v1 offset:12288
v_mfma_f32_16x16x32_bf16 v[144:147], v[16:19], a[172:175], 0
ds_read_b128 a[212:215], v1 offset:13312
v_mfma_f32_16x16x32_bf16 v[144:147], v[52:55], a[192:195], v[144:147]
ds_read_b128 a[216:219], v1 offset:14336
v_mfma_f32_16x16x32_bf16 v[160:163], v[16:19], a[176:179], 0
ds_read_b128 a[220:223], v1 offset:15360
v_mfma_f32_16x16x32_bf16 v[160:163], v[52:55], a[196:199], v[160:163]
ds_read_b128 a[224:227], v1 offset:16384
v_mfma_f32_16x16x32_bf16 v[100:103], v[56:59], a[160:163], 0
s_waitcnt vmcnt(24)
v_cndmask_b32_dpp v24, v68, v64, vcc quad_perm:[1,0,3,2] row_mask:0xf bank_mask:0xf
v_cndmask_b32_dpp v25, v69, v65, vcc quad_perm:[1,0,3,2] row_mask:0xf bank_mask:0xf
v_mfma_f32_16x16x32_bf16 v[100:103], v[20:23], a[180:183], v[100:103]
ds_read_b128 a[228:231], v1 offset:17408
v_cndmask_b32_dpp v26, v70, v66, vcc quad_perm:[1,0,3,2] row_mask:0xf bank_mask:0xf
v_mfma_f32_16x16x32_bf16 v[116:119], v[56:59], a[164:167], 0
ds_read_b128 a[232:235], v1 offset:18432
v_cndmask_b32_dpp v27, v71, v67, vcc quad_perm:[1,0,3,2] row_mask:0xf bank_mask:0xf
v_mfma_f32_16x16x32_bf16 v[116:119], v[20:23], a[184:187], v[116:119]
ds_read_b128 a[236:239], v1 offset:19456
s_mov_b64 vcc, s[78:79]
v_cndmask_b32_dpp v68, v64, v68, vcc quad_perm:[1,0,3,2] row_mask:0xf bank_mask:0xf
v_mfma_f32_16x16x32_bf16 v[132:135], v[56:59], a[168:171], 0
v_cndmask_b32_dpp v69, v65, v69, vcc quad_perm:[1,0,3,2] row_mask:0xf bank_mask:0xf
v_cndmask_b32_dpp v70, v66, v70, vcc quad_perm:[1,0,3,2] row_mask:0xf bank_mask:0xf
v_mfma_f32_16x16x32_bf16 v[132:135], v[20:23], a[188:191], v[132:135]
v_cndmask_b32_dpp v71, v67, v71, vcc quad_perm:[1,0,3,2] row_mask:0xf bank_mask:0xf
v_mfma_f32_16x16x32_bf16 v[148:151], v[56:59], a[172:175], 0
v_mfma_f32_16x16x32_bf16 v[148:151], v[20:23], a[192:195], v[148:151]
v_mfma_f32_16x16x32_bf16 v[164:167], v[56:59], a[176:179], 0
buffer_load_dwordx4 v[48:51], v4, s[36:39], s74 offen nt
v_mfma_f32_16x16x32_bf16 v[164:167], v[20:23], a[196:199], v[164:167]
buffer_load_dwordx4 v[52:55], v4, s[36:39], s74 offen offset:512 nt
s_waitcnt lgkmcnt(0)
v_mfma_f32_16x16x32_bf16 v[96:99], v[24:27], a[200:203], v[96:99]
v_cndmask_b32_dpp v28, v72, v76, vcc quad_perm:[1,0,3,2] row_mask:0xf bank_mask:0xf
v_cndmask_b32_dpp v29, v73, v77, vcc quad_perm:[1,0,3,2] row_mask:0xf bank_mask:0xf
v_mfma_f32_16x16x32_bf16 v[96:99], v[68:71], a[220:223], v[96:99]
v_cndmask_b32_dpp v30, v74, v78, vcc quad_perm:[1,0,3,2] row_mask:0xf bank_mask:0xf
v_cndmask_b32_dpp v31, v75, v79, vcc quad_perm:[1,0,3,2] row_mask:0xf bank_mask:0xf
v_mfma_f32_16x16x32_bf16 v[112:115], v[24:27], a[204:207], v[112:115]
s_mov_b64 vcc, s[76:77]
v_cndmask_b32_dpp v72, v76, v72, vcc quad_perm:[1,0,3,2] row_mask:0xf bank_mask:0xf
v_cndmask_b32_dpp v73, v77, v73, vcc quad_perm:[1,0,3,2] row_mask:0xf bank_mask:0xf
v_mfma_f32_16x16x32_bf16 v[112:115], v[68:71], a[224:227], v[112:115]
v_cndmask_b32_dpp v74, v78, v74, vcc quad_perm:[1,0,3,2] row_mask:0xf bank_mask:0xf
v_cndmask_b32_dpp v75, v79, v75, vcc quad_perm:[1,0,3,2] row_mask:0xf bank_mask:0xf
v_mfma_f32_16x16x32_bf16 v[128:131], v[24:27], a[208:211], v[128:131]
buffer_load_dwordx4 v[56:59], v4, s[36:39], s74 offen offset:2048 nt
v_mfma_f32_16x16x32_bf16 v[128:131], v[68:71], a[228:231], v[128:131]
buffer_load_dwordx4 v[60:63], v4, s[36:39], s74 offen offset:2560 nt
v_mfma_f32_16x16x32_bf16 v[144:147], v[24:27], a[212:215], v[144:147]
v_mfma_f32_16x16x32_bf16 v[144:147], v[68:71], a[232:235], v[144:147]
v_mfma_f32_16x16x32_bf16 v[160:163], v[24:27], a[216:219], v[160:163]
v_mfma_f32_16x16x32_bf16 v[160:163], v[68:71], a[236:239], v[160:163]
v_mfma_f32_16x16x32_bf16 v[100:103], v[72:75], a[200:203], v[100:103]
s_waitcnt vmcnt(24)
v_cndmask_b32_dpp v16, v84, v80, vcc quad_perm:[1,0,3,2] row_mask:0xf bank_mask:0xf
v_cndmask_b32_dpp v17, v85, v81, vcc quad_perm:[1,0,3,2] row_mask:0xf bank_mask:0xf
v_mfma_f32_16x16x32_bf16 v[100:103], v[28:31], a[220:223], v[100:103]
v_cndmask_b32_dpp v18, v86, v82, vcc quad_perm:[1,0,3,2] row_mask:0xf bank_mask:0xf
v_cndmask_b32_dpp v19, v87, v83, vcc quad_perm:[1,0,3,2] row_mask:0xf bank_mask:0xf
v_mfma_f32_16x16x32_bf16 v[116:119], v[72:75], a[204:207], v[116:119]
s_mov_b64 vcc, s[78:79]
v_cndmask_b32_dpp v84, v80, v84, vcc quad_perm:[1,0,3,2] row_mask:0xf bank_mask:0xf
v_cndmask_b32_dpp v85, v81, v85, vcc quad_perm:[1,0,3,2] row_mask:0xf bank_mask:0xf
v_mfma_f32_16x16x32_bf16 v[116:119], v[28:31], a[224:227], v[116:119]
v_cndmask_b32_dpp v86, v82, v86, vcc quad_perm:[1,0,3,2] row_mask:0xf bank_mask:0xf
v_cndmask_b32_dpp v87, v83, v87, vcc quad_perm:[1,0,3,2] row_mask:0xf bank_mask:0xf
v_mfma_f32_16x16x32_bf16 v[132:135], v[72:75], a[208:211], v[132:135]
v_mfma_f32_16x16x32_bf16 v[132:135], v[28:31], a[228:231], v[132:135]
v_mfma_f32_16x16x32_bf16 v[148:151], v[72:75], a[212:215], v[148:151]
v_mfma_f32_16x16x32_bf16 v[148:151], v[28:31], a[232:235], v[148:151]
v_mfma_f32_16x16x32_bf16 v[164:167], v[72:75], a[216:219], v[164:167]
buffer_load_dwordx4 v[64:67], v4, s[36:39], s74 offen offset:128 nt
v_mfma_f32_16x16x32_bf16 v[164:167], v[28:31], a[236:239], v[164:167]
buffer_load_dwordx4 v[68:71], v4, s[36:39], s74 offen offset:640 nt
v_mfma_f32_16x16x32_bf16 v[104:107], v[16:19], a[160:163], 0
v_cndmask_b32_dpp v20, v88, v92, vcc quad_perm:[1,0,3,2] row_mask:0xf bank_mask:0xf
v_cndmask_b32_dpp v21, v89, v93, vcc quad_perm:[1,0,3,2] row_mask:0xf bank_mask:0xf
v_mfma_f32_16x16x32_bf16 v[104:107], v[84:87], a[180:183], v[104:107]
v_cndmask_b32_dpp v22, v90, v94, vcc quad_perm:[1,0,3,2] row_mask:0xf bank_mask:0xf
v_cndmask_b32_dpp v23, v91, v95, vcc quad_perm:[1,0,3,2] row_mask:0xf bank_mask:0xf
v_mfma_f32_16x16x32_bf16 v[120:123], v[16:19], a[164:167], 0
s_mov_b64 vcc, s[76:77]
v_cndmask_b32_dpp v88, v92, v88, vcc quad_perm:[1,0,3,2] row_mask:0xf bank_mask:0xf
v_cndmask_b32_dpp v89, v93, v89, vcc quad_perm:[1,0,3,2] row_mask:0xf bank_mask:0xf
v_mfma_f32_16x16x32_bf16 v[120:123], v[84:87], a[184:187], v[120:123]
v_cndmask_b32_dpp v90, v94, v90, vcc quad_perm:[1,0,3,2] row_mask:0xf bank_mask:0xf
v_cndmask_b32_dpp v91, v95, v91, vcc quad_perm:[1,0,3,2] row_mask:0xf bank_mask:0xf
v_mfma_f32_16x16x32_bf16 v[136:139], v[16:19], a[168:171], 0
buffer_load_dwordx4 v[72:75], v4, s[36:39], s74 offen offset:2176 nt
v_mfma_f32_16x16x32_bf16 v[136:139], v[84:87], a[188:191], v[136:139]
buffer_load_dwordx4 v[76:79], v4, s[36:39], s74 offen offset:2688 nt
v_mfma_f32_16x16x32_bf16 v[152:155], v[16:19], a[172:175], 0
v_mfma_f32_16x16x32_bf16 v[152:155], v[84:87], a[192:195], v[152:155]
v_mfma_f32_16x16x32_bf16 v[168:171], v[16:19], a[176:179], 0
v_mfma_f32_16x16x32_bf16 v[168:171], v[84:87], a[196:199], v[168:171]
v_mfma_f32_16x16x32_bf16 v[108:111], v[88:91], a[160:163], 0
s_waitcnt vmcnt(24)
v_cndmask_b32_dpp v24, v244, v240, vcc quad_perm:[1,0,3,2] row_mask:0xf bank_mask:0xf
v_cndmask_b32_dpp v25, v245, v241, vcc quad_perm:[1,0,3,2] row_mask:0xf bank_mask:0xf
v_mfma_f32_16x16x32_bf16 v[108:111], v[20:23], a[180:183], v[108:111]
v_cndmask_b32_dpp v26, v246, v242, vcc quad_perm:[1,0,3,2] row_mask:0xf bank_mask:0xf
v_cndmask_b32_dpp v27, v247, v243, vcc quad_perm:[1,0,3,2] row_mask:0xf bank_mask:0xf
v_mfma_f32_16x16x32_bf16 v[124:127], v[88:91], a[164:167], 0
s_mov_b64 vcc, s[78:79]
v_cndmask_b32_dpp v244, v240, v244, vcc quad_perm:[1,0,3,2] row_mask:0xf bank_mask:0xf
v_cndmask_b32_dpp v245, v241, v245, vcc quad_perm:[1,0,3,2] row_mask:0xf bank_mask:0xf
v_mfma_f32_16x16x32_bf16 v[124:127], v[20:23], a[184:187], v[124:127]
v_cndmask_b32_dpp v246, v242, v246, vcc quad_perm:[1,0,3,2] row_mask:0xf bank_mask:0xf
v_cndmask_b32_dpp v247, v243, v247, vcc quad_perm:[1,0,3,2] row_mask:0xf bank_mask:0xf
v_mfma_f32_16x16x32_bf16 v[140:143], v[88:91], a[168:171], 0
v_mfma_f32_16x16x32_bf16 v[140:143], v[20:23], a[188:191], v[140:143]
v_mfma_f32_16x16x32_bf16 v[156:159], v[88:91], a[172:175], 0
v_mfma_f32_16x16x32_bf16 v[156:159], v[20:23], a[192:195], v[156:159]
v_mfma_f32_16x16x32_bf16 v[172:175], v[88:91], a[176:179], 0
buffer_load_dwordx4 v[80:83], v4, s[36:39], s75 offen nt
v_mfma_f32_16x16x32_bf16 v[172:175], v[20:23], a[196:199], v[172:175]
buffer_load_dwordx4 v[84:87], v4, s[36:39], s75 offen offset:512 nt
v_mfma_f32_16x16x32_bf16 v[104:107], v[24:27], a[200:203], v[104:107]
v_cndmask_b32_dpp v28, v248, v252, vcc quad_perm:[1,0,3,2] row_mask:0xf bank_mask:0xf
v_cndmask_b32_dpp v29, v249, v253, vcc quad_perm:[1,0,3,2] row_mask:0xf bank_mask:0xf
v_mfma_f32_16x16x32_bf16 v[104:107], v[244:247], a[220:223], v[104:107]
v_cndmask_b32_dpp v30, v250, v254, vcc quad_perm:[1,0,3,2] row_mask:0xf bank_mask:0xf
v_cndmask_b32_dpp v31, v251, v255, vcc quad_perm:[1,0,3,2] row_mask:0xf bank_mask:0xf
v_mfma_f32_16x16x32_bf16 v[120:123], v[24:27], a[204:207], v[120:123]
ds_read_b128 a[160:163], v1 offset:20480
s_mov_b64 vcc, s[76:77]
v_cndmask_b32_dpp v248, v252, v248, vcc quad_perm:[1,0,3,2] row_mask:0xf bank_mask:0xf
v_mfma_f32_16x16x32_bf16 v[120:123], v[244:247], a[224:227], v[120:123]
ds_read_b128 a[164:167], v1 offset:21504
v_cndmask_b32_dpp v249, v253, v249, vcc quad_perm:[1,0,3,2] row_mask:0xf bank_mask:0xf
v_mfma_f32_16x16x32_bf16 v[136:139], v[24:27], a[208:211], v[136:139]
ds_read_b128 a[168:171], v1 offset:22528
v_cndmask_b32_dpp v250, v254, v250, vcc quad_perm:[1,0,3,2] row_mask:0xf bank_mask:0xf
v_mfma_f32_16x16x32_bf16 v[136:139], v[244:247], a[228:231], v[136:139]
ds_read_b128 a[172:175], v1 offset:23552
v_cndmask_b32_dpp v251, v255, v251, vcc quad_perm:[1,0,3,2] row_mask:0xf bank_mask:0xf
v_mfma_f32_16x16x32_bf16 v[152:155], v[24:27], a[212:215], v[152:155]
ds_read_b128 a[176:179], v1 offset:24576
v_mfma_f32_16x16x32_bf16 v[152:155], v[244:247], a[232:235], v[152:155]
ds_read_b128 a[180:183], v1 offset:25600
v_mfma_f32_16x16x32_bf16 v[168:171], v[24:27], a[216:219], v[168:171]
ds_read_b128 a[184:187], v1 offset:26624
v_mfma_f32_16x16x32_bf16 v[168:171], v[244:247], a[236:239], v[168:171]
ds_read_b128 a[188:191], v1 offset:27648
v_mfma_f32_16x16x32_bf16 v[108:111], v[248:251], a[200:203], v[108:111]
s_waitcnt vmcnt(22)
v_cndmask_b32_dpp v16, v180, v176, vcc quad_perm:[1,0,3,2] row_mask:0xf bank_mask:0xf
v_cndmask_b32_dpp v17, v181, v177, vcc quad_perm:[1,0,3,2] row_mask:0xf bank_mask:0xf
)"
R"(v_mfma_f32_16x16x32_bf16 v[108:111], v[28:31], a[220:223], v[108:111]
v_cndmask_b32_dpp v18, v182, v178, vcc quad_perm:[1,0,3,2] row_mask:0xf bank_mask:0xf
v_cndmask_b32_dpp v19, v183, v179, vcc quad_perm:[1,0,3,2] row_mask:0xf bank_mask:0xf
v_mfma_f32_16x16x32_bf16 v[124:127], v[248:251], a[204:207], v[124:127]
ds_read_b128 a[192:195], v1 offset:28672
s_mov_b64 vcc, s[78:79]
v_cndmask_b32_dpp v180, v176, v180, vcc quad_perm:[1,0,3,2] row_mask:0xf bank_mask:0xf
v_mfma_f32_16x16x32_bf16 v[124:127], v[28:31], a[224:227], v[124:127]
ds_read_b128 a[196:199], v1 offset:29696
v_cndmask_b32_dpp v181, v177, v181, vcc quad_perm:[1,0,3,2] row_mask:0xf bank_mask:0xf
v_mfma_f32_16x16x32_bf16 v[140:143], v[248:251], a[208:211], v[140:143]
v_cndmask_b32_dpp v182, v178, v182, vcc quad_perm:[1,0,3,2] row_mask:0xf bank_mask:0xf
v_cndmask_b32_dpp v183, v179, v183, vcc quad_perm:[1,0,3,2] row_mask:0xf bank_mask:0xf
v_mfma_f32_16x16x32_bf16 v[140:143], v[28:31], a[228:231], v[140:143]
buffer_load_dwordx4 v[88:91], v4, s[36:39], s75 offen offset:2048 nt
v_mfma_f32_16x16x32_bf16 v[156:159], v[248:251], a[212:215], v[156:159]
buffer_load_dwordx4 v[92:95], v4, s[36:39], s75 offen offset:2560 nt
v_mfma_f32_16x16x32_bf16 v[156:159], v[28:31], a[232:235], v[156:159]
v_mfma_f32_16x16x32_bf16 v[172:175], v[248:251], a[216:219], v[172:175]
v_mfma_f32_16x16x32_bf16 v[172:175], v[28:31], a[236:239], v[172:175]
s_waitcnt lgkmcnt(0)
v_mfma_f32_16x16x32_bf16 v[96:99], v[16:19], a[160:163], v[96:99]
v_cndmask_b32_dpp v20, v184, v188, vcc quad_perm:[1,0,3,2] row_mask:0xf bank_mask:0xf
v_cndmask_b32_dpp v21, v185, v189, vcc quad_perm:[1,0,3,2] row_mask:0xf bank_mask:0xf
v_mfma_f32_16x16x32_bf16 v[96:99], v[180:183], a[180:183], v[96:99]
v_cndmask_b32_dpp v22, v186, v190, vcc quad_perm:[1,0,3,2] row_mask:0xf bank_mask:0xf
v_cndmask_b32_dpp v23, v187, v191, vcc quad_perm:[1,0,3,2] row_mask:0xf bank_mask:0xf
v_mfma_f32_16x16x32_bf16 v[112:115], v[16:19], a[164:167], v[112:115]
ds_read_b128 a[200:203], v1 offset:30720
s_mov_b64 vcc, s[76:77]
v_cndmask_b32_dpp v184, v188, v184, vcc quad_perm:[1,0,3,2] row_mask:0xf bank_mask:0xf
v_mfma_f32_16x16x32_bf16 v[112:115], v[180:183], a[184:187], v[112:115]
ds_read_b128 a[204:207], v1 offset:31744
v_cndmask_b32_dpp v185, v189, v185, vcc quad_perm:[1,0,3,2] row_mask:0xf bank_mask:0xf
v_mfma_f32_16x16x32_bf16 v[128:131], v[16:19], a[168:171], v[128:131]
ds_read_b128 a[208:211], v1 offset:32768
v_cndmask_b32_dpp v186, v190, v186, vcc quad_perm:[1,0,3,2] row_mask:0xf bank_mask:0xf
v_mfma_f32_16x16x32_bf16 v[128:131], v[180:183], a[188:191], v[128:131]
ds_read_b128 a[212:215], v1 offset:33792
v_cndmask_b32_dpp v187, v191, v187, vcc quad_perm:[1,0,3,2] row_mask:0xf bank_mask:0xf
v_mfma_f32_16x16x32_bf16 v[144:147], v[16:19], a[172:175], v[144:147]
ds_read_b128 a[216:219], v1 offset:34816
v_mfma_f32_16x16x32_bf16 v[144:147], v[180:183], a[192:195], v[144:147]
ds_read_b128 a[220:223], v1 offset:35840
v_mfma_f32_16x16x32_bf16 v[160:163], v[16:19], a[176:179], v[160:163]
ds_read_b128 a[224:227], v1 offset:36864
v_mfma_f32_16x16x32_bf16 v[160:163], v[180:183], a[196:199], v[160:163]
ds_read_b128 a[228:231], v1 offset:37888
v_mfma_f32_16x16x32_bf16 v[100:103], v[184:187], a[160:163], v[100:103]
s_waitcnt vmcnt(20)
v_cndmask_b32_dpp v24, v196, v192, vcc quad_perm:[1,0,3,2] row_mask:0xf bank_mask:0xf
v_cndmask_b32_dpp v25, v197, v193, vcc quad_perm:[1,0,3,2] row_mask:0xf bank_mask:0xf
v_mfma_f32_16x16x32_bf16 v[100:103], v[20:23], a[180:183], v[100:103]
v_cndmask_b32_dpp v26, v198, v194, vcc quad_perm:[1,0,3,2] row_mask:0xf bank_mask:0xf
v_cndmask_b32_dpp v27, v199, v195, vcc quad_perm:[1,0,3,2] row_mask:0xf bank_mask:0xf
v_mfma_f32_16x16x32_bf16 v[116:119], v[184:187], a[164:167], v[116:119]
ds_read_b128 a[232:235], v1 offset:38912
s_mov_b64 vcc, s[78:79]
v_cndmask_b32_dpp v196, v192, v196, vcc quad_perm:[1,0,3,2] row_mask:0xf bank_mask:0xf
v_mfma_f32_16x16x32_bf16 v[116:119], v[20:23], a[184:187], v[116:119]
ds_read_b128 a[236:239], v1 offset:39936
v_cndmask_b32_dpp v197, v193, v197, vcc quad_perm:[1,0,3,2] row_mask:0xf bank_mask:0xf
v_mfma_f32_16x16x32_bf16 v[132:135], v[184:187], a[168:171], v[132:135]
v_cndmask_b32_dpp v198, v194, v198, vcc quad_perm:[1,0,3,2] row_mask:0xf bank_mask:0xf
v_cndmask_b32_dpp v199, v195, v199, vcc quad_perm:[1,0,3,2] row_mask:0xf bank_mask:0xf
v_mfma_f32_16x16x32_bf16 v[132:135], v[20:23], a[188:191], v[132:135]
v_mfma_f32_16x16x32_bf16 v[148:151], v[184:187], a[172:175], v[148:151]
v_mfma_f32_16x16x32_bf16 v[148:151], v[20:23], a[192:195], v[148:151]
v_mfma_f32_16x16x32_bf16 v[164:167], v[184:187], a[176:179], v[164:167]
v_mfma_f32_16x16x32_bf16 v[164:167], v[20:23], a[196:199], v[164:167]
s_waitcnt lgkmcnt(0)
v_mfma_f32_16x16x32_bf16 v[96:99], v[24:27], a[200:203], v[96:99]
v_cndmask_b32_dpp v28, v200, v204, vcc quad_perm:[1,0,3,2] row_mask:0xf bank_mask:0xf
v_cndmask_b32_dpp v29, v201, v205, vcc quad_perm:[1,0,3,2] row_mask:0xf bank_mask:0xf
v_mfma_f32_16x16x32_bf16 v[96:99], v[196:199], a[220:223], v[96:99]
v_cndmask_b32_dpp v30, v202, v206, vcc quad_perm:[1,0,3,2] row_mask:0xf bank_mask:0xf
v_cndmask_b32_dpp v31, v203, v207, vcc quad_perm:[1,0,3,2] row_mask:0xf bank_mask:0xf
v_mfma_f32_16x16x32_bf16 v[112:115], v[24:27], a[204:207], v[112:115]
s_mov_b64 vcc, s[76:77]
v_cndmask_b32_dpp v200, v204, v200, vcc quad_perm:[1,0,3,2] row_mask:0xf bank_mask:0xf
v_cndmask_b32_dpp v201, v205, v201, vcc quad_perm:[1,0,3,2] row_mask:0xf bank_mask:0xf
v_mfma_f32_16x16x32_bf16 v[112:115], v[196:199], a[224:227], v[112:115]
v_cndmask_b32_dpp v202, v206, v202, vcc quad_perm:[1,0,3,2] row_mask:0xf bank_mask:0xf
v_cndmask_b32_dpp v203, v207, v203, vcc quad_perm:[1,0,3,2] row_mask:0xf bank_mask:0xf
v_mfma_f32_16x16x32_bf16 v[128:131], v[24:27], a[208:211], v[128:131]
v_mfma_f32_16x16x32_bf16 v[128:131], v[196:199], a[228:231], v[128:131]
v_mfma_f32_16x16x32_bf16 v[144:147], v[24:27], a[212:215], v[144:147]
v_mfma_f32_16x16x32_bf16 v[144:147], v[196:199], a[232:235], v[144:147]
v_mfma_f32_16x16x32_bf16 v[160:163], v[24:27], a[216:219], v[160:163]
v_mfma_f32_16x16x32_bf16 v[160:163], v[196:199], a[236:239], v[160:163]
v_mfma_f32_16x16x32_bf16 v[100:103], v[200:203], a[200:203], v[100:103]
s_waitcnt vmcnt(16)
v_cndmask_b32_dpp v16, v36, v32, vcc quad_perm:[1,0,3,2] row_mask:0xf bank_mask:0xf
v_cndmask_b32_dpp v17, v37, v33, vcc quad_perm:[1,0,3,2] row_mask:0xf bank_mask:0xf
v_mfma_f32_16x16x32_bf16 v[100:103], v[28:31], a[220:223], v[100:103]
v_cndmask_b32_dpp v18, v38, v34, vcc quad_perm:[1,0,3,2] row_mask:0xf bank_mask:0xf
v_cndmask_b32_dpp v19, v39, v35, vcc quad_perm:[1,0,3,2] row_mask:0xf bank_mask:0xf
v_mfma_f32_16x16x32_bf16 v[116:119], v[200:203], a[204:207], v[116:119]
s_mov_b64 vcc, s[78:79]
v_cndmask_b32_dpp v36, v32, v36, vcc quad_perm:[1,0,3,2] row_mask:0xf bank_mask:0xf
v_cndmask_b32_dpp v37, v33, v37, vcc quad_perm:[1,0,3,2] row_mask:0xf bank_mask:0xf
v_mfma_f32_16x16x32_bf16 v[116:119], v[28:31], a[224:227], v[116:119]
v_cndmask_b32_dpp v38, v34, v38, vcc quad_perm:[1,0,3,2] row_mask:0xf bank_mask:0xf
v_cndmask_b32_dpp v39, v35, v39, vcc quad_perm:[1,0,3,2] row_mask:0xf bank_mask:0xf
v_mfma_f32_16x16x32_bf16 v[132:135], v[200:203], a[208:211], v[132:135]
v_mfma_f32_16x16x32_bf16 v[132:135], v[28:31], a[228:231], v[132:135]
v_mfma_f32_16x16x32_bf16 v[148:151], v[200:203], a[212:215], v[148:151]
v_mfma_f32_16x16x32_bf16 v[148:151], v[28:31], a[232:235], v[148:151]
v_mfma_f32_16x16x32_bf16 v[164:167], v[200:203], a[216:219], v[164:167]
v_mfma_f32_16x16x32_bf16 v[164:167], v[28:31], a[236:239], v[164:167]
v_mfma_f32_16x16x32_bf16 v[104:107], v[16:19], a[160:163], v[104:107]
v_cndmask_b32_dpp v20, v40, v44, vcc quad_perm:[1,0,3,2] row_mask:0xf bank_mask:0xf
v_cndmask_b32_dpp v21, v41, v45, vcc quad_perm:[1,0,3,2] row_mask:0xf bank_mask:0xf
v_mfma_f32_16x16x32_bf16 v[104:107], v[36:39], a[180:183], v[104:107]
v_cndmask_b32_dpp v22, v42, v46, vcc quad_perm:[1,0,3,2] row_mask:0xf bank_mask:0xf
v_cndmask_b32_dpp v23, v43, v47, vcc quad_perm:[1,0,3,2] row_mask:0xf bank_mask:0xf
v_mfma_f32_16x16x32_bf16 v[120:123], v[16:19], a[164:167], v[120:123]
s_mov_b64 vcc, s[76:77]
v_cndmask_b32_dpp v40, v44, v40, vcc quad_perm:[1,0,3,2] row_mask:0xf bank_mask:0xf
v_cndmask_b32_dpp v41, v45, v41, vcc quad_perm:[1,0,3,2] row_mask:0xf bank_mask:0xf
v_mfma_f32_16x16x32_bf16 v[120:123], v[36:39], a[184:187], v[120:123]
v_cndmask_b32_dpp v42, v46, v42, vcc quad_perm:[1,0,3,2] row_mask:0xf bank_mask:0xf
v_cndmask_b32_dpp v43, v47, v43, vcc quad_perm:[1,0,3,2] row_mask:0xf bank_mask:0xf
v_mfma_f32_16x16x32_bf16 v[136:139], v[16:19], a[168:171], v[136:139]
v_mfma_f32_16x16x32_bf16 v[136:139], v[36:39], a[188:191], v[136:139]
v_mfma_f32_16x16x32_bf16 v[152:155], v[16:19], a[172:175], v[152:155]
v_mfma_f32_16x16x32_bf16 v[152:155], v[36:39], a[192:195], v[152:155]
v_mfma_f32_16x16x32_bf16 v[168:171], v[16:19], a[176:179], v[168:171]
v_mfma_f32_16x16x32_bf16 v[168:171], v[36:39], a[196:199], v[168:171]
v_mfma_f32_16x16x32_bf16 v[108:111], v[40:43], a[160:163], v[108:111]
s_waitcnt vmcnt(12)
v_cndmask_b32_dpp v24, v212, v208, vcc quad_perm:[1,0,3,2] row_mask:0xf bank_mask:0xf
v_cndmask_b32_dpp v25, v213, v209, vcc quad_perm:[1,0,3,2] row_mask:0xf bank_mask:0xf
v_mfma_f32_16x16x32_bf16 v[108:111], v[20:23], a[180:183], v[108:111]
v_cndmask_b32_dpp v26, v214, v210, vcc quad_perm:[1,0,3,2] row_mask:0xf bank_mask:0xf
v_cndmask_b32_dpp v27, v215, v211, vcc quad_perm:[1,0,3,2] row_mask:0xf bank_mask:0xf
v_mfma_f32_16x16x32_bf16 v[124:127], v[40:43], a[164:167], v[124:127]
s_mov_b64 vcc, s[78:79]
v_cndmask_b32_dpp v212, v208, v212, vcc quad_perm:[1,0,3,2] row_mask:0xf bank_mask:0xf
v_cndmask_b32_dpp v213, v209, v213, vcc quad_perm:[1,0,3,2] row_mask:0xf bank_mask:0xf
v_mfma_f32_16x16x32_bf16 v[124:127], v[20:23], a[184:187], v[124:127]
v_cndmask_b32_dpp v214, v210, v214, vcc quad_perm:[1,0,3,2] row_mask:0xf bank_mask:0xf
v_cndmask_b32_dpp v215, v211, v215, vcc quad_perm:[1,0,3,2] row_mask:0xf bank_mask:0xf
v_mfma_f32_16x16x32_bf16 v[140:143], v[40:43], a[168:171], v[140:143]
v_mfma_f32_16x16x32_bf16 v[140:143], v[20:23], a[188:191], v[140:143]
v_mfma_f32_16x16x32_bf16 v[156:159], v[40:43], a[172:175], v[156:159]
v_mfma_f32_16x16x32_bf16 v[156:159], v[20:23], a[192:195], v[156:159]
v_mfma_f32_16x16x32_bf16 v[172:175], v[40:43], a[176:179], v[172:175]
buffer_load_dwordx4 v[32:35], v4, s[36:39], s75 offen offset:128 nt
v_mfma_f32_16x16x32_bf16 v[172:175], v[20:23], a[196:199], v[172:175]
buffer_load_dwordx4 v[36:39], v4, s[36:39], s75 offen offset:640 nt
v_mfma_f32_16x16x32_bf16 v[104:107], v[24:27], a[200:203], v[104:107]
v_cndmask_b32_dpp v28, v216, v220, vcc quad_perm:[1,0,3,2] row_mask:0xf bank_mask:0xf
v_cndmask_b32_dpp v29, v217, v221, vcc quad_perm:[1,0,3,2] row_mask:0xf bank_mask:0xf
v_mfma_f32_16x16x32_bf16 v[104:107], v[212:215], a[220:223], v[104:107]
v_cndmask_b32_dpp v30, v218, v222, vcc quad_perm:[1,0,3,2] row_mask:0xf bank_mask:0xf
v_cndmask_b32_dpp v31, v219, v223, vcc quad_perm:[1,0,3,2] row_mask:0xf bank_mask:0xf
v_mfma_f32_16x16x32_bf16 v[120:123], v[24:27], a[204:207], v[120:123]
ds_read_b128 a[160:163], v1 offset:0
s_mov_b64 vcc, s[76:77]
v_cndmask_b32_dpp v216, v220, v216, vcc quad_perm:[1,0,3,2] row_mask:0xf bank_mask:0xf
v_mfma_f32_16x16x32_bf16 v[120:123], v[212:215], a[224:227], v[120:123]
ds_read_b128 a[164:167], v1 offset:1024
v_cndmask_b32_dpp v217, v221, v217, vcc quad_perm:[1,0,3,2] row_mask:0xf bank_mask:0xf
v_mfma_f32_16x16x32_bf16 v[136:139], v[24:27], a[208:211], v[136:139]
ds_read_b128 a[168:171], v1 offset:2048
v_cndmask_b32_dpp v218, v222, v218, vcc quad_perm:[1,0,3,2] row_mask:0xf bank_mask:0xf
v_mfma_f32_16x16x32_bf16 v[136:139], v[212:215], a[228:231], v[136:139]
ds_read_b128 a[172:175], v1 offset:3072
v_cndmask_b32_dpp v219, v223, v219, vcc quad_perm:[1,0,3,2] row_mask:0xf bank_mask:0xf
v_mfma_f32_16x16x32_bf16 v[152:155], v[24:27], a[212:215], v[152:155]
ds_read_b128 a[176:179], v1 offset:4096
v_mfma_f32_16x16x32_bf16 v[152:155], v[212:215], a[232:235], v[152:155]
ds_read_b128 a[180:183], v1 offset:5120
v_mfma_f32_16x16x32_bf16 v[168:171], v[24:27], a[216:219], v[168:171]
ds_read_b128 a[184:187], v1 offset:6144
v_mfma_f32_16x16x32_bf16 v[168:171], v[212:215], a[236:239], v[168:171]
ds_read_b128 a[188:191], v1 offset:7168
v_mfma_f32_16x16x32_bf16 v[108:111], v[216:219], a[200:203], v[108:111]
s_waitcnt vmcnt(10)
v_cndmask_b32_dpp v16, v52, v48, vcc quad_perm:[1,0,3,2] row_mask:0xf bank_mask:0xf
v_cndmask_b32_dpp v17, v53, v49, vcc quad_perm:[1,0,3,2] row_mask:0xf bank_mask:0xf
v_mfma_f32_16x16x32_bf16 v[108:111], v[28:31], a[220:223], v[108:111]
v_cndmask_b32_dpp v18, v54, v50, vcc quad_perm:[1,0,3,2] row_mask:0xf bank_mask:0xf
v_cndmask_b32_dpp v19, v55, v51, vcc quad_perm:[1,0,3,2] row_mask:0xf bank_mask:0xf
v_mfma_f32_16x16x32_bf16 v[124:127], v[216:219], a[204:207], v[124:127]
ds_read_b128 a[192:195], v1 offset:8192
s_mov_b64 vcc, s[78:79]
v_cndmask_b32_dpp v52, v48, v52, vcc quad_perm:[1,0,3,2] row_mask:0xf bank_mask:0xf
v_mfma_f32_16x16x32_bf16 v[124:127], v[28:31], a[224:227], v[124:127]
ds_read_b128 a[196:199], v1 offset:9216
v_cndmask_b32_dpp v53, v49, v53, vcc quad_perm:[1,0,3,2] row_mask:0xf bank_mask:0xf
v_mfma_f32_16x16x32_bf16 v[140:143], v[216:219], a[208:211], v[140:143]
v_cndmask_b32_dpp v54, v50, v54, vcc quad_perm:[1,0,3,2] row_mask:0xf bank_mask:0xf
v_cndmask_b32_dpp v55, v51, v55, vcc quad_perm:[1,0,3,2] row_mask:0xf bank_mask:0xf
v_mfma_f32_16x16x32_bf16 v[140:143], v[28:31], a[228:231], v[140:143]
buffer_load_dwordx4 v[40:43], v4, s[36:39], s75 offen offset:2176 nt
v_mfma_f32_16x16x32_bf16 v[156:159], v[216:219], a[212:215], v[156:159]
buffer_load_dwordx4 v[44:47], v4, s[36:39], s75 offen offset:2688 nt
v_mfma_f32_16x16x32_bf16 v[156:159], v[28:31], a[232:235], v[156:159]
)"
R"(v_mfma_f32_16x16x32_bf16 v[172:175], v[216:219], a[216:219], v[172:175]
v_mfma_f32_16x16x32_bf16 v[172:175], v[28:31], a[236:239], v[172:175]
s_waitcnt lgkmcnt(0)
v_mfma_f32_16x16x32_bf16 v[176:179], v[16:19], a[160:163], 0
v_cndmask_b32_dpp v20, v56, v60, vcc quad_perm:[1,0,3,2] row_mask:0xf bank_mask:0xf
v_cndmask_b32_dpp v21, v57, v61, vcc quad_perm:[1,0,3,2] row_mask:0xf bank_mask:0xf
v_mfma_f32_16x16x32_bf16 v[176:179], v[52:55], a[180:183], v[176:179]
v_cndmask_b32_dpp v22, v58, v62, vcc quad_perm:[1,0,3,2] row_mask:0xf bank_mask:0xf
v_cndmask_b32_dpp v23, v59, v63, vcc quad_perm:[1,0,3,2] row_mask:0xf bank_mask:0xf
v_mfma_f32_16x16x32_bf16 v[192:195], v[16:19], a[164:167], 0
ds_read_b128 a[200:203], v1 offset:10240
s_mov_b64 vcc, s[76:77]
v_cndmask_b32_dpp v56, v60, v56, vcc quad_perm:[1,0,3,2] row_mask:0xf bank_mask:0xf
v_mfma_f32_16x16x32_bf16 v[192:195], v[52:55], a[184:187], v[192:195]
ds_read_b128 a[204:207], v1 offset:11264
v_cndmask_b32_dpp v57, v61, v57, vcc quad_perm:[1,0,3,2] row_mask:0xf bank_mask:0xf
v_mfma_f32_16x16x32_bf16 v[208:211], v[16:19], a[168:171], 0
ds_read_b128 a[208:211], v1 offset:12288
v_cndmask_b32_dpp v58, v62, v58, vcc quad_perm:[1,0,3,2] row_mask:0xf bank_mask:0xf
v_mfma_f32_16x16x32_bf16 v[208:211], v[52:55], a[188:191], v[208:211]
ds_read_b128 a[212:215], v1 offset:13312
v_cndmask_b32_dpp v59, v63, v59, vcc quad_perm:[1,0,3,2] row_mask:0xf bank_mask:0xf
v_mfma_f32_16x16x32_bf16 v[224:227], v[16:19], a[172:175], 0
ds_read_b128 a[216:219], v1 offset:14336
v_mfma_f32_16x16x32_bf16 v[224:227], v[52:55], a[192:195], v[224:227]
ds_read_b128 a[220:223], v1 offset:15360
v_mfma_f32_16x16x32_bf16 v[240:243], v[16:19], a[176:179], 0
ds_read_b128 a[224:227], v1 offset:16384
v_mfma_f32_16x16x32_bf16 v[240:243], v[52:55], a[196:199], v[240:243]
ds_read_b128 a[228:231], v1 offset:17408
v_mfma_f32_16x16x32_bf16 v[180:183], v[56:59], a[160:163], 0
s_waitcnt vmcnt(8)
v_cndmask_b32_dpp v24, v68, v64, vcc quad_perm:[1,0,3,2] row_mask:0xf bank_mask:0xf
v_cndmask_b32_dpp v25, v69, v65, vcc quad_perm:[1,0,3,2] row_mask:0xf bank_mask:0xf
v_mfma_f32_16x16x32_bf16 v[180:183], v[20:23], a[180:183], v[180:183]
v_cndmask_b32_dpp v26, v70, v66, vcc quad_perm:[1,0,3,2] row_mask:0xf bank_mask:0xf
v_cndmask_b32_dpp v27, v71, v67, vcc quad_perm:[1,0,3,2] row_mask:0xf bank_mask:0xf
v_mfma_f32_16x16x32_bf16 v[196:199], v[56:59], a[164:167], 0
ds_read_b128 a[232:235], v1 offset:18432
s_mov_b64 vcc, s[78:79]
v_cndmask_b32_dpp v68, v64, v68, vcc quad_perm:[1,0,3,2] row_mask:0xf bank_mask:0xf
v_mfma_f32_16x16x32_bf16 v[196:199], v[20:23], a[184:187], v[196:199]
ds_read_b128 a[236:239], v1 offset:19456
v_cndmask_b32_dpp v69, v65, v69, vcc quad_perm:[1,0,3,2] row_mask:0xf bank_mask:0xf
v_mfma_f32_16x16x32_bf16 v[212:215], v[56:59], a[168:171], 0
v_cndmask_b32_dpp v70, v66, v70, vcc quad_perm:[1,0,3,2] row_mask:0xf bank_mask:0xf
v_cndmask_b32_dpp v71, v67, v71, vcc quad_perm:[1,0,3,2] row_mask:0xf bank_mask:0xf
v_mfma_f32_16x16x32_bf16 v[212:215], v[20:23], a[188:191], v[212:215]
v_max3_f32 v11, v96, v97, v98
v_max3_f32 v12, v112, v113, v114
v_mfma_f32_16x16x32_bf16 v[228:231], v[56:59], a[172:175], 0
v_mfma_f32_16x16x32_bf16 v[228:231], v[20:23], a[192:195], v[228:231]
v_max3_f32 v13, v128, v129, v130
v_max3_f32 v14, v144, v145, v146
v_mfma_f32_16x16x32_bf16 v[244:247], v[56:59], a[176:179], 0
v_max3_f32 v15, v160, v161, v162
v_max3_f32 v11, v11, v99, v100
v_mfma_f32_16x16x32_bf16 v[244:247], v[20:23], a[196:199], v[244:247]
s_waitcnt lgkmcnt(0)
v_max3_f32 v12, v12, v115, v116
v_max3_f32 v13, v13, v131, v132
v_mfma_f32_16x16x32_bf16 v[176:179], v[24:27], a[200:203], v[176:179]
v_cndmask_b32_dpp v28, v72, v76, vcc quad_perm:[1,0,3,2] row_mask:0xf bank_mask:0xf
v_cndmask_b32_dpp v29, v73, v77, vcc quad_perm:[1,0,3,2] row_mask:0xf bank_mask:0xf
v_mfma_f32_16x16x32_bf16 v[176:179], v[68:71], a[220:223], v[176:179]
v_cndmask_b32_dpp v30, v74, v78, vcc quad_perm:[1,0,3,2] row_mask:0xf bank_mask:0xf
v_cndmask_b32_dpp v31, v75, v79, vcc quad_perm:[1,0,3,2] row_mask:0xf bank_mask:0xf
v_mfma_f32_16x16x32_bf16 v[192:195], v[24:27], a[204:207], v[192:195]
s_mov_b64 vcc, s[76:77]
v_cndmask_b32_dpp v72, v76, v72, vcc quad_perm:[1,0,3,2] row_mask:0xf bank_mask:0xf
v_cndmask_b32_dpp v73, v77, v73, vcc quad_perm:[1,0,3,2] row_mask:0xf bank_mask:0xf
v_mfma_f32_16x16x32_bf16 v[192:195], v[68:71], a[224:227], v[192:195]
v_cndmask_b32_dpp v74, v78, v74, vcc quad_perm:[1,0,3,2] row_mask:0xf bank_mask:0xf
v_cndmask_b32_dpp v75, v79, v75, vcc quad_perm:[1,0,3,2] row_mask:0xf bank_mask:0xf
v_mfma_f32_16x16x32_bf16 v[208:211], v[24:27], a[208:211], v[208:211]
buffer_load_dwordx4 v[48:51], v4, s[36:39], s74 offen offset:256 nt
v_mfma_f32_16x16x32_bf16 v[208:211], v[68:71], a[228:231], v[208:211]
v_max3_f32 v14, v14, v147, v148
v_max3_f32 v15, v15, v163, v164
v_mfma_f32_16x16x32_bf16 v[224:227], v[24:27], a[212:215], v[224:227]
v_max3_f32 v11, v11, v101, v102
v_max3_f32 v12, v12, v117, v118
v_mfma_f32_16x16x32_bf16 v[224:227], v[68:71], a[232:235], v[224:227]
v_max3_f32 v13, v13, v133, v134
v_max3_f32 v14, v14, v149, v150
v_mfma_f32_16x16x32_bf16 v[240:243], v[24:27], a[216:219], v[240:243]
buffer_load_dwordx4 v[52:55], v4, s[36:39], s74 offen offset:768 nt
v_mfma_f32_16x16x32_bf16 v[240:243], v[68:71], a[236:239], v[240:243]
v_max3_f32 v15, v15, v165, v166
v_max3_f32 v11, v11, v103, v104
v_mfma_f32_16x16x32_bf16 v[180:183], v[72:75], a[200:203], v[180:183]
s_waitcnt vmcnt(6)
v_cndmask_b32_dpp v16, v84, v80, vcc quad_perm:[1,0,3,2] row_mask:0xf bank_mask:0xf
v_cndmask_b32_dpp v17, v85, v81, vcc quad_perm:[1,0,3,2] row_mask:0xf bank_mask:0xf
v_mfma_f32_16x16x32_bf16 v[180:183], v[28:31], a[220:223], v[180:183]
v_cndmask_b32_dpp v18, v86, v82, vcc quad_perm:[1,0,3,2] row_mask:0xf bank_mask:0xf
v_cndmask_b32_dpp v19, v87, v83, vcc quad_perm:[1,0,3,2] row_mask:0xf bank_mask:0xf
v_mfma_f32_16x16x32_bf16 v[196:199], v[72:75], a[204:207], v[196:199]
s_mov_b64 vcc, s[78:79]
v_cndmask_b32_dpp v84, v80, v84, vcc quad_perm:[1,0,3,2] row_mask:0xf bank_mask:0xf
v_cndmask_b32_dpp v85, v81, v85, vcc quad_perm:[1,0,3,2] row_mask:0xf bank_mask:0xf
v_mfma_f32_16x16x32_bf16 v[196:199], v[28:31], a[224:227], v[196:199]
v_cndmask_b32_dpp v86, v82, v86, vcc quad_perm:[1,0,3,2] row_mask:0xf bank_mask:0xf
v_cndmask_b32_dpp v87, v83, v87, vcc quad_perm:[1,0,3,2] row_mask:0xf bank_mask:0xf
v_mfma_f32_16x16x32_bf16 v[212:215], v[72:75], a[208:211], v[212:215]
v_max3_f32 v12, v12, v119, v120
v_max3_f32 v13, v13, v135, v136
v_mfma_f32_16x16x32_bf16 v[212:215], v[28:31], a[228:231], v[212:215]
v_max3_f32 v14, v14, v151, v152
v_max3_f32 v15, v15, v167, v168
v_mfma_f32_16x16x32_bf16 v[228:231], v[72:75], a[212:215], v[228:231]
buffer_load_dwordx4 v[56:59], v4, s[36:39], s74 offen offset:2304 nt
v_mfma_f32_16x16x32_bf16 v[228:231], v[28:31], a[232:235], v[228:231]
v_max3_f32 v11, v11, v105, v106
v_max3_f32 v12, v12, v121, v122
v_mfma_f32_16x16x32_bf16 v[244:247], v[72:75], a[216:219], v[244:247]
v_max3_f32 v13, v13, v137, v138
v_max3_f32 v14, v14, v153, v154
v_mfma_f32_16x16x32_bf16 v[244:247], v[28:31], a[236:239], v[244:247]
v_max3_f32 v15, v15, v169, v170
v_max3_f32 v11, v11, v107, v108
v_mfma_f32_16x16x32_bf16 v[184:187], v[16:19], a[160:163], 0
v_cndmask_b32_dpp v20, v88, v92, vcc quad_perm:[1,0,3,2] row_mask:0xf bank_mask:0xf
v_cndmask_b32_dpp v21, v89, v93, vcc quad_perm:[1,0,3,2] row_mask:0xf bank_mask:0xf
v_mfma_f32_16x16x32_bf16 v[184:187], v[84:87], a[180:183], v[184:187]
v_cndmask_b32_dpp v22, v90, v94, vcc quad_perm:[1,0,3,2] row_mask:0xf bank_mask:0xf
v_cndmask_b32_dpp v23, v91, v95, vcc quad_perm:[1,0,3,2] row_mask:0xf bank_mask:0xf
v_mfma_f32_16x16x32_bf16 v[200:203], v[16:19], a[164:167], 0
s_mov_b64 vcc, s[76:77]
v_cndmask_b32_dpp v88, v92, v88, vcc quad_perm:[1,0,3,2] row_mask:0xf bank_mask:0xf
v_cndmask_b32_dpp v89, v93, v89, vcc quad_perm:[1,0,3,2] row_mask:0xf bank_mask:0xf
v_mfma_f32_16x16x32_bf16 v[200:203], v[84:87], a[184:187], v[200:203]
v_cndmask_b32_dpp v90, v94, v90, vcc quad_perm:[1,0,3,2] row_mask:0xf bank_mask:0xf
v_cndmask_b32_dpp v91, v95, v91, vcc quad_perm:[1,0,3,2] row_mask:0xf bank_mask:0xf
v_mfma_f32_16x16x32_bf16 v[216:219], v[16:19], a[168:171], 0
buffer_load_dwordx4 v[60:63], v4, s[36:39], s74 offen offset:2816 nt
v_mfma_f32_16x16x32_bf16 v[216:219], v[84:87], a[188:191], v[216:219]
v_max3_f32 v12, v12, v123, v124
v_max3_f32 v13, v13, v139, v140
v_mfma_f32_16x16x32_bf16 v[232:235], v[16:19], a[172:175], 0
v_max3_f32 v14, v14, v155, v156
v_max3_f32 v15, v15, v171, v172
v_mfma_f32_16x16x32_bf16 v[232:235], v[84:87], a[192:195], v[232:235]
v_max3_f32 v11, v11, v109, v110
v_max3_f32 v12, v12, v125, v126
v_mfma_f32_16x16x32_bf16 v[248:251], v[16:19], a[176:179], 0
buffer_load_dwordx4 v[64:67], v4, s[36:39], s74 offen offset:384 nt
v_mfma_f32_16x16x32_bf16 v[248:251], v[84:87], a[196:199], v[248:251]
v_max3_f32 v13, v13, v141, v142
v_max3_f32 v14, v14, v157, v158
v_mfma_f32_16x16x32_bf16 v[188:191], v[88:91], a[160:163], 0
buffer_load_dwordx4 v[68:71], v4, s[36:39], s74 offen offset:896 nt
buffer_load_dwordx4 v[72:75], v4, s[36:39], s74 offen offset:2432 nt
buffer_load_dwordx4 v[76:79], v4, s[36:39], s74 offen offset:2944 nt
s_waitcnt vmcnt(8)
v_cndmask_b32_dpp v24, v36, v32, vcc quad_perm:[1,0,3,2] row_mask:0xf bank_mask:0xf
v_cndmask_b32_dpp v25, v37, v33, vcc quad_perm:[1,0,3,2] row_mask:0xf bank_mask:0xf
v_mfma_f32_16x16x32_bf16 v[188:191], v[20:23], a[180:183], v[188:191]
v_cndmask_b32_dpp v26, v38, v34, vcc quad_perm:[1,0,3,2] row_mask:0xf bank_mask:0xf
v_cndmask_b32_dpp v27, v39, v35, vcc quad_perm:[1,0,3,2] row_mask:0xf bank_mask:0xf
v_mfma_f32_16x16x32_bf16 v[204:207], v[88:91], a[164:167], 0
s_mov_b64 vcc, s[78:79]
v_cndmask_b32_dpp v36, v32, v36, vcc quad_perm:[1,0,3,2] row_mask:0xf bank_mask:0xf
v_cndmask_b32_dpp v37, v33, v37, vcc quad_perm:[1,0,3,2] row_mask:0xf bank_mask:0xf
v_mfma_f32_16x16x32_bf16 v[204:207], v[20:23], a[184:187], v[204:207]
v_cndmask_b32_dpp v38, v34, v38, vcc quad_perm:[1,0,3,2] row_mask:0xf bank_mask:0xf
v_cndmask_b32_dpp v39, v35, v39, vcc quad_perm:[1,0,3,2] row_mask:0xf bank_mask:0xf
v_mfma_f32_16x16x32_bf16 v[220:223], v[88:91], a[168:171], 0
v_max3_f32 v15, v15, v173, v174
v_max_f32 v11, v11, v111
v_mfma_f32_16x16x32_bf16 v[220:223], v[20:23], a[188:191], v[220:223]
v_max_f32 v12, v12, v127
v_max_f32 v13, v13, v143
v_mfma_f32_16x16x32_bf16 v[236:239], v[88:91], a[172:175], 0
v_mfma_f32_16x16x32_bf16 v[236:239], v[20:23], a[192:195], v[236:239]
v_max_f32 v14, v14, v159
v_max_f32 v15, v15, v175
v_mfma_f32_16x16x32_bf16 v[252:255], v[88:91], a[176:179], 0
buffer_load_dwordx4 v[80:83], v4, s[36:39], s75 offen offset:256 nt
v_mfma_f32_16x16x32_bf16 v[252:255], v[20:23], a[196:199], v[252:255]
buffer_load_dwordx4 v[84:87], v4, s[36:39], s75 offen offset:768 nt
v_mfma_f32_16x16x32_bf16 v[184:187], v[24:27], a[200:203], v[184:187]
v_cndmask_b32_dpp v28, v40, v44, vcc quad_perm:[1,0,3,2] row_mask:0xf bank_mask:0xf
v_cndmask_b32_dpp v29, v41, v45, vcc quad_perm:[1,0,3,2] row_mask:0xf bank_mask:0xf
v_mfma_f32_16x16x32_bf16 v[184:187], v[36:39], a[220:223], v[184:187]
v_cndmask_b32_dpp v30, v42, v46, vcc quad_perm:[1,0,3,2] row_mask:0xf bank_mask:0xf
v_cndmask_b32_dpp v31, v43, v47, vcc quad_perm:[1,0,3,2] row_mask:0xf bank_mask:0xf
v_mfma_f32_16x16x32_bf16 v[200:203], v[24:27], a[204:207], v[200:203]
ds_read_b128 a[160:163], v1 offset:20480
s_mov_b64 vcc, s[76:77]
v_cndmask_b32_dpp v40, v44, v40, vcc quad_perm:[1,0,3,2] row_mask:0xf bank_mask:0xf
v_mfma_f32_16x16x32_bf16 v[200:203], v[36:39], a[224:227], v[200:203]
ds_read_b128 a[164:167], v1 offset:21504
v_cndmask_b32_dpp v41, v45, v41, vcc quad_perm:[1,0,3,2] row_mask:0xf bank_mask:0xf
v_mfma_f32_16x16x32_bf16 v[216:219], v[24:27], a[208:211], v[216:219]
ds_read_b128 a[168:171], v1 offset:22528
v_cndmask_b32_dpp v42, v46, v42, vcc quad_perm:[1,0,3,2] row_mask:0xf bank_mask:0xf
v_mfma_f32_16x16x32_bf16 v[216:219], v[36:39], a[228:231], v[216:219]
ds_read_b128 a[172:175], v1 offset:23552
v_cndmask_b32_dpp v43, v47, v43, vcc quad_perm:[1,0,3,2] row_mask:0xf bank_mask:0xf
)"
R"(v_mfma_f32_16x16x32_bf16 v[232:235], v[24:27], a[212:215], v[232:235]
ds_read_b128 a[176:179], v1 offset:24576
v_mfma_f32_16x16x32_bf16 v[232:235], v[36:39], a[232:235], v[232:235]
ds_read_b128 a[180:183], v1 offset:25600
v_mfma_f32_16x16x32_bf16 v[248:251], v[24:27], a[216:219], v[248:251]
ds_read_b128 a[184:187], v1 offset:26624
v_mfma_f32_16x16x32_bf16 v[248:251], v[36:39], a[236:239], v[248:251]
ds_read_b128 a[188:191], v1 offset:27648
v_mfma_f32_16x16x32_bf16 v[188:191], v[40:43], a[200:203], v[188:191]
buffer_load_dwordx4 v[88:91], v4, s[36:39], s75 offen offset:2304 nt
buffer_load_dwordx4 v[92:95], v4, s[36:39], s75 offen offset:2816 nt
s_waitcnt vmcnt(8)
v_cndmask_b32_dpp v16, v52, v48, vcc quad_perm:[1,0,3,2] row_mask:0xf bank_mask:0xf
v_cndmask_b32_dpp v17, v53, v49, vcc quad_perm:[1,0,3,2] row_mask:0xf bank_mask:0xf
v_mfma_f32_16x16x32_bf16 v[188:191], v[28:31], a[220:223], v[188:191]
v_cndmask_b32_dpp v18, v54, v50, vcc quad_perm:[1,0,3,2] row_mask:0xf bank_mask:0xf
v_cndmask_b32_dpp v19, v55, v51, vcc quad_perm:[1,0,3,2] row_mask:0xf bank_mask:0xf
v_mfma_f32_16x16x32_bf16 v[204:207], v[40:43], a[204:207], v[204:207]
ds_read_b128 a[192:195], v1 offset:28672
s_mov_b64 vcc, s[78:79]
v_cndmask_b32_dpp v52, v48, v52, vcc quad_perm:[1,0,3,2] row_mask:0xf bank_mask:0xf
v_mfma_f32_16x16x32_bf16 v[204:207], v[28:31], a[224:227], v[204:207]
ds_read_b128 a[196:199], v1 offset:29696
v_cndmask_b32_dpp v53, v49, v53, vcc quad_perm:[1,0,3,2] row_mask:0xf bank_mask:0xf
v_mfma_f32_16x16x32_bf16 v[220:223], v[40:43], a[208:211], v[220:223]
v_cndmask_b32_dpp v54, v50, v54, vcc quad_perm:[1,0,3,2] row_mask:0xf bank_mask:0xf
v_cndmask_b32_dpp v55, v51, v55, vcc quad_perm:[1,0,3,2] row_mask:0xf bank_mask:0xf
v_mfma_f32_16x16x32_bf16 v[220:223], v[28:31], a[228:231], v[220:223]
v_mfma_f32_16x16x32_bf16 v[236:239], v[40:43], a[212:215], v[236:239]
v_mfma_f32_16x16x32_bf16 v[236:239], v[28:31], a[232:235], v[236:239]
v_mfma_f32_16x16x32_bf16 v[252:255], v[40:43], a[216:219], v[252:255]
buffer_load_dwordx4 v[32:35], v4, s[36:39], s75 offen offset:384 nt
v_mfma_f32_16x16x32_bf16 v[252:255], v[28:31], a[236:239], v[252:255]
buffer_load_dwordx4 v[36:39], v4, s[36:39], s75 offen offset:896 nt
s_waitcnt lgkmcnt(0)
v_mfma_f32_16x16x32_bf16 v[176:179], v[16:19], a[160:163], v[176:179]
v_cndmask_b32_dpp v20, v56, v60, vcc quad_perm:[1,0,3,2] row_mask:0xf bank_mask:0xf
v_cndmask_b32_dpp v21, v57, v61, vcc quad_perm:[1,0,3,2] row_mask:0xf bank_mask:0xf
v_mfma_f32_16x16x32_bf16 v[176:179], v[52:55], a[180:183], v[176:179]
v_cndmask_b32_dpp v22, v58, v62, vcc quad_perm:[1,0,3,2] row_mask:0xf bank_mask:0xf
v_cndmask_b32_dpp v23, v59, v63, vcc quad_perm:[1,0,3,2] row_mask:0xf bank_mask:0xf
v_mfma_f32_16x16x32_bf16 v[192:195], v[16:19], a[164:167], v[192:195]
ds_read_b128 a[200:203], v1 offset:30720
s_mov_b64 vcc, s[76:77]
v_cndmask_b32_dpp v56, v60, v56, vcc quad_perm:[1,0,3,2] row_mask:0xf bank_mask:0xf
v_mfma_f32_16x16x32_bf16 v[192:195], v[52:55], a[184:187], v[192:195]
ds_read_b128 a[204:207], v1 offset:31744
v_cndmask_b32_dpp v57, v61, v57, vcc quad_perm:[1,0,3,2] row_mask:0xf bank_mask:0xf
v_mfma_f32_16x16x32_bf16 v[208:211], v[16:19], a[168:171], v[208:211]
ds_read_b128 a[208:211], v1 offset:32768
v_cndmask_b32_dpp v58, v62, v58, vcc quad_perm:[1,0,3,2] row_mask:0xf bank_mask:0xf
v_mfma_f32_16x16x32_bf16 v[208:211], v[52:55], a[188:191], v[208:211]
ds_read_b128 a[212:215], v1 offset:33792
v_cndmask_b32_dpp v59, v63, v59, vcc quad_perm:[1,0,3,2] row_mask:0xf bank_mask:0xf
v_mfma_f32_16x16x32_bf16 v[224:227], v[16:19], a[172:175], v[224:227]
ds_read_b128 a[216:219], v1 offset:34816
v_mfma_f32_16x16x32_bf16 v[224:227], v[52:55], a[192:195], v[224:227]
ds_read_b128 a[220:223], v1 offset:35840
v_mfma_f32_16x16x32_bf16 v[240:243], v[16:19], a[176:179], v[240:243]
ds_read_b128 a[224:227], v1 offset:36864
v_mfma_f32_16x16x32_bf16 v[240:243], v[52:55], a[196:199], v[240:243]
ds_read_b128 a[228:231], v1 offset:37888
v_mfma_f32_16x16x32_bf16 v[180:183], v[56:59], a[160:163], v[180:183]
buffer_load_dwordx4 v[40:43], v4, s[36:39], s75 offen offset:2432 nt
buffer_load_dwordx4 v[44:47], v4, s[36:39], s75 offen offset:2944 nt
s_waitcnt vmcnt(8)
v_cndmask_b32_dpp v24, v68, v64, vcc quad_perm:[1,0,3,2] row_mask:0xf bank_mask:0xf
v_cndmask_b32_dpp v25, v69, v65, vcc quad_perm:[1,0,3,2] row_mask:0xf bank_mask:0xf
v_mfma_f32_16x16x32_bf16 v[180:183], v[20:23], a[180:183], v[180:183]
v_cndmask_b32_dpp v26, v70, v66, vcc quad_perm:[1,0,3,2] row_mask:0xf bank_mask:0xf
v_cndmask_b32_dpp v27, v71, v67, vcc quad_perm:[1,0,3,2] row_mask:0xf bank_mask:0xf
v_mfma_f32_16x16x32_bf16 v[196:199], v[56:59], a[164:167], v[196:199]
ds_read_b128 a[232:235], v1 offset:38912
s_mov_b64 vcc, s[78:79]
v_cndmask_b32_dpp v68, v64, v68, vcc quad_perm:[1,0,3,2] row_mask:0xf bank_mask:0xf
v_mfma_f32_16x16x32_bf16 v[196:199], v[20:23], a[184:187], v[196:199]
ds_read_b128 a[236:239], v1 offset:39936
v_cndmask_b32_dpp v69, v65, v69, vcc quad_perm:[1,0,3,2] row_mask:0xf bank_mask:0xf
v_mfma_f32_16x16x32_bf16 v[212:215], v[56:59], a[168:171], v[212:215]
v_cndmask_b32_dpp v70, v66, v70, vcc quad_perm:[1,0,3,2] row_mask:0xf bank_mask:0xf
v_cndmask_b32_dpp v71, v67, v71, vcc quad_perm:[1,0,3,2] row_mask:0xf bank_mask:0xf
v_mfma_f32_16x16x32_bf16 v[212:215], v[20:23], a[188:191], v[212:215]
v_mfma_f32_16x16x32_bf16 v[228:231], v[56:59], a[172:175], v[228:231]
v_mfma_f32_16x16x32_bf16 v[228:231], v[20:23], a[192:195], v[228:231]
v_mfma_f32_16x16x32_bf16 v[244:247], v[56:59], a[176:179], v[244:247]
s_mov_b32 s80, 0
v_mfma_f32_16x16x32_bf16 v[244:247], v[20:23], a[196:199], v[244:247]
buffer_load_dwordx2 v[48:49], v5, s[40:43], s80 offen nt
s_waitcnt lgkmcnt(0)
v_mfma_f32_16x16x32_bf16 v[176:179], v[24:27], a[200:203], v[176:179]
v_cndmask_b32_dpp v28, v72, v76, vcc quad_perm:[1,0,3,2] row_mask:0xf bank_mask:0xf
v_cndmask_b32_dpp v29, v73, v77, vcc quad_perm:[1,0,3,2] row_mask:0xf bank_mask:0xf
v_mfma_f32_16x16x32_bf16 v[176:179], v[68:71], a[220:223], v[176:179]
v_cndmask_b32_dpp v30, v74, v78, vcc quad_perm:[1,0,3,2] row_mask:0xf bank_mask:0xf
v_cndmask_b32_dpp v31, v75, v79, vcc quad_perm:[1,0,3,2] row_mask:0xf bank_mask:0xf
v_mfma_f32_16x16x32_bf16 v[192:195], v[24:27], a[204:207], v[192:195]
s_mov_b64 vcc, s[76:77]
v_cndmask_b32_dpp v72, v76, v72, vcc quad_perm:[1,0,3,2] row_mask:0xf bank_mask:0xf
v_cndmask_b32_dpp v73, v77, v73, vcc quad_perm:[1,0,3,2] row_mask:0xf bank_mask:0xf
v_mfma_f32_16x16x32_bf16 v[192:195], v[68:71], a[224:227], v[192:195]
v_cndmask_b32_dpp v74, v78, v74, vcc quad_perm:[1,0,3,2] row_mask:0xf bank_mask:0xf
v_cndmask_b32_dpp v75, v79, v75, vcc quad_perm:[1,0,3,2] row_mask:0xf bank_mask:0xf
v_mfma_f32_16x16x32_bf16 v[208:211], v[24:27], a[208:211], v[208:211]
buffer_load_dwordx2 v[50:51], v5, s[40:43], s80 offen offset:512 nt
v_mfma_f32_16x16x32_bf16 v[208:211], v[68:71], a[228:231], v[208:211]
buffer_load_dwordx2 v[52:53], v5, s[40:43], s80 offen offset:1024 nt
v_mfma_f32_16x16x32_bf16 v[224:227], v[24:27], a[212:215], v[224:227]
buffer_load_dwordx2 v[54:55], v5, s[40:43], s80 offen offset:1536 nt
v_mfma_f32_16x16x32_bf16 v[224:227], v[68:71], a[232:235], v[224:227]
buffer_load_dwordx2 v[56:57], v5, s[40:43], s80 offen offset:2048 nt
v_mfma_f32_16x16x32_bf16 v[240:243], v[24:27], a[216:219], v[240:243]
buffer_load_dwordx2 v[58:59], v5, s[40:43], s80 offen offset:2560 nt
v_mfma_f32_16x16x32_bf16 v[240:243], v[68:71], a[236:239], v[240:243]
buffer_load_dwordx2 v[60:61], v5, s[40:43], s80 offen offset:3072 nt
v_mfma_f32_16x16x32_bf16 v[180:183], v[72:75], a[200:203], v[180:183]
buffer_load_dwordx2 v[62:63], v5, s[40:43], s80 offen offset:3584 nt
s_waitcnt vmcnt(12)
v_cndmask_b32_dpp v16, v84, v80, vcc quad_perm:[1,0,3,2] row_mask:0xf bank_mask:0xf
v_cndmask_b32_dpp v17, v85, v81, vcc quad_perm:[1,0,3,2] row_mask:0xf bank_mask:0xf
v_mfma_f32_16x16x32_bf16 v[180:183], v[28:31], a[220:223], v[180:183]
v_cndmask_b32_dpp v18, v86, v82, vcc quad_perm:[1,0,3,2] row_mask:0xf bank_mask:0xf
v_cndmask_b32_dpp v19, v87, v83, vcc quad_perm:[1,0,3,2] row_mask:0xf bank_mask:0xf
v_mfma_f32_16x16x32_bf16 v[196:199], v[72:75], a[204:207], v[196:199]
s_mov_b64 vcc, s[78:79]
v_cndmask_b32_dpp v84, v80, v84, vcc quad_perm:[1,0,3,2] row_mask:0xf bank_mask:0xf
v_cndmask_b32_dpp v85, v81, v85, vcc quad_perm:[1,0,3,2] row_mask:0xf bank_mask:0xf
v_mfma_f32_16x16x32_bf16 v[196:199], v[28:31], a[224:227], v[196:199]
v_cndmask_b32_dpp v86, v82, v86, vcc quad_perm:[1,0,3,2] row_mask:0xf bank_mask:0xf
v_cndmask_b32_dpp v87, v83, v87, vcc quad_perm:[1,0,3,2] row_mask:0xf bank_mask:0xf
v_mfma_f32_16x16x32_bf16 v[212:215], v[72:75], a[208:211], v[212:215]
v_mfma_f32_16x16x32_bf16 v[212:215], v[28:31], a[228:231], v[212:215]
v_mfma_f32_16x16x32_bf16 v[228:231], v[72:75], a[212:215], v[228:231]
v_mfma_f32_16x16x32_bf16 v[228:231], v[28:31], a[232:235], v[228:231]
v_mfma_f32_16x16x32_bf16 v[244:247], v[72:75], a[216:219], v[244:247]
s_add_u32 s80, s80, 16384
v_mfma_f32_16x16x32_bf16 v[244:247], v[28:31], a[236:239], v[244:247]
buffer_load_dwordx2 v[64:65], v5, s[40:43], s80 offen nt
v_mfma_f32_16x16x32_bf16 v[184:187], v[16:19], a[160:163], v[184:187]
v_cndmask_b32_dpp v20, v88, v92, vcc quad_perm:[1,0,3,2] row_mask:0xf bank_mask:0xf
v_cndmask_b32_dpp v21, v89, v93, vcc quad_perm:[1,0,3,2] row_mask:0xf bank_mask:0xf
v_mfma_f32_16x16x32_bf16 v[184:187], v[84:87], a[180:183], v[184:187]
v_cndmask_b32_dpp v22, v90, v94, vcc quad_perm:[1,0,3,2] row_mask:0xf bank_mask:0xf
v_cndmask_b32_dpp v23, v91, v95, vcc quad_perm:[1,0,3,2] row_mask:0xf bank_mask:0xf
v_mfma_f32_16x16x32_bf16 v[200:203], v[16:19], a[164:167], v[200:203]
s_mov_b64 vcc, s[76:77]
v_cndmask_b32_dpp v88, v92, v88, vcc quad_perm:[1,0,3,2] row_mask:0xf bank_mask:0xf
v_cndmask_b32_dpp v89, v93, v89, vcc quad_perm:[1,0,3,2] row_mask:0xf bank_mask:0xf
v_mfma_f32_16x16x32_bf16 v[200:203], v[84:87], a[184:187], v[200:203]
v_cndmask_b32_dpp v90, v94, v90, vcc quad_perm:[1,0,3,2] row_mask:0xf bank_mask:0xf
v_cndmask_b32_dpp v91, v95, v91, vcc quad_perm:[1,0,3,2] row_mask:0xf bank_mask:0xf
v_mfma_f32_16x16x32_bf16 v[216:219], v[16:19], a[168:171], v[216:219]
buffer_load_dwordx2 v[66:67], v5, s[40:43], s80 offen offset:512 nt
v_mfma_f32_16x16x32_bf16 v[216:219], v[84:87], a[188:191], v[216:219]
buffer_load_dwordx2 v[68:69], v5, s[40:43], s80 offen offset:1024 nt
v_mfma_f32_16x16x32_bf16 v[232:235], v[16:19], a[172:175], v[232:235]
buffer_load_dwordx2 v[70:71], v5, s[40:43], s80 offen offset:1536 nt
v_mfma_f32_16x16x32_bf16 v[232:235], v[84:87], a[192:195], v[232:235]
buffer_load_dwordx2 v[72:73], v5, s[40:43], s80 offen offset:2048 nt
v_mfma_f32_16x16x32_bf16 v[248:251], v[16:19], a[176:179], v[248:251]
v_mfma_f32_16x16x32_bf16 v[248:251], v[84:87], a[196:199], v[248:251]
v_mfma_f32_16x16x32_bf16 v[188:191], v[88:91], a[160:163], v[188:191]
buffer_load_dwordx2 v[74:75], v5, s[40:43], s80 offen offset:2560 nt
buffer_load_dwordx2 v[76:77], v5, s[40:43], s80 offen offset:3072 nt
buffer_load_dwordx2 v[78:79], v5, s[40:43], s80 offen offset:3584 nt
s_waitcnt vmcnt(16)
v_cndmask_b32_dpp v24, v36, v32, vcc quad_perm:[1,0,3,2] row_mask:0xf bank_mask:0xf
v_cndmask_b32_dpp v25, v37, v33, vcc quad_perm:[1,0,3,2] row_mask:0xf bank_mask:0xf
v_mfma_f32_16x16x32_bf16 v[188:191], v[20:23], a[180:183], v[188:191]
v_cndmask_b32_dpp v26, v38, v34, vcc quad_perm:[1,0,3,2] row_mask:0xf bank_mask:0xf
v_cndmask_b32_dpp v27, v39, v35, vcc quad_perm:[1,0,3,2] row_mask:0xf bank_mask:0xf
v_mfma_f32_16x16x32_bf16 v[204:207], v[88:91], a[164:167], v[204:207]
s_mov_b64 vcc, s[78:79]
v_cndmask_b32_dpp v36, v32, v36, vcc quad_perm:[1,0,3,2] row_mask:0xf bank_mask:0xf
v_cndmask_b32_dpp v37, v33, v37, vcc quad_perm:[1,0,3,2] row_mask:0xf bank_mask:0xf
v_mfma_f32_16x16x32_bf16 v[204:207], v[20:23], a[184:187], v[204:207]
v_cndmask_b32_dpp v38, v34, v38, vcc quad_perm:[1,0,3,2] row_mask:0xf bank_mask:0xf
v_cndmask_b32_dpp v39, v35, v39, vcc quad_perm:[1,0,3,2] row_mask:0xf bank_mask:0xf
v_mfma_f32_16x16x32_bf16 v[220:223], v[88:91], a[168:171], v[220:223]
v_mfma_f32_16x16x32_bf16 v[220:223], v[20:23], a[188:191], v[220:223]
v_mfma_f32_16x16x32_bf16 v[236:239], v[88:91], a[172:175], v[236:239]
v_mfma_f32_16x16x32_bf16 v[236:239], v[20:23], a[192:195], v[236:239]
v_mfma_f32_16x16x32_bf16 v[252:255], v[88:91], a[176:179], v[252:255]
s_mov_b32 s80, 32768
v_mfma_f32_16x16x32_bf16 v[252:255], v[20:23], a[196:199], v[252:255]
buffer_load_dwordx2 v[80:81], v5, s[40:43], s80 offen nt
v_mfma_f32_16x16x32_bf16 v[184:187], v[24:27], a[200:203], v[184:187]
v_cndmask_b32_dpp v28, v40, v44, vcc quad_perm:[1,0,3,2] row_mask:0xf bank_mask:0xf
v_cndmask_b32_dpp v29, v41, v45, vcc quad_perm:[1,0,3,2] row_mask:0xf bank_mask:0xf
v_mfma_f32_16x16x32_bf16 v[184:187], v[36:39], a[220:223], v[184:187]
v_cndmask_b32_dpp v30, v42, v46, vcc quad_perm:[1,0,3,2] row_mask:0xf bank_mask:0xf
v_cndmask_b32_dpp v31, v43, v47, vcc quad_perm:[1,0,3,2] row_mask:0xf bank_mask:0xf
v_mfma_f32_16x16x32_bf16 v[200:203], v[24:27], a[204:207], v[200:203]
s_mov_b64 vcc, s[76:77]
v_cndmask_b32_dpp v40, v44, v40, vcc quad_perm:[1,0,3,2] row_mask:0xf bank_mask:0xf
v_cndmask_b32_dpp v41, v45, v41, vcc quad_perm:[1,0,3,2] row_mask:0xf bank_mask:0xf
v_mfma_f32_16x16x32_bf16 v[200:203], v[36:39], a[224:227], v[200:203]
v_cndmask_b32_dpp v42, v46, v42, vcc quad_perm:[1,0,3,2] row_mask:0xf bank_mask:0xf
v_cndmask_b32_dpp v43, v47, v43, vcc quad_perm:[1,0,3,2] row_mask:0xf bank_mask:0xf
v_mfma_f32_16x16x32_bf16 v[216:219], v[24:27], a[208:211], v[216:219]
buffer_load_dwordx2 v[82:83], v5, s[40:43], s80 offen offset:512 nt
)"
R"(v_mfma_f32_16x16x32_bf16 v[216:219], v[36:39], a[228:231], v[216:219]
buffer_load_dwordx2 v[84:85], v5, s[40:43], s80 offen offset:1024 nt
v_mfma_f32_16x16x32_bf16 v[232:235], v[24:27], a[212:215], v[232:235]
buffer_load_dwordx2 v[86:87], v5, s[40:43], s80 offen offset:1536 nt
v_mfma_f32_16x16x32_bf16 v[232:235], v[36:39], a[232:235], v[232:235]
buffer_load_dwordx2 v[88:89], v5, s[40:43], s80 offen offset:2048 nt
v_mfma_f32_16x16x32_bf16 v[248:251], v[24:27], a[216:219], v[248:251]
buffer_load_dwordx2 v[90:91], v5, s[40:43], s80 offen offset:2560 nt
v_mfma_f32_16x16x32_bf16 v[248:251], v[36:39], a[236:239], v[248:251]
buffer_load_dwordx2 v[92:93], v5, s[40:43], s80 offen offset:3072 nt
v_mfma_f32_16x16x32_bf16 v[188:191], v[40:43], a[200:203], v[188:191]
v_mfma_f32_16x16x32_bf16 v[188:191], v[28:31], a[220:223], v[188:191]
v_mfma_f32_16x16x32_bf16 v[204:207], v[40:43], a[204:207], v[204:207]
v_mfma_f32_16x16x32_bf16 v[204:207], v[28:31], a[224:227], v[204:207]
v_mfma_f32_16x16x32_bf16 v[220:223], v[40:43], a[208:211], v[220:223]
buffer_load_dwordx2 v[94:95], v5, s[40:43], s80 offen offset:3584 nt
v_mfma_f32_16x16x32_bf16 v[220:223], v[28:31], a[228:231], v[220:223]
v_mfma_f32_16x16x32_bf16 v[236:239], v[40:43], a[212:215], v[236:239]
v_mfma_f32_16x16x32_bf16 v[236:239], v[28:31], a[232:235], v[236:239]
v_mfma_f32_16x16x32_bf16 v[252:255], v[40:43], a[216:219], v[252:255]
s_add_u32 s80, s80, 16384
v_mfma_f32_16x16x32_bf16 v[252:255], v[28:31], a[236:239], v[252:255]
v_max3_f32 v11, v11, v176, v177
v_max3_f32 v12, v12, v192, v193
v_max3_f32 v13, v13, v208, v209
v_max3_f32 v14, v14, v224, v225
v_max3_f32 v15, v15, v240, v241
v_max3_f32 v11, v11, v178, v179
v_max3_f32 v12, v12, v194, v195
v_max3_f32 v13, v13, v210, v211
v_max3_f32 v14, v14, v226, v227
v_max3_f32 v15, v15, v242, v243
v_max3_f32 v11, v11, v180, v181
v_max3_f32 v12, v12, v196, v197
v_max3_f32 v13, v13, v212, v213
v_max3_f32 v14, v14, v228, v229
v_max3_f32 v15, v15, v244, v245
v_max3_f32 v11, v11, v182, v183
v_max3_f32 v12, v12, v198, v199
v_max3_f32 v13, v13, v214, v215
v_max3_f32 v14, v14, v230, v231
v_max3_f32 v15, v15, v246, v247
v_max3_f32 v11, v11, v184, v185
v_max3_f32 v12, v12, v200, v201
v_max3_f32 v13, v13, v216, v217
v_max3_f32 v14, v14, v232, v233
v_max3_f32 v15, v15, v248, v249
v_max3_f32 v11, v11, v186, v187
v_max3_f32 v12, v12, v202, v203
v_max3_f32 v13, v13, v218, v219
v_max3_f32 v14, v14, v234, v235
v_max3_f32 v15, v15, v250, v251
v_max3_f32 v11, v11, v188, v189
v_max3_f32 v12, v12, v204, v205
v_max3_f32 v13, v13, v220, v221
v_max3_f32 v14, v14, v236, v237
buffer_load_dwordx2 v[32:33], v5, s[40:43], s80 offen nt
buffer_load_dwordx2 v[34:35], v5, s[40:43], s80 offen offset:512 nt
buffer_load_dwordx2 v[36:37], v5, s[40:43], s80 offen offset:1024 nt
v_max3_f32 v15, v15, v252, v253
v_max3_f32 v11, v11, v190, v191
v_max3_f32 v12, v12, v206, v207
v_max3_f32 v13, v13, v222, v223
v_max3_f32 v14, v14, v238, v239
v_max3_f32 v15, v15, v254, v255
v_mov_b32 v21, 0x3f803f80
v_accvgpr_write_b32 a160, v21
v_accvgpr_write_b32 a161, v21
v_accvgpr_write_b32 a162, v21
v_accvgpr_write_b32 a163, v21
v_mov_b32 v16, v11
v_mov_b32 v17, v12
v_mov_b32 v18, v13
v_mov_b32 v19, v14
v_mov_b32 v20, v15
v_permlane32_swap_b32 v16, v11
v_permlane32_swap_b32 v17, v12
v_permlane32_swap_b32 v18, v13
v_permlane32_swap_b32 v19, v14
v_permlane32_swap_b32 v20, v15
v_max_f32 v11, v16, v11
v_max_f32 v12, v17, v12
v_max_f32 v13, v18, v13
v_max_f32 v14, v19, v14
v_max_f32 v15, v20, v15
v_mov_b32 v16, v11
v_mov_b32 v17, v12
v_mov_b32 v18, v13
v_mov_b32 v19, v14
v_mov_b32 v20, v15
v_permlane16_swap_b32 v16, v11
v_permlane16_swap_b32 v17, v12
v_permlane16_swap_b32 v18, v13
v_permlane16_swap_b32 v19, v14
v_permlane16_swap_b32 v20, v15
v_max_f32 v11, v16, v11
v_max_f32 v12, v17, v12
v_max_f32 v13, v18, v13
v_max_f32 v14, v19, v14
v_max_f32 v15, v20, v15
v_accvgpr_read_b32 v9, a248
v_accvgpr_read_b32 v0, a247
s_barrier
buffer_load_dwordx2 v[38:39], v5, s[40:43], s80 offen offset:1536 nt
buffer_load_dwordx2 v[40:41], v5, s[40:43], s80 offen offset:2048 nt
buffer_load_dwordx2 v[42:43], v5, s[40:43], s80 offen offset:2560 nt
ds_write_b32 v9, v11 offset:0
ds_write_b32 v9, v12 offset:256
ds_write_b32 v9, v13 offset:512
ds_write_b32 v9, v14 offset:768
ds_write_b32 v9, v15 offset:1024
v_and_b32 v10, 0xf0, v1
s_waitcnt lgkmcnt(0)
s_barrier
s_add_u32 s91, s90, -1
s_or_b32 s91, s91, s92
s_cmp_lg_u32 s91, 0
s_cbranch_scc1 LpubB_%=
s_mov_b64 exec, 1
v_accvgpr_read_b32 v16, a252
v_accvgpr_read_b32 v17, a253
s_xor_b32 s91, s61, 0
v_add_u32 v16, 1, v16
v_add_u32 v17, 1, v17
v_mov_b32 v18, s91
buffer_store_dwordx2 v[16:17], v18, s[56:59], 0 offen sc1
s_mov_b64 exec, -1
LpubB_%=:
s_nop 1
buffer_load_dwordx2 v[44:45], v5, s[40:43], s80 offen offset:3072 nt
buffer_load_dwordx2 v[46:47], v5, s[40:43], s80 offen offset:3584 nt
ds_read_b128 v[16:19], v10 offset:0
ds_read_b128 v[20:23], v10 offset:256
ds_read_b128 v[24:27], v10 offset:512
ds_read_b128 v[28:31], v10 offset:768
s_waitcnt lgkmcnt(3)
v_max3_f32 v16, v16, v17, v18
v_max_f32 v16, v16, v19
v_mul_f32 v11, s86, v16
ds_read_b128 v[16:19], v10 offset:1024
s_waitcnt lgkmcnt(3)
v_max3_f32 v20, v20, v21, v22
v_max_f32 v20, v20, v23
v_mul_f32 v12, s86, v20
s_waitcnt lgkmcnt(2)
v_max3_f32 v24, v24, v25, v26
v_max_f32 v24, v24, v27
v_mul_f32 v13, s86, v24
s_waitcnt lgkmcnt(1)
v_max3_f32 v28, v28, v29, v30
v_max_f32 v28, v28, v31
v_mul_f32 v14, s86, v28
s_waitcnt lgkmcnt(0)
v_max3_f32 v16, v16, v17, v18
v_max_f32 v16, v16, v19
v_mul_f32 v15, s86, v16
v_mov_b32 v16, v11
v_pk_fma_f32 v[96:97], v[96:97], s[86:87], v[16:17] op_sel_hi:[1,1,0] neg_lo:[0,0,1] neg_hi:[0,0,1]
v_pk_fma_f32 v[98:99], v[98:99], s[86:87], v[16:17] op_sel_hi:[1,1,0] neg_lo:[0,0,1] neg_hi:[0,0,1]
v_pk_fma_f32 v[100:101], v[100:101], s[86:87], v[16:17] op_sel_hi:[1,1,0] neg_lo:[0,0,1] neg_hi:[0,0,1]
v_pk_fma_f32 v[102:103], v[102:103], s[86:87], v[16:17] op_sel_hi:[1,1,0] neg_lo:[0,0,1] neg_hi:[0,0,1]
v_pk_fma_f32 v[104:105], v[104:105], s[86:87], v[16:17] op_sel_hi:[1,1,0] neg_lo:[0,0,1] neg_hi:[0,0,1]
v_pk_fma_f32 v[106:107], v[106:107], s[86:87], v[16:17] op_sel_hi:[1,1,0] neg_lo:[0,0,1] neg_hi:[0,0,1]
v_pk_fma_f32 v[108:109], v[108:109], s[86:87], v[16:17] op_sel_hi:[1,1,0] neg_lo:[0,0,1] neg_hi:[0,0,1]
v_pk_fma_f32 v[110:111], v[110:111], s[86:87], v[16:17] op_sel_hi:[1,1,0] neg_lo:[0,0,1] neg_hi:[0,0,1]
v_exp_f32 v96, v96
v_exp_f32 v97, v97
v_exp_f32 v98, v98
v_exp_f32 v99, v99
v_exp_f32 v100, v100
v_exp_f32 v101, v101
v_exp_f32 v102, v102
v_exp_f32 v103, v103
v_exp_f32 v104, v104
v_exp_f32 v105, v105
v_exp_f32 v106, v106
v_exp_f32 v107, v107
v_exp_f32 v108, v108
v_exp_f32 v109, v109
v_exp_f32 v110, v110
v_exp_f32 v111, v111
v_cvt_pk_bf16_f32 v96, v96, v97
v_cvt_pk_bf16_f32 v97, v98, v99
v_cvt_pk_bf16_f32 v98, v100, v101
v_cvt_pk_bf16_f32 v99, v102, v103
v_cvt_pk_bf16_f32 v104, v104, v105
v_cvt_pk_bf16_f32 v105, v106, v107
v_cvt_pk_bf16_f32 v106, v108, v109
v_cvt_pk_bf16_f32 v107, v110, v111
v_mfma_f32_16x16x32_bf16 a[164:167], a[160:163], v[96:99], 0
ds_write_b128 v0, v[96:99] offset:0
v_mfma_f32_16x16x32_bf16 a[164:167], a[160:163], v[104:107], a[164:167]
ds_write_b128 v0, v[104:107] offset:5120
s_mov_b32 s80, 65536
s_waitcnt lgkmcnt(1)
buffer_load_dwordx2 v[96:97], v5, s[40:43], s80 offen nt
buffer_load_dwordx2 v[98:99], v5, s[40:43], s80 offen offset:512 nt
buffer_load_dwordx2 v[100:101], v5, s[40:43], s80 offen offset:1024 nt
v_pk_fma_f32 v[176:177], v[176:177], s[86:87], v[16:17] op_sel_hi:[1,1,0] neg_lo:[0,0,1] neg_hi:[0,0,1]
buffer_load_dwordx2 v[102:103], v5, s[40:43], s80 offen offset:1536 nt
)"
R"(s_waitcnt lgkmcnt(0)
buffer_load_dwordx2 v[104:105], v5, s[40:43], s80 offen offset:2048 nt
buffer_load_dwordx2 v[106:107], v5, s[40:43], s80 offen offset:2560 nt
v_pk_fma_f32 v[178:179], v[178:179], s[86:87], v[16:17] op_sel_hi:[1,1,0] neg_lo:[0,0,1] neg_hi:[0,0,1]
v_pk_fma_f32 v[180:181], v[180:181], s[86:87], v[16:17] op_sel_hi:[1,1,0] neg_lo:[0,0,1] neg_hi:[0,0,1]
v_pk_fma_f32 v[182:183], v[182:183], s[86:87], v[16:17] op_sel_hi:[1,1,0] neg_lo:[0,0,1] neg_hi:[0,0,1]
v_pk_fma_f32 v[184:185], v[184:185], s[86:87], v[16:17] op_sel_hi:[1,1,0] neg_lo:[0,0,1] neg_hi:[0,0,1]
v_pk_fma_f32 v[186:187], v[186:187], s[86:87], v[16:17] op_sel_hi:[1,1,0] neg_lo:[0,0,1] neg_hi:[0,0,1]
v_pk_fma_f32 v[188:189], v[188:189], s[86:87], v[16:17] op_sel_hi:[1,1,0] neg_lo:[0,0,1] neg_hi:[0,0,1]
v_pk_fma_f32 v[190:191], v[190:191], s[86:87], v[16:17] op_sel_hi:[1,1,0] neg_lo:[0,0,1] neg_hi:[0,0,1]
v_exp_f32 v176, v176
v_exp_f32 v177, v177
v_exp_f32 v178, v178
v_exp_f32 v179, v179
v_exp_f32 v180, v180
v_exp_f32 v181, v181
v_exp_f32 v182, v182
v_exp_f32 v183, v183
v_exp_f32 v184, v184
v_exp_f32 v185, v185
v_exp_f32 v186, v186
v_exp_f32 v187, v187
v_exp_f32 v188, v188
v_exp_f32 v189, v189
v_exp_f32 v190, v190
v_exp_f32 v191, v191
buffer_load_dwordx2 v[108:109], v5, s[40:43], s80 offen offset:3072 nt
buffer_load_dwordx2 v[110:111], v5, s[40:43], s80 offen offset:3584 nt
v_cvt_pk_bf16_f32 v176, v176, v177
v_cvt_pk_bf16_f32 v177, v178, v179
v_cvt_pk_bf16_f32 v178, v180, v181
v_cvt_pk_bf16_f32 v179, v182, v183
v_cvt_pk_bf16_f32 v184, v184, v185
v_cvt_pk_bf16_f32 v185, v186, v187
v_cvt_pk_bf16_f32 v186, v188, v189
v_cvt_pk_bf16_f32 v187, v190, v191
v_mfma_f32_16x16x32_bf16 a[164:167], a[160:163], v[176:179], a[164:167]
ds_write_b128 v0, v[176:179] offset:10240
v_mfma_f32_16x16x32_bf16 a[164:167], a[160:163], v[184:187], a[164:167]
ds_write_b128 v0, v[184:187] offset:15360
v_mov_b32 v16, v12
v_pk_fma_f32 v[112:113], v[112:113], s[86:87], v[16:17] op_sel_hi:[1,1,0] neg_lo:[0,0,1] neg_hi:[0,0,1]
v_pk_fma_f32 v[114:115], v[114:115], s[86:87], v[16:17] op_sel_hi:[1,1,0] neg_lo:[0,0,1] neg_hi:[0,0,1]
v_pk_fma_f32 v[116:117], v[116:117], s[86:87], v[16:17] op_sel_hi:[1,1,0] neg_lo:[0,0,1] neg_hi:[0,0,1]
v_pk_fma_f32 v[118:119], v[118:119], s[86:87], v[16:17] op_sel_hi:[1,1,0] neg_lo:[0,0,1] neg_hi:[0,0,1]
v_pk_fma_f32 v[120:121], v[120:121], s[86:87], v[16:17] op_sel_hi:[1,1,0] neg_lo:[0,0,1] neg_hi:[0,0,1]
v_pk_fma_f32 v[122:123], v[122:123], s[86:87], v[16:17] op_sel_hi:[1,1,0] neg_lo:[0,0,1] neg_hi:[0,0,1]
v_pk_fma_f32 v[124:125], v[124:125], s[86:87], v[16:17] op_sel_hi:[1,1,0] neg_lo:[0,0,1] neg_hi:[0,0,1]
v_pk_fma_f32 v[126:127], v[126:127], s[86:87], v[16:17] op_sel_hi:[1,1,0] neg_lo:[0,0,1] neg_hi:[0,0,1]
v_exp_f32 v112, v112
v_exp_f32 v113, v113
v_exp_f32 v114, v114
v_exp_f32 v115, v115
v_exp_f32 v116, v116
v_exp_f32 v117, v117
v_exp_f32 v118, v118
v_exp_f32 v119, v119
v_exp_f32 v120, v120
v_exp_f32 v121, v121
v_exp_f32 v122, v122
v_exp_f32 v123, v123
v_exp_f32 v124, v124
v_exp_f32 v125, v125
v_exp_f32 v126, v126
v_exp_f32 v127, v127
v_cvt_pk_bf16_f32 v112, v112, v113
v_cvt_pk_bf16_f32 v113, v114, v115
v_cvt_pk_bf16_f32 v114, v116, v117
v_cvt_pk_bf16_f32 v115, v118, v119
v_cvt_pk_bf16_f32 v120, v120, v121
v_cvt_pk_bf16_f32 v121, v122, v123
v_cvt_pk_bf16_f32 v122, v124, v125
v_cvt_pk_bf16_f32 v123, v126, v127
v_mfma_f32_16x16x32_bf16 a[168:171], a[160:163], v[112:115], 0
ds_write_b128 v0, v[112:115] offset:1024
v_mfma_f32_16x16x32_bf16 a[168:171], a[160:163], v[120:123], a[168:171]
ds_write_b128 v0, v[120:123] offset:6144
s_add_u32 s80, s80, 16384
s_waitcnt lgkmcnt(1)
buffer_load_dwordx2 v[112:113], v5, s[40:43], s80 offen nt
buffer_load_dwordx2 v[114:115], v5, s[40:43], s80 offen offset:512 nt
buffer_load_dwordx2 v[116:117], v5, s[40:43], s80 offen offset:1024 nt
v_pk_fma_f32 v[192:193], v[192:193], s[86:87], v[16:17] op_sel_hi:[1,1,0] neg_lo:[0,0,1] neg_hi:[0,0,1]
buffer_load_dwordx2 v[118:119], v5, s[40:43], s80 offen offset:1536 nt
s_waitcnt lgkmcnt(0)
buffer_load_dwordx2 v[120:121], v5, s[40:43], s80 offen offset:2048 nt
buffer_load_dwordx2 v[122:123], v5, s[40:43], s80 offen offset:2560 nt
buffer_load_dwordx2 v[124:125], v5, s[40:43], s80 offen offset:3072 nt
v_pk_fma_f32 v[194:195], v[194:195], s[86:87], v[16:17] op_sel_hi:[1,1,0] neg_lo:[0,0,1] neg_hi:[0,0,1]
v_pk_fma_f32 v[196:197], v[196:197], s[86:87], v[16:17] op_sel_hi:[1,1,0] neg_lo:[0,0,1] neg_hi:[0,0,1]
v_pk_fma_f32 v[198:199], v[198:199], s[86:87], v[16:17] op_sel_hi:[1,1,0] neg_lo:[0,0,1] neg_hi:[0,0,1]
v_pk_fma_f32 v[200:201], v[200:201], s[86:87], v[16:17] op_sel_hi:[1,1,0] neg_lo:[0,0,1] neg_hi:[0,0,1]
v_pk_fma_f32 v[202:203], v[202:203], s[86:87], v[16:17] op_sel_hi:[1,1,0] neg_lo:[0,0,1] neg_hi:[0,0,1]
v_pk_fma_f32 v[204:205], v[204:205], s[86:87], v[16:17] op_sel_hi:[1,1,0] neg_lo:[0,0,1] neg_hi:[0,0,1]
v_pk_fma_f32 v[206:207], v[206:207], s[86:87], v[16:17] op_sel_hi:[1,1,0] neg_lo:[0,0,1] neg_hi:[0,0,1]
v_exp_f32 v192, v192
v_exp_f32 v193, v193
v_exp_f32 v194, v194
v_exp_f32 v195, v195
v_exp_f32 v196, v196
v_exp_f32 v197, v197
v_exp_f32 v198, v198
v_exp_f32 v199, v199
v_exp_f32 v200, v200
v_exp_f32 v201, v201
v_exp_f32 v202, v202
v_exp_f32 v203, v203
v_exp_f32 v204, v204
v_exp_f32 v205, v205
v_exp_f32 v206, v206
v_exp_f32 v207, v207
v_cvt_pk_bf16_f32 v192, v192, v193
v_cvt_pk_bf16_f32 v193, v194, v195
v_cvt_pk_bf16_f32 v194, v196, v197
v_cvt_pk_bf16_f32 v195, v198, v199
v_cvt_pk_bf16_f32 v200, v200, v201
v_cvt_pk_bf16_f32 v201, v202, v203
v_cvt_pk_bf16_f32 v202, v204, v205
v_cvt_pk_bf16_f32 v203, v206, v207
v_mfma_f32_16x16x32_bf16 a[168:171], a[160:163], v[192:195], a[168:171]
buffer_load_dwordx2 v[126:127], v5, s[40:43], s80 offen offset:3584 nt
ds_write_b128 v0, v[192:195] offset:11264
v_mfma_f32_16x16x32_bf16 a[168:171], a[160:163], v[200:203], a[168:171]
ds_write_b128 v0, v[200:203] offset:16384
v_mov_b32 v16, v13
v_pk_fma_f32 v[128:129], v[128:129], s[86:87], v[16:17] op_sel_hi:[1,1,0] neg_lo:[0,0,1] neg_hi:[0,0,1]
v_pk_fma_f32 v[130:131], v[130:131], s[86:87], v[16:17] op_sel_hi:[1,1,0] neg_lo:[0,0,1] neg_hi:[0,0,1]
v_pk_fma_f32 v[132:133], v[132:133], s[86:87], v[16:17] op_sel_hi:[1,1,0] neg_lo:[0,0,1] neg_hi:[0,0,1]
v_pk_fma_f32 v[134:135], v[134:135], s[86:87], v[16:17] op_sel_hi:[1,1,0] neg_lo:[0,0,1] neg_hi:[0,0,1]
v_pk_fma_f32 v[136:137], v[136:137], s[86:87], v[16:17] op_sel_hi:[1,1,0] neg_lo:[0,0,1] neg_hi:[0,0,1]
v_pk_fma_f32 v[138:139], v[138:139], s[86:87], v[16:17] op_sel_hi:[1,1,0] neg_lo:[0,0,1] neg_hi:[0,0,1]
v_pk_fma_f32 v[140:141], v[140:141], s[86:87], v[16:17] op_sel_hi:[1,1,0] neg_lo:[0,0,1] neg_hi:[0,0,1]
v_pk_fma_f32 v[142:143], v[142:143], s[86:87], v[16:17] op_sel_hi:[1,1,0] neg_lo:[0,0,1] neg_hi:[0,0,1]
v_exp_f32 v128, v128
v_exp_f32 v129, v129
v_exp_f32 v130, v130
v_exp_f32 v131, v131
v_exp_f32 v132, v132
v_exp_f32 v133, v133
v_exp_f32 v134, v134
v_exp_f32 v135, v135
v_exp_f32 v136, v136
v_exp_f32 v137, v137
v_exp_f32 v138, v138
v_exp_f32 v139, v139
v_exp_f32 v140, v140
v_exp_f32 v141, v141
v_exp_f32 v142, v142
v_exp_f32 v143, v143
v_cvt_pk_bf16_f32 v128, v128, v129
v_cvt_pk_bf16_f32 v129, v130, v131
v_cvt_pk_bf16_f32 v130, v132, v133
v_cvt_pk_bf16_f32 v131, v134, v135
v_cvt_pk_bf16_f32 v136, v136, v137
v_cvt_pk_bf16_f32 v137, v138, v139
v_cvt_pk_bf16_f32 v138, v140, v141
v_cvt_pk_bf16_f32 v139, v142, v143
v_mfma_f32_16x16x32_bf16 a[172:175], a[160:163], v[128:131], 0
ds_write_b128 v0, v[128:131] offset:2048
v_mfma_f32_16x16x32_bf16 a[172:175], a[160:163], v[136:139], a[172:175]
ds_write_b128 v0, v[136:139] offset:7168
s_mov_b32 s80, 98304
s_waitcnt lgkmcnt(1)
buffer_load_dwordx2 v[128:129], v5, s[40:43], s80 offen nt
buffer_load_dwordx2 v[130:131], v5, s[40:43], s80 offen offset:512 nt
buffer_load_dwordx2 v[132:133], v5, s[40:43], s80 offen offset:1024 nt
v_pk_fma_f32 v[208:209], v[208:209], s[86:87], v[16:17] op_sel_hi:[1,1,0] neg_lo:[0,0,1] neg_hi:[0,0,1]
v_pk_fma_f32 v[210:211], v[210:211], s[86:87], v[16:17] op_sel_hi:[1,1,0] neg_lo:[0,0,1] neg_hi:[0,0,1]
v_pk_fma_f32 v[212:213], v[212:213], s[86:87], v[16:17] op_sel_hi:[1,1,0] neg_lo:[0,0,1] neg_hi:[0,0,1]
v_pk_fma_f32 v[214:215], v[214:215], s[86:87], v[16:17] op_sel_hi:[1,1,0] neg_lo:[0,0,1] neg_hi:[0,0,1]
v_pk_fma_f32 v[216:217], v[216:217], s[86:87], v[16:17] op_sel_hi:[1,1,0] neg_lo:[0,0,1] neg_hi:[0,0,1]
v_pk_fma_f32 v[218:219], v[218:219], s[86:87], v[16:17] op_sel_hi:[1,1,0] neg_lo:[0,0,1] neg_hi:[0,0,1]
v_pk_fma_f32 v[220:221], v[220:221], s[86:87], v[16:17] op_sel_hi:[1,1,0] neg_lo:[0,0,1] neg_hi:[0,0,1]
v_pk_fma_f32 v[222:223], v[222:223], s[86:87], v[16:17] op_sel_hi:[1,1,0] neg_lo:[0,0,1] neg_hi:[0,0,1]
v_exp_f32 v208, v208
v_exp_f32 v209, v209
v_exp_f32 v210, v210
v_exp_f32 v211, v211
v_exp_f32 v212, v212
v_exp_f32 v213, v213
v_exp_f32 v214, v214
v_exp_f32 v215, v215
v_exp_f32 v216, v216
buffer_load_dwordx2 v[134:135], v5, s[40:43], s80 offen offset:1536 nt
s_waitcnt lgkmcnt(0)
buffer_load_dwordx2 v[136:137], v5, s[40:43], s80 offen offset:2048 nt
buffer_load_dwordx2 v[138:139], v5, s[40:43], s80 offen offset:2560 nt
v_exp_f32 v217, v217
v_exp_f32 v218, v218
v_exp_f32 v219, v219
v_exp_f32 v220, v220
v_exp_f32 v221, v221
v_exp_f32 v222, v222
v_exp_f32 v223, v223
v_cvt_pk_bf16_f32 v208, v208, v209
v_cvt_pk_bf16_f32 v209, v210, v211
v_cvt_pk_bf16_f32 v210, v212, v213
v_cvt_pk_bf16_f32 v211, v214, v215
v_cvt_pk_bf16_f32 v216, v216, v217
v_cvt_pk_bf16_f32 v217, v218, v219
)"
R"(v_cvt_pk_bf16_f32 v218, v220, v221
v_cvt_pk_bf16_f32 v219, v222, v223
v_mfma_f32_16x16x32_bf16 a[172:175], a[160:163], v[208:211], a[172:175]
ds_write_b128 v0, v[208:211] offset:12288
v_mfma_f32_16x16x32_bf16 a[172:175], a[160:163], v[216:219], a[172:175]
ds_write_b128 v0, v[216:219] offset:17408
v_mov_b32 v16, v14
v_pk_fma_f32 v[144:145], v[144:145], s[86:87], v[16:17] op_sel_hi:[1,1,0] neg_lo:[0,0,1] neg_hi:[0,0,1]
v_pk_fma_f32 v[146:147], v[146:147], s[86:87], v[16:17] op_sel_hi:[1,1,0] neg_lo:[0,0,1] neg_hi:[0,0,1]
v_pk_fma_f32 v[148:149], v[148:149], s[86:87], v[16:17] op_sel_hi:[1,1,0] neg_lo:[0,0,1] neg_hi:[0,0,1]
v_pk_fma_f32 v[150:151], v[150:151], s[86:87], v[16:17] op_sel_hi:[1,1,0] neg_lo:[0,0,1] neg_hi:[0,0,1]
v_pk_fma_f32 v[152:153], v[152:153], s[86:87], v[16:17] op_sel_hi:[1,1,0] neg_lo:[0,0,1] neg_hi:[0,0,1]
v_pk_fma_f32 v[154:155], v[154:155], s[86:87], v[16:17] op_sel_hi:[1,1,0] neg_lo:[0,0,1] neg_hi:[0,0,1]
v_pk_fma_f32 v[156:157], v[156:157], s[86:87], v[16:17] op_sel_hi:[1,1,0] neg_lo:[0,0,1] neg_hi:[0,0,1]
buffer_load_dwordx2 v[140:141], v5, s[40:43], s80 offen offset:3072 nt
buffer_load_dwordx2 v[142:143], v5, s[40:43], s80 offen offset:3584 nt
v_pk_fma_f32 v[158:159], v[158:159], s[86:87], v[16:17] op_sel_hi:[1,1,0] neg_lo:[0,0,1] neg_hi:[0,0,1]
v_exp_f32 v144, v144
v_exp_f32 v145, v145
v_exp_f32 v146, v146
v_exp_f32 v147, v147
v_exp_f32 v148, v148
v_exp_f32 v149, v149
v_exp_f32 v150, v150
v_exp_f32 v151, v151
v_exp_f32 v152, v152
v_exp_f32 v153, v153
v_exp_f32 v154, v154
v_exp_f32 v155, v155
v_exp_f32 v156, v156
v_exp_f32 v157, v157
v_exp_f32 v158, v158
v_exp_f32 v159, v159
v_cvt_pk_bf16_f32 v144, v144, v145
v_cvt_pk_bf16_f32 v145, v146, v147
v_cvt_pk_bf16_f32 v146, v148, v149
v_cvt_pk_bf16_f32 v147, v150, v151
v_cvt_pk_bf16_f32 v152, v152, v153
v_cvt_pk_bf16_f32 v153, v154, v155
v_cvt_pk_bf16_f32 v154, v156, v157
v_cvt_pk_bf16_f32 v155, v158, v159
v_mfma_f32_16x16x32_bf16 a[176:179], a[160:163], v[144:147], 0
ds_write_b128 v0, v[144:147] offset:3072
v_mfma_f32_16x16x32_bf16 a[176:179], a[160:163], v[152:155], a[176:179]
ds_write_b128 v0, v[152:155] offset:8192
s_add_u32 s80, s80, 16384
s_waitcnt lgkmcnt(1)
buffer_load_dwordx2 v[144:145], v5, s[40:43], s80 offen nt
s_waitcnt vmcnt(50)
buffer_load_dwordx2 v[146:147], v5, s[40:43], s80 offen offset:512 nt
buffer_load_dwordx2 v[148:149], v5, s[40:43], s80 offen offset:1024 nt
v_pk_fma_f32 v[224:225], v[224:225], s[86:87], v[16:17] op_sel_hi:[1,1,0] neg_lo:[0,0,1] neg_hi:[0,0,1]
v_pk_fma_f32 v[226:227], v[226:227], s[86:87], v[16:17] op_sel_hi:[1,1,0] neg_lo:[0,0,1] neg_hi:[0,0,1]
v_pk_fma_f32 v[228:229], v[228:229], s[86:87], v[16:17] op_sel_hi:[1,1,0] neg_lo:[0,0,1] neg_hi:[0,0,1]
v_pk_fma_f32 v[230:231], v[230:231], s[86:87], v[16:17] op_sel_hi:[1,1,0] neg_lo:[0,0,1] neg_hi:[0,0,1]
v_pk_fma_f32 v[232:233], v[232:233], s[86:87], v[16:17] op_sel_hi:[1,1,0] neg_lo:[0,0,1] neg_hi:[0,0,1]
v_pk_fma_f32 v[234:235], v[234:235], s[86:87], v[16:17] op_sel_hi:[1,1,0] neg_lo:[0,0,1] neg_hi:[0,0,1]
v_pk_fma_f32 v[236:237], v[236:237], s[86:87], v[16:17] op_sel_hi:[1,1,0] neg_lo:[0,0,1] neg_hi:[0,0,1]
v_pk_fma_f32 v[238:239], v[238:239], s[86:87], v[16:17] op_sel_hi:[1,1,0] neg_lo:[0,0,1] neg_hi:[0,0,1]
v_exp_f32 v224, v224
v_exp_f32 v225, v225
v_exp_f32 v226, v226
v_exp_f32 v227, v227
v_exp_f32 v228, v228
v_exp_f32 v229, v229
v_exp_f32 v230, v230
v_exp_f32 v231, v231
v_exp_f32 v232, v232
buffer_load_dwordx2 v[150:151], v5, s[40:43], s80 offen offset:1536 nt
s_waitcnt lgkmcnt(0)
buffer_load_dwordx2 v[152:153], v5, s[40:43], s80 offen offset:2048 nt
buffer_load_dwordx2 v[154:155], v5, s[40:43], s80 offen offset:2560 nt
v_exp_f32 v233, v233
v_exp_f32 v234, v234
v_exp_f32 v235, v235
v_exp_f32 v236, v236
v_exp_f32 v237, v237
v_exp_f32 v238, v238
v_exp_f32 v239, v239
v_cvt_pk_bf16_f32 v224, v224, v225
v_cvt_pk_bf16_f32 v225, v226, v227
v_cvt_pk_bf16_f32 v226, v228, v229
v_cvt_pk_bf16_f32 v227, v230, v231
v_cvt_pk_bf16_f32 v232, v232, v233
v_cvt_pk_bf16_f32 v233, v234, v235
v_cvt_pk_bf16_f32 v234, v236, v237
v_cvt_pk_bf16_f32 v235, v238, v239
v_mfma_f32_16x16x32_bf16 a[176:179], a[160:163], v[224:227], a[176:179]
ds_write_b128 v0, v[224:227] offset:13312
v_mfma_f32_16x16x32_bf16 a[176:179], a[160:163], v[232:235], a[176:179]
ds_write_b128 v0, v[232:235] offset:18432
v_mov_b32 v16, v15
v_pk_fma_f32 v[160:161], v[160:161], s[86:87], v[16:17] op_sel_hi:[1,1,0] neg_lo:[0,0,1] neg_hi:[0,0,1]
v_pk_fma_f32 v[162:163], v[162:163], s[86:87], v[16:17] op_sel_hi:[1,1,0] neg_lo:[0,0,1] neg_hi:[0,0,1]
v_pk_fma_f32 v[164:165], v[164:165], s[86:87], v[16:17] op_sel_hi:[1,1,0] neg_lo:[0,0,1] neg_hi:[0,0,1]
v_pk_fma_f32 v[166:167], v[166:167], s[86:87], v[16:17] op_sel_hi:[1,1,0] neg_lo:[0,0,1] neg_hi:[0,0,1]
v_pk_fma_f32 v[168:169], v[168:169], s[86:87], v[16:17] op_sel_hi:[1,1,0] neg_lo:[0,0,1] neg_hi:[0,0,1]
v_pk_fma_f32 v[170:171], v[170:171], s[86:87], v[16:17] op_sel_hi:[1,1,0] neg_lo:[0,0,1] neg_hi:[0,0,1]
v_pk_fma_f32 v[172:173], v[172:173], s[86:87], v[16:17] op_sel_hi:[1,1,0] neg_lo:[0,0,1] neg_hi:[0,0,1]
buffer_load_dwordx2 v[156:157], v5, s[40:43], s80 offen offset:3072 nt
buffer_load_dwordx2 v[158:159], v5, s[40:43], s80 offen offset:3584 nt
v_pk_fma_f32 v[174:175], v[174:175], s[86:87], v[16:17] op_sel_hi:[1,1,0] neg_lo:[0,0,1] neg_hi:[0,0,1]
v_exp_f32 v160, v160
v_exp_f32 v161, v161
v_exp_f32 v162, v162
v_exp_f32 v163, v163
v_exp_f32 v164, v164
v_exp_f32 v165, v165
v_exp_f32 v166, v166
v_exp_f32 v167, v167
v_exp_f32 v168, v168
v_exp_f32 v169, v169
v_exp_f32 v170, v170
v_exp_f32 v171, v171
v_exp_f32 v172, v172
v_exp_f32 v173, v173
v_exp_f32 v174, v174
v_exp_f32 v175, v175
v_cvt_pk_bf16_f32 v160, v160, v161
v_cvt_pk_bf16_f32 v161, v162, v163
v_cvt_pk_bf16_f32 v162, v164, v165
v_cvt_pk_bf16_f32 v163, v166, v167
v_cvt_pk_bf16_f32 v168, v168, v169
v_cvt_pk_bf16_f32 v169, v170, v171
v_cvt_pk_bf16_f32 v170, v172, v173
v_cvt_pk_bf16_f32 v171, v174, v175
v_mfma_f32_16x16x32_bf16 a[180:183], a[160:163], v[160:163], 0
ds_write_b128 v0, v[160:163] offset:4096
v_mfma_f32_16x16x32_bf16 a[180:183], a[160:163], v[168:171], a[180:183]
ds_write_b128 v0, v[168:171] offset:9216
s_mov_b32 s80, 131072
s_waitcnt lgkmcnt(1)
buffer_load_dwordx2 v[160:161], v5, s[40:43], s80 offen nt
s_waitcnt vmcnt(50)
buffer_load_dwordx2 v[162:163], v5, s[40:43], s80 offen offset:512 nt
buffer_load_dwordx2 v[164:165], v5, s[40:43], s80 offen offset:1024 nt
v_pk_fma_f32 v[240:241], v[240:241], s[86:87], v[16:17] op_sel_hi:[1,1,0] neg_lo:[0,0,1] neg_hi:[0,0,1]
v_pk_fma_f32 v[242:243], v[242:243], s[86:87], v[16:17] op_sel_hi:[1,1,0] neg_lo:[0,0,1] neg_hi:[0,0,1]
v_pk_fma_f32 v[244:245], v[244:245], s[86:87], v[16:17] op_sel_hi:[1,1,0] neg_lo:[0,0,1] neg_hi:[0,0,1]
v_pk_fma_f32 v[246:247], v[246:247], s[86:87], v[16:17] op_sel_hi:[1,1,0] neg_lo:[0,0,1] neg_hi:[0,0,1]
v_pk_fma_f32 v[248:249], v[248:249], s[86:87], v[16:17] op_sel_hi:[1,1,0] neg_lo:[0,0,1] neg_hi:[0,0,1]
v_pk_fma_f32 v[250:251], v[250:251], s[86:87], v[16:17] op_sel_hi:[1,1,0] neg_lo:[0,0,1] neg_hi:[0,0,1]
v_pk_fma_f32 v[252:253], v[252:253], s[86:87], v[16:17] op_sel_hi:[1,1,0] neg_lo:[0,0,1] neg_hi:[0,0,1]
v_pk_fma_f32 v[254:255], v[254:255], s[86:87], v[16:17] op_sel_hi:[1,1,0] neg_lo:[0,0,1] neg_hi:[0,0,1]
v_exp_f32 v240, v240
v_exp_f32 v241, v241
v_exp_f32 v242, v242
v_exp_f32 v243, v243
v_exp_f32 v244, v244
v_exp_f32 v245, v245
v_exp_f32 v246, v246
v_exp_f32 v247, v247
v_exp_f32 v248, v248
buffer_load_dwordx2 v[166:167], v5, s[40:43], s80 offen offset:1536 nt
s_waitcnt lgkmcnt(0)
buffer_load_dwordx2 v[168:169], v5, s[40:43], s80 offen offset:2048 nt
buffer_load_dwordx2 v[170:171], v5, s[40:43], s80 offen offset:2560 nt
v_exp_f32 v249, v249
v_exp_f32 v250, v250
v_exp_f32 v251, v251
v_exp_f32 v252, v252
v_exp_f32 v253, v253
v_exp_f32 v254, v254
v_exp_f32 v255, v255
v_cvt_pk_bf16_f32 v240, v240, v241
v_cvt_pk_bf16_f32 v241, v242, v243
v_cvt_pk_bf16_f32 v242, v244, v245
v_cvt_pk_bf16_f32 v243, v246, v247
v_cvt_pk_bf16_f32 v248, v248, v249
v_cvt_pk_bf16_f32 v249, v250, v251
v_cvt_pk_bf16_f32 v250, v252, v253
v_cvt_pk_bf16_f32 v251, v254, v255
v_mfma_f32_16x16x32_bf16 a[180:183], a[160:163], v[240:243], a[180:183]
ds_write_b128 v0, v[240:243] offset:14336
v_mfma_f32_16x16x32_bf16 a[180:183], a[160:163], v[248:251], a[180:183]
ds_write_b128 v0, v[248:251] offset:19456
ds_write_b32 v9, a164 offset:1280
ds_write_b32 v9, a168 offset:1536
buffer_load_dwordx2 v[172:173], v5, s[40:43], s80 offen offset:3072 nt
buffer_load_dwordx2 v[174:175], v5, s[40:43], s80 offen offset:3584 nt
ds_write_b32 v9, a172 offset:1792
ds_write_b32 v9, a176 offset:2048
s_nop 0
ds_write_b32 v9, a180 offset:2304
s_mov_b32 vcc_lo, 0x00ff00ff
s_mov_b32 vcc_hi, 0x00ff00ff
v_perm_b32 v16, v50, v48, s84
v_perm_b32 v17, v54, v52, s84
v_perm_b32 v18, v58, v56, s84
v_perm_b32 v19, v62, v60, s84
v_perm_b32 v20, v66, v64, s84
v_perm_b32 v21, v70, v68, s84
v_perm_b32 v22, v74, v72, s84
s_waitcnt vmcnt(56)
v_perm_b32 v23, v78, v76, s84
s_waitcnt lgkmcnt(0)
s_barrier
ds_read_b128 a[160:163], v2 offset:16384
ds_read_b128 a[164:167], v2 offset:17408
ds_read_b128 a[168:171], v2 offset:18432
)"
R"(ds_read_b128 a[172:175], v2 offset:19456
ds_read_b128 a[176:179], v2 offset:20480
ds_read_b128 a[180:183], v2 offset:21504
ds_read_b128 a[184:187], v2 offset:22528
ds_read_b128 a[188:191], v2 offset:23552
ds_read_b128 a[192:195], v2 offset:24576
ds_read_b128 a[196:199], v2 offset:25600
s_waitcnt lgkmcnt(5)
v_mfma_f32_16x16x32_bf16 v[176:179], v[16:19], a[160:163], 0
s_waitcnt lgkmcnt(0)
v_perm_b32 v24, v50, v48, s85
v_perm_b32 v25, v54, v52, s85
v_mfma_f32_16x16x32_bf16 v[176:179], v[20:23], a[180:183], v[176:179]
v_perm_b32 v26, v58, v56, s85
v_perm_b32 v27, v62, v60, s85
v_mfma_f32_16x16x32_bf16 v[192:195], v[16:19], a[164:167], 0
v_perm_b32 v28, v66, v64, s85
v_perm_b32 v29, v70, v68, s85
v_mfma_f32_16x16x32_bf16 v[192:195], v[20:23], a[184:187], v[192:195]
v_perm_b32 v30, v74, v72, s85
v_perm_b32 v31, v78, v76, s85
v_mfma_f32_16x16x32_bf16 v[208:211], v[16:19], a[168:171], 0
ds_read_b128 a[200:203], v2 offset:26624
v_mfma_f32_16x16x32_bf16 v[208:211], v[20:23], a[188:191], v[208:211]
ds_read_b128 a[220:223], v2 offset:31744
v_mfma_f32_16x16x32_bf16 v[224:227], v[16:19], a[172:175], 0
v_mfma_f32_16x16x32_bf16 v[224:227], v[20:23], a[192:195], v[224:227]
v_mfma_f32_16x16x32_bf16 v[240:243], v[16:19], a[176:179], 0
v_mfma_f32_16x16x32_bf16 v[240:243], v[20:23], a[196:199], v[240:243]
v_mfma_f32_16x16x32_bf16 v[180:183], v[24:27], a[160:163], 0
v_perm_b32 v16, v51, v49, s84
v_perm_b32 v17, v55, v53, s84
v_mfma_f32_16x16x32_bf16 v[180:183], v[28:31], a[180:183], v[180:183]
v_perm_b32 v18, v59, v57, s84
v_perm_b32 v19, v63, v61, s84
v_mfma_f32_16x16x32_bf16 v[196:199], v[24:27], a[164:167], 0
ds_read_b128 a[204:207], v2 offset:27648
v_perm_b32 v20, v67, v65, s84
v_mfma_f32_16x16x32_bf16 v[196:199], v[28:31], a[184:187], v[196:199]
ds_read_b128 a[224:227], v2 offset:32768
v_perm_b32 v21, v71, v69, s84
v_mfma_f32_16x16x32_bf16 v[212:215], v[24:27], a[168:171], 0
v_perm_b32 v22, v75, v73, s84
v_perm_b32 v23, v79, v77, s84
v_mfma_f32_16x16x32_bf16 v[212:215], v[28:31], a[188:191], v[212:215]
v_mfma_f32_16x16x32_bf16 v[228:231], v[24:27], a[172:175], 0
ds_read_b128 a[208:211], v2 offset:28672
v_mfma_f32_16x16x32_bf16 v[228:231], v[28:31], a[192:195], v[228:231]
ds_read_b128 a[228:231], v2 offset:33792
v_mfma_f32_16x16x32_bf16 v[244:247], v[24:27], a[176:179], 0
v_mfma_f32_16x16x32_bf16 v[244:247], v[28:31], a[196:199], v[244:247]
v_mfma_f32_16x16x32_bf16 v[184:187], v[16:19], a[160:163], 0
v_perm_b32 v24, v51, v49, s85
v_perm_b32 v25, v55, v53, s85
v_mfma_f32_16x16x32_bf16 v[184:187], v[20:23], a[180:183], v[184:187]
v_perm_b32 v26, v59, v57, s85
v_perm_b32 v27, v63, v61, s85
v_mfma_f32_16x16x32_bf16 v[200:203], v[16:19], a[164:167], 0
s_add_u32 s80, s80, 16384
v_perm_b32 v28, v67, v65, s85
v_perm_b32 v29, v71, v69, s85
v_mfma_f32_16x16x32_bf16 v[200:203], v[20:23], a[184:187], v[200:203]
v_perm_b32 v30, v75, v73, s85
v_perm_b32 v31, v79, v77, s85
v_mfma_f32_16x16x32_bf16 v[216:219], v[16:19], a[168:171], 0
ds_read_b128 a[212:215], v2 offset:29696
v_mfma_f32_16x16x32_bf16 v[216:219], v[20:23], a[188:191], v[216:219]
ds_read_b128 a[232:235], v2 offset:34816
v_mfma_f32_16x16x32_bf16 v[232:235], v[16:19], a[172:175], 0
buffer_load_dwordx2 v[48:49], v5, s[40:43], s80 offen nt
v_mfma_f32_16x16x32_bf16 v[232:235], v[20:23], a[192:195], v[232:235]
buffer_load_dwordx2 v[50:51], v5, s[40:43], s80 offen offset:512 nt
v_mfma_f32_16x16x32_bf16 v[248:251], v[16:19], a[176:179], 0
ds_read_b128 a[216:219], v2 offset:30720
v_mfma_f32_16x16x32_bf16 v[248:251], v[20:23], a[196:199], v[248:251]
ds_read_b128 a[236:239], v2 offset:35840
v_mfma_f32_16x16x32_bf16 v[188:191], v[24:27], a[160:163], 0
s_waitcnt vmcnt(42)
v_perm_b32 v16, v82, v80, s84
v_perm_b32 v17, v86, v84, s84
v_mfma_f32_16x16x32_bf16 v[188:191], v[28:31], a[180:183], v[188:191]
v_perm_b32 v18, v90, v88, s84
v_perm_b32 v19, v94, v92, s84
v_mfma_f32_16x16x32_bf16 v[204:207], v[24:27], a[164:167], 0
v_perm_b32 v20, v34, v32, s84
v_perm_b32 v21, v38, v36, s84
v_mfma_f32_16x16x32_bf16 v[204:207], v[28:31], a[184:187], v[204:207]
v_perm_b32 v22, v42, v40, s84
v_perm_b32 v23, v46, v44, s84
v_mfma_f32_16x16x32_bf16 v[220:223], v[24:27], a[168:171], 0
buffer_load_dwordx2 v[52:53], v5, s[40:43], s80 offen offset:1024 nt
v_mfma_f32_16x16x32_bf16 v[220:223], v[28:31], a[188:191], v[220:223]
buffer_load_dwordx2 v[54:55], v5, s[40:43], s80 offen offset:1536 nt
v_mfma_f32_16x16x32_bf16 v[236:239], v[24:27], a[172:175], 0
buffer_load_dwordx2 v[56:57], v5, s[40:43], s80 offen offset:2048 nt
v_mfma_f32_16x16x32_bf16 v[236:239], v[28:31], a[192:195], v[236:239]
buffer_load_dwordx2 v[58:59], v5, s[40:43], s80 offen offset:2560 nt
v_mfma_f32_16x16x32_bf16 v[252:255], v[24:27], a[176:179], 0
buffer_load_dwordx2 v[60:61], v5, s[40:43], s80 offen offset:3072 nt
v_mfma_f32_16x16x32_bf16 v[252:255], v[28:31], a[196:199], v[252:255]
buffer_load_dwordx2 v[62:63], v5, s[40:43], s80 offen offset:3584 nt
s_waitcnt lgkmcnt(1)
v_mfma_f32_16x16x32_bf16 v[176:179], v[16:19], a[200:203], v[176:179]
s_mov_b32 s80, 163840
v_perm_b32 v24, v82, v80, s85
v_perm_b32 v25, v86, v84, s85
v_mfma_f32_16x16x32_bf16 v[176:179], v[20:23], a[220:223], v[176:179]
v_perm_b32 v26, v90, v88, s85
v_perm_b32 v27, v94, v92, s85
v_mfma_f32_16x16x32_bf16 v[192:195], v[16:19], a[204:207], v[192:195]
v_perm_b32 v28, v34, v32, s85
v_perm_b32 v29, v38, v36, s85
v_mfma_f32_16x16x32_bf16 v[192:195], v[20:23], a[224:227], v[192:195]
v_perm_b32 v30, v42, v40, s85
v_perm_b32 v31, v46, v44, s85
v_mfma_f32_16x16x32_bf16 v[208:211], v[16:19], a[208:211], v[208:211]
ds_read_b128 a[160:163], v2 offset:36864
v_mfma_f32_16x16x32_bf16 v[208:211], v[20:23], a[228:231], v[208:211]
ds_read_b128 a[180:183], v2 offset:41984
v_mfma_f32_16x16x32_bf16 v[224:227], v[16:19], a[212:215], v[224:227]
buffer_load_dwordx2 v[64:65], v5, s[40:43], s80 offen nt
v_mfma_f32_16x16x32_bf16 v[224:227], v[20:23], a[232:235], v[224:227]
buffer_load_dwordx2 v[66:67], v5, s[40:43], s80 offen offset:512 nt
v_mfma_f32_16x16x32_bf16 v[240:243], v[16:19], a[216:219], v[240:243]
ds_read_b128 a[164:167], v2 offset:37888
s_waitcnt lgkmcnt(3)
v_mfma_f32_16x16x32_bf16 v[240:243], v[20:23], a[236:239], v[240:243]
ds_read_b128 a[184:187], v2 offset:43008
v_mfma_f32_16x16x32_bf16 v[180:183], v[24:27], a[200:203], v[180:183]
v_perm_b32 v16, v83, v81, s84
v_perm_b32 v17, v87, v85, s84
v_mfma_f32_16x16x32_bf16 v[180:183], v[28:31], a[220:223], v[180:183]
v_perm_b32 v18, v91, v89, s84
v_perm_b32 v19, v95, v93, s84
v_mfma_f32_16x16x32_bf16 v[196:199], v[24:27], a[204:207], v[196:199]
v_perm_b32 v20, v35, v33, s84
v_perm_b32 v21, v39, v37, s84
v_mfma_f32_16x16x32_bf16 v[196:199], v[28:31], a[224:227], v[196:199]
v_perm_b32 v22, v43, v41, s84
v_perm_b32 v23, v47, v45, s84
v_mfma_f32_16x16x32_bf16 v[212:215], v[24:27], a[208:211], v[212:215]
buffer_load_dwordx2 v[68:69], v5, s[40:43], s80 offen offset:1024 nt
v_mfma_f32_16x16x32_bf16 v[212:215], v[28:31], a[228:231], v[212:215]
ds_read_b128 a[168:171], v2 offset:38912
v_mfma_f32_16x16x32_bf16 v[228:231], v[24:27], a[212:215], v[228:231]
ds_read_b128 a[188:191], v2 offset:44032
v_mfma_f32_16x16x32_bf16 v[228:231], v[28:31], a[232:235], v[228:231]
buffer_load_dwordx2 v[70:71], v5, s[40:43], s80 offen offset:1536 nt
v_mfma_f32_16x16x32_bf16 v[244:247], v[24:27], a[216:219], v[244:247]
buffer_load_dwordx2 v[72:73], v5, s[40:43], s80 offen offset:2048 nt
v_mfma_f32_16x16x32_bf16 v[244:247], v[28:31], a[236:239], v[244:247]
buffer_load_dwordx2 v[74:75], v5, s[40:43], s80 offen offset:2560 nt
v_mfma_f32_16x16x32_bf16 v[184:187], v[16:19], a[200:203], v[184:187]
v_perm_b32 v24, v83, v81, s85
v_perm_b32 v25, v87, v85, s85
v_mfma_f32_16x16x32_bf16 v[184:187], v[20:23], a[220:223], v[184:187]
v_perm_b32 v26, v91, v89, s85
v_perm_b32 v27, v95, v93, s85
v_mfma_f32_16x16x32_bf16 v[200:203], v[16:19], a[204:207], v[200:203]
v_perm_b32 v28, v35, v33, s85
v_perm_b32 v29, v39, v37, s85
v_mfma_f32_16x16x32_bf16 v[200:203], v[20:23], a[224:227], v[200:203]
v_perm_b32 v30, v43, v41, s85
v_perm_b32 v31, v47, v45, s85
v_mfma_f32_16x16x32_bf16 v[216:219], v[16:19], a[208:211], v[216:219]
ds_read_b128 a[172:175], v2 offset:39936
v_mfma_f32_16x16x32_bf16 v[216:219], v[20:23], a[228:231], v[216:219]
ds_read_b128 a[192:195], v2 offset:45056
v_mfma_f32_16x16x32_bf16 v[232:235], v[16:19], a[212:215], v[232:235]
buffer_load_dwordx2 v[76:77], v5, s[40:43], s80 offen offset:3072 nt
v_mfma_f32_16x16x32_bf16 v[232:235], v[20:23], a[232:235], v[232:235]
buffer_load_dwordx2 v[78:79], v5, s[40:43], s80 offen offset:3584 nt
v_mfma_f32_16x16x32_bf16 v[248:251], v[16:19], a[216:219], v[248:251]
s_add_u32 s80, s80, 16384
ds_read_b128 a[176:179], v2 offset:40960
v_mfma_f32_16x16x32_bf16 v[248:251], v[20:23], a[236:239], v[248:251]
ds_read_b128 a[196:199], v2 offset:46080
v_mfma_f32_16x16x32_bf16 v[188:191], v[24:27], a[200:203], v[188:191]
s_waitcnt vmcnt(40)
v_perm_b32 v16, v98, v96, s84
v_perm_b32 v17, v102, v100, s84
v_mfma_f32_16x16x32_bf16 v[188:191], v[28:31], a[220:223], v[188:191]
v_perm_b32 v18, v106, v104, s84
v_perm_b32 v19, v110, v108, s84
v_mfma_f32_16x16x32_bf16 v[204:207], v[24:27], a[204:207], v[204:207]
v_perm_b32 v20, v114, v112, s84
v_perm_b32 v21, v118, v116, s84
v_mfma_f32_16x16x32_bf16 v[204:207], v[28:31], a[224:227], v[204:207]
v_perm_b32 v22, v122, v120, s84
v_perm_b32 v23, v126, v124, s84
v_mfma_f32_16x16x32_bf16 v[220:223], v[24:27], a[208:211], v[220:223]
buffer_load_dwordx2 v[32:33], v5, s[40:43], s80 offen nt
v_mfma_f32_16x16x32_bf16 v[220:223], v[28:31], a[228:231], v[220:223]
buffer_load_dwordx2 v[34:35], v5, s[40:43], s80 offen offset:512 nt
v_mfma_f32_16x16x32_bf16 v[236:239], v[24:27], a[212:215], v[236:239]
buffer_load_dwordx2 v[36:37], v5, s[40:43], s80 offen offset:1024 nt
v_mfma_f32_16x16x32_bf16 v[236:239], v[28:31], a[232:235], v[236:239]
buffer_load_dwordx2 v[38:39], v5, s[40:43], s80 offen offset:1536 nt
v_mfma_f32_16x16x32_bf16 v[252:255], v[24:27], a[216:219], v[252:255]
v_mfma_f32_16x16x32_bf16 v[252:255], v[28:31], a[236:239], v[252:255]
)"
R"(s_waitcnt lgkmcnt(1)
v_mfma_f32_16x16x32_bf16 v[176:179], v[16:19], a[160:163], v[176:179]
v_perm_b32 v24, v98, v96, s85
v_perm_b32 v25, v102, v100, s85
v_mfma_f32_16x16x32_bf16 v[176:179], v[20:23], a[180:183], v[176:179]
v_perm_b32 v26, v106, v104, s85
v_perm_b32 v27, v110, v108, s85
v_mfma_f32_16x16x32_bf16 v[192:195], v[16:19], a[164:167], v[192:195]
v_perm_b32 v28, v114, v112, s85
v_perm_b32 v29, v118, v116, s85
v_mfma_f32_16x16x32_bf16 v[192:195], v[20:23], a[184:187], v[192:195]
v_perm_b32 v30, v122, v120, s85
v_perm_b32 v31, v126, v124, s85
v_mfma_f32_16x16x32_bf16 v[208:211], v[16:19], a[168:171], v[208:211]
ds_read_b128 a[200:203], v2 offset:47104
v_mfma_f32_16x16x32_bf16 v[208:211], v[20:23], a[188:191], v[208:211]
ds_read_b128 a[220:223], v2 offset:52224
v_mfma_f32_16x16x32_bf16 v[224:227], v[16:19], a[172:175], v[224:227]
v_mfma_f32_16x16x32_bf16 v[224:227], v[20:23], a[192:195], v[224:227]
v_mfma_f32_16x16x32_bf16 v[240:243], v[16:19], a[176:179], v[240:243]
ds_read_b128 a[204:207], v2 offset:48128
s_waitcnt lgkmcnt(3)
v_mfma_f32_16x16x32_bf16 v[240:243], v[20:23], a[196:199], v[240:243]
ds_read_b128 a[224:227], v2 offset:53248
v_mfma_f32_16x16x32_bf16 v[180:183], v[24:27], a[160:163], v[180:183]
v_perm_b32 v16, v99, v97, s84
v_perm_b32 v17, v103, v101, s84
v_mfma_f32_16x16x32_bf16 v[180:183], v[28:31], a[180:183], v[180:183]
v_perm_b32 v18, v107, v105, s84
v_perm_b32 v19, v111, v109, s84
v_mfma_f32_16x16x32_bf16 v[196:199], v[24:27], a[164:167], v[196:199]
v_perm_b32 v20, v115, v113, s84
v_perm_b32 v21, v119, v117, s84
v_mfma_f32_16x16x32_bf16 v[196:199], v[28:31], a[184:187], v[196:199]
v_perm_b32 v22, v123, v121, s84
v_perm_b32 v23, v127, v125, s84
v_mfma_f32_16x16x32_bf16 v[212:215], v[24:27], a[168:171], v[212:215]
v_mfma_f32_16x16x32_bf16 v[212:215], v[28:31], a[188:191], v[212:215]
ds_read_b128 a[208:211], v2 offset:49152
v_mfma_f32_16x16x32_bf16 v[228:231], v[24:27], a[172:175], v[228:231]
ds_read_b128 a[228:231], v2 offset:54272
v_mfma_f32_16x16x32_bf16 v[228:231], v[28:31], a[192:195], v[228:231]
v_mfma_f32_16x16x32_bf16 v[244:247], v[24:27], a[176:179], v[244:247]
v_mfma_f32_16x16x32_bf16 v[244:247], v[28:31], a[196:199], v[244:247]
v_mfma_f32_16x16x32_bf16 v[184:187], v[16:19], a[160:163], v[184:187]
v_perm_b32 v24, v99, v97, s85
v_perm_b32 v25, v103, v101, s85
v_mfma_f32_16x16x32_bf16 v[184:187], v[20:23], a[180:183], v[184:187]
v_perm_b32 v26, v107, v105, s85
v_perm_b32 v27, v111, v109, s85
v_mfma_f32_16x16x32_bf16 v[200:203], v[16:19], a[164:167], v[200:203]
v_perm_b32 v28, v115, v113, s85
v_perm_b32 v29, v119, v117, s85
v_mfma_f32_16x16x32_bf16 v[200:203], v[20:23], a[184:187], v[200:203]
v_perm_b32 v30, v123, v121, s85
v_perm_b32 v31, v127, v125, s85
v_mfma_f32_16x16x32_bf16 v[216:219], v[16:19], a[168:171], v[216:219]
ds_read_b128 a[212:215], v2 offset:50176
v_mfma_f32_16x16x32_bf16 v[216:219], v[20:23], a[188:191], v[216:219]
ds_read_b128 a[232:235], v2 offset:55296
v_mfma_f32_16x16x32_bf16 v[232:235], v[16:19], a[172:175], v[232:235]
v_mfma_f32_16x16x32_bf16 v[232:235], v[20:23], a[192:195], v[232:235]
v_mfma_f32_16x16x32_bf16 v[248:251], v[16:19], a[176:179], v[248:251]
ds_read_b128 a[216:219], v2 offset:51200
v_mfma_f32_16x16x32_bf16 v[248:251], v[20:23], a[196:199], v[248:251]
ds_read_b128 a[236:239], v2 offset:56320
v_mfma_f32_16x16x32_bf16 v[188:191], v[24:27], a[160:163], v[188:191]
s_waitcnt vmcnt(28)
v_perm_b32 v16, v130, v128, s84
v_perm_b32 v17, v134, v132, s84
v_mfma_f32_16x16x32_bf16 v[188:191], v[28:31], a[180:183], v[188:191]
v_perm_b32 v18, v138, v136, s84
v_perm_b32 v19, v142, v140, s84
v_mfma_f32_16x16x32_bf16 v[204:207], v[24:27], a[164:167], v[204:207]
v_perm_b32 v20, v146, v144, s84
v_perm_b32 v21, v150, v148, s84
v_mfma_f32_16x16x32_bf16 v[204:207], v[28:31], a[184:187], v[204:207]
v_perm_b32 v22, v154, v152, s84
v_perm_b32 v23, v158, v156, s84
v_mfma_f32_16x16x32_bf16 v[220:223], v[24:27], a[168:171], v[220:223]
buffer_load_dwordx2 v[40:41], v5, s[40:43], s80 offen offset:2048 nt
v_mfma_f32_16x16x32_bf16 v[220:223], v[28:31], a[188:191], v[220:223]
buffer_load_dwordx2 v[42:43], v5, s[40:43], s80 offen offset:2560 nt
v_mfma_f32_16x16x32_bf16 v[236:239], v[24:27], a[172:175], v[236:239]
v_mfma_f32_16x16x32_bf16 v[236:239], v[28:31], a[192:195], v[236:239]
v_mfma_f32_16x16x32_bf16 v[252:255], v[24:27], a[176:179], v[252:255]
buffer_load_dwordx2 v[44:45], v5, s[40:43], s80 offen offset:3072 nt
v_mfma_f32_16x16x32_bf16 v[252:255], v[28:31], a[196:199], v[252:255]
s_waitcnt lgkmcnt(1)
v_mfma_f32_16x16x32_bf16 v[176:179], v[16:19], a[200:203], v[176:179]
v_perm_b32 v24, v130, v128, s85
v_perm_b32 v25, v134, v132, s85
v_mfma_f32_16x16x32_bf16 v[176:179], v[20:23], a[220:223], v[176:179]
v_perm_b32 v26, v138, v136, s85
v_perm_b32 v27, v142, v140, s85
v_mfma_f32_16x16x32_bf16 v[192:195], v[16:19], a[204:207], v[192:195]
v_perm_b32 v28, v146, v144, s85
v_perm_b32 v29, v150, v148, s85
v_mfma_f32_16x16x32_bf16 v[192:195], v[20:23], a[224:227], v[192:195]
v_perm_b32 v30, v154, v152, s85
v_perm_b32 v31, v158, v156, s85
v_mfma_f32_16x16x32_bf16 v[208:211], v[16:19], a[208:211], v[208:211]
ds_read_b128 a[160:163], v2 offset:57344
v_mfma_f32_16x16x32_bf16 v[208:211], v[20:23], a[228:231], v[208:211]
ds_read_b128 a[180:183], v2 offset:62464
v_mfma_f32_16x16x32_bf16 v[224:227], v[16:19], a[212:215], v[224:227]
buffer_load_dwordx2 v[46:47], v5, s[40:43], s80 offen offset:3584 nt
v_mfma_f32_16x16x32_bf16 v[224:227], v[20:23], a[232:235], v[224:227]
s_mov_b32 s80, 196608
v_mfma_f32_16x16x32_bf16 v[240:243], v[16:19], a[216:219], v[240:243]
ds_read_b128 a[164:167], v2 offset:58368
s_waitcnt lgkmcnt(3)
v_mfma_f32_16x16x32_bf16 v[240:243], v[20:23], a[236:239], v[240:243]
ds_read_b128 a[184:187], v2 offset:63488
v_mfma_f32_16x16x32_bf16 v[180:183], v[24:27], a[200:203], v[180:183]
v_perm_b32 v16, v131, v129, s84
v_perm_b32 v17, v135, v133, s84
v_mfma_f32_16x16x32_bf16 v[180:183], v[28:31], a[220:223], v[180:183]
v_perm_b32 v18, v139, v137, s84
v_perm_b32 v19, v143, v141, s84
v_mfma_f32_16x16x32_bf16 v[196:199], v[24:27], a[204:207], v[196:199]
v_perm_b32 v20, v147, v145, s84
v_perm_b32 v21, v151, v149, s84
v_mfma_f32_16x16x32_bf16 v[196:199], v[28:31], a[224:227], v[196:199]
v_perm_b32 v22, v155, v153, s84
v_perm_b32 v23, v159, v157, s84
v_mfma_f32_16x16x32_bf16 v[212:215], v[24:27], a[208:211], v[212:215]
buffer_load_dwordx2 v[96:97], v5, s[40:43], s80 offen nt
v_mfma_f32_16x16x32_bf16 v[212:215], v[28:31], a[228:231], v[212:215]
ds_read_b128 a[168:171], v2 offset:59392
v_mfma_f32_16x16x32_bf16 v[228:231], v[24:27], a[212:215], v[228:231]
ds_read_b128 a[188:191], v2 offset:64512
v_mfma_f32_16x16x32_bf16 v[228:231], v[28:31], a[232:235], v[228:231]
buffer_load_dwordx2 v[98:99], v5, s[40:43], s80 offen offset:512 nt
v_mfma_f32_16x16x32_bf16 v[244:247], v[24:27], a[216:219], v[244:247]
buffer_load_dwordx2 v[100:101], v5, s[40:43], s80 offen offset:1024 nt
v_mfma_f32_16x16x32_bf16 v[244:247], v[28:31], a[236:239], v[244:247]
v_mfma_f32_16x16x32_bf16 v[184:187], v[16:19], a[200:203], v[184:187]
v_perm_b32 v24, v131, v129, s85
v_perm_b32 v25, v135, v133, s85
v_mfma_f32_16x16x32_bf16 v[184:187], v[20:23], a[220:223], v[184:187]
v_perm_b32 v26, v139, v137, s85
v_perm_b32 v27, v143, v141, s85
v_mfma_f32_16x16x32_bf16 v[200:203], v[16:19], a[204:207], v[200:203]
v_perm_b32 v28, v147, v145, s85
v_perm_b32 v29, v151, v149, s85
v_mfma_f32_16x16x32_bf16 v[200:203], v[20:23], a[224:227], v[200:203]
v_perm_b32 v30, v155, v153, s85
v_perm_b32 v31, v159, v157, s85
v_mfma_f32_16x16x32_bf16 v[216:219], v[16:19], a[208:211], v[216:219]
ds_read_b128 a[172:175], v2 offset:60416
v_mfma_f32_16x16x32_bf16 v[216:219], v[20:23], a[228:231], v[216:219]
ds_read_b128 a[192:195], v3 offset:0
v_mfma_f32_16x16x32_bf16 v[232:235], v[16:19], a[212:215], v[232:235]
buffer_load_dwordx2 v[102:103], v5, s[40:43], s80 offen offset:1536 nt
v_mfma_f32_16x16x32_bf16 v[232:235], v[20:23], a[232:235], v[232:235]
buffer_load_dwordx2 v[104:105], v5, s[40:43], s80 offen offset:2048 nt
v_mfma_f32_16x16x32_bf16 v[248:251], v[16:19], a[216:219], v[248:251]
ds_read_b128 a[176:179], v2 offset:61440
v_mfma_f32_16x16x32_bf16 v[248:251], v[20:23], a[236:239], v[248:251]
ds_read_b128 a[196:199], v3 offset:1024
v_mfma_f32_16x16x32_bf16 v[188:191], v[24:27], a[200:203], v[188:191]
s_waitcnt vmcnt(21)
v_perm_b32 v16, v162, v160, s84
v_perm_b32 v17, v166, v164, s84
v_mfma_f32_16x16x32_bf16 v[188:191], v[28:31], a[220:223], v[188:191]
v_perm_b32 v18, v170, v168, s84
v_perm_b32 v19, v174, v172, s84
v_mfma_f32_16x16x32_bf16 v[204:207], v[24:27], a[204:207], v[204:207]
v_perm_b32 v20, v50, v48, s84
v_perm_b32 v21, v54, v52, s84
v_mfma_f32_16x16x32_bf16 v[204:207], v[28:31], a[224:227], v[204:207]
v_perm_b32 v22, v58, v56, s84
v_perm_b32 v23, v62, v60, s84
v_mfma_f32_16x16x32_bf16 v[220:223], v[24:27], a[208:211], v[220:223]
buffer_load_dwordx2 v[106:107], v5, s[40:43], s80 offen offset:2560 nt
v_mfma_f32_16x16x32_bf16 v[220:223], v[28:31], a[228:231], v[220:223]
buffer_load_dwordx2 v[108:109], v5, s[40:43], s80 offen offset:3072 nt
v_mfma_f32_16x16x32_bf16 v[236:239], v[24:27], a[212:215], v[236:239]
v_mfma_f32_16x16x32_bf16 v[236:239], v[28:31], a[232:235], v[236:239]
v_mfma_f32_16x16x32_bf16 v[252:255], v[24:27], a[216:219], v[252:255]
v_mfma_f32_16x16x32_bf16 v[252:255], v[28:31], a[236:239], v[252:255]
s_waitcnt lgkmcnt(1)
v_mfma_f32_16x16x32_bf16 v[176:179], v[16:19], a[160:163], v[176:179]
v_perm_b32 v24, v162, v160, s85
v_perm_b32 v25, v166, v164, s85
v_mfma_f32_16x16x32_bf16 v[176:179], v[20:23], a[180:183], v[176:179]
v_perm_b32 v26, v170, v168, s85
v_perm_b32 v27, v174, v172, s85
v_mfma_f32_16x16x32_bf16 v[192:195], v[16:19], a[164:167], v[192:195]
v_perm_b32 v28, v50, v48, s85
v_perm_b32 v29, v54, v52, s85
v_mfma_f32_16x16x32_bf16 v[192:195], v[20:23], a[184:187], v[192:195]
v_perm_b32 v30, v58, v56, s85
v_perm_b32 v31, v62, v60, s85
v_mfma_f32_16x16x32_bf16 v[208:211], v[16:19], a[168:171], v[208:211]
ds_read_b128 a[200:203], v3 offset:2048
v_mfma_f32_16x16x32_bf16 v[208:211], v[20:23], a[188:191], v[208:211]
ds_read_b128 a[220:223], v3 offset:7168
v_mfma_f32_16x16x32_bf16 v[224:227], v[16:19], a[172:175], v[224:227]
)"
R"(buffer_load_dwordx2 v[110:111], v5, s[40:43], s80 offen offset:3584 nt
v_mfma_f32_16x16x32_bf16 v[224:227], v[20:23], a[192:195], v[224:227]
s_add_u32 s80, s80, 16384
v_mfma_f32_16x16x32_bf16 v[240:243], v[16:19], a[176:179], v[240:243]
ds_read_b128 a[204:207], v3 offset:3072
s_waitcnt lgkmcnt(3)
v_mfma_f32_16x16x32_bf16 v[240:243], v[20:23], a[196:199], v[240:243]
ds_read_b128 a[224:227], v3 offset:8192
v_mfma_f32_16x16x32_bf16 v[180:183], v[24:27], a[160:163], v[180:183]
v_perm_b32 v16, v163, v161, s84
v_perm_b32 v17, v167, v165, s84
v_mfma_f32_16x16x32_bf16 v[180:183], v[28:31], a[180:183], v[180:183]
v_perm_b32 v18, v171, v169, s84
v_perm_b32 v19, v175, v173, s84
v_mfma_f32_16x16x32_bf16 v[196:199], v[24:27], a[164:167], v[196:199]
v_perm_b32 v20, v51, v49, s84
v_perm_b32 v21, v55, v53, s84
v_mfma_f32_16x16x32_bf16 v[196:199], v[28:31], a[184:187], v[196:199]
v_perm_b32 v22, v59, v57, s84
v_perm_b32 v23, v63, v61, s84
v_mfma_f32_16x16x32_bf16 v[212:215], v[24:27], a[168:171], v[212:215]
buffer_load_dwordx2 v[112:113], v5, s[40:43], s80 offen nt
v_mfma_f32_16x16x32_bf16 v[212:215], v[28:31], a[188:191], v[212:215]
ds_read_b128 a[208:211], v3 offset:4096
v_mfma_f32_16x16x32_bf16 v[228:231], v[24:27], a[172:175], v[228:231]
ds_read_b128 a[228:231], v3 offset:9216
v_mfma_f32_16x16x32_bf16 v[228:231], v[28:31], a[192:195], v[228:231]
buffer_load_dwordx2 v[114:115], v5, s[40:43], s80 offen offset:512 nt
v_mfma_f32_16x16x32_bf16 v[244:247], v[24:27], a[176:179], v[244:247]
buffer_load_dwordx2 v[116:117], v5, s[40:43], s80 offen offset:1024 nt
v_mfma_f32_16x16x32_bf16 v[244:247], v[28:31], a[196:199], v[244:247]
buffer_load_dwordx2 v[118:119], v5, s[40:43], s80 offen offset:1536 nt
v_mfma_f32_16x16x32_bf16 v[184:187], v[16:19], a[160:163], v[184:187]
v_perm_b32 v24, v163, v161, s85
v_perm_b32 v25, v167, v165, s85
v_mfma_f32_16x16x32_bf16 v[184:187], v[20:23], a[180:183], v[184:187]
v_perm_b32 v26, v171, v169, s85
v_perm_b32 v27, v175, v173, s85
v_mfma_f32_16x16x32_bf16 v[200:203], v[16:19], a[164:167], v[200:203]
v_perm_b32 v28, v51, v49, s85
v_perm_b32 v29, v55, v53, s85
v_mfma_f32_16x16x32_bf16 v[200:203], v[20:23], a[184:187], v[200:203]
v_perm_b32 v30, v59, v57, s85
v_perm_b32 v31, v63, v61, s85
v_mfma_f32_16x16x32_bf16 v[216:219], v[16:19], a[168:171], v[216:219]
ds_read_b128 a[212:215], v3 offset:5120
v_mfma_f32_16x16x32_bf16 v[216:219], v[20:23], a[188:191], v[216:219]
ds_read_b128 a[232:235], v3 offset:10240
v_mfma_f32_16x16x32_bf16 v[232:235], v[16:19], a[172:175], v[232:235]
buffer_load_dwordx2 v[120:121], v5, s[40:43], s80 offen offset:2048 nt
v_mfma_f32_16x16x32_bf16 v[232:235], v[20:23], a[192:195], v[232:235]
v_mfma_f32_16x16x32_bf16 v[248:251], v[16:19], a[176:179], v[248:251]
ds_read_b128 a[216:219], v3 offset:6144
v_mfma_f32_16x16x32_bf16 v[248:251], v[20:23], a[196:199], v[248:251]
ds_read_b128 a[236:239], v3 offset:11264
v_mfma_f32_16x16x32_bf16 v[188:191], v[24:27], a[160:163], v[188:191]
buffer_load_dwordx2 v[122:123], v5, s[40:43], s80 offen offset:2560 nt
buffer_load_dwordx2 v[124:125], v5, s[40:43], s80 offen offset:3072 nt
buffer_load_dwordx2 v[126:127], v5, s[40:43], s80 offen offset:3584 nt
s_mov_b32 s80, 229376
buffer_load_dwordx2 v[128:129], v5, s[40:43], s80 offen nt
buffer_load_dwordx2 v[130:131], v5, s[40:43], s80 offen offset:512 nt
buffer_load_dwordx2 v[132:133], v5, s[40:43], s80 offen offset:1024 nt
s_waitcnt vmcnt(19)
v_perm_b32 v16, v66, v64, s84
v_perm_b32 v17, v70, v68, s84
v_mfma_f32_16x16x32_bf16 v[188:191], v[28:31], a[180:183], v[188:191]
v_perm_b32 v18, v74, v72, s84
v_perm_b32 v19, v78, v76, s84
v_mfma_f32_16x16x32_bf16 v[204:207], v[24:27], a[164:167], v[204:207]
v_perm_b32 v20, v34, v32, s84
v_perm_b32 v21, v38, v36, s84
v_mfma_f32_16x16x32_bf16 v[204:207], v[28:31], a[184:187], v[204:207]
v_perm_b32 v22, v42, v40, s84
v_perm_b32 v23, v46, v44, s84
v_mfma_f32_16x16x32_bf16 v[220:223], v[24:27], a[168:171], v[220:223]
buffer_load_dwordx2 v[134:135], v5, s[40:43], s80 offen offset:1536 nt
v_mfma_f32_16x16x32_bf16 v[220:223], v[28:31], a[188:191], v[220:223]
buffer_load_dwordx2 v[136:137], v5, s[40:43], s80 offen offset:2048 nt
v_mfma_f32_16x16x32_bf16 v[236:239], v[24:27], a[172:175], v[236:239]
v_mfma_f32_16x16x32_bf16 v[236:239], v[28:31], a[192:195], v[236:239]
v_mfma_f32_16x16x32_bf16 v[252:255], v[24:27], a[176:179], v[252:255]
v_mfma_f32_16x16x32_bf16 v[252:255], v[28:31], a[196:199], v[252:255]
s_waitcnt lgkmcnt(1)
v_mfma_f32_16x16x32_bf16 v[176:179], v[16:19], a[200:203], v[176:179]
v_perm_b32 v24, v66, v64, s85
v_perm_b32 v25, v70, v68, s85
v_mfma_f32_16x16x32_bf16 v[176:179], v[20:23], a[220:223], v[176:179]
v_perm_b32 v26, v74, v72, s85
v_perm_b32 v27, v78, v76, s85
v_mfma_f32_16x16x32_bf16 v[192:195], v[16:19], a[204:207], v[192:195]
v_perm_b32 v28, v34, v32, s85
v_perm_b32 v29, v38, v36, s85
v_mfma_f32_16x16x32_bf16 v[192:195], v[20:23], a[224:227], v[192:195]
v_perm_b32 v30, v42, v40, s85
v_perm_b32 v31, v46, v44, s85
v_mfma_f32_16x16x32_bf16 v[208:211], v[16:19], a[208:211], v[208:211]
ds_read_b128 a[160:163], v3 offset:12288
v_mfma_f32_16x16x32_bf16 v[208:211], v[20:23], a[228:231], v[208:211]
ds_read_b128 a[180:183], v3 offset:17408
v_mfma_f32_16x16x32_bf16 v[224:227], v[16:19], a[212:215], v[224:227]
buffer_load_dwordx2 v[138:139], v5, s[40:43], s80 offen offset:2560 nt
v_mfma_f32_16x16x32_bf16 v[224:227], v[20:23], a[232:235], v[224:227]
buffer_load_dwordx2 v[140:141], v5, s[40:43], s80 offen offset:3072 nt
v_mfma_f32_16x16x32_bf16 v[240:243], v[16:19], a[216:219], v[240:243]
ds_read_b128 a[164:167], v3 offset:13312
s_waitcnt lgkmcnt(3)
v_mfma_f32_16x16x32_bf16 v[240:243], v[20:23], a[236:239], v[240:243]
ds_read_b128 a[184:187], v3 offset:18432
v_mfma_f32_16x16x32_bf16 v[180:183], v[24:27], a[200:203], v[180:183]
v_perm_b32 v16, v67, v65, s84
v_perm_b32 v17, v71, v69, s84
v_mfma_f32_16x16x32_bf16 v[180:183], v[28:31], a[220:223], v[180:183]
v_perm_b32 v18, v75, v73, s84
v_perm_b32 v19, v79, v77, s84
v_mfma_f32_16x16x32_bf16 v[196:199], v[24:27], a[204:207], v[196:199]
v_perm_b32 v20, v35, v33, s84
v_perm_b32 v21, v39, v37, s84
v_mfma_f32_16x16x32_bf16 v[196:199], v[28:31], a[224:227], v[196:199]
v_perm_b32 v22, v43, v41, s84
v_perm_b32 v23, v47, v45, s84
v_mfma_f32_16x16x32_bf16 v[212:215], v[24:27], a[208:211], v[212:215]
buffer_load_dwordx2 v[142:143], v5, s[40:43], s80 offen offset:3584 nt
v_mfma_f32_16x16x32_bf16 v[212:215], v[28:31], a[228:231], v[212:215]
s_add_u32 s80, s80, 16384
ds_read_b128 a[168:171], v3 offset:14336
v_mfma_f32_16x16x32_bf16 v[228:231], v[24:27], a[212:215], v[228:231]
ds_read_b128 a[188:191], v3 offset:19456
v_mfma_f32_16x16x32_bf16 v[228:231], v[28:31], a[232:235], v[228:231]
buffer_load_dwordx2 v[144:145], v5, s[40:43], s80 offen nt
v_mfma_f32_16x16x32_bf16 v[244:247], v[24:27], a[216:219], v[244:247]
v_mfma_f32_16x16x32_bf16 v[244:247], v[28:31], a[236:239], v[244:247]
buffer_load_dwordx2 v[146:147], v5, s[40:43], s80 offen offset:512 nt
v_mfma_f32_16x16x32_bf16 v[184:187], v[16:19], a[200:203], v[184:187]
v_perm_b32 v24, v67, v65, s85
v_perm_b32 v25, v71, v69, s85
v_mfma_f32_16x16x32_bf16 v[184:187], v[20:23], a[220:223], v[184:187]
v_perm_b32 v26, v75, v73, s85
v_perm_b32 v27, v79, v77, s85
v_mfma_f32_16x16x32_bf16 v[200:203], v[16:19], a[204:207], v[200:203]
v_perm_b32 v28, v35, v33, s85
v_perm_b32 v29, v39, v37, s85
v_mfma_f32_16x16x32_bf16 v[200:203], v[20:23], a[224:227], v[200:203]
v_perm_b32 v30, v43, v41, s85
v_perm_b32 v31, v47, v45, s85
v_mfma_f32_16x16x32_bf16 v[216:219], v[16:19], a[208:211], v[216:219]
ds_read_b128 a[172:175], v3 offset:15360
v_mfma_f32_16x16x32_bf16 v[216:219], v[20:23], a[228:231], v[216:219]
ds_read_b128 a[192:195], v3 offset:20480
v_mfma_f32_16x16x32_bf16 v[232:235], v[16:19], a[212:215], v[232:235]
buffer_load_dwordx2 v[148:149], v5, s[40:43], s80 offen offset:1024 nt
v_mfma_f32_16x16x32_bf16 v[232:235], v[20:23], a[232:235], v[232:235]
v_mfma_f32_16x16x32_bf16 v[248:251], v[16:19], a[216:219], v[248:251]
ds_read_b128 a[176:179], v3 offset:16384
v_mfma_f32_16x16x32_bf16 v[248:251], v[20:23], a[236:239], v[248:251]
ds_read_b128 a[196:199], v3 offset:21504
v_mfma_f32_16x16x32_bf16 v[188:191], v[24:27], a[200:203], v[188:191]
buffer_load_dwordx2 v[150:151], v5, s[40:43], s80 offen offset:1536 nt
buffer_load_dwordx2 v[152:153], v5, s[40:43], s80 offen offset:2048 nt
buffer_load_dwordx2 v[154:155], v5, s[40:43], s80 offen offset:2560 nt
buffer_load_dwordx2 v[156:157], v5, s[40:43], s80 offen offset:3072 nt
buffer_load_dwordx2 v[158:159], v5, s[40:43], s80 offen offset:3584 nt
s_add_u32 s82, s95, 0
s_and_b32 s82, s82, 7
s_mul_i32 s82, s82, 28672
buffer_load_dwordx4 v[160:163], v7, s[48:51], s82 offen
buffer_load_dwordx4 v[164:167], v7, s[48:51], s82 offen offset:896
s_waitcnt vmcnt(18)
v_perm_b32 v16, v98, v96, s84
v_perm_b32 v17, v102, v100, s84
v_mfma_f32_16x16x32_bf16 v[188:191], v[28:31], a[220:223], v[188:191]
v_perm_b32 v18, v106, v104, s84
v_perm_b32 v19, v110, v108, s84
v_mfma_f32_16x16x32_bf16 v[204:207], v[24:27], a[204:207], v[204:207]
v_perm_b32 v20, v114, v112, s84
v_perm_b32 v21, v118, v116, s84
v_mfma_f32_16x16x32_bf16 v[204:207], v[28:31], a[224:227], v[204:207]
v_perm_b32 v22, v122, v120, s84
v_perm_b32 v23, v126, v124, s84
v_mfma_f32_16x16x32_bf16 v[220:223], v[24:27], a[208:211], v[220:223]
v_mfma_f32_16x16x32_bf16 v[220:223], v[28:31], a[228:231], v[220:223]
buffer_load_dwordx4 v[168:171], v7, s[48:51], s82 offen offset:1792
v_mfma_f32_16x16x32_bf16 v[236:239], v[24:27], a[212:215], v[236:239]
v_mfma_f32_16x16x32_bf16 v[236:239], v[28:31], a[232:235], v[236:239]
v_mfma_f32_16x16x32_bf16 v[252:255], v[24:27], a[216:219], v[252:255]
v_mfma_f32_16x16x32_bf16 v[252:255], v[28:31], a[236:239], v[252:255]
s_waitcnt lgkmcnt(1)
v_mfma_f32_16x16x32_bf16 v[176:179], v[16:19], a[160:163], v[176:179]
v_perm_b32 v24, v98, v96, s85
v_perm_b32 v25, v102, v100, s85
v_mfma_f32_16x16x32_bf16 v[176:179], v[20:23], a[180:183], v[176:179]
v_perm_b32 v26, v106, v104, s85
v_perm_b32 v27, v110, v108, s85
v_mfma_f32_16x16x32_bf16 v[192:195], v[16:19], a[164:167], v[192:195]
v_perm_b32 v28, v114, v112, s85
v_perm_b32 v29, v118, v116, s85
v_mfma_f32_16x16x32_bf16 v[192:195], v[20:23], a[184:187], v[192:195]
v_perm_b32 v30, v122, v120, s85
v_perm_b32 v31, v126, v124, s85
v_mfma_f32_16x16x32_bf16 v[208:211], v[16:19], a[168:171], v[208:211]
)"
R"(ds_read_b128 a[200:203], v3 offset:22528
v_mfma_f32_16x16x32_bf16 v[208:211], v[20:23], a[188:191], v[208:211]
ds_read_b128 a[220:223], v3 offset:27648
v_mfma_f32_16x16x32_bf16 v[224:227], v[16:19], a[172:175], v[224:227]
buffer_load_dwordx4 v[172:175], v7, s[48:51], s82 offen offset:2688
v_mfma_f32_16x16x32_bf16 v[224:227], v[20:23], a[192:195], v[224:227]
s_add_u32 s83, s82, 3584
v_mfma_f32_16x16x32_bf16 v[240:243], v[16:19], a[176:179], v[240:243]
ds_read_b128 a[204:207], v3 offset:23552
s_waitcnt lgkmcnt(3)
v_mfma_f32_16x16x32_bf16 v[240:243], v[20:23], a[196:199], v[240:243]
ds_read_b128 a[224:227], v3 offset:28672
v_mfma_f32_16x16x32_bf16 v[180:183], v[24:27], a[160:163], v[180:183]
v_perm_b32 v16, v99, v97, s84
v_perm_b32 v17, v103, v101, s84
v_mfma_f32_16x16x32_bf16 v[180:183], v[28:31], a[180:183], v[180:183]
v_perm_b32 v18, v107, v105, s84
v_perm_b32 v19, v111, v109, s84
v_mfma_f32_16x16x32_bf16 v[196:199], v[24:27], a[164:167], v[196:199]
v_perm_b32 v20, v115, v113, s84
v_perm_b32 v21, v119, v117, s84
v_mfma_f32_16x16x32_bf16 v[196:199], v[28:31], a[184:187], v[196:199]
v_perm_b32 v22, v123, v121, s84
v_perm_b32 v23, v127, v125, s84
v_mfma_f32_16x16x32_bf16 v[212:215], v[24:27], a[168:171], v[212:215]
buffer_load_dwordx4 v[32:35], v7, s[48:51], s83 offen
v_mfma_f32_16x16x32_bf16 v[212:215], v[28:31], a[188:191], v[212:215]
ds_read_b128 a[208:211], v3 offset:24576
v_mfma_f32_16x16x32_bf16 v[228:231], v[24:27], a[172:175], v[228:231]
ds_read_b128 a[228:231], v3 offset:29696
v_mfma_f32_16x16x32_bf16 v[228:231], v[28:31], a[192:195], v[228:231]
v_mfma_f32_16x16x32_bf16 v[244:247], v[24:27], a[176:179], v[244:247]
v_mfma_f32_16x16x32_bf16 v[244:247], v[28:31], a[196:199], v[244:247]
buffer_load_dwordx4 v[36:39], v7, s[48:51], s83 offen offset:896
v_mfma_f32_16x16x32_bf16 v[184:187], v[16:19], a[160:163], v[184:187]
v_perm_b32 v24, v99, v97, s85
v_perm_b32 v25, v103, v101, s85
v_mfma_f32_16x16x32_bf16 v[184:187], v[20:23], a[180:183], v[184:187]
v_perm_b32 v26, v107, v105, s85
v_perm_b32 v27, v111, v109, s85
v_mfma_f32_16x16x32_bf16 v[200:203], v[16:19], a[164:167], v[200:203]
v_perm_b32 v28, v115, v113, s85
v_perm_b32 v29, v119, v117, s85
v_mfma_f32_16x16x32_bf16 v[200:203], v[20:23], a[184:187], v[200:203]
v_perm_b32 v30, v123, v121, s85
v_perm_b32 v31, v127, v125, s85
v_mfma_f32_16x16x32_bf16 v[216:219], v[16:19], a[168:171], v[216:219]
ds_read_b128 a[212:215], v3 offset:25600
v_mfma_f32_16x16x32_bf16 v[216:219], v[20:23], a[188:191], v[216:219]
ds_read_b128 a[232:235], v3 offset:30720
v_mfma_f32_16x16x32_bf16 v[232:235], v[16:19], a[172:175], v[232:235]
v_mfma_f32_16x16x32_bf16 v[232:235], v[20:23], a[192:195], v[232:235]
v_mfma_f32_16x16x32_bf16 v[248:251], v[16:19], a[176:179], v[248:251]
ds_read_b128 a[216:219], v3 offset:26624
v_mfma_f32_16x16x32_bf16 v[248:251], v[20:23], a[196:199], v[248:251]
ds_read_b128 a[236:239], v3 offset:31744
v_mfma_f32_16x16x32_bf16 v[188:191], v[24:27], a[160:163], v[188:191]
buffer_load_dwordx4 v[40:43], v7, s[48:51], s83 offen offset:1792
buffer_load_dwordx4 v[44:47], v7, s[48:51], s83 offen offset:2688
s_add_u32 s82, s95, 1
s_and_b32 s82, s82, 7
s_mul_i32 s82, s82, 28672
buffer_load_dwordx4 v[96:99], v7, s[48:51], s82 offen
buffer_load_dwordx4 v[100:103], v7, s[48:51], s82 offen offset:896
buffer_load_dwordx4 v[104:107], v7, s[48:51], s82 offen offset:1792
s_waitcnt vmcnt(11)
v_perm_b32 v16, v130, v128, s84
v_perm_b32 v17, v134, v132, s84
v_mfma_f32_16x16x32_bf16 v[188:191], v[28:31], a[180:183], v[188:191]
v_perm_b32 v18, v138, v136, s84
v_perm_b32 v19, v142, v140, s84
v_mfma_f32_16x16x32_bf16 v[204:207], v[24:27], a[164:167], v[204:207]
v_perm_b32 v20, v146, v144, s84
v_perm_b32 v21, v150, v148, s84
v_mfma_f32_16x16x32_bf16 v[204:207], v[28:31], a[184:187], v[204:207]
v_perm_b32 v22, v154, v152, s84
v_perm_b32 v23, v158, v156, s84
v_mfma_f32_16x16x32_bf16 v[220:223], v[24:27], a[168:171], v[220:223]
buffer_load_dwordx4 v[108:111], v7, s[48:51], s82 offen offset:2688
v_mfma_f32_16x16x32_bf16 v[220:223], v[28:31], a[188:191], v[220:223]
s_add_u32 s83, s82, 3584
v_mfma_f32_16x16x32_bf16 v[236:239], v[24:27], a[172:175], v[236:239]
buffer_load_dwordx4 v[112:115], v7, s[48:51], s83 offen
v_mfma_f32_16x16x32_bf16 v[236:239], v[28:31], a[192:195], v[236:239]
buffer_load_dwordx4 v[116:119], v7, s[48:51], s83 offen offset:896
v_mfma_f32_16x16x32_bf16 v[252:255], v[24:27], a[176:179], v[252:255]
v_mfma_f32_16x16x32_bf16 v[252:255], v[28:31], a[196:199], v[252:255]
s_waitcnt lgkmcnt(1)
v_mfma_f32_16x16x32_bf16 v[176:179], v[16:19], a[200:203], v[176:179]
v_perm_b32 v24, v130, v128, s85
v_perm_b32 v25, v134, v132, s85
v_mfma_f32_16x16x32_bf16 v[176:179], v[20:23], a[220:223], v[176:179]
v_perm_b32 v26, v138, v136, s85
v_perm_b32 v27, v142, v140, s85
v_mfma_f32_16x16x32_bf16 v[192:195], v[16:19], a[204:207], v[192:195]
v_perm_b32 v28, v146, v144, s85
v_perm_b32 v29, v150, v148, s85
v_mfma_f32_16x16x32_bf16 v[192:195], v[20:23], a[224:227], v[192:195]
v_perm_b32 v30, v154, v152, s85
v_perm_b32 v31, v158, v156, s85
v_mfma_f32_16x16x32_bf16 v[208:211], v[16:19], a[208:211], v[208:211]
buffer_load_dwordx4 v[120:123], v7, s[48:51], s83 offen offset:1792
v_mfma_f32_16x16x32_bf16 v[208:211], v[20:23], a[228:231], v[208:211]
buffer_load_dwordx4 v[124:127], v7, s[48:51], s83 offen offset:2688
v_mfma_f32_16x16x32_bf16 v[224:227], v[16:19], a[212:215], v[224:227]
s_add_u32 s82, s95, 2
s_and_b32 s82, s82, 7
s_mul_i32 s82, s82, 28672
v_mfma_f32_16x16x32_bf16 v[224:227], v[20:23], a[232:235], v[224:227]
buffer_load_dwordx4 v[48:51], v7, s[48:51], s82 offen
v_mfma_f32_16x16x32_bf16 v[240:243], v[16:19], a[216:219], v[240:243]
s_waitcnt lgkmcnt(0)
v_mfma_f32_16x16x32_bf16 v[240:243], v[20:23], a[236:239], v[240:243]
buffer_load_dwordx4 v[52:55], v7, s[48:51], s82 offen offset:896
v_mfma_f32_16x16x32_bf16 v[180:183], v[24:27], a[200:203], v[180:183]
v_perm_b32 v16, v131, v129, s84
v_perm_b32 v17, v135, v133, s84
v_mfma_f32_16x16x32_bf16 v[180:183], v[28:31], a[220:223], v[180:183]
v_perm_b32 v18, v139, v137, s84
v_perm_b32 v19, v143, v141, s84
v_mfma_f32_16x16x32_bf16 v[196:199], v[24:27], a[204:207], v[196:199]
v_perm_b32 v20, v147, v145, s84
v_perm_b32 v21, v151, v149, s84
v_mfma_f32_16x16x32_bf16 v[196:199], v[28:31], a[224:227], v[196:199]
v_perm_b32 v22, v155, v153, s84
v_perm_b32 v23, v159, v157, s84
v_mfma_f32_16x16x32_bf16 v[212:215], v[24:27], a[208:211], v[212:215]
buffer_load_dwordx4 v[56:59], v7, s[48:51], s82 offen offset:1792
v_mfma_f32_16x16x32_bf16 v[212:215], v[28:31], a[228:231], v[212:215]
buffer_load_dwordx4 v[60:63], v7, s[48:51], s82 offen offset:2688
v_mfma_f32_16x16x32_bf16 v[228:231], v[24:27], a[212:215], v[228:231]
s_add_u32 s83, s82, 3584
v_mfma_f32_16x16x32_bf16 v[228:231], v[28:31], a[232:235], v[228:231]
v_mfma_f32_16x16x32_bf16 v[244:247], v[24:27], a[216:219], v[244:247]
v_mfma_f32_16x16x32_bf16 v[244:247], v[28:31], a[236:239], v[244:247]
buffer_load_dwordx4 v[64:67], v7, s[48:51], s83 offen
v_mfma_f32_16x16x32_bf16 v[184:187], v[16:19], a[200:203], v[184:187]
v_perm_b32 v24, v131, v129, s85
v_perm_b32 v25, v135, v133, s85
v_mfma_f32_16x16x32_bf16 v[184:187], v[20:23], a[220:223], v[184:187]
v_perm_b32 v26, v139, v137, s85
v_perm_b32 v27, v143, v141, s85
v_mfma_f32_16x16x32_bf16 v[200:203], v[16:19], a[204:207], v[200:203]
v_perm_b32 v28, v147, v145, s85
v_perm_b32 v29, v151, v149, s85
v_mfma_f32_16x16x32_bf16 v[200:203], v[20:23], a[224:227], v[200:203]
v_perm_b32 v30, v155, v153, s85
v_perm_b32 v31, v159, v157, s85
v_mfma_f32_16x16x32_bf16 v[216:219], v[16:19], a[208:211], v[216:219]
buffer_load_dwordx4 v[68:71], v7, s[48:51], s83 offen offset:896
v_mfma_f32_16x16x32_bf16 v[216:219], v[20:23], a[228:231], v[216:219]
buffer_load_dwordx4 v[72:75], v7, s[48:51], s83 offen offset:1792
v_mfma_f32_16x16x32_bf16 v[232:235], v[16:19], a[212:215], v[232:235]
v_mfma_f32_16x16x32_bf16 v[232:235], v[20:23], a[232:235], v[232:235]
v_mfma_f32_16x16x32_bf16 v[248:251], v[16:19], a[216:219], v[248:251]
buffer_load_dwordx4 v[76:79], v7, s[48:51], s83 offen offset:2688
v_mfma_f32_16x16x32_bf16 v[248:251], v[20:23], a[236:239], v[248:251]
s_add_u32 s82, s95, 3
s_and_b32 s82, s82, 7
s_mul_i32 s82, s82, 28672
v_mfma_f32_16x16x32_bf16 v[188:191], v[24:27], a[200:203], v[188:191]
v_mfma_f32_16x16x32_bf16 v[188:191], v[28:31], a[220:223], v[188:191]
buffer_load_dwordx4 v[128:131], v7, s[48:51], s82 offen
v_mfma_f32_16x16x32_bf16 v[204:207], v[24:27], a[204:207], v[204:207]
v_mfma_f32_16x16x32_bf16 v[204:207], v[28:31], a[224:227], v[204:207]
v_mfma_f32_16x16x32_bf16 v[220:223], v[24:27], a[208:211], v[220:223]
buffer_load_dwordx4 v[132:135], v7, s[48:51], s82 offen offset:896
v_mfma_f32_16x16x32_bf16 v[220:223], v[28:31], a[228:231], v[220:223]
v_mfma_f32_16x16x32_bf16 v[236:239], v[24:27], a[212:215], v[236:239]
v_mfma_f32_16x16x32_bf16 v[236:239], v[28:31], a[232:235], v[236:239]
buffer_load_dwordx4 v[136:139], v7, s[48:51], s82 offen offset:1792
v_mfma_f32_16x16x32_bf16 v[252:255], v[24:27], a[216:219], v[252:255]
v_mfma_f32_16x16x32_bf16 v[252:255], v[28:31], a[236:239], v[252:255]
s_mov_b64 vcc, s[76:77]
ds_read_b128 v[16:19], v10 offset:1280
ds_read_b128 v[20:23], v10 offset:1536
ds_read_b128 v[24:27], v10 offset:1792
ds_read_b128 v[28:31], v10 offset:2048
s_waitcnt lgkmcnt(3)
v_add_f32 v16, v16, v17
v_add_f32 v18, v18, v19
v_add_f32 v16, v16, v18
v_rcp_f32 v11, v16
ds_read_b128 v[16:19], v10 offset:2304
s_waitcnt lgkmcnt(3)
v_add_f32 v20, v20, v21
buffer_load_dwordx4 v[140:143], v7, s[48:51], s82 offen offset:2688
s_add_u32 s83, s82, 3584
buffer_load_dwordx4 v[144:147], v7, s[48:51], s83 offen
v_add_f32 v22, v22, v23
v_add_f32 v20, v20, v22
v_rcp_f32 v12, v20
s_waitcnt lgkmcnt(2)
v_add_f32 v24, v24, v25
v_add_f32 v26, v26, v27
v_add_f32 v24, v24, v26
v_rcp_f32 v13, v24
s_waitcnt lgkmcnt(1)
v_add_f32 v28, v28, v29
v_add_f32 v30, v30, v31
)"
R"(v_add_f32 v28, v28, v30
v_rcp_f32 v14, v28
s_waitcnt lgkmcnt(0)
v_add_f32 v16, v16, v17
v_add_f32 v18, v18, v19
v_add_f32 v16, v16, v18
v_rcp_f32 v15, v16
buffer_load_dwordx4 v[148:151], v7, s[48:51], s83 offen offset:896
buffer_load_dwordx4 v[152:155], v7, s[48:51], s83 offen offset:1792
v_accvgpr_read_b32 v9, a246
s_barrier
v_mov_b32 v24, v11
v_pk_mul_f32 v[176:177], v[176:177], v[24:25] op_sel_hi:[1,0]
v_pk_mul_f32 v[178:179], v[178:179], v[24:25] op_sel_hi:[1,0]
v_pk_mul_f32 v[180:181], v[180:181], v[24:25] op_sel_hi:[1,0]
v_pk_mul_f32 v[182:183], v[182:183], v[24:25] op_sel_hi:[1,0]
v_pk_mul_f32 v[184:185], v[184:185], v[24:25] op_sel_hi:[1,0]
v_pk_mul_f32 v[186:187], v[186:187], v[24:25] op_sel_hi:[1,0]
buffer_load_dwordx4 v[156:159], v7, s[48:51], s83 offen offset:2688
s_add_u32 s82, s95, 4
s_and_b32 s82, s82, 7
s_mul_i32 s82, s82, 28672
buffer_load_dwordx4 v[80:83], v7, s[48:51], s82 offen
v_pk_mul_f32 v[188:189], v[188:189], v[24:25] op_sel_hi:[1,0]
v_pk_mul_f32 v[190:191], v[190:191], v[24:25] op_sel_hi:[1,0]
v_cvt_pk_bf16_f32 v16, v176, v180
v_cvt_pk_bf16_f32 v17, v184, v188
v_cvt_pk_bf16_f32 v18, v177, v181
v_cvt_pk_bf16_f32 v19, v185, v189
v_cvt_pk_bf16_f32 v20, v178, v182
v_cvt_pk_bf16_f32 v21, v186, v190
v_cvt_pk_bf16_f32 v22, v179, v183
v_cvt_pk_bf16_f32 v23, v187, v191
ds_write_b128 v9, v[16:19] offset:0
ds_write_b128 v9, v[20:23] offset:256
buffer_load_dwordx4 v[84:87], v7, s[48:51], s82 offen offset:896
buffer_load_dwordx4 v[88:91], v7, s[48:51], s82 offen offset:1792
v_mov_b32 v24, v12
v_pk_mul_f32 v[192:193], v[192:193], v[24:25] op_sel_hi:[1,0]
v_pk_mul_f32 v[194:195], v[194:195], v[24:25] op_sel_hi:[1,0]
v_pk_mul_f32 v[196:197], v[196:197], v[24:25] op_sel_hi:[1,0]
v_pk_mul_f32 v[198:199], v[198:199], v[24:25] op_sel_hi:[1,0]
v_pk_mul_f32 v[200:201], v[200:201], v[24:25] op_sel_hi:[1,0]
v_pk_mul_f32 v[202:203], v[202:203], v[24:25] op_sel_hi:[1,0]
v_pk_mul_f32 v[204:205], v[204:205], v[24:25] op_sel_hi:[1,0]
v_pk_mul_f32 v[206:207], v[206:207], v[24:25] op_sel_hi:[1,0]
v_cvt_pk_bf16_f32 v16, v192, v196
v_cvt_pk_bf16_f32 v17, v200, v204
v_cvt_pk_bf16_f32 v18, v193, v197
v_cvt_pk_bf16_f32 v19, v201, v205
v_cvt_pk_bf16_f32 v20, v194, v198
buffer_load_dwordx4 v[92:95], v7, s[48:51], s82 offen offset:2688
s_add_u32 s83, s82, 3584
buffer_load_dwordx4 v[176:179], v7, s[48:51], s83 offen
v_cvt_pk_bf16_f32 v21, v202, v206
v_cvt_pk_bf16_f32 v22, v195, v199
v_cvt_pk_bf16_f32 v23, v203, v207
ds_write_b128 v9, v[16:19] offset:1024
ds_write_b128 v9, v[20:23] offset:1280
v_mov_b32 v24, v13
v_pk_mul_f32 v[208:209], v[208:209], v[24:25] op_sel_hi:[1,0]
buffer_load_dwordx4 v[180:183], v7, s[48:51], s83 offen offset:896
buffer_load_dwordx4 v[184:187], v7, s[48:51], s83 offen offset:1792
v_pk_mul_f32 v[210:211], v[210:211], v[24:25] op_sel_hi:[1,0]
v_pk_mul_f32 v[212:213], v[212:213], v[24:25] op_sel_hi:[1,0]
v_pk_mul_f32 v[214:215], v[214:215], v[24:25] op_sel_hi:[1,0]
v_pk_mul_f32 v[216:217], v[216:217], v[24:25] op_sel_hi:[1,0]
v_pk_mul_f32 v[218:219], v[218:219], v[24:25] op_sel_hi:[1,0]
v_pk_mul_f32 v[220:221], v[220:221], v[24:25] op_sel_hi:[1,0]
v_pk_mul_f32 v[222:223], v[222:223], v[24:25] op_sel_hi:[1,0]
v_cvt_pk_bf16_f32 v16, v208, v212
v_cvt_pk_bf16_f32 v17, v216, v220
v_cvt_pk_bf16_f32 v18, v209, v213
v_cvt_pk_bf16_f32 v19, v217, v221
v_cvt_pk_bf16_f32 v20, v210, v214
v_cvt_pk_bf16_f32 v21, v218, v222
v_cvt_pk_bf16_f32 v22, v211, v215
v_cvt_pk_bf16_f32 v23, v219, v223
ds_write_b128 v9, v[16:19] offset:2048
buffer_load_dwordx4 v[188:191], v7, s[48:51], s83 offen offset:2688
s_add_u32 s82, s95, 5
s_and_b32 s82, s82, 7
s_mul_i32 s82, s82, 28672
buffer_load_dwordx4 v[192:195], v7, s[48:51], s82 offen
ds_write_b128 v9, v[20:23] offset:2304
v_mov_b32 v24, v14
v_pk_mul_f32 v[224:225], v[224:225], v[24:25] op_sel_hi:[1,0]
v_pk_mul_f32 v[226:227], v[226:227], v[24:25] op_sel_hi:[1,0]
v_pk_mul_f32 v[228:229], v[228:229], v[24:25] op_sel_hi:[1,0]
v_pk_mul_f32 v[230:231], v[230:231], v[24:25] op_sel_hi:[1,0]
v_pk_mul_f32 v[232:233], v[232:233], v[24:25] op_sel_hi:[1,0]
v_pk_mul_f32 v[234:235], v[234:235], v[24:25] op_sel_hi:[1,0]
v_pk_mul_f32 v[236:237], v[236:237], v[24:25] op_sel_hi:[1,0]
v_pk_mul_f32 v[238:239], v[238:239], v[24:25] op_sel_hi:[1,0]
buffer_load_dwordx4 v[196:199], v7, s[48:51], s82 offen offset:896
buffer_load_dwordx4 v[200:203], v7, s[48:51], s82 offen offset:1792
v_cvt_pk_bf16_f32 v16, v224, v228
v_cvt_pk_bf16_f32 v17, v232, v236
v_cvt_pk_bf16_f32 v18, v225, v229
v_cvt_pk_bf16_f32 v19, v233, v237
v_cvt_pk_bf16_f32 v20, v226, v230
v_cvt_pk_bf16_f32 v21, v234, v238
v_cvt_pk_bf16_f32 v22, v227, v231
v_cvt_pk_bf16_f32 v23, v235, v239
ds_write_b128 v9, v[16:19] offset:3072
ds_write_b128 v9, v[20:23] offset:3328
buffer_load_dwordx4 v[204:207], v7, s[48:51], s82 offen offset:2688
s_add_u32 s83, s82, 3584
buffer_load_dwordx4 v[208:211], v7, s[48:51], s83 offen
v_mov_b32 v24, v15
v_pk_mul_f32 v[240:241], v[240:241], v[24:25] op_sel_hi:[1,0]
v_pk_mul_f32 v[242:243], v[242:243], v[24:25] op_sel_hi:[1,0]
v_pk_mul_f32 v[244:245], v[244:245], v[24:25] op_sel_hi:[1,0]
v_pk_mul_f32 v[246:247], v[246:247], v[24:25] op_sel_hi:[1,0]
v_pk_mul_f32 v[248:249], v[248:249], v[24:25] op_sel_hi:[1,0]
v_pk_mul_f32 v[250:251], v[250:251], v[24:25] op_sel_hi:[1,0]
v_pk_mul_f32 v[252:253], v[252:253], v[24:25] op_sel_hi:[1,0]
v_pk_mul_f32 v[254:255], v[254:255], v[24:25] op_sel_hi:[1,0]
v_cvt_pk_bf16_f32 v16, v240, v244
v_cvt_pk_bf16_f32 v17, v248, v252
v_cvt_pk_bf16_f32 v18, v241, v245
v_cvt_pk_bf16_f32 v19, v249, v253
v_cvt_pk_bf16_f32 v20, v242, v246
v_cvt_pk_bf16_f32 v21, v250, v254
v_cvt_pk_bf16_f32 v22, v243, v247
buffer_load_dwordx4 v[212:215], v7, s[48:51], s83 offen offset:896
buffer_load_dwordx4 v[216:219], v7, s[48:51], s83 offen offset:1792
v_cvt_pk_bf16_f32 v23, v251, v255
ds_write_b128 v9, v[16:19] offset:4096
ds_write_b128 v9, v[20:23] offset:4352
s_waitcnt vmcnt(39)
v_perm_b32 v16, v164, v160, s84
v_perm_b32 v17, v172, v168, s84
v_perm_b32 v18, v36, v32, s84
v_perm_b32 v19, v44, v40, s84
s_waitcnt vmcnt(31)
v_perm_b32 v20, v100, v96, s84
buffer_load_dwordx4 v[220:223], v7, s[48:51], s83 offen offset:2688
s_add_u32 s82, s95, 6
s_and_b32 s82, s82, 7
s_mul_i32 s82, s82, 28672
buffer_load_dwordx4 v[224:227], v7, s[48:51], s82 offen
v_perm_b32 v21, v108, v104, s84
v_perm_b32 v22, v116, v112, s84
v_perm_b32 v23, v124, v120, s84
s_waitcnt lgkmcnt(0)
s_barrier
ds_read_b128 a[160:163], v1 offset:0
ds_read_b128 a[164:167], v1 offset:1024
buffer_load_dwordx4 v[228:231], v7, s[48:51], s82 offen offset:896
buffer_load_dwordx4 v[232:235], v7, s[48:51], s82 offen offset:1792
ds_read_b128 a[168:171], v1 offset:2048
ds_read_b128 a[172:175], v1 offset:3072
ds_read_b128 a[176:179], v1 offset:4096
ds_read_b128 a[180:183], v1 offset:5120
ds_read_b128 a[184:187], v1 offset:6144
ds_read_b128 a[188:191], v1 offset:7168
ds_read_b128 a[192:195], v1 offset:8192
ds_read_b128 a[196:199], v1 offset:9216
s_waitcnt lgkmcnt(5)
v_mfma_f32_16x16x32_bf16 a[0:3], v[16:19], a[160:163], 0
s_waitcnt lgkmcnt(0)
v_perm_b32 v24, v164, v160, s85
v_perm_b32 v25, v172, v168, s85
v_mfma_f32_16x16x32_bf16 a[0:3], v[20:23], a[180:183], a[0:3]
v_perm_b32 v26, v36, v32, s85
v_perm_b32 v27, v44, v40, s85
v_mfma_f32_16x16x32_bf16 a[4:7], v[16:19], a[164:167], 0
v_perm_b32 v28, v100, v96, s85
v_perm_b32 v29, v108, v104, s85
v_mfma_f32_16x16x32_bf16 a[4:7], v[20:23], a[184:187], a[4:7]
v_perm_b32 v30, v116, v112, s85
v_perm_b32 v31, v124, v120, s85
v_mfma_f32_16x16x32_bf16 a[8:11], v[16:19], a[168:171], 0
buffer_load_dwordx4 v[236:239], v7, s[48:51], s82 offen offset:2688
v_mfma_f32_16x16x32_bf16 a[8:11], v[20:23], a[188:191], a[8:11]
s_add_u32 s83, s82, 3584
v_mfma_f32_16x16x32_bf16 a[12:15], v[16:19], a[172:175], 0
buffer_load_dwordx4 v[240:243], v7, s[48:51], s83 offen
v_mfma_f32_16x16x32_bf16 a[12:15], v[20:23], a[192:195], a[12:15]
buffer_load_dwordx4 v[244:247], v7, s[48:51], s83 offen offset:896
v_mfma_f32_16x16x32_bf16 a[16:19], v[16:19], a[176:179], 0
ds_read_b128 a[200:203], v1 offset:10240
v_mfma_f32_16x16x32_bf16 a[16:19], v[20:23], a[196:199], a[16:19]
ds_read_b128 a[220:223], v1 offset:15360
v_mfma_f32_16x16x32_bf16 a[20:23], v[24:27], a[160:163], 0
v_perm_b32 v16, v165, v161, s84
v_perm_b32 v17, v173, v169, s84
v_mfma_f32_16x16x32_bf16 a[20:23], v[28:31], a[180:183], a[20:23]
v_perm_b32 v18, v37, v33, s84
v_perm_b32 v19, v45, v41, s84
v_mfma_f32_16x16x32_bf16 a[24:27], v[24:27], a[164:167], 0
v_perm_b32 v20, v101, v97, s84
v_perm_b32 v21, v109, v105, s84
v_mfma_f32_16x16x32_bf16 a[24:27], v[28:31], a[184:187], a[24:27]
v_perm_b32 v22, v117, v113, s84
v_perm_b32 v23, v125, v121, s84
v_mfma_f32_16x16x32_bf16 a[28:31], v[24:27], a[168:171], 0
buffer_load_dwordx4 v[248:251], v7, s[48:51], s83 offen offset:1792
v_mfma_f32_16x16x32_bf16 a[28:31], v[28:31], a[188:191], a[28:31]
)"
R"(buffer_load_dwordx4 v[252:255], v7, s[48:51], s83 offen offset:2688
v_mfma_f32_16x16x32_bf16 a[32:35], v[24:27], a[172:175], 0
v_mfma_f32_16x16x32_bf16 a[32:35], v[28:31], a[192:195], a[32:35]
v_mfma_f32_16x16x32_bf16 a[36:39], v[24:27], a[176:179], 0
v_mfma_f32_16x16x32_bf16 a[36:39], v[28:31], a[196:199], a[36:39]
v_mfma_f32_16x16x32_bf16 a[40:43], v[16:19], a[160:163], 0
v_perm_b32 v24, v165, v161, s85
v_perm_b32 v25, v173, v169, s85
v_mfma_f32_16x16x32_bf16 a[40:43], v[20:23], a[180:183], a[40:43]
v_perm_b32 v26, v37, v33, s85
v_perm_b32 v27, v45, v41, s85
v_mfma_f32_16x16x32_bf16 a[44:47], v[16:19], a[164:167], 0
v_perm_b32 v28, v101, v97, s85
v_perm_b32 v29, v109, v105, s85
v_mfma_f32_16x16x32_bf16 a[44:47], v[20:23], a[184:187], a[44:47]
v_perm_b32 v30, v117, v113, s85
v_perm_b32 v31, v125, v121, s85
v_mfma_f32_16x16x32_bf16 a[48:51], v[16:19], a[168:171], 0
ds_read_b128 a[204:207], v1 offset:11264
v_mfma_f32_16x16x32_bf16 a[48:51], v[20:23], a[188:191], a[48:51]
ds_read_b128 a[224:227], v1 offset:16384
v_mfma_f32_16x16x32_bf16 a[52:55], v[16:19], a[172:175], 0
v_mfma_f32_16x16x32_bf16 a[52:55], v[20:23], a[192:195], a[52:55]
v_mfma_f32_16x16x32_bf16 a[56:59], v[16:19], a[176:179], 0
v_mfma_f32_16x16x32_bf16 a[56:59], v[20:23], a[196:199], a[56:59]
v_mfma_f32_16x16x32_bf16 a[60:63], v[24:27], a[160:163], 0
v_perm_b32 v16, v166, v162, s84
v_perm_b32 v17, v174, v170, s84
v_mfma_f32_16x16x32_bf16 a[60:63], v[28:31], a[180:183], a[60:63]
v_perm_b32 v18, v38, v34, s84
v_perm_b32 v19, v46, v42, s84
v_mfma_f32_16x16x32_bf16 a[64:67], v[24:27], a[164:167], 0
v_perm_b32 v20, v102, v98, s84
v_perm_b32 v21, v110, v106, s84
v_mfma_f32_16x16x32_bf16 a[64:67], v[28:31], a[184:187], a[64:67]
v_perm_b32 v22, v118, v114, s84
v_perm_b32 v23, v126, v122, s84
v_mfma_f32_16x16x32_bf16 a[68:71], v[24:27], a[168:171], 0
v_mfma_f32_16x16x32_bf16 a[68:71], v[28:31], a[188:191], a[68:71]
v_mfma_f32_16x16x32_bf16 a[72:75], v[24:27], a[172:175], 0
ds_read_b128 a[208:211], v1 offset:12288
v_mfma_f32_16x16x32_bf16 a[72:75], v[28:31], a[192:195], a[72:75]
ds_read_b128 a[228:231], v1 offset:17408
v_mfma_f32_16x16x32_bf16 a[76:79], v[24:27], a[176:179], 0
v_mfma_f32_16x16x32_bf16 a[76:79], v[28:31], a[196:199], a[76:79]
v_mfma_f32_16x16x32_bf16 a[80:83], v[16:19], a[160:163], 0
v_perm_b32 v24, v166, v162, s85
v_perm_b32 v25, v174, v170, s85
v_mfma_f32_16x16x32_bf16 a[80:83], v[20:23], a[180:183], a[80:83]
v_perm_b32 v26, v38, v34, s85
v_perm_b32 v27, v46, v42, s85
v_mfma_f32_16x16x32_bf16 a[84:87], v[16:19], a[164:167], 0
v_perm_b32 v28, v102, v98, s85
v_perm_b32 v29, v110, v106, s85
v_mfma_f32_16x16x32_bf16 a[84:87], v[20:23], a[184:187], a[84:87]
v_perm_b32 v30, v118, v114, s85
v_perm_b32 v31, v126, v122, s85
v_mfma_f32_16x16x32_bf16 a[88:91], v[16:19], a[168:171], 0
v_mfma_f32_16x16x32_bf16 a[88:91], v[20:23], a[188:191], a[88:91]
v_mfma_f32_16x16x32_bf16 a[92:95], v[16:19], a[172:175], 0
v_mfma_f32_16x16x32_bf16 a[92:95], v[20:23], a[192:195], a[92:95]
v_mfma_f32_16x16x32_bf16 a[96:99], v[16:19], a[176:179], 0
v_mfma_f32_16x16x32_bf16 a[96:99], v[20:23], a[196:199], a[96:99]
v_mfma_f32_16x16x32_bf16 a[100:103], v[24:27], a[160:163], 0
v_perm_b32 v16, v167, v163, s84
v_perm_b32 v17, v175, v171, s84
v_mfma_f32_16x16x32_bf16 a[100:103], v[28:31], a[180:183], a[100:103]
v_perm_b32 v18, v39, v35, s84
v_perm_b32 v19, v47, v43, s84
v_mfma_f32_16x16x32_bf16 a[104:107], v[24:27], a[164:167], 0
ds_read_b128 a[212:215], v1 offset:13312
v_perm_b32 v20, v103, v99, s84
v_mfma_f32_16x16x32_bf16 a[104:107], v[28:31], a[184:187], a[104:107]
ds_read_b128 a[232:235], v1 offset:18432
v_perm_b32 v21, v111, v107, s84
v_mfma_f32_16x16x32_bf16 a[108:111], v[24:27], a[168:171], 0
v_perm_b32 v22, v119, v115, s84
v_perm_b32 v23, v127, v123, s84
v_mfma_f32_16x16x32_bf16 a[108:111], v[28:31], a[188:191], a[108:111]
v_mfma_f32_16x16x32_bf16 a[112:115], v[24:27], a[172:175], 0
v_mfma_f32_16x16x32_bf16 a[112:115], v[28:31], a[192:195], a[112:115]
v_mfma_f32_16x16x32_bf16 a[116:119], v[24:27], a[176:179], 0
v_mfma_f32_16x16x32_bf16 a[116:119], v[28:31], a[196:199], a[116:119]
v_mfma_f32_16x16x32_bf16 a[120:123], v[16:19], a[160:163], 0
v_perm_b32 v24, v167, v163, s85
v_perm_b32 v25, v175, v171, s85
v_mfma_f32_16x16x32_bf16 a[120:123], v[20:23], a[180:183], a[120:123]
s_add_u32 s82, s95, 7
s_and_b32 s82, s82, 7
s_mul_i32 s82, s82, 28672
v_perm_b32 v26, v39, v35, s85
v_perm_b32 v27, v47, v43, s85
v_mfma_f32_16x16x32_bf16 a[124:127], v[16:19], a[164:167], 0
v_perm_b32 v28, v103, v99, s85
v_perm_b32 v29, v111, v107, s85
v_mfma_f32_16x16x32_bf16 a[124:127], v[20:23], a[184:187], a[124:127]
v_perm_b32 v30, v119, v115, s85
v_perm_b32 v31, v127, v123, s85
v_mfma_f32_16x16x32_bf16 a[128:131], v[16:19], a[168:171], 0
buffer_load_dwordx4 v[160:163], v7, s[48:51], s82 offen
ds_read_b128 a[216:219], v1 offset:14336
v_mfma_f32_16x16x32_bf16 a[128:131], v[20:23], a[188:191], a[128:131]
buffer_load_dwordx4 v[164:167], v7, s[48:51], s82 offen offset:896
ds_read_b128 a[236:239], v1 offset:19456
v_mfma_f32_16x16x32_bf16 a[132:135], v[16:19], a[172:175], 0
buffer_load_dwordx4 v[168:171], v7, s[48:51], s82 offen offset:1792
v_mfma_f32_16x16x32_bf16 a[132:135], v[20:23], a[192:195], a[132:135]
buffer_load_dwordx4 v[172:175], v7, s[48:51], s82 offen offset:2688
v_mfma_f32_16x16x32_bf16 a[136:139], v[16:19], a[176:179], 0
s_add_u32 s83, s82, 3584
v_mfma_f32_16x16x32_bf16 a[136:139], v[20:23], a[196:199], a[136:139]
buffer_load_dwordx4 v[96:99], v7, s[48:51], s83 offen
v_mfma_f32_16x16x32_bf16 a[140:143], v[24:27], a[160:163], 0
s_waitcnt vmcnt(37)
v_perm_b32 v16, v52, v48, s84
v_perm_b32 v17, v60, v56, s84
v_mfma_f32_16x16x32_bf16 a[140:143], v[28:31], a[180:183], a[140:143]
v_perm_b32 v18, v68, v64, s84
v_perm_b32 v19, v76, v72, s84
v_mfma_f32_16x16x32_bf16 a[144:147], v[24:27], a[164:167], 0
s_waitcnt vmcnt(29)
v_perm_b32 v20, v132, v128, s84
v_perm_b32 v21, v140, v136, s84
v_mfma_f32_16x16x32_bf16 a[144:147], v[28:31], a[184:187], a[144:147]
v_perm_b32 v22, v148, v144, s84
v_perm_b32 v23, v156, v152, s84
v_mfma_f32_16x16x32_bf16 a[148:151], v[24:27], a[168:171], 0
buffer_load_dwordx4 v[100:103], v7, s[48:51], s83 offen offset:896
v_mfma_f32_16x16x32_bf16 a[148:151], v[28:31], a[188:191], a[148:151]
buffer_load_dwordx4 v[104:107], v7, s[48:51], s83 offen offset:1792
v_mfma_f32_16x16x32_bf16 a[152:155], v[24:27], a[172:175], 0
buffer_load_dwordx4 v[108:111], v7, s[48:51], s83 offen offset:2688
v_mfma_f32_16x16x32_bf16 a[152:155], v[28:31], a[192:195], a[152:155]
s_add_u32 s72, s88, s89
v_mfma_f32_16x16x32_bf16 a[156:159], v[24:27], a[176:179], 0
s_add_u32 s73, s72, s66
v_mfma_f32_16x16x32_bf16 a[156:159], v[28:31], a[196:199], a[156:159]
s_add_u32 s74, s72, s68
s_waitcnt lgkmcnt(1)
v_mfma_f32_16x16x32_bf16 a[0:3], v[16:19], a[200:203], a[0:3]
s_add_u32 s75, s72, s70
v_perm_b32 v24, v52, v48, s85
v_perm_b32 v25, v60, v56, s85
v_mfma_f32_16x16x32_bf16 a[0:3], v[20:23], a[220:223], a[0:3]
v_perm_b32 v26, v68, v64, s85
v_perm_b32 v27, v76, v72, s85
v_mfma_f32_16x16x32_bf16 a[4:7], v[16:19], a[204:207], a[4:7]
v_perm_b32 v28, v132, v128, s85
v_perm_b32 v29, v140, v136, s85
v_mfma_f32_16x16x32_bf16 a[4:7], v[20:23], a[224:227], a[4:7]
v_perm_b32 v30, v148, v144, s85
v_perm_b32 v31, v156, v152, s85
v_mfma_f32_16x16x32_bf16 a[8:11], v[16:19], a[208:211], a[8:11]
buffer_load_dwordx4 v[32:35], v4, s[36:39], s72 offen nt
v_mfma_f32_16x16x32_bf16 a[8:11], v[20:23], a[228:231], a[8:11]
buffer_load_dwordx4 v[36:39], v4, s[36:39], s72 offen offset:512 nt
v_mfma_f32_16x16x32_bf16 a[12:15], v[16:19], a[212:215], a[12:15]
ds_read_b128 a[160:163], v1 offset:20480
v_mfma_f32_16x16x32_bf16 a[12:15], v[20:23], a[232:235], a[12:15]
ds_read_b128 a[180:183], v1 offset:25600
v_mfma_f32_16x16x32_bf16 a[16:19], v[16:19], a[216:219], a[16:19]
buffer_load_dwordx4 v[40:43], v4, s[36:39], s72 offen offset:2048 nt
s_waitcnt lgkmcnt(2)
v_mfma_f32_16x16x32_bf16 a[16:19], v[20:23], a[236:239], a[16:19]
buffer_load_dwordx4 v[44:47], v4, s[36:39], s72 offen offset:2560 nt
v_mfma_f32_16x16x32_bf16 a[20:23], v[24:27], a[200:203], a[20:23]
v_perm_b32 v16, v53, v49, s84
v_perm_b32 v17, v61, v57, s84
v_mfma_f32_16x16x32_bf16 a[20:23], v[28:31], a[220:223], a[20:23]
v_perm_b32 v18, v69, v65, s84
v_perm_b32 v19, v77, v73, s84
v_mfma_f32_16x16x32_bf16 a[24:27], v[24:27], a[204:207], a[24:27]
v_perm_b32 v20, v133, v129, s84
v_perm_b32 v21, v141, v137, s84
v_mfma_f32_16x16x32_bf16 a[24:27], v[28:31], a[224:227], a[24:27]
v_perm_b32 v22, v149, v145, s84
v_perm_b32 v23, v157, v153, s84
v_mfma_f32_16x16x32_bf16 a[28:31], v[24:27], a[208:211], a[28:31]
v_mfma_f32_16x16x32_bf16 a[28:31], v[28:31], a[228:231], a[28:31]
v_mfma_f32_16x16x32_bf16 a[32:35], v[24:27], a[212:215], a[32:35]
v_mfma_f32_16x16x32_bf16 a[32:35], v[28:31], a[232:235], a[32:35]
v_mfma_f32_16x16x32_bf16 a[36:39], v[24:27], a[216:219], a[36:39]
v_mfma_f32_16x16x32_bf16 a[36:39], v[28:31], a[236:239], a[36:39]
v_mfma_f32_16x16x32_bf16 a[40:43], v[16:19], a[200:203], a[40:43]
v_perm_b32 v24, v53, v49, s85
v_perm_b32 v25, v61, v57, s85
v_mfma_f32_16x16x32_bf16 a[40:43], v[20:23], a[220:223], a[40:43]
v_perm_b32 v26, v69, v65, s85
v_perm_b32 v27, v77, v73, s85
v_mfma_f32_16x16x32_bf16 a[44:47], v[16:19], a[204:207], a[44:47]
ds_read_b128 a[164:167], v1 offset:21504
v_perm_b32 v28, v133, v129, s85
v_mfma_f32_16x16x32_bf16 a[44:47], v[20:23], a[224:227], a[44:47]
ds_read_b128 a[184:187], v1 offset:26624
v_perm_b32 v29, v141, v137, s85
v_mfma_f32_16x16x32_bf16 a[48:51], v[16:19], a[208:211], a[48:51]
v_perm_b32 v30, v149, v145, s85
v_perm_b32 v31, v157, v153, s85
v_mfma_f32_16x16x32_bf16 a[48:51], v[20:23], a[228:231], a[48:51]
v_mfma_f32_16x16x32_bf16 a[52:55], v[16:19], a[212:215], a[52:55]
)"
R"(v_mfma_f32_16x16x32_bf16 a[52:55], v[20:23], a[232:235], a[52:55]
v_mfma_f32_16x16x32_bf16 a[56:59], v[16:19], a[216:219], a[56:59]
v_mfma_f32_16x16x32_bf16 a[56:59], v[20:23], a[236:239], a[56:59]
v_mfma_f32_16x16x32_bf16 a[60:63], v[24:27], a[200:203], a[60:63]
v_perm_b32 v16, v54, v50, s84
v_perm_b32 v17, v62, v58, s84
v_mfma_f32_16x16x32_bf16 a[60:63], v[28:31], a[220:223], a[60:63]
v_perm_b32 v18, v70, v66, s84
v_perm_b32 v19, v78, v74, s84
v_mfma_f32_16x16x32_bf16 a[64:67], v[24:27], a[204:207], a[64:67]
v_perm_b32 v20, v134, v130, s84
v_perm_b32 v21, v142, v138, s84
v_mfma_f32_16x16x32_bf16 a[64:67], v[28:31], a[224:227], a[64:67]
v_perm_b32 v22, v150, v146, s84
v_perm_b32 v23, v158, v154, s84
v_mfma_f32_16x16x32_bf16 a[68:71], v[24:27], a[208:211], a[68:71]
v_mfma_f32_16x16x32_bf16 a[68:71], v[28:31], a[228:231], a[68:71]
ds_read_b128 a[168:171], v1 offset:22528
v_mfma_f32_16x16x32_bf16 a[72:75], v[24:27], a[212:215], a[72:75]
ds_read_b128 a[188:191], v1 offset:27648
v_mfma_f32_16x16x32_bf16 a[72:75], v[28:31], a[232:235], a[72:75]
v_mfma_f32_16x16x32_bf16 a[76:79], v[24:27], a[216:219], a[76:79]
v_mfma_f32_16x16x32_bf16 a[76:79], v[28:31], a[236:239], a[76:79]
v_mfma_f32_16x16x32_bf16 a[80:83], v[16:19], a[200:203], a[80:83]
v_perm_b32 v24, v54, v50, s85
v_perm_b32 v25, v62, v58, s85
v_mfma_f32_16x16x32_bf16 a[80:83], v[20:23], a[220:223], a[80:83]
v_perm_b32 v26, v70, v66, s85
v_perm_b32 v27, v78, v74, s85
v_mfma_f32_16x16x32_bf16 a[84:87], v[16:19], a[204:207], a[84:87]
v_perm_b32 v28, v134, v130, s85
v_perm_b32 v29, v142, v138, s85
v_mfma_f32_16x16x32_bf16 a[84:87], v[20:23], a[224:227], a[84:87]
v_perm_b32 v30, v150, v146, s85
v_perm_b32 v31, v158, v154, s85
v_mfma_f32_16x16x32_bf16 a[88:91], v[16:19], a[208:211], a[88:91]
v_mfma_f32_16x16x32_bf16 a[88:91], v[20:23], a[228:231], a[88:91]
v_mfma_f32_16x16x32_bf16 a[92:95], v[16:19], a[212:215], a[92:95]
v_mfma_f32_16x16x32_bf16 a[92:95], v[20:23], a[232:235], a[92:95]
v_mfma_f32_16x16x32_bf16 a[96:99], v[16:19], a[216:219], a[96:99]
v_mfma_f32_16x16x32_bf16 a[96:99], v[20:23], a[236:239], a[96:99]
v_mfma_f32_16x16x32_bf16 a[100:103], v[24:27], a[200:203], a[100:103]
v_perm_b32 v16, v55, v51, s84
v_perm_b32 v17, v63, v59, s84
v_mfma_f32_16x16x32_bf16 a[100:103], v[28:31], a[220:223], a[100:103]
v_perm_b32 v18, v71, v67, s84
v_perm_b32 v19, v79, v75, s84
v_mfma_f32_16x16x32_bf16 a[104:107], v[24:27], a[204:207], a[104:107]
ds_read_b128 a[172:175], v1 offset:23552
v_perm_b32 v20, v135, v131, s84
v_mfma_f32_16x16x32_bf16 a[104:107], v[28:31], a[224:227], a[104:107]
ds_read_b128 a[192:195], v1 offset:28672
v_perm_b32 v21, v143, v139, s84
v_mfma_f32_16x16x32_bf16 a[108:111], v[24:27], a[208:211], a[108:111]
v_perm_b32 v22, v151, v147, s84
v_perm_b32 v23, v159, v155, s84
v_mfma_f32_16x16x32_bf16 a[108:111], v[28:31], a[228:231], a[108:111]
v_mfma_f32_16x16x32_bf16 a[112:115], v[24:27], a[212:215], a[112:115]
v_mfma_f32_16x16x32_bf16 a[112:115], v[28:31], a[232:235], a[112:115]
v_mfma_f32_16x16x32_bf16 a[116:119], v[24:27], a[216:219], a[116:119]
v_mfma_f32_16x16x32_bf16 a[116:119], v[28:31], a[236:239], a[116:119]
v_mfma_f32_16x16x32_bf16 a[120:123], v[16:19], a[200:203], a[120:123]
v_perm_b32 v24, v55, v51, s85
v_perm_b32 v25, v63, v59, s85
v_mfma_f32_16x16x32_bf16 a[120:123], v[20:23], a[220:223], a[120:123]
v_perm_b32 v26, v71, v67, s85
v_perm_b32 v27, v79, v75, s85
v_mfma_f32_16x16x32_bf16 a[124:127], v[16:19], a[204:207], a[124:127]
v_perm_b32 v28, v135, v131, s85
v_perm_b32 v29, v143, v139, s85
v_mfma_f32_16x16x32_bf16 a[124:127], v[20:23], a[224:227], a[124:127]
v_perm_b32 v30, v151, v147, s85
v_perm_b32 v31, v159, v155, s85
v_mfma_f32_16x16x32_bf16 a[128:131], v[16:19], a[208:211], a[128:131]
buffer_load_dwordx4 v[48:51], v4, s[36:39], s72 offen offset:128 nt
ds_read_b128 a[176:179], v1 offset:24576
v_mfma_f32_16x16x32_bf16 a[128:131], v[20:23], a[228:231], a[128:131]
buffer_load_dwordx4 v[52:55], v4, s[36:39], s72 offen offset:640 nt
ds_read_b128 a[196:199], v1 offset:29696
v_mfma_f32_16x16x32_bf16 a[132:135], v[16:19], a[212:215], a[132:135]
buffer_load_dwordx4 v[56:59], v4, s[36:39], s72 offen offset:2176 nt
v_mfma_f32_16x16x32_bf16 a[132:135], v[20:23], a[232:235], a[132:135]
buffer_load_dwordx4 v[60:63], v4, s[36:39], s72 offen offset:2688 nt
v_mfma_f32_16x16x32_bf16 a[136:139], v[16:19], a[216:219], a[136:139]
buffer_load_dwordx4 v[64:67], v4, s[36:39], s73 offen nt
v_mfma_f32_16x16x32_bf16 a[136:139], v[20:23], a[236:239], a[136:139]
buffer_load_dwordx4 v[68:71], v4, s[36:39], s73 offen offset:512 nt
v_mfma_f32_16x16x32_bf16 a[140:143], v[24:27], a[200:203], a[140:143]
s_waitcnt vmcnt(34)
v_perm_b32 v16, v84, v80, s84
v_perm_b32 v17, v92, v88, s84
v_mfma_f32_16x16x32_bf16 a[140:143], v[28:31], a[220:223], a[140:143]
v_perm_b32 v18, v180, v176, s84
v_perm_b32 v19, v188, v184, s84
v_mfma_f32_16x16x32_bf16 a[144:147], v[24:27], a[204:207], a[144:147]
s_waitcnt vmcnt(26)
v_perm_b32 v20, v196, v192, s84
v_perm_b32 v21, v204, v200, s84
v_mfma_f32_16x16x32_bf16 a[144:147], v[28:31], a[224:227], a[144:147]
v_perm_b32 v22, v212, v208, s84
v_perm_b32 v23, v220, v216, s84
v_mfma_f32_16x16x32_bf16 a[148:151], v[24:27], a[208:211], a[148:151]
buffer_load_dwordx4 v[72:75], v4, s[36:39], s73 offen offset:2048 nt
v_mfma_f32_16x16x32_bf16 a[148:151], v[28:31], a[228:231], a[148:151]
buffer_load_dwordx4 v[76:79], v4, s[36:39], s73 offen offset:2560 nt
v_mfma_f32_16x16x32_bf16 a[152:155], v[24:27], a[212:215], a[152:155]
v_mfma_f32_16x16x32_bf16 a[152:155], v[28:31], a[232:235], a[152:155]
v_mfma_f32_16x16x32_bf16 a[156:159], v[24:27], a[216:219], a[156:159]
v_mfma_f32_16x16x32_bf16 a[156:159], v[28:31], a[236:239], a[156:159]
s_waitcnt lgkmcnt(1)
v_mfma_f32_16x16x32_bf16 a[0:3], v[16:19], a[160:163], a[0:3]
v_perm_b32 v24, v84, v80, s85
v_perm_b32 v25, v92, v88, s85
v_mfma_f32_16x16x32_bf16 a[0:3], v[20:23], a[180:183], a[0:3]
v_perm_b32 v26, v180, v176, s85
v_perm_b32 v27, v188, v184, s85
v_mfma_f32_16x16x32_bf16 a[4:7], v[16:19], a[164:167], a[4:7]
v_perm_b32 v28, v196, v192, s85
v_perm_b32 v29, v204, v200, s85
v_mfma_f32_16x16x32_bf16 a[4:7], v[20:23], a[184:187], a[4:7]
v_perm_b32 v30, v212, v208, s85
v_perm_b32 v31, v220, v216, s85
v_mfma_f32_16x16x32_bf16 a[8:11], v[16:19], a[168:171], a[8:11]
v_mfma_f32_16x16x32_bf16 a[8:11], v[20:23], a[188:191], a[8:11]
v_mfma_f32_16x16x32_bf16 a[12:15], v[16:19], a[172:175], a[12:15]
ds_read_b128 a[200:203], v1 offset:30720
v_mfma_f32_16x16x32_bf16 a[12:15], v[20:23], a[192:195], a[12:15]
ds_read_b128 a[220:223], v1 offset:35840
v_mfma_f32_16x16x32_bf16 a[16:19], v[16:19], a[176:179], a[16:19]
s_waitcnt lgkmcnt(2)
v_mfma_f32_16x16x32_bf16 a[16:19], v[20:23], a[196:199], a[16:19]
v_mfma_f32_16x16x32_bf16 a[20:23], v[24:27], a[160:163], a[20:23]
v_perm_b32 v16, v85, v81, s84
v_perm_b32 v17, v93, v89, s84
v_mfma_f32_16x16x32_bf16 a[20:23], v[28:31], a[180:183], a[20:23]
v_perm_b32 v18, v181, v177, s84
v_perm_b32 v19, v189, v185, s84
v_mfma_f32_16x16x32_bf16 a[24:27], v[24:27], a[164:167], a[24:27]
v_perm_b32 v20, v197, v193, s84
v_perm_b32 v21, v205, v201, s84
v_mfma_f32_16x16x32_bf16 a[24:27], v[28:31], a[184:187], a[24:27]
v_perm_b32 v22, v213, v209, s84
v_perm_b32 v23, v221, v217, s84
v_mfma_f32_16x16x32_bf16 a[28:31], v[24:27], a[168:171], a[28:31]
v_mfma_f32_16x16x32_bf16 a[28:31], v[28:31], a[188:191], a[28:31]
v_mfma_f32_16x16x32_bf16 a[32:35], v[24:27], a[172:175], a[32:35]
v_mfma_f32_16x16x32_bf16 a[32:35], v[28:31], a[192:195], a[32:35]
v_mfma_f32_16x16x32_bf16 a[36:39], v[24:27], a[176:179], a[36:39]
v_mfma_f32_16x16x32_bf16 a[36:39], v[28:31], a[196:199], a[36:39]
v_mfma_f32_16x16x32_bf16 a[40:43], v[16:19], a[160:163], a[40:43]
v_perm_b32 v24, v85, v81, s85
v_perm_b32 v25, v93, v89, s85
v_mfma_f32_16x16x32_bf16 a[40:43], v[20:23], a[180:183], a[40:43]
v_perm_b32 v26, v181, v177, s85
v_perm_b32 v27, v189, v185, s85
v_mfma_f32_16x16x32_bf16 a[44:47], v[16:19], a[164:167], a[44:47]
ds_read_b128 a[204:207], v1 offset:31744
v_perm_b32 v28, v197, v193, s85
v_mfma_f32_16x16x32_bf16 a[44:47], v[20:23], a[184:187], a[44:47]
ds_read_b128 a[224:227], v1 offset:36864
v_perm_b32 v29, v205, v201, s85
v_mfma_f32_16x16x32_bf16 a[48:51], v[16:19], a[168:171], a[48:51]
v_perm_b32 v30, v213, v209, s85
v_perm_b32 v31, v221, v217, s85
v_mfma_f32_16x16x32_bf16 a[48:51], v[20:23], a[188:191], a[48:51]
v_mfma_f32_16x16x32_bf16 a[52:55], v[16:19], a[172:175], a[52:55]
v_mfma_f32_16x16x32_bf16 a[52:55], v[20:23], a[192:195], a[52:55]
v_mfma_f32_16x16x32_bf16 a[56:59], v[16:19], a[176:179], a[56:59]
v_mfma_f32_16x16x32_bf16 a[56:59], v[20:23], a[196:199], a[56:59]
v_mfma_f32_16x16x32_bf16 a[60:63], v[24:27], a[160:163], a[60:63]
v_perm_b32 v16, v86, v82, s84
v_perm_b32 v17, v94, v90, s84
v_mfma_f32_16x16x32_bf16 a[60:63], v[28:31], a[180:183], a[60:63]
v_perm_b32 v18, v182, v178, s84
v_perm_b32 v19, v190, v186, s84
v_mfma_f32_16x16x32_bf16 a[64:67], v[24:27], a[164:167], a[64:67]
v_perm_b32 v20, v198, v194, s84
v_perm_b32 v21, v206, v202, s84
v_mfma_f32_16x16x32_bf16 a[64:67], v[28:31], a[184:187], a[64:67]
v_perm_b32 v22, v214, v210, s84
v_perm_b32 v23, v222, v218, s84
v_mfma_f32_16x16x32_bf16 a[68:71], v[24:27], a[168:171], a[68:71]
v_mfma_f32_16x16x32_bf16 a[68:71], v[28:31], a[188:191], a[68:71]
ds_read_b128 a[208:211], v1 offset:32768
v_mfma_f32_16x16x32_bf16 a[72:75], v[24:27], a[172:175], a[72:75]
ds_read_b128 a[228:231], v1 offset:37888
v_mfma_f32_16x16x32_bf16 a[72:75], v[28:31], a[192:195], a[72:75]
v_mfma_f32_16x16x32_bf16 a[76:79], v[24:27], a[176:179], a[76:79]
v_mfma_f32_16x16x32_bf16 a[76:79], v[28:31], a[196:199], a[76:79]
v_mfma_f32_16x16x32_bf16 a[80:83], v[16:19], a[160:163], a[80:83]
v_perm_b32 v24, v86, v82, s85
v_perm_b32 v25, v94, v90, s85
v_mfma_f32_16x16x32_bf16 a[80:83], v[20:23], a[180:183], a[80:83]
v_perm_b32 v26, v182, v178, s85
v_perm_b32 v27, v190, v186, s85
v_mfma_f32_16x16x32_bf16 a[84:87], v[16:19], a[164:167], a[84:87]
v_perm_b32 v28, v198, v194, s85
v_perm_b32 v29, v206, v202, s85
v_mfma_f32_16x16x32_bf16 a[84:87], v[20:23], a[184:187], a[84:87]
v_perm_b32 v30, v214, v210, s85
)"
R"(v_perm_b32 v31, v222, v218, s85
v_mfma_f32_16x16x32_bf16 a[88:91], v[16:19], a[168:171], a[88:91]
v_mfma_f32_16x16x32_bf16 a[88:91], v[20:23], a[188:191], a[88:91]
v_mfma_f32_16x16x32_bf16 a[92:95], v[16:19], a[172:175], a[92:95]
v_mfma_f32_16x16x32_bf16 a[92:95], v[20:23], a[192:195], a[92:95]
v_mfma_f32_16x16x32_bf16 a[96:99], v[16:19], a[176:179], a[96:99]
v_mfma_f32_16x16x32_bf16 a[96:99], v[20:23], a[196:199], a[96:99]
v_mfma_f32_16x16x32_bf16 a[100:103], v[24:27], a[160:163], a[100:103]
v_perm_b32 v16, v87, v83, s84
v_perm_b32 v17, v95, v91, s84
v_mfma_f32_16x16x32_bf16 a[100:103], v[28:31], a[180:183], a[100:103]
v_perm_b32 v18, v183, v179, s84
v_perm_b32 v19, v191, v187, s84
v_mfma_f32_16x16x32_bf16 a[104:107], v[24:27], a[164:167], a[104:107]
ds_read_b128 a[212:215], v1 offset:33792
v_perm_b32 v20, v199, v195, s84
v_mfma_f32_16x16x32_bf16 a[104:107], v[28:31], a[184:187], a[104:107]
ds_read_b128 a[232:235], v1 offset:38912
v_perm_b32 v21, v207, v203, s84
v_mfma_f32_16x16x32_bf16 a[108:111], v[24:27], a[168:171], a[108:111]
v_perm_b32 v22, v215, v211, s84
v_perm_b32 v23, v223, v219, s84
v_mfma_f32_16x16x32_bf16 a[108:111], v[28:31], a[188:191], a[108:111]
v_mfma_f32_16x16x32_bf16 a[112:115], v[24:27], a[172:175], a[112:115]
v_mfma_f32_16x16x32_bf16 a[112:115], v[28:31], a[192:195], a[112:115]
v_mfma_f32_16x16x32_bf16 a[116:119], v[24:27], a[176:179], a[116:119]
v_mfma_f32_16x16x32_bf16 a[116:119], v[28:31], a[196:199], a[116:119]
v_mfma_f32_16x16x32_bf16 a[120:123], v[16:19], a[160:163], a[120:123]
v_perm_b32 v24, v87, v83, s85
v_perm_b32 v25, v95, v91, s85
v_mfma_f32_16x16x32_bf16 a[120:123], v[20:23], a[180:183], a[120:123]
v_perm_b32 v26, v183, v179, s85
v_perm_b32 v27, v191, v187, s85
v_mfma_f32_16x16x32_bf16 a[124:127], v[16:19], a[164:167], a[124:127]
v_perm_b32 v28, v199, v195, s85
v_perm_b32 v29, v207, v203, s85
v_mfma_f32_16x16x32_bf16 a[124:127], v[20:23], a[184:187], a[124:127]
v_perm_b32 v30, v215, v211, s85
v_perm_b32 v31, v223, v219, s85
v_mfma_f32_16x16x32_bf16 a[128:131], v[16:19], a[168:171], a[128:131]
ds_read_b128 a[216:219], v1 offset:34816
v_mfma_f32_16x16x32_bf16 a[128:131], v[20:23], a[188:191], a[128:131]
ds_read_b128 a[236:239], v1 offset:39936
v_mfma_f32_16x16x32_bf16 a[132:135], v[16:19], a[172:175], a[132:135]
buffer_load_dwordx4 v[80:83], v4, s[36:39], s73 offen offset:128 nt
v_mfma_f32_16x16x32_bf16 a[132:135], v[20:23], a[192:195], a[132:135]
buffer_load_dwordx4 v[84:87], v4, s[36:39], s73 offen offset:640 nt
v_mfma_f32_16x16x32_bf16 a[136:139], v[16:19], a[176:179], a[136:139]
buffer_load_dwordx4 v[88:91], v4, s[36:39], s73 offen offset:2176 nt
v_mfma_f32_16x16x32_bf16 a[136:139], v[20:23], a[196:199], a[136:139]
buffer_load_dwordx4 v[92:95], v4, s[36:39], s73 offen offset:2688 nt
v_mfma_f32_16x16x32_bf16 a[140:143], v[24:27], a[160:163], a[140:143]
s_waitcnt vmcnt(24)
v_perm_b32 v16, v228, v224, s84
v_perm_b32 v17, v236, v232, s84
v_mfma_f32_16x16x32_bf16 a[140:143], v[28:31], a[180:183], a[140:143]
v_perm_b32 v18, v244, v240, s84
v_perm_b32 v19, v252, v248, s84
v_mfma_f32_16x16x32_bf16 a[144:147], v[24:27], a[164:167], a[144:147]
s_waitcnt vmcnt(16)
v_perm_b32 v20, v164, v160, s84
v_perm_b32 v21, v172, v168, s84
v_mfma_f32_16x16x32_bf16 a[144:147], v[28:31], a[184:187], a[144:147]
v_perm_b32 v22, v100, v96, s84
v_perm_b32 v23, v108, v104, s84
v_mfma_f32_16x16x32_bf16 a[148:151], v[24:27], a[168:171], a[148:151]
buffer_load_dwordx4 v[176:179], v4, s[36:39], s72 offen offset:256 nt
v_mfma_f32_16x16x32_bf16 a[148:151], v[28:31], a[188:191], a[148:151]
buffer_load_dwordx4 v[180:183], v4, s[36:39], s72 offen offset:768 nt
v_mfma_f32_16x16x32_bf16 a[152:155], v[24:27], a[172:175], a[152:155]
v_mfma_f32_16x16x32_bf16 a[152:155], v[28:31], a[192:195], a[152:155]
v_mfma_f32_16x16x32_bf16 a[156:159], v[24:27], a[176:179], a[156:159]
v_mfma_f32_16x16x32_bf16 a[156:159], v[28:31], a[196:199], a[156:159]
s_waitcnt lgkmcnt(1)
v_mfma_f32_16x16x32_bf16 a[0:3], v[16:19], a[200:203], a[0:3]
v_perm_b32 v24, v228, v224, s85
v_perm_b32 v25, v236, v232, s85
v_mfma_f32_16x16x32_bf16 a[0:3], v[20:23], a[220:223], a[0:3]
v_perm_b32 v26, v244, v240, s85
v_perm_b32 v27, v252, v248, s85
v_mfma_f32_16x16x32_bf16 a[4:7], v[16:19], a[204:207], a[4:7]
v_perm_b32 v28, v164, v160, s85
v_perm_b32 v29, v172, v168, s85
v_mfma_f32_16x16x32_bf16 a[4:7], v[20:23], a[224:227], a[4:7]
v_perm_b32 v30, v100, v96, s85
v_perm_b32 v31, v108, v104, s85
v_mfma_f32_16x16x32_bf16 a[8:11], v[16:19], a[208:211], a[8:11]
buffer_load_dwordx4 v[184:187], v4, s[36:39], s72 offen offset:2304 nt
v_mfma_f32_16x16x32_bf16 a[8:11], v[20:23], a[228:231], a[8:11]
v_mfma_f32_16x16x32_bf16 a[12:15], v[16:19], a[212:215], a[12:15]
v_mfma_f32_16x16x32_bf16 a[12:15], v[20:23], a[232:235], a[12:15]
v_mfma_f32_16x16x32_bf16 a[16:19], v[16:19], a[216:219], a[16:19]
s_waitcnt lgkmcnt(0)
v_mfma_f32_16x16x32_bf16 a[16:19], v[20:23], a[236:239], a[16:19]
v_mfma_f32_16x16x32_bf16 a[20:23], v[24:27], a[200:203], a[20:23]
v_perm_b32 v16, v229, v225, s84
v_perm_b32 v17, v237, v233, s84
v_mfma_f32_16x16x32_bf16 a[20:23], v[28:31], a[220:223], a[20:23]
v_perm_b32 v18, v245, v241, s84
v_perm_b32 v19, v253, v249, s84
v_mfma_f32_16x16x32_bf16 a[24:27], v[24:27], a[204:207], a[24:27]
v_perm_b32 v20, v165, v161, s84
v_perm_b32 v21, v173, v169, s84
v_mfma_f32_16x16x32_bf16 a[24:27], v[28:31], a[224:227], a[24:27]
v_perm_b32 v22, v101, v97, s84
v_perm_b32 v23, v109, v105, s84
v_mfma_f32_16x16x32_bf16 a[28:31], v[24:27], a[208:211], a[28:31]
buffer_load_dwordx4 v[188:191], v4, s[36:39], s72 offen offset:2816 nt
v_mfma_f32_16x16x32_bf16 a[28:31], v[28:31], a[228:231], a[28:31]
v_mfma_f32_16x16x32_bf16 a[32:35], v[24:27], a[212:215], a[32:35]
v_mfma_f32_16x16x32_bf16 a[32:35], v[28:31], a[232:235], a[32:35]
v_mfma_f32_16x16x32_bf16 a[36:39], v[24:27], a[216:219], a[36:39]
buffer_load_dwordx4 v[192:195], v4, s[36:39], s72 offen offset:384 nt
v_mfma_f32_16x16x32_bf16 a[36:39], v[28:31], a[236:239], a[36:39]
v_mfma_f32_16x16x32_bf16 a[40:43], v[16:19], a[200:203], a[40:43]
v_perm_b32 v24, v229, v225, s85
v_perm_b32 v25, v237, v233, s85
v_mfma_f32_16x16x32_bf16 a[40:43], v[20:23], a[220:223], a[40:43]
v_perm_b32 v26, v245, v241, s85
v_perm_b32 v27, v253, v249, s85
v_mfma_f32_16x16x32_bf16 a[44:47], v[16:19], a[204:207], a[44:47]
v_perm_b32 v28, v165, v161, s85
v_perm_b32 v29, v173, v169, s85
v_mfma_f32_16x16x32_bf16 a[44:47], v[20:23], a[224:227], a[44:47]
v_perm_b32 v30, v101, v97, s85
v_perm_b32 v31, v109, v105, s85
v_mfma_f32_16x16x32_bf16 a[48:51], v[16:19], a[208:211], a[48:51]
v_mfma_f32_16x16x32_bf16 a[48:51], v[20:23], a[228:231], a[48:51]
v_mfma_f32_16x16x32_bf16 a[52:55], v[16:19], a[212:215], a[52:55]
buffer_load_dwordx4 v[196:199], v4, s[36:39], s72 offen offset:896 nt
v_mfma_f32_16x16x32_bf16 a[52:55], v[20:23], a[232:235], a[52:55]
v_mfma_f32_16x16x32_bf16 a[56:59], v[16:19], a[216:219], a[56:59]
v_mfma_f32_16x16x32_bf16 a[56:59], v[20:23], a[236:239], a[56:59]
v_mfma_f32_16x16x32_bf16 a[60:63], v[24:27], a[200:203], a[60:63]
v_perm_b32 v16, v230, v226, s84
v_perm_b32 v17, v238, v234, s84
v_mfma_f32_16x16x32_bf16 a[60:63], v[28:31], a[220:223], a[60:63]
v_perm_b32 v18, v246, v242, s84
v_perm_b32 v19, v254, v250, s84
v_mfma_f32_16x16x32_bf16 a[64:67], v[24:27], a[204:207], a[64:67]
v_perm_b32 v20, v166, v162, s84
v_perm_b32 v21, v174, v170, s84
v_mfma_f32_16x16x32_bf16 a[64:67], v[28:31], a[224:227], a[64:67]
v_perm_b32 v22, v102, v98, s84
v_perm_b32 v23, v110, v106, s84
v_mfma_f32_16x16x32_bf16 a[68:71], v[24:27], a[208:211], a[68:71]
buffer_load_dwordx4 v[200:203], v4, s[36:39], s72 offen offset:2432 nt
v_mfma_f32_16x16x32_bf16 a[68:71], v[28:31], a[228:231], a[68:71]
v_mfma_f32_16x16x32_bf16 a[72:75], v[24:27], a[212:215], a[72:75]
v_mfma_f32_16x16x32_bf16 a[72:75], v[28:31], a[232:235], a[72:75]
v_mfma_f32_16x16x32_bf16 a[76:79], v[24:27], a[216:219], a[76:79]
v_mfma_f32_16x16x32_bf16 a[76:79], v[28:31], a[236:239], a[76:79]
v_mfma_f32_16x16x32_bf16 a[80:83], v[16:19], a[200:203], a[80:83]
v_perm_b32 v24, v230, v226, s85
v_perm_b32 v25, v238, v234, s85
v_mfma_f32_16x16x32_bf16 a[80:83], v[20:23], a[220:223], a[80:83]
v_perm_b32 v26, v246, v242, s85
v_perm_b32 v27, v254, v250, s85
v_mfma_f32_16x16x32_bf16 a[84:87], v[16:19], a[204:207], a[84:87]
v_perm_b32 v28, v166, v162, s85
v_perm_b32 v29, v174, v170, s85
v_mfma_f32_16x16x32_bf16 a[84:87], v[20:23], a[224:227], a[84:87]
v_perm_b32 v30, v102, v98, s85
v_perm_b32 v31, v110, v106, s85
v_mfma_f32_16x16x32_bf16 a[88:91], v[16:19], a[208:211], a[88:91]
buffer_load_dwordx4 v[204:207], v4, s[36:39], s72 offen offset:2944 nt
v_mfma_f32_16x16x32_bf16 a[88:91], v[20:23], a[228:231], a[88:91]
v_mfma_f32_16x16x32_bf16 a[92:95], v[16:19], a[212:215], a[92:95]
v_mfma_f32_16x16x32_bf16 a[92:95], v[20:23], a[232:235], a[92:95]
v_mfma_f32_16x16x32_bf16 a[96:99], v[16:19], a[216:219], a[96:99]
v_mfma_f32_16x16x32_bf16 a[96:99], v[20:23], a[236:239], a[96:99]
v_mfma_f32_16x16x32_bf16 a[100:103], v[24:27], a[200:203], a[100:103]
v_perm_b32 v16, v231, v227, s84
v_perm_b32 v17, v239, v235, s84
v_mfma_f32_16x16x32_bf16 a[100:103], v[28:31], a[220:223], a[100:103]
v_perm_b32 v18, v247, v243, s84
v_perm_b32 v19, v255, v251, s84
v_mfma_f32_16x16x32_bf16 a[104:107], v[24:27], a[204:207], a[104:107]
v_perm_b32 v20, v167, v163, s84
v_perm_b32 v21, v175, v171, s84
v_mfma_f32_16x16x32_bf16 a[104:107], v[28:31], a[224:227], a[104:107]
v_perm_b32 v22, v103, v99, s84
v_perm_b32 v23, v111, v107, s84
v_mfma_f32_16x16x32_bf16 a[108:111], v[24:27], a[208:211], a[108:111]
v_mfma_f32_16x16x32_bf16 a[108:111], v[28:31], a[228:231], a[108:111]
v_mfma_f32_16x16x32_bf16 a[112:115], v[24:27], a[212:215], a[112:115]
v_mfma_f32_16x16x32_bf16 a[112:115], v[28:31], a[232:235], a[112:115]
v_mfma_f32_16x16x32_bf16 a[116:119], v[24:27], a[216:219], a[116:119]
v_mfma_f32_16x16x32_bf16 a[116:119], v[28:31], a[236:239], a[116:119]
v_mfma_f32_16x16x32_bf16 a[120:123], v[16:19], a[200:203], a[120:123]
v_perm_b32 v24, v231, v227, s85
v_perm_b32 v25, v239, v235, s85
v_mfma_f32_16x16x32_bf16 a[120:123], v[20:23], a[220:223], a[120:123]
v_perm_b32 v26, v247, v243, s85
v_perm_b32 v27, v255, v251, s85
v_mfma_f32_16x16x32_bf16 a[124:127], v[16:19], a[204:207], a[124:127]
v_perm_b32 v28, v167, v163, s85
v_perm_b32 v29, v175, v171, s85
v_mfma_f32_16x16x32_bf16 a[124:127], v[20:23], a[224:227], a[124:127]
v_perm_b32 v30, v103, v99, s85
)"
R"(v_perm_b32 v31, v111, v107, s85
v_mfma_f32_16x16x32_bf16 a[128:131], v[16:19], a[208:211], a[128:131]
v_mfma_f32_16x16x32_bf16 a[128:131], v[20:23], a[228:231], a[128:131]
v_mfma_f32_16x16x32_bf16 a[132:135], v[16:19], a[212:215], a[132:135]
v_mfma_f32_16x16x32_bf16 a[132:135], v[20:23], a[232:235], a[132:135]
v_mfma_f32_16x16x32_bf16 a[136:139], v[16:19], a[216:219], a[136:139]
buffer_load_dwordx4 v[208:211], v4, s[36:39], s73 offen offset:256 nt
v_mfma_f32_16x16x32_bf16 a[136:139], v[20:23], a[236:239], a[136:139]
v_mfma_f32_16x16x32_bf16 a[140:143], v[24:27], a[200:203], a[140:143]
v_mfma_f32_16x16x32_bf16 a[140:143], v[28:31], a[220:223], a[140:143]
v_mfma_f32_16x16x32_bf16 a[144:147], v[24:27], a[204:207], a[144:147]
v_mfma_f32_16x16x32_bf16 a[144:147], v[28:31], a[224:227], a[144:147]
v_mfma_f32_16x16x32_bf16 a[148:151], v[24:27], a[208:211], a[148:151]
v_mfma_f32_16x16x32_bf16 a[148:151], v[28:31], a[228:231], a[148:151]
v_mfma_f32_16x16x32_bf16 a[152:155], v[24:27], a[212:215], a[152:155]
buffer_load_dwordx4 v[212:215], v4, s[36:39], s73 offen offset:768 nt
v_mfma_f32_16x16x32_bf16 a[152:155], v[28:31], a[232:235], a[152:155]
v_mfma_f32_16x16x32_bf16 a[156:159], v[24:27], a[216:219], a[156:159]
v_mfma_f32_16x16x32_bf16 a[156:159], v[28:31], a[236:239], a[156:159]
ds_read_b128 a[160:163], v1 offset:40960
ds_read_b128 a[164:167], v1 offset:41984
ds_read_b128 a[168:171], v1 offset:43008
ds_read_b128 a[172:175], v1 offset:44032
ds_read_b128 a[176:179], v1 offset:45056
ds_read_b128 a[180:183], v1 offset:46080
ds_read_b128 a[184:187], v1 offset:47104
ds_read_b128 a[188:191], v1 offset:48128
ds_read_b128 a[192:195], v1 offset:49152
ds_read_b128 a[196:199], v1 offset:50176
s_waitcnt vmcnt(22)
v_cndmask_b32_dpp v16, v36, v32, vcc quad_perm:[1,0,3,2] row_mask:0xf bank_mask:0xf
v_cndmask_b32_dpp v17, v37, v33, vcc quad_perm:[1,0,3,2] row_mask:0xf bank_mask:0xf
v_cndmask_b32_dpp v18, v38, v34, vcc quad_perm:[1,0,3,2] row_mask:0xf bank_mask:0xf
v_cndmask_b32_dpp v19, v39, v35, vcc quad_perm:[1,0,3,2] row_mask:0xf bank_mask:0xf
s_mov_b64 vcc, s[78:79]
v_cndmask_b32_dpp v36, v32, v36, vcc quad_perm:[1,0,3,2] row_mask:0xf bank_mask:0xf
v_cndmask_b32_dpp v37, v33, v37, vcc quad_perm:[1,0,3,2] row_mask:0xf bank_mask:0xf
v_cndmask_b32_dpp v38, v34, v38, vcc quad_perm:[1,0,3,2] row_mask:0xf bank_mask:0xf
v_cndmask_b32_dpp v39, v35, v39, vcc quad_perm:[1,0,3,2] row_mask:0xf bank_mask:0xf
s_waitcnt lgkmcnt(0)
v_mfma_f32_16x16x32_bf16 v[96:99], v[16:19], a[160:163], 0
v_cndmask_b32_dpp v20, v40, v44, vcc quad_perm:[1,0,3,2] row_mask:0xf bank_mask:0xf
v_cndmask_b32_dpp v21, v41, v45, vcc quad_perm:[1,0,3,2] row_mask:0xf bank_mask:0xf
v_mfma_f32_16x16x32_bf16 v[96:99], v[36:39], a[180:183], v[96:99]
v_cndmask_b32_dpp v22, v42, v46, vcc quad_perm:[1,0,3,2] row_mask:0xf bank_mask:0xf
v_cndmask_b32_dpp v23, v43, v47, vcc quad_perm:[1,0,3,2] row_mask:0xf bank_mask:0xf
v_mfma_f32_16x16x32_bf16 v[112:115], v[16:19], a[164:167], 0
s_mov_b64 vcc, s[76:77]
v_cndmask_b32_dpp v40, v44, v40, vcc quad_perm:[1,0,3,2] row_mask:0xf bank_mask:0xf
v_cndmask_b32_dpp v41, v45, v41, vcc quad_perm:[1,0,3,2] row_mask:0xf bank_mask:0xf
v_mfma_f32_16x16x32_bf16 v[112:115], v[36:39], a[184:187], v[112:115]
ds_read_b128 a[200:203], v1 offset:51200
v_cndmask_b32_dpp v42, v46, v42, vcc quad_perm:[1,0,3,2] row_mask:0xf bank_mask:0xf
v_mfma_f32_16x16x32_bf16 v[128:131], v[16:19], a[168:171], 0
ds_read_b128 a[204:207], v1 offset:52224
v_cndmask_b32_dpp v43, v47, v43, vcc quad_perm:[1,0,3,2] row_mask:0xf bank_mask:0xf
v_mfma_f32_16x16x32_bf16 v[128:131], v[36:39], a[188:191], v[128:131]
ds_read_b128 a[208:211], v1 offset:53248
v_mfma_f32_16x16x32_bf16 v[144:147], v[16:19], a[172:175], 0
ds_read_b128 a[212:215], v1 offset:54272
v_mfma_f32_16x16x32_bf16 v[144:147], v[36:39], a[192:195], v[144:147]
ds_read_b128 a[216:219], v1 offset:55296
v_mfma_f32_16x16x32_bf16 v[160:163], v[16:19], a[176:179], 0
ds_read_b128 a[220:223], v1 offset:56320
v_mfma_f32_16x16x32_bf16 v[160:163], v[36:39], a[196:199], v[160:163]
ds_read_b128 a[224:227], v1 offset:57344
v_mfma_f32_16x16x32_bf16 v[100:103], v[40:43], a[160:163], 0
s_waitcnt vmcnt(18)
v_cndmask_b32_dpp v24, v52, v48, vcc quad_perm:[1,0,3,2] row_mask:0xf bank_mask:0xf
v_cndmask_b32_dpp v25, v53, v49, vcc quad_perm:[1,0,3,2] row_mask:0xf bank_mask:0xf
v_mfma_f32_16x16x32_bf16 v[100:103], v[20:23], a[180:183], v[100:103]
ds_read_b128 a[228:231], v1 offset:58368
v_cndmask_b32_dpp v26, v54, v50, vcc quad_perm:[1,0,3,2] row_mask:0xf bank_mask:0xf
v_mfma_f32_16x16x32_bf16 v[116:119], v[40:43], a[164:167], 0
ds_read_b128 a[232:235], v1 offset:59392
v_cndmask_b32_dpp v27, v55, v51, vcc quad_perm:[1,0,3,2] row_mask:0xf bank_mask:0xf
v_mfma_f32_16x16x32_bf16 v[116:119], v[20:23], a[184:187], v[116:119]
ds_read_b128 a[236:239], v1 offset:60416
s_mov_b64 vcc, s[78:79]
v_cndmask_b32_dpp v52, v48, v52, vcc quad_perm:[1,0,3,2] row_mask:0xf bank_mask:0xf
v_mfma_f32_16x16x32_bf16 v[132:135], v[40:43], a[168:171], 0
v_cndmask_b32_dpp v53, v49, v53, vcc quad_perm:[1,0,3,2] row_mask:0xf bank_mask:0xf
v_cndmask_b32_dpp v54, v50, v54, vcc quad_perm:[1,0,3,2] row_mask:0xf bank_mask:0xf
v_mfma_f32_16x16x32_bf16 v[132:135], v[20:23], a[188:191], v[132:135]
v_cndmask_b32_dpp v55, v51, v55, vcc quad_perm:[1,0,3,2] row_mask:0xf bank_mask:0xf
v_mfma_f32_16x16x32_bf16 v[148:151], v[40:43], a[172:175], 0
buffer_load_dwordx4 v[216:219], v4, s[36:39], s73 offen offset:2304 nt
v_mfma_f32_16x16x32_bf16 v[148:151], v[20:23], a[192:195], v[148:151]
buffer_load_dwordx4 v[220:223], v4, s[36:39], s73 offen offset:2816 nt
v_mfma_f32_16x16x32_bf16 v[164:167], v[40:43], a[176:179], 0
buffer_load_dwordx4 v[224:227], v4, s[36:39], s73 offen offset:384 nt
v_mfma_f32_16x16x32_bf16 v[164:167], v[20:23], a[196:199], v[164:167]
s_waitcnt lgkmcnt(0)
v_mfma_f32_16x16x32_bf16 v[96:99], v[24:27], a[200:203], v[96:99]
v_cndmask_b32_dpp v28, v56, v60, vcc quad_perm:[1,0,3,2] row_mask:0xf bank_mask:0xf
v_cndmask_b32_dpp v29, v57, v61, vcc quad_perm:[1,0,3,2] row_mask:0xf bank_mask:0xf
v_mfma_f32_16x16x32_bf16 v[96:99], v[52:55], a[220:223], v[96:99]
v_cndmask_b32_dpp v30, v58, v62, vcc quad_perm:[1,0,3,2] row_mask:0xf bank_mask:0xf
v_cndmask_b32_dpp v31, v59, v63, vcc quad_perm:[1,0,3,2] row_mask:0xf bank_mask:0xf
v_mfma_f32_16x16x32_bf16 v[112:115], v[24:27], a[204:207], v[112:115]
s_mov_b64 vcc, s[76:77]
v_cndmask_b32_dpp v56, v60, v56, vcc quad_perm:[1,0,3,2] row_mask:0xf bank_mask:0xf
v_cndmask_b32_dpp v57, v61, v57, vcc quad_perm:[1,0,3,2] row_mask:0xf bank_mask:0xf
v_mfma_f32_16x16x32_bf16 v[112:115], v[52:55], a[224:227], v[112:115]
v_cndmask_b32_dpp v58, v62, v58, vcc quad_perm:[1,0,3,2] row_mask:0xf bank_mask:0xf
v_cndmask_b32_dpp v59, v63, v59, vcc quad_perm:[1,0,3,2] row_mask:0xf bank_mask:0xf
v_mfma_f32_16x16x32_bf16 v[128:131], v[24:27], a[208:211], v[128:131]
buffer_load_dwordx4 v[228:231], v4, s[36:39], s73 offen offset:896 nt
v_mfma_f32_16x16x32_bf16 v[128:131], v[52:55], a[228:231], v[128:131]
v_mfma_f32_16x16x32_bf16 v[144:147], v[24:27], a[212:215], v[144:147]
v_mfma_f32_16x16x32_bf16 v[144:147], v[52:55], a[232:235], v[144:147]
v_mfma_f32_16x16x32_bf16 v[160:163], v[24:27], a[216:219], v[160:163]
v_mfma_f32_16x16x32_bf16 v[160:163], v[52:55], a[236:239], v[160:163]
buffer_load_dwordx4 v[232:235], v4, s[36:39], s73 offen offset:2432 nt
v_mfma_f32_16x16x32_bf16 v[100:103], v[56:59], a[200:203], v[100:103]
s_waitcnt vmcnt(19)
v_cndmask_b32_dpp v16, v68, v64, vcc quad_perm:[1,0,3,2] row_mask:0xf bank_mask:0xf
v_cndmask_b32_dpp v17, v69, v65, vcc quad_perm:[1,0,3,2] row_mask:0xf bank_mask:0xf
v_mfma_f32_16x16x32_bf16 v[100:103], v[28:31], a[220:223], v[100:103]
v_cndmask_b32_dpp v18, v70, v66, vcc quad_perm:[1,0,3,2] row_mask:0xf bank_mask:0xf
v_cndmask_b32_dpp v19, v71, v67, vcc quad_perm:[1,0,3,2] row_mask:0xf bank_mask:0xf
v_mfma_f32_16x16x32_bf16 v[116:119], v[56:59], a[204:207], v[116:119]
s_mov_b64 vcc, s[78:79]
v_cndmask_b32_dpp v68, v64, v68, vcc quad_perm:[1,0,3,2] row_mask:0xf bank_mask:0xf
v_cndmask_b32_dpp v69, v65, v69, vcc quad_perm:[1,0,3,2] row_mask:0xf bank_mask:0xf
v_mfma_f32_16x16x32_bf16 v[116:119], v[28:31], a[224:227], v[116:119]
v_cndmask_b32_dpp v70, v66, v70, vcc quad_perm:[1,0,3,2] row_mask:0xf bank_mask:0xf
v_cndmask_b32_dpp v71, v67, v71, vcc quad_perm:[1,0,3,2] row_mask:0xf bank_mask:0xf
v_mfma_f32_16x16x32_bf16 v[132:135], v[56:59], a[208:211], v[132:135]
v_mfma_f32_16x16x32_bf16 v[132:135], v[28:31], a[228:231], v[132:135]
v_mfma_f32_16x16x32_bf16 v[148:151], v[56:59], a[212:215], v[148:151]
v_mfma_f32_16x16x32_bf16 v[148:151], v[28:31], a[232:235], v[148:151]
buffer_load_dwordx4 v[236:239], v4, s[36:39], s73 offen offset:2944 nt
v_mfma_f32_16x16x32_bf16 v[164:167], v[56:59], a[216:219], v[164:167]
v_mfma_f32_16x16x32_bf16 v[164:167], v[28:31], a[236:239], v[164:167]
v_mfma_f32_16x16x32_bf16 v[104:107], v[16:19], a[160:163], 0
v_cndmask_b32_dpp v20, v72, v76, vcc quad_perm:[1,0,3,2] row_mask:0xf bank_mask:0xf
v_cndmask_b32_dpp v21, v73, v77, vcc quad_perm:[1,0,3,2] row_mask:0xf bank_mask:0xf
v_mfma_f32_16x16x32_bf16 v[104:107], v[68:71], a[180:183], v[104:107]
v_cndmask_b32_dpp v22, v74, v78, vcc quad_perm:[1,0,3,2] row_mask:0xf bank_mask:0xf
v_cndmask_b32_dpp v23, v75, v79, vcc quad_perm:[1,0,3,2] row_mask:0xf bank_mask:0xf
v_mfma_f32_16x16x32_bf16 v[120:123], v[16:19], a[164:167], 0
s_mov_b64 vcc, s[76:77]
v_cndmask_b32_dpp v72, v76, v72, vcc quad_perm:[1,0,3,2] row_mask:0xf bank_mask:0xf
v_cndmask_b32_dpp v73, v77, v73, vcc quad_perm:[1,0,3,2] row_mask:0xf bank_mask:0xf
v_mfma_f32_16x16x32_bf16 v[120:123], v[68:71], a[184:187], v[120:123]
v_cndmask_b32_dpp v74, v78, v74, vcc quad_perm:[1,0,3,2] row_mask:0xf bank_mask:0xf
v_cndmask_b32_dpp v75, v79, v75, vcc quad_perm:[1,0,3,2] row_mask:0xf bank_mask:0xf
v_mfma_f32_16x16x32_bf16 v[136:139], v[16:19], a[168:171], 0
v_mfma_f32_16x16x32_bf16 v[136:139], v[68:71], a[188:191], v[136:139]
buffer_load_dwordx4 v[32:35], v4, s[36:39], s74 offen nt
v_mfma_f32_16x16x32_bf16 v[152:155], v[16:19], a[172:175], 0
v_mfma_f32_16x16x32_bf16 v[152:155], v[68:71], a[192:195], v[152:155]
v_mfma_f32_16x16x32_bf16 v[168:171], v[16:19], a[176:179], 0
v_mfma_f32_16x16x32_bf16 v[168:171], v[68:71], a[196:199], v[168:171]
v_mfma_f32_16x16x32_bf16 v[108:111], v[72:75], a[160:163], 0
s_waitcnt vmcnt(17)
v_cndmask_b32_dpp v24, v84, v80, vcc quad_perm:[1,0,3,2] row_mask:0xf bank_mask:0xf
v_cndmask_b32_dpp v25, v85, v81, vcc quad_perm:[1,0,3,2] row_mask:0xf bank_mask:0xf
v_mfma_f32_16x16x32_bf16 v[108:111], v[20:23], a[180:183], v[108:111]
v_cndmask_b32_dpp v26, v86, v82, vcc quad_perm:[1,0,3,2] row_mask:0xf bank_mask:0xf
v_cndmask_b32_dpp v27, v87, v83, vcc quad_perm:[1,0,3,2] row_mask:0xf bank_mask:0xf
v_mfma_f32_16x16x32_bf16 v[124:127], v[72:75], a[164:167], 0
s_mov_b64 vcc, s[78:79]
v_cndmask_b32_dpp v84, v80, v84, vcc quad_perm:[1,0,3,2] row_mask:0xf bank_mask:0xf
v_cndmask_b32_dpp v85, v81, v85, vcc quad_perm:[1,0,3,2] row_mask:0xf bank_mask:0xf
v_mfma_f32_16x16x32_bf16 v[124:127], v[20:23], a[184:187], v[124:127]
v_cndmask_b32_dpp v86, v82, v86, vcc quad_perm:[1,0,3,2] row_mask:0xf bank_mask:0xf
v_cndmask_b32_dpp v87, v83, v87, vcc quad_perm:[1,0,3,2] row_mask:0xf bank_mask:0xf
v_mfma_f32_16x16x32_bf16 v[140:143], v[72:75], a[168:171], 0
buffer_load_dwordx4 v[36:39], v4, s[36:39], s74 offen offset:512 nt
v_mfma_f32_16x16x32_bf16 v[140:143], v[20:23], a[188:191], v[140:143]
v_mfma_f32_16x16x32_bf16 v[156:159], v[72:75], a[172:175], 0
v_mfma_f32_16x16x32_bf16 v[156:159], v[20:23], a[192:195], v[156:159]
v_mfma_f32_16x16x32_bf16 v[172:175], v[72:75], a[176:179], 0
v_mfma_f32_16x16x32_bf16 v[172:175], v[20:23], a[196:199], v[172:175]
v_mfma_f32_16x16x32_bf16 v[104:107], v[24:27], a[200:203], v[104:107]
v_cndmask_b32_dpp v28, v88, v92, vcc quad_perm:[1,0,3,2] row_mask:0xf bank_mask:0xf
v_cndmask_b32_dpp v29, v89, v93, vcc quad_perm:[1,0,3,2] row_mask:0xf bank_mask:0xf
v_mfma_f32_16x16x32_bf16 v[104:107], v[84:87], a[220:223], v[104:107]
v_cndmask_b32_dpp v30, v90, v94, vcc quad_perm:[1,0,3,2] row_mask:0xf bank_mask:0xf
v_cndmask_b32_dpp v31, v91, v95, vcc quad_perm:[1,0,3,2] row_mask:0xf bank_mask:0xf
v_mfma_f32_16x16x32_bf16 v[120:123], v[24:27], a[204:207], v[120:123]
ds_read_b128 a[160:163], v1 offset:61440
s_mov_b64 vcc, s[76:77]
v_cndmask_b32_dpp v88, v92, v88, vcc quad_perm:[1,0,3,2] row_mask:0xf bank_mask:0xf
v_mfma_f32_16x16x32_bf16 v[120:123], v[84:87], a[224:227], v[120:123]
ds_read_b128 a[164:167], v1 offset:62464
v_cndmask_b32_dpp v89, v93, v89, vcc quad_perm:[1,0,3,2] row_mask:0xf bank_mask:0xf
v_mfma_f32_16x16x32_bf16 v[136:139], v[24:27], a[208:211], v[136:139]
ds_read_b128 a[168:171], v1 offset:63488
v_cndmask_b32_dpp v90, v94, v90, vcc quad_perm:[1,0,3,2] row_mask:0xf bank_mask:0xf
v_mfma_f32_16x16x32_bf16 v[136:139], v[84:87], a[228:231], v[136:139]
ds_read_b128 a[172:175], v1 offset:64512
v_cndmask_b32_dpp v91, v95, v91, vcc quad_perm:[1,0,3,2] row_mask:0xf bank_mask:0xf
v_mfma_f32_16x16x32_bf16 v[152:155], v[24:27], a[212:215], v[152:155]
ds_read_b128 a[176:179], v2 offset:0
v_mfma_f32_16x16x32_bf16 v[152:155], v[84:87], a[232:235], v[152:155]
ds_read_b128 a[180:183], v2 offset:1024
v_mfma_f32_16x16x32_bf16 v[168:171], v[24:27], a[216:219], v[168:171]
)"
R"(ds_read_b128 a[184:187], v2 offset:2048
v_mfma_f32_16x16x32_bf16 v[168:171], v[84:87], a[236:239], v[168:171]
ds_read_b128 a[188:191], v2 offset:3072
v_mfma_f32_16x16x32_bf16 v[108:111], v[88:91], a[200:203], v[108:111]
s_waitcnt vmcnt(14)
v_cndmask_b32_dpp v16, v180, v176, vcc quad_perm:[1,0,3,2] row_mask:0xf bank_mask:0xf
v_cndmask_b32_dpp v17, v181, v177, vcc quad_perm:[1,0,3,2] row_mask:0xf bank_mask:0xf
v_mfma_f32_16x16x32_bf16 v[108:111], v[28:31], a[220:223], v[108:111]
v_cndmask_b32_dpp v18, v182, v178, vcc quad_perm:[1,0,3,2] row_mask:0xf bank_mask:0xf
v_cndmask_b32_dpp v19, v183, v179, vcc quad_perm:[1,0,3,2] row_mask:0xf bank_mask:0xf
v_mfma_f32_16x16x32_bf16 v[124:127], v[88:91], a[204:207], v[124:127]
ds_read_b128 a[192:195], v2 offset:4096
s_mov_b64 vcc, s[78:79]
v_cndmask_b32_dpp v180, v176, v180, vcc quad_perm:[1,0,3,2] row_mask:0xf bank_mask:0xf
v_mfma_f32_16x16x32_bf16 v[124:127], v[28:31], a[224:227], v[124:127]
ds_read_b128 a[196:199], v2 offset:5120
v_cndmask_b32_dpp v181, v177, v181, vcc quad_perm:[1,0,3,2] row_mask:0xf bank_mask:0xf
v_mfma_f32_16x16x32_bf16 v[140:143], v[88:91], a[208:211], v[140:143]
v_cndmask_b32_dpp v182, v178, v182, vcc quad_perm:[1,0,3,2] row_mask:0xf bank_mask:0xf
v_cndmask_b32_dpp v183, v179, v183, vcc quad_perm:[1,0,3,2] row_mask:0xf bank_mask:0xf
v_mfma_f32_16x16x32_bf16 v[140:143], v[28:31], a[228:231], v[140:143]
buffer_load_dwordx4 v[40:43], v4, s[36:39], s74 offen offset:2048 nt
v_mfma_f32_16x16x32_bf16 v[156:159], v[88:91], a[212:215], v[156:159]
buffer_load_dwordx4 v[44:47], v4, s[36:39], s74 offen offset:2560 nt
v_mfma_f32_16x16x32_bf16 v[156:159], v[28:31], a[232:235], v[156:159]
buffer_load_dwordx4 v[48:51], v4, s[36:39], s74 offen offset:128 nt
v_mfma_f32_16x16x32_bf16 v[172:175], v[88:91], a[216:219], v[172:175]
v_mfma_f32_16x16x32_bf16 v[172:175], v[28:31], a[236:239], v[172:175]
s_waitcnt lgkmcnt(0)
v_mfma_f32_16x16x32_bf16 v[96:99], v[16:19], a[160:163], v[96:99]
v_cndmask_b32_dpp v20, v184, v188, vcc quad_perm:[1,0,3,2] row_mask:0xf bank_mask:0xf
v_cndmask_b32_dpp v21, v185, v189, vcc quad_perm:[1,0,3,2] row_mask:0xf bank_mask:0xf
v_mfma_f32_16x16x32_bf16 v[96:99], v[180:183], a[180:183], v[96:99]
v_cndmask_b32_dpp v22, v186, v190, vcc quad_perm:[1,0,3,2] row_mask:0xf bank_mask:0xf
v_cndmask_b32_dpp v23, v187, v191, vcc quad_perm:[1,0,3,2] row_mask:0xf bank_mask:0xf
v_mfma_f32_16x16x32_bf16 v[112:115], v[16:19], a[164:167], v[112:115]
ds_read_b128 a[200:203], v2 offset:6144
s_mov_b64 vcc, s[76:77]
v_cndmask_b32_dpp v184, v188, v184, vcc quad_perm:[1,0,3,2] row_mask:0xf bank_mask:0xf
v_mfma_f32_16x16x32_bf16 v[112:115], v[180:183], a[184:187], v[112:115]
ds_read_b128 a[204:207], v2 offset:7168
v_cndmask_b32_dpp v185, v189, v185, vcc quad_perm:[1,0,3,2] row_mask:0xf bank_mask:0xf
v_mfma_f32_16x16x32_bf16 v[128:131], v[16:19], a[168:171], v[128:131]
ds_read_b128 a[208:211], v2 offset:8192
v_cndmask_b32_dpp v186, v190, v186, vcc quad_perm:[1,0,3,2] row_mask:0xf bank_mask:0xf
v_mfma_f32_16x16x32_bf16 v[128:131], v[180:183], a[188:191], v[128:131]
ds_read_b128 a[212:215], v2 offset:9216
v_cndmask_b32_dpp v187, v191, v187, vcc quad_perm:[1,0,3,2] row_mask:0xf bank_mask:0xf
v_mfma_f32_16x16x32_bf16 v[144:147], v[16:19], a[172:175], v[144:147]
ds_read_b128 a[216:219], v2 offset:10240
v_mfma_f32_16x16x32_bf16 v[144:147], v[180:183], a[192:195], v[144:147]
ds_read_b128 a[220:223], v2 offset:11264
v_mfma_f32_16x16x32_bf16 v[160:163], v[16:19], a[176:179], v[160:163]
ds_read_b128 a[224:227], v2 offset:12288
v_mfma_f32_16x16x32_bf16 v[160:163], v[180:183], a[196:199], v[160:163]
ds_read_b128 a[228:231], v2 offset:13312
v_mfma_f32_16x16x32_bf16 v[100:103], v[184:187], a[160:163], v[100:103]
s_waitcnt vmcnt(13)
v_cndmask_b32_dpp v24, v196, v192, vcc quad_perm:[1,0,3,2] row_mask:0xf bank_mask:0xf
v_cndmask_b32_dpp v25, v197, v193, vcc quad_perm:[1,0,3,2] row_mask:0xf bank_mask:0xf
v_mfma_f32_16x16x32_bf16 v[100:103], v[20:23], a[180:183], v[100:103]
v_cndmask_b32_dpp v26, v198, v194, vcc quad_perm:[1,0,3,2] row_mask:0xf bank_mask:0xf
v_cndmask_b32_dpp v27, v199, v195, vcc quad_perm:[1,0,3,2] row_mask:0xf bank_mask:0xf
v_mfma_f32_16x16x32_bf16 v[116:119], v[184:187], a[164:167], v[116:119]
ds_read_b128 a[232:235], v2 offset:14336
s_mov_b64 vcc, s[78:79]
v_cndmask_b32_dpp v196, v192, v196, vcc quad_perm:[1,0,3,2] row_mask:0xf bank_mask:0xf
v_mfma_f32_16x16x32_bf16 v[116:119], v[20:23], a[184:187], v[116:119]
ds_read_b128 a[236:239], v2 offset:15360
v_cndmask_b32_dpp v197, v193, v197, vcc quad_perm:[1,0,3,2] row_mask:0xf bank_mask:0xf
v_mfma_f32_16x16x32_bf16 v[132:135], v[184:187], a[168:171], v[132:135]
v_cndmask_b32_dpp v198, v194, v198, vcc quad_perm:[1,0,3,2] row_mask:0xf bank_mask:0xf
v_cndmask_b32_dpp v199, v195, v199, vcc quad_perm:[1,0,3,2] row_mask:0xf bank_mask:0xf
v_mfma_f32_16x16x32_bf16 v[132:135], v[20:23], a[188:191], v[132:135]
buffer_load_dwordx4 v[52:55], v4, s[36:39], s74 offen offset:640 nt
v_mfma_f32_16x16x32_bf16 v[148:151], v[184:187], a[172:175], v[148:151]
v_mfma_f32_16x16x32_bf16 v[148:151], v[20:23], a[192:195], v[148:151]
v_mfma_f32_16x16x32_bf16 v[164:167], v[184:187], a[176:179], v[164:167]
buffer_load_dwordx4 v[56:59], v4, s[36:39], s74 offen offset:2176 nt
v_mfma_f32_16x16x32_bf16 v[164:167], v[20:23], a[196:199], v[164:167]
s_waitcnt lgkmcnt(0)
v_mfma_f32_16x16x32_bf16 v[96:99], v[24:27], a[200:203], v[96:99]
v_cndmask_b32_dpp v28, v200, v204, vcc quad_perm:[1,0,3,2] row_mask:0xf bank_mask:0xf
v_cndmask_b32_dpp v29, v201, v205, vcc quad_perm:[1,0,3,2] row_mask:0xf bank_mask:0xf
v_mfma_f32_16x16x32_bf16 v[96:99], v[196:199], a[220:223], v[96:99]
v_cndmask_b32_dpp v30, v202, v206, vcc quad_perm:[1,0,3,2] row_mask:0xf bank_mask:0xf
v_cndmask_b32_dpp v31, v203, v207, vcc quad_perm:[1,0,3,2] row_mask:0xf bank_mask:0xf
v_mfma_f32_16x16x32_bf16 v[112:115], v[24:27], a[204:207], v[112:115]
s_mov_b64 vcc, s[76:77]
v_cndmask_b32_dpp v200, v204, v200, vcc quad_perm:[1,0,3,2] row_mask:0xf bank_mask:0xf
v_cndmask_b32_dpp v201, v205, v201, vcc quad_perm:[1,0,3,2] row_mask:0xf bank_mask:0xf
v_mfma_f32_16x16x32_bf16 v[112:115], v[196:199], a[224:227], v[112:115]
v_cndmask_b32_dpp v202, v206, v202, vcc quad_perm:[1,0,3,2] row_mask:0xf bank_mask:0xf
v_cndmask_b32_dpp v203, v207, v203, vcc quad_perm:[1,0,3,2] row_mask:0xf bank_mask:0xf
v_mfma_f32_16x16x32_bf16 v[128:131], v[24:27], a[208:211], v[128:131]
v_mfma_f32_16x16x32_bf16 v[128:131], v[196:199], a[228:231], v[128:131]
v_mfma_f32_16x16x32_bf16 v[144:147], v[24:27], a[212:215], v[144:147]
buffer_load_dwordx4 v[60:63], v4, s[36:39], s74 offen offset:2688 nt
v_mfma_f32_16x16x32_bf16 v[144:147], v[196:199], a[232:235], v[144:147]
v_mfma_f32_16x16x32_bf16 v[160:163], v[24:27], a[216:219], v[160:163]
v_mfma_f32_16x16x32_bf16 v[160:163], v[196:199], a[236:239], v[160:163]
v_mfma_f32_16x16x32_bf16 v[100:103], v[200:203], a[200:203], v[100:103]
s_waitcnt vmcnt(12)
v_cndmask_b32_dpp v16, v212, v208, vcc quad_perm:[1,0,3,2] row_mask:0xf bank_mask:0xf
v_cndmask_b32_dpp v17, v213, v209, vcc quad_perm:[1,0,3,2] row_mask:0xf bank_mask:0xf
v_mfma_f32_16x16x32_bf16 v[100:103], v[28:31], a[220:223], v[100:103]
v_cndmask_b32_dpp v18, v214, v210, vcc quad_perm:[1,0,3,2] row_mask:0xf bank_mask:0xf
v_cndmask_b32_dpp v19, v215, v211, vcc quad_perm:[1,0,3,2] row_mask:0xf bank_mask:0xf
v_mfma_f32_16x16x32_bf16 v[116:119], v[200:203], a[204:207], v[116:119]
s_mov_b64 vcc, s[78:79]
v_cndmask_b32_dpp v212, v208, v212, vcc quad_perm:[1,0,3,2] row_mask:0xf bank_mask:0xf
v_cndmask_b32_dpp v213, v209, v213, vcc quad_perm:[1,0,3,2] row_mask:0xf bank_mask:0xf
v_mfma_f32_16x16x32_bf16 v[116:119], v[28:31], a[224:227], v[116:119]
v_cndmask_b32_dpp v214, v210, v214, vcc quad_perm:[1,0,3,2] row_mask:0xf bank_mask:0xf
v_cndmask_b32_dpp v215, v211, v215, vcc quad_perm:[1,0,3,2] row_mask:0xf bank_mask:0xf
v_mfma_f32_16x16x32_bf16 v[132:135], v[200:203], a[208:211], v[132:135]
buffer_load_dwordx4 v[64:67], v4, s[36:39], s75 offen nt
v_mfma_f32_16x16x32_bf16 v[132:135], v[28:31], a[228:231], v[132:135]
v_mfma_f32_16x16x32_bf16 v[148:151], v[200:203], a[212:215], v[148:151]
v_mfma_f32_16x16x32_bf16 v[148:151], v[28:31], a[232:235], v[148:151]
v_mfma_f32_16x16x32_bf16 v[164:167], v[200:203], a[216:219], v[164:167]
v_mfma_f32_16x16x32_bf16 v[164:167], v[28:31], a[236:239], v[164:167]
v_mfma_f32_16x16x32_bf16 v[104:107], v[16:19], a[160:163], v[104:107]
v_cndmask_b32_dpp v20, v216, v220, vcc quad_perm:[1,0,3,2] row_mask:0xf bank_mask:0xf
v_cndmask_b32_dpp v21, v217, v221, vcc quad_perm:[1,0,3,2] row_mask:0xf bank_mask:0xf
v_mfma_f32_16x16x32_bf16 v[104:107], v[212:215], a[180:183], v[104:107]
v_cndmask_b32_dpp v22, v218, v222, vcc quad_perm:[1,0,3,2] row_mask:0xf bank_mask:0xf
v_cndmask_b32_dpp v23, v219, v223, vcc quad_perm:[1,0,3,2] row_mask:0xf bank_mask:0xf
v_mfma_f32_16x16x32_bf16 v[120:123], v[16:19], a[164:167], v[120:123]
s_mov_b64 vcc, s[76:77]
v_cndmask_b32_dpp v216, v220, v216, vcc quad_perm:[1,0,3,2] row_mask:0xf bank_mask:0xf
v_cndmask_b32_dpp v217, v221, v217, vcc quad_perm:[1,0,3,2] row_mask:0xf bank_mask:0xf
v_mfma_f32_16x16x32_bf16 v[120:123], v[212:215], a[184:187], v[120:123]
v_cndmask_b32_dpp v218, v222, v218, vcc quad_perm:[1,0,3,2] row_mask:0xf bank_mask:0xf
v_cndmask_b32_dpp v219, v223, v219, vcc quad_perm:[1,0,3,2] row_mask:0xf bank_mask:0xf
v_mfma_f32_16x16x32_bf16 v[136:139], v[16:19], a[168:171], v[136:139]
buffer_load_dwordx4 v[68:71], v4, s[36:39], s75 offen offset:512 nt
v_mfma_f32_16x16x32_bf16 v[136:139], v[212:215], a[188:191], v[136:139]
v_mfma_f32_16x16x32_bf16 v[152:155], v[16:19], a[172:175], v[152:155]
v_mfma_f32_16x16x32_bf16 v[152:155], v[212:215], a[192:195], v[152:155]
v_mfma_f32_16x16x32_bf16 v[168:171], v[16:19], a[176:179], v[168:171]
v_mfma_f32_16x16x32_bf16 v[168:171], v[212:215], a[196:199], v[168:171]
v_mfma_f32_16x16x32_bf16 v[108:111], v[216:219], a[160:163], v[108:111]
buffer_load_dwordx4 v[72:75], v4, s[36:39], s75 offen offset:2048 nt
s_waitcnt vmcnt(11)
v_cndmask_b32_dpp v24, v228, v224, vcc quad_perm:[1,0,3,2] row_mask:0xf bank_mask:0xf
v_cndmask_b32_dpp v25, v229, v225, vcc quad_perm:[1,0,3,2] row_mask:0xf bank_mask:0xf
v_mfma_f32_16x16x32_bf16 v[108:111], v[20:23], a[180:183], v[108:111]
v_cndmask_b32_dpp v26, v230, v226, vcc quad_perm:[1,0,3,2] row_mask:0xf bank_mask:0xf
v_cndmask_b32_dpp v27, v231, v227, vcc quad_perm:[1,0,3,2] row_mask:0xf bank_mask:0xf
v_mfma_f32_16x16x32_bf16 v[124:127], v[216:219], a[164:167], v[124:127]
s_mov_b64 vcc, s[78:79]
v_cndmask_b32_dpp v228, v224, v228, vcc quad_perm:[1,0,3,2] row_mask:0xf bank_mask:0xf
v_cndmask_b32_dpp v229, v225, v229, vcc quad_perm:[1,0,3,2] row_mask:0xf bank_mask:0xf
v_mfma_f32_16x16x32_bf16 v[124:127], v[20:23], a[184:187], v[124:127]
v_cndmask_b32_dpp v230, v226, v230, vcc quad_perm:[1,0,3,2] row_mask:0xf bank_mask:0xf
v_cndmask_b32_dpp v231, v227, v231, vcc quad_perm:[1,0,3,2] row_mask:0xf bank_mask:0xf
v_mfma_f32_16x16x32_bf16 v[140:143], v[216:219], a[168:171], v[140:143]
buffer_load_dwordx4 v[76:79], v4, s[36:39], s75 offen offset:2560 nt
v_mfma_f32_16x16x32_bf16 v[140:143], v[20:23], a[188:191], v[140:143]
v_mfma_f32_16x16x32_bf16 v[156:159], v[216:219], a[172:175], v[156:159]
v_mfma_f32_16x16x32_bf16 v[156:159], v[20:23], a[192:195], v[156:159]
v_mfma_f32_16x16x32_bf16 v[172:175], v[216:219], a[176:179], v[172:175]
buffer_load_dwordx4 v[80:83], v4, s[36:39], s75 offen offset:128 nt
v_mfma_f32_16x16x32_bf16 v[172:175], v[20:23], a[196:199], v[172:175]
v_mfma_f32_16x16x32_bf16 v[104:107], v[24:27], a[200:203], v[104:107]
v_cndmask_b32_dpp v28, v232, v236, vcc quad_perm:[1,0,3,2] row_mask:0xf bank_mask:0xf
v_cndmask_b32_dpp v29, v233, v237, vcc quad_perm:[1,0,3,2] row_mask:0xf bank_mask:0xf
v_mfma_f32_16x16x32_bf16 v[104:107], v[228:231], a[220:223], v[104:107]
v_cndmask_b32_dpp v30, v234, v238, vcc quad_perm:[1,0,3,2] row_mask:0xf bank_mask:0xf
v_cndmask_b32_dpp v31, v235, v239, vcc quad_perm:[1,0,3,2] row_mask:0xf bank_mask:0xf
v_mfma_f32_16x16x32_bf16 v[120:123], v[24:27], a[204:207], v[120:123]
ds_read_b128 a[160:163], v1 offset:40960
s_mov_b64 vcc, s[76:77]
v_cndmask_b32_dpp v232, v236, v232, vcc quad_perm:[1,0,3,2] row_mask:0xf bank_mask:0xf
v_mfma_f32_16x16x32_bf16 v[120:123], v[228:231], a[224:227], v[120:123]
ds_read_b128 a[164:167], v1 offset:41984
v_cndmask_b32_dpp v233, v237, v233, vcc quad_perm:[1,0,3,2] row_mask:0xf bank_mask:0xf
v_mfma_f32_16x16x32_bf16 v[136:139], v[24:27], a[208:211], v[136:139]
ds_read_b128 a[168:171], v1 offset:43008
v_cndmask_b32_dpp v234, v238, v234, vcc quad_perm:[1,0,3,2] row_mask:0xf bank_mask:0xf
v_mfma_f32_16x16x32_bf16 v[136:139], v[228:231], a[228:231], v[136:139]
ds_read_b128 a[172:175], v1 offset:44032
v_cndmask_b32_dpp v235, v239, v235, vcc quad_perm:[1,0,3,2] row_mask:0xf bank_mask:0xf
v_mfma_f32_16x16x32_bf16 v[152:155], v[24:27], a[212:215], v[152:155]
ds_read_b128 a[176:179], v1 offset:45056
v_mfma_f32_16x16x32_bf16 v[152:155], v[228:231], a[232:235], v[152:155]
ds_read_b128 a[180:183], v1 offset:46080
v_mfma_f32_16x16x32_bf16 v[168:171], v[24:27], a[216:219], v[168:171]
ds_read_b128 a[184:187], v1 offset:47104
v_mfma_f32_16x16x32_bf16 v[168:171], v[228:231], a[236:239], v[168:171]
ds_read_b128 a[188:191], v1 offset:48128
v_mfma_f32_16x16x32_bf16 v[108:111], v[232:235], a[200:203], v[108:111]
buffer_load_dwordx4 v[84:87], v4, s[36:39], s75 offen offset:640 nt
buffer_load_dwordx4 v[88:91], v4, s[36:39], s75 offen offset:2176 nt
s_waitcnt vmcnt(11)
v_cndmask_b32_dpp v16, v36, v32, vcc quad_perm:[1,0,3,2] row_mask:0xf bank_mask:0xf
v_cndmask_b32_dpp v17, v37, v33, vcc quad_perm:[1,0,3,2] row_mask:0xf bank_mask:0xf
v_mfma_f32_16x16x32_bf16 v[108:111], v[28:31], a[220:223], v[108:111]
v_cndmask_b32_dpp v18, v38, v34, vcc quad_perm:[1,0,3,2] row_mask:0xf bank_mask:0xf
)"
R"(v_cndmask_b32_dpp v19, v39, v35, vcc quad_perm:[1,0,3,2] row_mask:0xf bank_mask:0xf
v_mfma_f32_16x16x32_bf16 v[124:127], v[232:235], a[204:207], v[124:127]
ds_read_b128 a[192:195], v1 offset:49152
s_mov_b64 vcc, s[78:79]
v_cndmask_b32_dpp v36, v32, v36, vcc quad_perm:[1,0,3,2] row_mask:0xf bank_mask:0xf
v_mfma_f32_16x16x32_bf16 v[124:127], v[28:31], a[224:227], v[124:127]
ds_read_b128 a[196:199], v1 offset:50176
v_cndmask_b32_dpp v37, v33, v37, vcc quad_perm:[1,0,3,2] row_mask:0xf bank_mask:0xf
v_mfma_f32_16x16x32_bf16 v[140:143], v[232:235], a[208:211], v[140:143]
v_cndmask_b32_dpp v38, v34, v38, vcc quad_perm:[1,0,3,2] row_mask:0xf bank_mask:0xf
v_cndmask_b32_dpp v39, v35, v39, vcc quad_perm:[1,0,3,2] row_mask:0xf bank_mask:0xf
v_mfma_f32_16x16x32_bf16 v[140:143], v[28:31], a[228:231], v[140:143]
buffer_load_dwordx4 v[92:95], v4, s[36:39], s75 offen offset:2688 nt
v_mfma_f32_16x16x32_bf16 v[156:159], v[232:235], a[212:215], v[156:159]
v_mfma_f32_16x16x32_bf16 v[156:159], v[28:31], a[232:235], v[156:159]
v_mfma_f32_16x16x32_bf16 v[172:175], v[232:235], a[216:219], v[172:175]
v_mfma_f32_16x16x32_bf16 v[172:175], v[28:31], a[236:239], v[172:175]
s_waitcnt lgkmcnt(0)
v_mfma_f32_16x16x32_bf16 v[176:179], v[16:19], a[160:163], 0
v_cndmask_b32_dpp v20, v40, v44, vcc quad_perm:[1,0,3,2] row_mask:0xf bank_mask:0xf
v_cndmask_b32_dpp v21, v41, v45, vcc quad_perm:[1,0,3,2] row_mask:0xf bank_mask:0xf
v_mfma_f32_16x16x32_bf16 v[176:179], v[36:39], a[180:183], v[176:179]
v_cndmask_b32_dpp v22, v42, v46, vcc quad_perm:[1,0,3,2] row_mask:0xf bank_mask:0xf
v_cndmask_b32_dpp v23, v43, v47, vcc quad_perm:[1,0,3,2] row_mask:0xf bank_mask:0xf
v_mfma_f32_16x16x32_bf16 v[192:195], v[16:19], a[164:167], 0
ds_read_b128 a[200:203], v1 offset:51200
s_mov_b64 vcc, s[76:77]
v_cndmask_b32_dpp v40, v44, v40, vcc quad_perm:[1,0,3,2] row_mask:0xf bank_mask:0xf
v_mfma_f32_16x16x32_bf16 v[192:195], v[36:39], a[184:187], v[192:195]
ds_read_b128 a[204:207], v1 offset:52224
v_cndmask_b32_dpp v41, v45, v41, vcc quad_perm:[1,0,3,2] row_mask:0xf bank_mask:0xf
v_mfma_f32_16x16x32_bf16 v[208:211], v[16:19], a[168:171], 0
ds_read_b128 a[208:211], v1 offset:53248
v_cndmask_b32_dpp v42, v46, v42, vcc quad_perm:[1,0,3,2] row_mask:0xf bank_mask:0xf
v_mfma_f32_16x16x32_bf16 v[208:211], v[36:39], a[188:191], v[208:211]
ds_read_b128 a[212:215], v1 offset:54272
v_cndmask_b32_dpp v43, v47, v43, vcc quad_perm:[1,0,3,2] row_mask:0xf bank_mask:0xf
v_mfma_f32_16x16x32_bf16 v[224:227], v[16:19], a[172:175], 0
ds_read_b128 a[216:219], v1 offset:55296
v_mfma_f32_16x16x32_bf16 v[224:227], v[36:39], a[192:195], v[224:227]
ds_read_b128 a[220:223], v1 offset:56320
v_mfma_f32_16x16x32_bf16 v[240:243], v[16:19], a[176:179], 0
ds_read_b128 a[224:227], v1 offset:57344
v_mfma_f32_16x16x32_bf16 v[240:243], v[36:39], a[196:199], v[240:243]
ds_read_b128 a[228:231], v1 offset:58368
v_mfma_f32_16x16x32_bf16 v[180:183], v[40:43], a[160:163], 0
s_waitcnt vmcnt(8)
v_cndmask_b32_dpp v24, v52, v48, vcc quad_perm:[1,0,3,2] row_mask:0xf bank_mask:0xf
v_cndmask_b32_dpp v25, v53, v49, vcc quad_perm:[1,0,3,2] row_mask:0xf bank_mask:0xf
v_mfma_f32_16x16x32_bf16 v[180:183], v[20:23], a[180:183], v[180:183]
v_cndmask_b32_dpp v26, v54, v50, vcc quad_perm:[1,0,3,2] row_mask:0xf bank_mask:0xf
v_cndmask_b32_dpp v27, v55, v51, vcc quad_perm:[1,0,3,2] row_mask:0xf bank_mask:0xf
v_mfma_f32_16x16x32_bf16 v[196:199], v[40:43], a[164:167], 0
ds_read_b128 a[232:235], v1 offset:59392
s_mov_b64 vcc, s[78:79]
v_cndmask_b32_dpp v52, v48, v52, vcc quad_perm:[1,0,3,2] row_mask:0xf bank_mask:0xf
v_mfma_f32_16x16x32_bf16 v[196:199], v[20:23], a[184:187], v[196:199]
ds_read_b128 a[236:239], v1 offset:60416
v_cndmask_b32_dpp v53, v49, v53, vcc quad_perm:[1,0,3,2] row_mask:0xf bank_mask:0xf
v_mfma_f32_16x16x32_bf16 v[212:215], v[40:43], a[168:171], 0
v_cndmask_b32_dpp v54, v50, v54, vcc quad_perm:[1,0,3,2] row_mask:0xf bank_mask:0xf
v_cndmask_b32_dpp v55, v51, v55, vcc quad_perm:[1,0,3,2] row_mask:0xf bank_mask:0xf
v_mfma_f32_16x16x32_bf16 v[212:215], v[20:23], a[188:191], v[212:215]
v_max3_f32 v11, v96, v97, v98
v_max3_f32 v12, v112, v113, v114
v_mfma_f32_16x16x32_bf16 v[228:231], v[40:43], a[172:175], 0
v_mfma_f32_16x16x32_bf16 v[228:231], v[20:23], a[192:195], v[228:231]
v_max3_f32 v13, v128, v129, v130
v_max3_f32 v14, v144, v145, v146
v_mfma_f32_16x16x32_bf16 v[244:247], v[40:43], a[176:179], 0
v_max3_f32 v15, v160, v161, v162
v_max3_f32 v11, v11, v99, v100
v_mfma_f32_16x16x32_bf16 v[244:247], v[20:23], a[196:199], v[244:247]
s_waitcnt lgkmcnt(0)
v_max3_f32 v12, v12, v115, v116
v_max3_f32 v13, v13, v131, v132
v_mfma_f32_16x16x32_bf16 v[176:179], v[24:27], a[200:203], v[176:179]
v_cndmask_b32_dpp v28, v56, v60, vcc quad_perm:[1,0,3,2] row_mask:0xf bank_mask:0xf
v_cndmask_b32_dpp v29, v57, v61, vcc quad_perm:[1,0,3,2] row_mask:0xf bank_mask:0xf
v_mfma_f32_16x16x32_bf16 v[176:179], v[52:55], a[220:223], v[176:179]
v_cndmask_b32_dpp v30, v58, v62, vcc quad_perm:[1,0,3,2] row_mask:0xf bank_mask:0xf
v_cndmask_b32_dpp v31, v59, v63, vcc quad_perm:[1,0,3,2] row_mask:0xf bank_mask:0xf
v_mfma_f32_16x16x32_bf16 v[192:195], v[24:27], a[204:207], v[192:195]
s_mov_b64 vcc, s[76:77]
v_cndmask_b32_dpp v56, v60, v56, vcc quad_perm:[1,0,3,2] row_mask:0xf bank_mask:0xf
v_cndmask_b32_dpp v57, v61, v57, vcc quad_perm:[1,0,3,2] row_mask:0xf bank_mask:0xf
v_mfma_f32_16x16x32_bf16 v[192:195], v[52:55], a[224:227], v[192:195]
v_cndmask_b32_dpp v58, v62, v58, vcc quad_perm:[1,0,3,2] row_mask:0xf bank_mask:0xf
v_cndmask_b32_dpp v59, v63, v59, vcc quad_perm:[1,0,3,2] row_mask:0xf bank_mask:0xf
v_mfma_f32_16x16x32_bf16 v[208:211], v[24:27], a[208:211], v[208:211]
buffer_load_dwordx4 v[32:35], v4, s[36:39], s74 offen offset:256 nt
v_mfma_f32_16x16x32_bf16 v[208:211], v[52:55], a[228:231], v[208:211]
v_max3_f32 v14, v14, v147, v148
v_max3_f32 v15, v15, v163, v164
v_mfma_f32_16x16x32_bf16 v[224:227], v[24:27], a[212:215], v[224:227]
v_max3_f32 v11, v11, v101, v102
v_max3_f32 v12, v12, v117, v118
v_mfma_f32_16x16x32_bf16 v[224:227], v[52:55], a[232:235], v[224:227]
v_max3_f32 v13, v13, v133, v134
v_max3_f32 v14, v14, v149, v150
v_mfma_f32_16x16x32_bf16 v[240:243], v[24:27], a[216:219], v[240:243]
buffer_load_dwordx4 v[36:39], v4, s[36:39], s74 offen offset:768 nt
v_mfma_f32_16x16x32_bf16 v[240:243], v[52:55], a[236:239], v[240:243]
v_max3_f32 v15, v15, v165, v166
v_max3_f32 v11, v11, v103, v104
v_mfma_f32_16x16x32_bf16 v[180:183], v[56:59], a[200:203], v[180:183]
buffer_load_dwordx4 v[40:43], v4, s[36:39], s74 offen offset:2304 nt
buffer_load_dwordx4 v[44:47], v4, s[36:39], s74 offen offset:2816 nt
s_waitcnt vmcnt(8)
v_cndmask_b32_dpp v16, v68, v64, vcc quad_perm:[1,0,3,2] row_mask:0xf bank_mask:0xf
v_cndmask_b32_dpp v17, v69, v65, vcc quad_perm:[1,0,3,2] row_mask:0xf bank_mask:0xf
v_mfma_f32_16x16x32_bf16 v[180:183], v[28:31], a[220:223], v[180:183]
v_cndmask_b32_dpp v18, v70, v66, vcc quad_perm:[1,0,3,2] row_mask:0xf bank_mask:0xf
v_cndmask_b32_dpp v19, v71, v67, vcc quad_perm:[1,0,3,2] row_mask:0xf bank_mask:0xf
v_mfma_f32_16x16x32_bf16 v[196:199], v[56:59], a[204:207], v[196:199]
s_mov_b64 vcc, s[78:79]
v_cndmask_b32_dpp v68, v64, v68, vcc quad_perm:[1,0,3,2] row_mask:0xf bank_mask:0xf
v_cndmask_b32_dpp v69, v65, v69, vcc quad_perm:[1,0,3,2] row_mask:0xf bank_mask:0xf
v_mfma_f32_16x16x32_bf16 v[196:199], v[28:31], a[224:227], v[196:199]
v_cndmask_b32_dpp v70, v66, v70, vcc quad_perm:[1,0,3,2] row_mask:0xf bank_mask:0xf
v_cndmask_b32_dpp v71, v67, v71, vcc quad_perm:[1,0,3,2] row_mask:0xf bank_mask:0xf
v_mfma_f32_16x16x32_bf16 v[212:215], v[56:59], a[208:211], v[212:215]
v_max3_f32 v12, v12, v119, v120
v_max3_f32 v13, v13, v135, v136
v_mfma_f32_16x16x32_bf16 v[212:215], v[28:31], a[228:231], v[212:215]
v_max3_f32 v14, v14, v151, v152
v_max3_f32 v15, v15, v167, v168
v_mfma_f32_16x16x32_bf16 v[228:231], v[56:59], a[212:215], v[228:231]
v_mfma_f32_16x16x32_bf16 v[228:231], v[28:31], a[232:235], v[228:231]
v_max3_f32 v11, v11, v105, v106
v_max3_f32 v12, v12, v121, v122
v_mfma_f32_16x16x32_bf16 v[244:247], v[56:59], a[216:219], v[244:247]
v_max3_f32 v13, v13, v137, v138
v_max3_f32 v14, v14, v153, v154
v_mfma_f32_16x16x32_bf16 v[244:247], v[28:31], a[236:239], v[244:247]
v_max3_f32 v15, v15, v169, v170
v_max3_f32 v11, v11, v107, v108
v_mfma_f32_16x16x32_bf16 v[184:187], v[16:19], a[160:163], 0
v_cndmask_b32_dpp v20, v72, v76, vcc quad_perm:[1,0,3,2] row_mask:0xf bank_mask:0xf
v_cndmask_b32_dpp v21, v73, v77, vcc quad_perm:[1,0,3,2] row_mask:0xf bank_mask:0xf
v_mfma_f32_16x16x32_bf16 v[184:187], v[68:71], a[180:183], v[184:187]
v_cndmask_b32_dpp v22, v74, v78, vcc quad_perm:[1,0,3,2] row_mask:0xf bank_mask:0xf
v_cndmask_b32_dpp v23, v75, v79, vcc quad_perm:[1,0,3,2] row_mask:0xf bank_mask:0xf
v_mfma_f32_16x16x32_bf16 v[200:203], v[16:19], a[164:167], 0
s_mov_b64 vcc, s[76:77]
v_cndmask_b32_dpp v72, v76, v72, vcc quad_perm:[1,0,3,2] row_mask:0xf bank_mask:0xf
v_cndmask_b32_dpp v73, v77, v73, vcc quad_perm:[1,0,3,2] row_mask:0xf bank_mask:0xf
v_mfma_f32_16x16x32_bf16 v[200:203], v[68:71], a[184:187], v[200:203]
v_cndmask_b32_dpp v74, v78, v74, vcc quad_perm:[1,0,3,2] row_mask:0xf bank_mask:0xf
v_cndmask_b32_dpp v75, v79, v75, vcc quad_perm:[1,0,3,2] row_mask:0xf bank_mask:0xf
v_mfma_f32_16x16x32_bf16 v[216:219], v[16:19], a[168:171], 0
buffer_load_dwordx4 v[48:51], v4, s[36:39], s74 offen offset:384 nt
v_mfma_f32_16x16x32_bf16 v[216:219], v[68:71], a[188:191], v[216:219]
v_max3_f32 v12, v12, v123, v124
v_max3_f32 v13, v13, v139, v140
v_mfma_f32_16x16x32_bf16 v[232:235], v[16:19], a[172:175], 0
v_max3_f32 v14, v14, v155, v156
v_max3_f32 v15, v15, v171, v172
v_mfma_f32_16x16x32_bf16 v[232:235], v[68:71], a[192:195], v[232:235]
v_max3_f32 v11, v11, v109, v110
v_max3_f32 v12, v12, v125, v126
v_mfma_f32_16x16x32_bf16 v[248:251], v[16:19], a[176:179], 0
buffer_load_dwordx4 v[52:55], v4, s[36:39], s74 offen offset:896 nt
v_mfma_f32_16x16x32_bf16 v[248:251], v[68:71], a[196:199], v[248:251]
v_max3_f32 v13, v13, v141, v142
v_max3_f32 v14, v14, v157, v158
v_mfma_f32_16x16x32_bf16 v[188:191], v[72:75], a[160:163], 0
buffer_load_dwordx4 v[56:59], v4, s[36:39], s74 offen offset:2432 nt
buffer_load_dwordx4 v[60:63], v4, s[36:39], s74 offen offset:2944 nt
s_waitcnt vmcnt(8)
v_cndmask_b32_dpp v24, v84, v80, vcc quad_perm:[1,0,3,2] row_mask:0xf bank_mask:0xf
v_cndmask_b32_dpp v25, v85, v81, vcc quad_perm:[1,0,3,2] row_mask:0xf bank_mask:0xf
v_mfma_f32_16x16x32_bf16 v[188:191], v[20:23], a[180:183], v[188:191]
v_cndmask_b32_dpp v26, v86, v82, vcc quad_perm:[1,0,3,2] row_mask:0xf bank_mask:0xf
v_cndmask_b32_dpp v27, v87, v83, vcc quad_perm:[1,0,3,2] row_mask:0xf bank_mask:0xf
v_mfma_f32_16x16x32_bf16 v[204:207], v[72:75], a[164:167], 0
s_mov_b64 vcc, s[78:79]
v_cndmask_b32_dpp v84, v80, v84, vcc quad_perm:[1,0,3,2] row_mask:0xf bank_mask:0xf
v_cndmask_b32_dpp v85, v81, v85, vcc quad_perm:[1,0,3,2] row_mask:0xf bank_mask:0xf
v_mfma_f32_16x16x32_bf16 v[204:207], v[20:23], a[184:187], v[204:207]
v_cndmask_b32_dpp v86, v82, v86, vcc quad_perm:[1,0,3,2] row_mask:0xf bank_mask:0xf
v_cndmask_b32_dpp v87, v83, v87, vcc quad_perm:[1,0,3,2] row_mask:0xf bank_mask:0xf
v_mfma_f32_16x16x32_bf16 v[220:223], v[72:75], a[168:171], 0
v_max3_f32 v15, v15, v173, v174
v_max_f32 v11, v11, v111
v_mfma_f32_16x16x32_bf16 v[220:223], v[20:23], a[188:191], v[220:223]
v_max_f32 v12, v12, v127
v_max_f32 v13, v13, v143
v_mfma_f32_16x16x32_bf16 v[236:239], v[72:75], a[172:175], 0
v_mfma_f32_16x16x32_bf16 v[236:239], v[20:23], a[192:195], v[236:239]
v_max_f32 v14, v14, v159
v_max_f32 v15, v15, v175
v_mfma_f32_16x16x32_bf16 v[252:255], v[72:75], a[176:179], 0
buffer_load_dwordx4 v[64:67], v4, s[36:39], s75 offen offset:256 nt
v_mfma_f32_16x16x32_bf16 v[252:255], v[20:23], a[196:199], v[252:255]
buffer_load_dwordx4 v[68:71], v4, s[36:39], s75 offen offset:768 nt
v_mfma_f32_16x16x32_bf16 v[184:187], v[24:27], a[200:203], v[184:187]
v_cndmask_b32_dpp v28, v88, v92, vcc quad_perm:[1,0,3,2] row_mask:0xf bank_mask:0xf
v_cndmask_b32_dpp v29, v89, v93, vcc quad_perm:[1,0,3,2] row_mask:0xf bank_mask:0xf
v_mfma_f32_16x16x32_bf16 v[184:187], v[84:87], a[220:223], v[184:187]
)"
R"(v_cndmask_b32_dpp v30, v90, v94, vcc quad_perm:[1,0,3,2] row_mask:0xf bank_mask:0xf
v_cndmask_b32_dpp v31, v91, v95, vcc quad_perm:[1,0,3,2] row_mask:0xf bank_mask:0xf
v_mfma_f32_16x16x32_bf16 v[200:203], v[24:27], a[204:207], v[200:203]
ds_read_b128 a[160:163], v1 offset:61440
s_mov_b64 vcc, s[76:77]
v_cndmask_b32_dpp v88, v92, v88, vcc quad_perm:[1,0,3,2] row_mask:0xf bank_mask:0xf
v_mfma_f32_16x16x32_bf16 v[200:203], v[84:87], a[224:227], v[200:203]
ds_read_b128 a[164:167], v1 offset:62464
v_cndmask_b32_dpp v89, v93, v89, vcc quad_perm:[1,0,3,2] row_mask:0xf bank_mask:0xf
v_mfma_f32_16x16x32_bf16 v[216:219], v[24:27], a[208:211], v[216:219]
ds_read_b128 a[168:171], v1 offset:63488
v_cndmask_b32_dpp v90, v94, v90, vcc quad_perm:[1,0,3,2] row_mask:0xf bank_mask:0xf
v_mfma_f32_16x16x32_bf16 v[216:219], v[84:87], a[228:231], v[216:219]
ds_read_b128 a[172:175], v1 offset:64512
v_cndmask_b32_dpp v91, v95, v91, vcc quad_perm:[1,0,3,2] row_mask:0xf bank_mask:0xf
v_mfma_f32_16x16x32_bf16 v[232:235], v[24:27], a[212:215], v[232:235]
ds_read_b128 a[176:179], v2 offset:0
v_mfma_f32_16x16x32_bf16 v[232:235], v[84:87], a[232:235], v[232:235]
ds_read_b128 a[180:183], v2 offset:1024
v_mfma_f32_16x16x32_bf16 v[248:251], v[24:27], a[216:219], v[248:251]
ds_read_b128 a[184:187], v2 offset:2048
v_mfma_f32_16x16x32_bf16 v[248:251], v[84:87], a[236:239], v[248:251]
ds_read_b128 a[188:191], v2 offset:3072
v_mfma_f32_16x16x32_bf16 v[188:191], v[88:91], a[200:203], v[188:191]
buffer_load_dwordx4 v[72:75], v4, s[36:39], s75 offen offset:2304 nt
buffer_load_dwordx4 v[76:79], v4, s[36:39], s75 offen offset:2816 nt
s_waitcnt vmcnt(8)
v_cndmask_b32_dpp v16, v36, v32, vcc quad_perm:[1,0,3,2] row_mask:0xf bank_mask:0xf
v_cndmask_b32_dpp v17, v37, v33, vcc quad_perm:[1,0,3,2] row_mask:0xf bank_mask:0xf
v_mfma_f32_16x16x32_bf16 v[188:191], v[28:31], a[220:223], v[188:191]
v_cndmask_b32_dpp v18, v38, v34, vcc quad_perm:[1,0,3,2] row_mask:0xf bank_mask:0xf
v_cndmask_b32_dpp v19, v39, v35, vcc quad_perm:[1,0,3,2] row_mask:0xf bank_mask:0xf
v_mfma_f32_16x16x32_bf16 v[204:207], v[88:91], a[204:207], v[204:207]
ds_read_b128 a[192:195], v2 offset:4096
s_mov_b64 vcc, s[78:79]
v_cndmask_b32_dpp v36, v32, v36, vcc quad_perm:[1,0,3,2] row_mask:0xf bank_mask:0xf
v_mfma_f32_16x16x32_bf16 v[204:207], v[28:31], a[224:227], v[204:207]
ds_read_b128 a[196:199], v2 offset:5120
v_cndmask_b32_dpp v37, v33, v37, vcc quad_perm:[1,0,3,2] row_mask:0xf bank_mask:0xf
v_mfma_f32_16x16x32_bf16 v[220:223], v[88:91], a[208:211], v[220:223]
v_cndmask_b32_dpp v38, v34, v38, vcc quad_perm:[1,0,3,2] row_mask:0xf bank_mask:0xf
v_cndmask_b32_dpp v39, v35, v39, vcc quad_perm:[1,0,3,2] row_mask:0xf bank_mask:0xf
v_mfma_f32_16x16x32_bf16 v[220:223], v[28:31], a[228:231], v[220:223]
v_mfma_f32_16x16x32_bf16 v[236:239], v[88:91], a[212:215], v[236:239]
v_mfma_f32_16x16x32_bf16 v[236:239], v[28:31], a[232:235], v[236:239]
v_mfma_f32_16x16x32_bf16 v[252:255], v[88:91], a[216:219], v[252:255]
buffer_load_dwordx4 v[80:83], v4, s[36:39], s75 offen offset:384 nt
v_mfma_f32_16x16x32_bf16 v[252:255], v[28:31], a[236:239], v[252:255]
buffer_load_dwordx4 v[84:87], v4, s[36:39], s75 offen offset:896 nt
s_waitcnt lgkmcnt(0)
v_mfma_f32_16x16x32_bf16 v[176:179], v[16:19], a[160:163], v[176:179]
v_cndmask_b32_dpp v20, v40, v44, vcc quad_perm:[1,0,3,2] row_mask:0xf bank_mask:0xf
v_cndmask_b32_dpp v21, v41, v45, vcc quad_perm:[1,0,3,2] row_mask:0xf bank_mask:0xf
v_mfma_f32_16x16x32_bf16 v[176:179], v[36:39], a[180:183], v[176:179]
v_cndmask_b32_dpp v22, v42, v46, vcc quad_perm:[1,0,3,2] row_mask:0xf bank_mask:0xf
v_cndmask_b32_dpp v23, v43, v47, vcc quad_perm:[1,0,3,2] row_mask:0xf bank_mask:0xf
v_mfma_f32_16x16x32_bf16 v[192:195], v[16:19], a[164:167], v[192:195]
ds_read_b128 a[200:203], v2 offset:6144
s_mov_b64 vcc, s[76:77]
v_cndmask_b32_dpp v40, v44, v40, vcc quad_perm:[1,0,3,2] row_mask:0xf bank_mask:0xf
v_mfma_f32_16x16x32_bf16 v[192:195], v[36:39], a[184:187], v[192:195]
ds_read_b128 a[204:207], v2 offset:7168
v_cndmask_b32_dpp v41, v45, v41, vcc quad_perm:[1,0,3,2] row_mask:0xf bank_mask:0xf
v_mfma_f32_16x16x32_bf16 v[208:211], v[16:19], a[168:171], v[208:211]
ds_read_b128 a[208:211], v2 offset:8192
v_cndmask_b32_dpp v42, v46, v42, vcc quad_perm:[1,0,3,2] row_mask:0xf bank_mask:0xf
v_mfma_f32_16x16x32_bf16 v[208:211], v[36:39], a[188:191], v[208:211]
ds_read_b128 a[212:215], v2 offset:9216
v_cndmask_b32_dpp v43, v47, v43, vcc quad_perm:[1,0,3,2] row_mask:0xf bank_mask:0xf
v_mfma_f32_16x16x32_bf16 v[224:227], v[16:19], a[172:175], v[224:227]
ds_read_b128 a[216:219], v2 offset:10240
v_mfma_f32_16x16x32_bf16 v[224:227], v[36:39], a[192:195], v[224:227]
ds_read_b128 a[220:223], v2 offset:11264
v_mfma_f32_16x16x32_bf16 v[240:243], v[16:19], a[176:179], v[240:243]
ds_read_b128 a[224:227], v2 offset:12288
v_mfma_f32_16x16x32_bf16 v[240:243], v[36:39], a[196:199], v[240:243]
ds_read_b128 a[228:231], v2 offset:13312
v_mfma_f32_16x16x32_bf16 v[180:183], v[40:43], a[160:163], v[180:183]
buffer_load_dwordx4 v[88:91], v4, s[36:39], s75 offen offset:2432 nt
buffer_load_dwordx4 v[92:95], v4, s[36:39], s75 offen offset:2944 nt
s_waitcnt vmcnt(8)
v_cndmask_b32_dpp v24, v52, v48, vcc quad_perm:[1,0,3,2] row_mask:0xf bank_mask:0xf
v_cndmask_b32_dpp v25, v53, v49, vcc quad_perm:[1,0,3,2] row_mask:0xf bank_mask:0xf
v_mfma_f32_16x16x32_bf16 v[180:183], v[20:23], a[180:183], v[180:183]
v_cndmask_b32_dpp v26, v54, v50, vcc quad_perm:[1,0,3,2] row_mask:0xf bank_mask:0xf
v_cndmask_b32_dpp v27, v55, v51, vcc quad_perm:[1,0,3,2] row_mask:0xf bank_mask:0xf
v_mfma_f32_16x16x32_bf16 v[196:199], v[40:43], a[164:167], v[196:199]
ds_read_b128 a[232:235], v2 offset:14336
s_mov_b64 vcc, s[78:79]
v_cndmask_b32_dpp v52, v48, v52, vcc quad_perm:[1,0,3,2] row_mask:0xf bank_mask:0xf
v_mfma_f32_16x16x32_bf16 v[196:199], v[20:23], a[184:187], v[196:199]
ds_read_b128 a[236:239], v2 offset:15360
v_cndmask_b32_dpp v53, v49, v53, vcc quad_perm:[1,0,3,2] row_mask:0xf bank_mask:0xf
v_mfma_f32_16x16x32_bf16 v[212:215], v[40:43], a[168:171], v[212:215]
v_cndmask_b32_dpp v54, v50, v54, vcc quad_perm:[1,0,3,2] row_mask:0xf bank_mask:0xf
v_cndmask_b32_dpp v55, v51, v55, vcc quad_perm:[1,0,3,2] row_mask:0xf bank_mask:0xf
v_mfma_f32_16x16x32_bf16 v[212:215], v[20:23], a[188:191], v[212:215]
v_mfma_f32_16x16x32_bf16 v[228:231], v[40:43], a[172:175], v[228:231]
v_mfma_f32_16x16x32_bf16 v[228:231], v[20:23], a[192:195], v[228:231]
v_mfma_f32_16x16x32_bf16 v[244:247], v[40:43], a[176:179], v[244:247]
s_add_u32 s80, s89, 0
v_mfma_f32_16x16x32_bf16 v[244:247], v[20:23], a[196:199], v[244:247]
buffer_load_dwordx2 v[32:33], v5, s[40:43], s80 offen nt
s_waitcnt lgkmcnt(0)
v_mfma_f32_16x16x32_bf16 v[176:179], v[24:27], a[200:203], v[176:179]
v_cndmask_b32_dpp v28, v56, v60, vcc quad_perm:[1,0,3,2] row_mask:0xf bank_mask:0xf
v_cndmask_b32_dpp v29, v57, v61, vcc quad_perm:[1,0,3,2] row_mask:0xf bank_mask:0xf
v_mfma_f32_16x16x32_bf16 v[176:179], v[52:55], a[220:223], v[176:179]
v_cndmask_b32_dpp v30, v58, v62, vcc quad_perm:[1,0,3,2] row_mask:0xf bank_mask:0xf
v_cndmask_b32_dpp v31, v59, v63, vcc quad_perm:[1,0,3,2] row_mask:0xf bank_mask:0xf
v_mfma_f32_16x16x32_bf16 v[192:195], v[24:27], a[204:207], v[192:195]
s_mov_b64 vcc, s[76:77]
v_cndmask_b32_dpp v56, v60, v56, vcc quad_perm:[1,0,3,2] row_mask:0xf bank_mask:0xf
v_cndmask_b32_dpp v57, v61, v57, vcc quad_perm:[1,0,3,2] row_mask:0xf bank_mask:0xf
v_mfma_f32_16x16x32_bf16 v[192:195], v[52:55], a[224:227], v[192:195]
v_cndmask_b32_dpp v58, v62, v58, vcc quad_perm:[1,0,3,2] row_mask:0xf bank_mask:0xf
v_cndmask_b32_dpp v59, v63, v59, vcc quad_perm:[1,0,3,2] row_mask:0xf bank_mask:0xf
v_mfma_f32_16x16x32_bf16 v[208:211], v[24:27], a[208:211], v[208:211]
buffer_load_dwordx2 v[34:35], v5, s[40:43], s80 offen offset:512 nt
v_mfma_f32_16x16x32_bf16 v[208:211], v[52:55], a[228:231], v[208:211]
buffer_load_dwordx2 v[36:37], v5, s[40:43], s80 offen offset:1024 nt
v_mfma_f32_16x16x32_bf16 v[224:227], v[24:27], a[212:215], v[224:227]
buffer_load_dwordx2 v[38:39], v5, s[40:43], s80 offen offset:1536 nt
v_mfma_f32_16x16x32_bf16 v[224:227], v[52:55], a[232:235], v[224:227]
buffer_load_dwordx2 v[40:41], v5, s[40:43], s80 offen offset:2048 nt
v_mfma_f32_16x16x32_bf16 v[240:243], v[24:27], a[216:219], v[240:243]
buffer_load_dwordx2 v[42:43], v5, s[40:43], s80 offen offset:2560 nt
v_mfma_f32_16x16x32_bf16 v[240:243], v[52:55], a[236:239], v[240:243]
buffer_load_dwordx2 v[44:45], v5, s[40:43], s80 offen offset:3072 nt
v_mfma_f32_16x16x32_bf16 v[180:183], v[56:59], a[200:203], v[180:183]
buffer_load_dwordx2 v[46:47], v5, s[40:43], s80 offen offset:3584 nt
s_waitcnt vmcnt(12)
v_cndmask_b32_dpp v16, v68, v64, vcc quad_perm:[1,0,3,2] row_mask:0xf bank_mask:0xf
v_cndmask_b32_dpp v17, v69, v65, vcc quad_perm:[1,0,3,2] row_mask:0xf bank_mask:0xf
v_mfma_f32_16x16x32_bf16 v[180:183], v[28:31], a[220:223], v[180:183]
v_cndmask_b32_dpp v18, v70, v66, vcc quad_perm:[1,0,3,2] row_mask:0xf bank_mask:0xf
v_cndmask_b32_dpp v19, v71, v67, vcc quad_perm:[1,0,3,2] row_mask:0xf bank_mask:0xf
v_mfma_f32_16x16x32_bf16 v[196:199], v[56:59], a[204:207], v[196:199]
s_mov_b64 vcc, s[78:79]
v_cndmask_b32_dpp v68, v64, v68, vcc quad_perm:[1,0,3,2] row_mask:0xf bank_mask:0xf
v_cndmask_b32_dpp v69, v65, v69, vcc quad_perm:[1,0,3,2] row_mask:0xf bank_mask:0xf
v_mfma_f32_16x16x32_bf16 v[196:199], v[28:31], a[224:227], v[196:199]
v_cndmask_b32_dpp v70, v66, v70, vcc quad_perm:[1,0,3,2] row_mask:0xf bank_mask:0xf
v_cndmask_b32_dpp v71, v67, v71, vcc quad_perm:[1,0,3,2] row_mask:0xf bank_mask:0xf
v_mfma_f32_16x16x32_bf16 v[212:215], v[56:59], a[208:211], v[212:215]
v_mfma_f32_16x16x32_bf16 v[212:215], v[28:31], a[228:231], v[212:215]
v_mfma_f32_16x16x32_bf16 v[228:231], v[56:59], a[212:215], v[228:231]
v_mfma_f32_16x16x32_bf16 v[228:231], v[28:31], a[232:235], v[228:231]
v_mfma_f32_16x16x32_bf16 v[244:247], v[56:59], a[216:219], v[244:247]
s_add_u32 s80, s80, 16384
v_mfma_f32_16x16x32_bf16 v[244:247], v[28:31], a[236:239], v[244:247]
buffer_load_dwordx2 v[48:49], v5, s[40:43], s80 offen nt
v_mfma_f32_16x16x32_bf16 v[184:187], v[16:19], a[160:163], v[184:187]
v_cndmask_b32_dpp v20, v72, v76, vcc quad_perm:[1,0,3,2] row_mask:0xf bank_mask:0xf
v_cndmask_b32_dpp v21, v73, v77, vcc quad_perm:[1,0,3,2] row_mask:0xf bank_mask:0xf
v_mfma_f32_16x16x32_bf16 v[184:187], v[68:71], a[180:183], v[184:187]
v_cndmask_b32_dpp v22, v74, v78, vcc quad_perm:[1,0,3,2] row_mask:0xf bank_mask:0xf
v_cndmask_b32_dpp v23, v75, v79, vcc quad_perm:[1,0,3,2] row_mask:0xf bank_mask:0xf
v_mfma_f32_16x16x32_bf16 v[200:203], v[16:19], a[164:167], v[200:203]
s_mov_b64 vcc, s[76:77]
v_cndmask_b32_dpp v72, v76, v72, vcc quad_perm:[1,0,3,2] row_mask:0xf bank_mask:0xf
v_cndmask_b32_dpp v73, v77, v73, vcc quad_perm:[1,0,3,2] row_mask:0xf bank_mask:0xf
v_mfma_f32_16x16x32_bf16 v[200:203], v[68:71], a[184:187], v[200:203]
v_cndmask_b32_dpp v74, v78, v74, vcc quad_perm:[1,0,3,2] row_mask:0xf bank_mask:0xf
v_cndmask_b32_dpp v75, v79, v75, vcc quad_perm:[1,0,3,2] row_mask:0xf bank_mask:0xf
v_mfma_f32_16x16x32_bf16 v[216:219], v[16:19], a[168:171], v[216:219]
buffer_load_dwordx2 v[50:51], v5, s[40:43], s80 offen offset:512 nt
v_mfma_f32_16x16x32_bf16 v[216:219], v[68:71], a[188:191], v[216:219]
buffer_load_dwordx2 v[52:53], v5, s[40:43], s80 offen offset:1024 nt
v_mfma_f32_16x16x32_bf16 v[232:235], v[16:19], a[172:175], v[232:235]
buffer_load_dwordx2 v[54:55], v5, s[40:43], s80 offen offset:1536 nt
v_mfma_f32_16x16x32_bf16 v[232:235], v[68:71], a[192:195], v[232:235]
buffer_load_dwordx2 v[56:57], v5, s[40:43], s80 offen offset:2048 nt
v_mfma_f32_16x16x32_bf16 v[248:251], v[16:19], a[176:179], v[248:251]
v_mfma_f32_16x16x32_bf16 v[248:251], v[68:71], a[196:199], v[248:251]
v_mfma_f32_16x16x32_bf16 v[188:191], v[72:75], a[160:163], v[188:191]
buffer_load_dwordx2 v[58:59], v5, s[40:43], s80 offen offset:2560 nt
buffer_load_dwordx2 v[60:61], v5, s[40:43], s80 offen offset:3072 nt
buffer_load_dwordx2 v[62:63], v5, s[40:43], s80 offen offset:3584 nt
s_waitcnt vmcnt(16)
v_cndmask_b32_dpp v24, v84, v80, vcc quad_perm:[1,0,3,2] row_mask:0xf bank_mask:0xf
v_cndmask_b32_dpp v25, v85, v81, vcc quad_perm:[1,0,3,2] row_mask:0xf bank_mask:0xf
v_mfma_f32_16x16x32_bf16 v[188:191], v[20:23], a[180:183], v[188:191]
v_cndmask_b32_dpp v26, v86, v82, vcc quad_perm:[1,0,3,2] row_mask:0xf bank_mask:0xf
v_cndmask_b32_dpp v27, v87, v83, vcc quad_perm:[1,0,3,2] row_mask:0xf bank_mask:0xf
v_mfma_f32_16x16x32_bf16 v[204:207], v[72:75], a[164:167], v[204:207]
s_mov_b64 vcc, s[78:79]
v_cndmask_b32_dpp v84, v80, v84, vcc quad_perm:[1,0,3,2] row_mask:0xf bank_mask:0xf
v_cndmask_b32_dpp v85, v81, v85, vcc quad_perm:[1,0,3,2] row_mask:0xf bank_mask:0xf
v_mfma_f32_16x16x32_bf16 v[204:207], v[20:23], a[184:187], v[204:207]
v_cndmask_b32_dpp v86, v82, v86, vcc quad_perm:[1,0,3,2] row_mask:0xf bank_mask:0xf
v_cndmask_b32_dpp v87, v83, v87, vcc quad_perm:[1,0,3,2] row_mask:0xf bank_mask:0xf
v_mfma_f32_16x16x32_bf16 v[220:223], v[72:75], a[168:171], v[220:223]
v_mfma_f32_16x16x32_bf16 v[220:223], v[20:23], a[188:191], v[220:223]
v_mfma_f32_16x16x32_bf16 v[236:239], v[72:75], a[172:175], v[236:239]
v_mfma_f32_16x16x32_bf16 v[236:239], v[20:23], a[192:195], v[236:239]
v_mfma_f32_16x16x32_bf16 v[252:255], v[72:75], a[176:179], v[252:255]
s_add_u32 s80, s89, 32768
v_mfma_f32_16x16x32_bf16 v[252:255], v[20:23], a[196:199], v[252:255]
buffer_load_dwordx2 v[64:65], v5, s[40:43], s80 offen nt
)"
R"(v_mfma_f32_16x16x32_bf16 v[184:187], v[24:27], a[200:203], v[184:187]
v_cndmask_b32_dpp v28, v88, v92, vcc quad_perm:[1,0,3,2] row_mask:0xf bank_mask:0xf
v_cndmask_b32_dpp v29, v89, v93, vcc quad_perm:[1,0,3,2] row_mask:0xf bank_mask:0xf
v_mfma_f32_16x16x32_bf16 v[184:187], v[84:87], a[220:223], v[184:187]
v_cndmask_b32_dpp v30, v90, v94, vcc quad_perm:[1,0,3,2] row_mask:0xf bank_mask:0xf
v_cndmask_b32_dpp v31, v91, v95, vcc quad_perm:[1,0,3,2] row_mask:0xf bank_mask:0xf
v_mfma_f32_16x16x32_bf16 v[200:203], v[24:27], a[204:207], v[200:203]
s_mov_b64 vcc, s[76:77]
v_cndmask_b32_dpp v88, v92, v88, vcc quad_perm:[1,0,3,2] row_mask:0xf bank_mask:0xf
v_cndmask_b32_dpp v89, v93, v89, vcc quad_perm:[1,0,3,2] row_mask:0xf bank_mask:0xf
v_mfma_f32_16x16x32_bf16 v[200:203], v[84:87], a[224:227], v[200:203]
v_cndmask_b32_dpp v90, v94, v90, vcc quad_perm:[1,0,3,2] row_mask:0xf bank_mask:0xf
v_cndmask_b32_dpp v91, v95, v91, vcc quad_perm:[1,0,3,2] row_mask:0xf bank_mask:0xf
v_mfma_f32_16x16x32_bf16 v[216:219], v[24:27], a[208:211], v[216:219]
buffer_load_dwordx2 v[66:67], v5, s[40:43], s80 offen offset:512 nt
v_mfma_f32_16x16x32_bf16 v[216:219], v[84:87], a[228:231], v[216:219]
buffer_load_dwordx2 v[68:69], v5, s[40:43], s80 offen offset:1024 nt
v_mfma_f32_16x16x32_bf16 v[232:235], v[24:27], a[212:215], v[232:235]
buffer_load_dwordx2 v[70:71], v5, s[40:43], s80 offen offset:1536 nt
v_mfma_f32_16x16x32_bf16 v[232:235], v[84:87], a[232:235], v[232:235]
buffer_load_dwordx2 v[72:73], v5, s[40:43], s80 offen offset:2048 nt
v_mfma_f32_16x16x32_bf16 v[248:251], v[24:27], a[216:219], v[248:251]
buffer_load_dwordx2 v[74:75], v5, s[40:43], s80 offen offset:2560 nt
v_mfma_f32_16x16x32_bf16 v[248:251], v[84:87], a[236:239], v[248:251]
buffer_load_dwordx2 v[76:77], v5, s[40:43], s80 offen offset:3072 nt
v_mfma_f32_16x16x32_bf16 v[188:191], v[88:91], a[200:203], v[188:191]
v_mfma_f32_16x16x32_bf16 v[188:191], v[28:31], a[220:223], v[188:191]
v_mfma_f32_16x16x32_bf16 v[204:207], v[88:91], a[204:207], v[204:207]
v_mfma_f32_16x16x32_bf16 v[204:207], v[28:31], a[224:227], v[204:207]
v_mfma_f32_16x16x32_bf16 v[220:223], v[88:91], a[208:211], v[220:223]
buffer_load_dwordx2 v[78:79], v5, s[40:43], s80 offen offset:3584 nt
v_mfma_f32_16x16x32_bf16 v[220:223], v[28:31], a[228:231], v[220:223]
v_mfma_f32_16x16x32_bf16 v[236:239], v[88:91], a[212:215], v[236:239]
v_mfma_f32_16x16x32_bf16 v[236:239], v[28:31], a[232:235], v[236:239]
v_mfma_f32_16x16x32_bf16 v[252:255], v[88:91], a[216:219], v[252:255]
s_add_u32 s80, s80, 16384
v_mfma_f32_16x16x32_bf16 v[252:255], v[28:31], a[236:239], v[252:255]
v_max3_f32 v11, v11, v176, v177
v_max3_f32 v12, v12, v192, v193
v_max3_f32 v13, v13, v208, v209
v_max3_f32 v14, v14, v224, v225
v_max3_f32 v15, v15, v240, v241
v_max3_f32 v11, v11, v178, v179
v_max3_f32 v12, v12, v194, v195
v_max3_f32 v13, v13, v210, v211
v_max3_f32 v14, v14, v226, v227
v_max3_f32 v15, v15, v242, v243
v_max3_f32 v11, v11, v180, v181
v_max3_f32 v12, v12, v196, v197
v_max3_f32 v13, v13, v212, v213
v_max3_f32 v14, v14, v228, v229
v_max3_f32 v15, v15, v244, v245
v_max3_f32 v11, v11, v182, v183
v_max3_f32 v12, v12, v198, v199
v_max3_f32 v13, v13, v214, v215
v_max3_f32 v14, v14, v230, v231
v_max3_f32 v15, v15, v246, v247
v_max3_f32 v11, v11, v184, v185
v_max3_f32 v12, v12, v200, v201
v_max3_f32 v13, v13, v216, v217
v_max3_f32 v14, v14, v232, v233
v_max3_f32 v15, v15, v248, v249
v_max3_f32 v11, v11, v186, v187
v_max3_f32 v12, v12, v202, v203
v_max3_f32 v13, v13, v218, v219
v_max3_f32 v14, v14, v234, v235
v_max3_f32 v15, v15, v250, v251
v_max3_f32 v11, v11, v188, v189
v_max3_f32 v12, v12, v204, v205
v_max3_f32 v13, v13, v220, v221
v_max3_f32 v14, v14, v236, v237
buffer_load_dwordx2 v[80:81], v5, s[40:43], s80 offen nt
buffer_load_dwordx2 v[82:83], v5, s[40:43], s80 offen offset:512 nt
buffer_load_dwordx2 v[84:85], v5, s[40:43], s80 offen offset:1024 nt
v_max3_f32 v15, v15, v252, v253
v_max3_f32 v11, v11, v190, v191
v_max3_f32 v12, v12, v206, v207
v_max3_f32 v13, v13, v222, v223
v_max3_f32 v14, v14, v238, v239
v_max3_f32 v15, v15, v254, v255
v_mov_b32 v21, 0x3f803f80
v_accvgpr_write_b32 a160, v21
v_accvgpr_write_b32 a161, v21
v_accvgpr_write_b32 a162, v21
v_accvgpr_write_b32 a163, v21
v_mov_b32 v16, v11
v_mov_b32 v17, v12
v_mov_b32 v18, v13
v_mov_b32 v19, v14
v_mov_b32 v20, v15
v_permlane32_swap_b32 v16, v11
v_permlane32_swap_b32 v17, v12
v_permlane32_swap_b32 v18, v13
v_permlane32_swap_b32 v19, v14
v_permlane32_swap_b32 v20, v15
v_max_f32 v11, v16, v11
v_max_f32 v12, v17, v12
v_max_f32 v13, v18, v13
v_max_f32 v14, v19, v14
v_max_f32 v15, v20, v15
v_mov_b32 v16, v11
v_mov_b32 v17, v12
v_mov_b32 v18, v13
v_mov_b32 v19, v14
v_mov_b32 v20, v15
v_permlane16_swap_b32 v16, v11
v_permlane16_swap_b32 v17, v12
v_permlane16_swap_b32 v18, v13
v_permlane16_swap_b32 v19, v14
v_permlane16_swap_b32 v20, v15
v_max_f32 v11, v16, v11
v_max_f32 v12, v17, v12
v_max_f32 v13, v18, v13
v_max_f32 v14, v19, v14
v_max_f32 v15, v20, v15
v_accvgpr_read_b32 v9, a248
v_accvgpr_read_b32 v0, a247
s_barrier
buffer_load_dwordx2 v[86:87], v5, s[40:43], s80 offen offset:1536 nt
buffer_load_dwordx2 v[88:89], v5, s[40:43], s80 offen offset:2048 nt
buffer_load_dwordx2 v[90:91], v5, s[40:43], s80 offen offset:2560 nt
ds_write_b32 v9, v11 offset:40960
ds_write_b32 v9, v12 offset:41216
ds_write_b32 v9, v13 offset:41472
ds_write_b32 v9, v14 offset:41728
ds_write_b32 v9, v15 offset:41984
v_and_b32 v10, 0xf0, v1
s_waitcnt lgkmcnt(0)
s_barrier
buffer_load_dwordx2 v[92:93], v5, s[40:43], s80 offen offset:3072 nt
buffer_load_dwordx2 v[94:95], v5, s[40:43], s80 offen offset:3584 nt
ds_read_b128 v[16:19], v10 offset:40960
ds_read_b128 v[20:23], v10 offset:41216
ds_read_b128 v[24:27], v10 offset:41472
ds_read_b128 v[28:31], v10 offset:41728
s_waitcnt lgkmcnt(3)
v_max3_f32 v16, v16, v17, v18
v_max_f32 v16, v16, v19
v_mul_f32 v11, s86, v16
ds_read_b128 v[16:19], v10 offset:41984
s_waitcnt lgkmcnt(3)
v_max3_f32 v20, v20, v21, v22
v_max_f32 v20, v20, v23
v_mul_f32 v12, s86, v20
s_waitcnt lgkmcnt(2)
v_max3_f32 v24, v24, v25, v26
v_max_f32 v24, v24, v27
v_mul_f32 v13, s86, v24
s_waitcnt lgkmcnt(1)
v_max3_f32 v28, v28, v29, v30
v_max_f32 v28, v28, v31
v_mul_f32 v14, s86, v28
s_waitcnt lgkmcnt(0)
v_max3_f32 v16, v16, v17, v18
v_max_f32 v16, v16, v19
v_mul_f32 v15, s86, v16
v_mov_b32 v16, v11
v_pk_fma_f32 v[96:97], v[96:97], s[86:87], v[16:17] op_sel_hi:[1,1,0] neg_lo:[0,0,1] neg_hi:[0,0,1]
v_pk_fma_f32 v[98:99], v[98:99], s[86:87], v[16:17] op_sel_hi:[1,1,0] neg_lo:[0,0,1] neg_hi:[0,0,1]
v_pk_fma_f32 v[100:101], v[100:101], s[86:87], v[16:17] op_sel_hi:[1,1,0] neg_lo:[0,0,1] neg_hi:[0,0,1]
v_pk_fma_f32 v[102:103], v[102:103], s[86:87], v[16:17] op_sel_hi:[1,1,0] neg_lo:[0,0,1] neg_hi:[0,0,1]
v_pk_fma_f32 v[104:105], v[104:105], s[86:87], v[16:17] op_sel_hi:[1,1,0] neg_lo:[0,0,1] neg_hi:[0,0,1]
v_pk_fma_f32 v[106:107], v[106:107], s[86:87], v[16:17] op_sel_hi:[1,1,0] neg_lo:[0,0,1] neg_hi:[0,0,1]
v_pk_fma_f32 v[108:109], v[108:109], s[86:87], v[16:17] op_sel_hi:[1,1,0] neg_lo:[0,0,1] neg_hi:[0,0,1]
v_pk_fma_f32 v[110:111], v[110:111], s[86:87], v[16:17] op_sel_hi:[1,1,0] neg_lo:[0,0,1] neg_hi:[0,0,1]
v_exp_f32 v96, v96
v_exp_f32 v97, v97
v_exp_f32 v98, v98
v_exp_f32 v99, v99
v_exp_f32 v100, v100
v_exp_f32 v101, v101
v_exp_f32 v102, v102
v_exp_f32 v103, v103
v_exp_f32 v104, v104
v_exp_f32 v105, v105
v_exp_f32 v106, v106
v_exp_f32 v107, v107
v_exp_f32 v108, v108
v_exp_f32 v109, v109
v_exp_f32 v110, v110
v_exp_f32 v111, v111
v_cvt_pk_bf16_f32 v96, v96, v97
v_cvt_pk_bf16_f32 v97, v98, v99
v_cvt_pk_bf16_f32 v98, v100, v101
v_cvt_pk_bf16_f32 v99, v102, v103
v_cvt_pk_bf16_f32 v104, v104, v105
v_cvt_pk_bf16_f32 v105, v106, v107
v_cvt_pk_bf16_f32 v106, v108, v109
v_cvt_pk_bf16_f32 v107, v110, v111
v_mfma_f32_16x16x32_bf16 a[164:167], a[160:163], v[96:99], 0
ds_write_b128 v0, v[96:99] offset:0
v_mfma_f32_16x16x32_bf16 a[164:167], a[160:163], v[104:107], a[164:167]
ds_write_b128 v0, v[104:107] offset:5120
s_add_u32 s80, s89, 65536
s_waitcnt lgkmcnt(1)
buffer_load_dwordx2 v[96:97], v5, s[40:43], s80 offen nt
buffer_load_dwordx2 v[98:99], v5, s[40:43], s80 offen offset:512 nt
buffer_load_dwordx2 v[100:101], v5, s[40:43], s80 offen offset:1024 nt
v_pk_fma_f32 v[176:177], v[176:177], s[86:87], v[16:17] op_sel_hi:[1,1,0] neg_lo:[0,0,1] neg_hi:[0,0,1]
v_pk_fma_f32 v[178:179], v[178:179], s[86:87], v[16:17] op_sel_hi:[1,1,0] neg_lo:[0,0,1] neg_hi:[0,0,1]
)"
R"(v_pk_fma_f32 v[180:181], v[180:181], s[86:87], v[16:17] op_sel_hi:[1,1,0] neg_lo:[0,0,1] neg_hi:[0,0,1]
v_pk_fma_f32 v[182:183], v[182:183], s[86:87], v[16:17] op_sel_hi:[1,1,0] neg_lo:[0,0,1] neg_hi:[0,0,1]
v_pk_fma_f32 v[184:185], v[184:185], s[86:87], v[16:17] op_sel_hi:[1,1,0] neg_lo:[0,0,1] neg_hi:[0,0,1]
v_pk_fma_f32 v[186:187], v[186:187], s[86:87], v[16:17] op_sel_hi:[1,1,0] neg_lo:[0,0,1] neg_hi:[0,0,1]
v_pk_fma_f32 v[188:189], v[188:189], s[86:87], v[16:17] op_sel_hi:[1,1,0] neg_lo:[0,0,1] neg_hi:[0,0,1]
v_pk_fma_f32 v[190:191], v[190:191], s[86:87], v[16:17] op_sel_hi:[1,1,0] neg_lo:[0,0,1] neg_hi:[0,0,1]
v_exp_f32 v176, v176
v_exp_f32 v177, v177
buffer_load_dwordx2 v[102:103], v5, s[40:43], s80 offen offset:1536 nt
s_waitcnt lgkmcnt(0)
buffer_load_dwordx2 v[104:105], v5, s[40:43], s80 offen offset:2048 nt
buffer_load_dwordx2 v[106:107], v5, s[40:43], s80 offen offset:2560 nt
v_exp_f32 v178, v178
v_exp_f32 v179, v179
v_exp_f32 v180, v180
v_exp_f32 v181, v181
v_exp_f32 v182, v182
v_exp_f32 v183, v183
v_exp_f32 v184, v184
v_exp_f32 v185, v185
v_exp_f32 v186, v186
v_exp_f32 v187, v187
v_exp_f32 v188, v188
v_exp_f32 v189, v189
v_exp_f32 v190, v190
v_exp_f32 v191, v191
v_cvt_pk_bf16_f32 v176, v176, v177
v_cvt_pk_bf16_f32 v177, v178, v179
v_cvt_pk_bf16_f32 v178, v180, v181
v_cvt_pk_bf16_f32 v179, v182, v183
v_cvt_pk_bf16_f32 v184, v184, v185
v_cvt_pk_bf16_f32 v185, v186, v187
v_cvt_pk_bf16_f32 v186, v188, v189
v_cvt_pk_bf16_f32 v187, v190, v191
v_mfma_f32_16x16x32_bf16 a[164:167], a[160:163], v[176:179], a[164:167]
ds_write_b128 v0, v[176:179] offset:10240
v_mfma_f32_16x16x32_bf16 a[164:167], a[160:163], v[184:187], a[164:167]
buffer_load_dwordx2 v[108:109], v5, s[40:43], s80 offen offset:3072 nt
buffer_load_dwordx2 v[110:111], v5, s[40:43], s80 offen offset:3584 nt
ds_write_b128 v0, v[184:187] offset:15360
v_mov_b32 v16, v12
v_pk_fma_f32 v[112:113], v[112:113], s[86:87], v[16:17] op_sel_hi:[1,1,0] neg_lo:[0,0,1] neg_hi:[0,0,1]
v_pk_fma_f32 v[114:115], v[114:115], s[86:87], v[16:17] op_sel_hi:[1,1,0] neg_lo:[0,0,1] neg_hi:[0,0,1]
v_pk_fma_f32 v[116:117], v[116:117], s[86:87], v[16:17] op_sel_hi:[1,1,0] neg_lo:[0,0,1] neg_hi:[0,0,1]
v_pk_fma_f32 v[118:119], v[118:119], s[86:87], v[16:17] op_sel_hi:[1,1,0] neg_lo:[0,0,1] neg_hi:[0,0,1]
v_pk_fma_f32 v[120:121], v[120:121], s[86:87], v[16:17] op_sel_hi:[1,1,0] neg_lo:[0,0,1] neg_hi:[0,0,1]
v_pk_fma_f32 v[122:123], v[122:123], s[86:87], v[16:17] op_sel_hi:[1,1,0] neg_lo:[0,0,1] neg_hi:[0,0,1]
v_pk_fma_f32 v[124:125], v[124:125], s[86:87], v[16:17] op_sel_hi:[1,1,0] neg_lo:[0,0,1] neg_hi:[0,0,1]
v_pk_fma_f32 v[126:127], v[126:127], s[86:87], v[16:17] op_sel_hi:[1,1,0] neg_lo:[0,0,1] neg_hi:[0,0,1]
v_exp_f32 v112, v112
v_exp_f32 v113, v113
v_exp_f32 v114, v114
v_exp_f32 v115, v115
v_exp_f32 v116, v116
v_exp_f32 v117, v117
v_exp_f32 v118, v118
v_exp_f32 v119, v119
v_exp_f32 v120, v120
v_exp_f32 v121, v121
v_exp_f32 v122, v122
v_exp_f32 v123, v123
v_exp_f32 v124, v124
v_exp_f32 v125, v125
v_exp_f32 v126, v126
v_exp_f32 v127, v127
v_cvt_pk_bf16_f32 v112, v112, v113
v_cvt_pk_bf16_f32 v113, v114, v115
v_cvt_pk_bf16_f32 v114, v116, v117
v_cvt_pk_bf16_f32 v115, v118, v119
v_cvt_pk_bf16_f32 v120, v120, v121
v_cvt_pk_bf16_f32 v121, v122, v123
v_cvt_pk_bf16_f32 v122, v124, v125
v_cvt_pk_bf16_f32 v123, v126, v127
v_mfma_f32_16x16x32_bf16 a[168:171], a[160:163], v[112:115], 0
ds_write_b128 v0, v[112:115] offset:1024
v_mfma_f32_16x16x32_bf16 a[168:171], a[160:163], v[120:123], a[168:171]
ds_write_b128 v0, v[120:123] offset:6144
s_add_u32 s80, s80, 16384
s_waitcnt lgkmcnt(1)
buffer_load_dwordx2 v[112:113], v5, s[40:43], s80 offen nt
buffer_load_dwordx2 v[114:115], v5, s[40:43], s80 offen offset:512 nt
buffer_load_dwordx2 v[116:117], v5, s[40:43], s80 offen offset:1024 nt
v_pk_fma_f32 v[192:193], v[192:193], s[86:87], v[16:17] op_sel_hi:[1,1,0] neg_lo:[0,0,1] neg_hi:[0,0,1]
v_pk_fma_f32 v[194:195], v[194:195], s[86:87], v[16:17] op_sel_hi:[1,1,0] neg_lo:[0,0,1] neg_hi:[0,0,1]
v_pk_fma_f32 v[196:197], v[196:197], s[86:87], v[16:17] op_sel_hi:[1,1,0] neg_lo:[0,0,1] neg_hi:[0,0,1]
v_pk_fma_f32 v[198:199], v[198:199], s[86:87], v[16:17] op_sel_hi:[1,1,0] neg_lo:[0,0,1] neg_hi:[0,0,1]
v_pk_fma_f32 v[200:201], v[200:201], s[86:87], v[16:17] op_sel_hi:[1,1,0] neg_lo:[0,0,1] neg_hi:[0,0,1]
buffer_load_dwordx2 v[118:119], v5, s[40:43], s80 offen offset:1536 nt
s_waitcnt lgkmcnt(0)
buffer_load_dwordx2 v[120:121], v5, s[40:43], s80 offen offset:2048 nt
buffer_load_dwordx2 v[122:123], v5, s[40:43], s80 offen offset:2560 nt
v_pk_fma_f32 v[202:203], v[202:203], s[86:87], v[16:17] op_sel_hi:[1,1,0] neg_lo:[0,0,1] neg_hi:[0,0,1]
v_pk_fma_f32 v[204:205], v[204:205], s[86:87], v[16:17] op_sel_hi:[1,1,0] neg_lo:[0,0,1] neg_hi:[0,0,1]
v_pk_fma_f32 v[206:207], v[206:207], s[86:87], v[16:17] op_sel_hi:[1,1,0] neg_lo:[0,0,1] neg_hi:[0,0,1]
v_exp_f32 v192, v192
v_exp_f32 v193, v193
v_exp_f32 v194, v194
v_exp_f32 v195, v195
v_exp_f32 v196, v196
v_exp_f32 v197, v197
v_exp_f32 v198, v198
v_exp_f32 v199, v199
v_exp_f32 v200, v200
v_exp_f32 v201, v201
v_exp_f32 v202, v202
v_exp_f32 v203, v203
v_exp_f32 v204, v204
v_exp_f32 v205, v205
v_exp_f32 v206, v206
v_exp_f32 v207, v207
v_cvt_pk_bf16_f32 v192, v192, v193
v_cvt_pk_bf16_f32 v193, v194, v195
v_cvt_pk_bf16_f32 v194, v196, v197
v_cvt_pk_bf16_f32 v195, v198, v199
v_cvt_pk_bf16_f32 v200, v200, v201
v_cvt_pk_bf16_f32 v201, v202, v203
v_cvt_pk_bf16_f32 v202, v204, v205
v_cvt_pk_bf16_f32 v203, v206, v207
v_mfma_f32_16x16x32_bf16 a[168:171], a[160:163], v[192:195], a[168:171]
ds_write_b128 v0, v[192:195] offset:11264
v_mfma_f32_16x16x32_bf16 a[168:171], a[160:163], v[200:203], a[168:171]
buffer_load_dwordx2 v[124:125], v5, s[40:43], s80 offen offset:3072 nt
buffer_load_dwordx2 v[126:127], v5, s[40:43], s80 offen offset:3584 nt
ds_write_b128 v0, v[200:203] offset:16384
v_mov_b32 v16, v13
v_pk_fma_f32 v[128:129], v[128:129], s[86:87], v[16:17] op_sel_hi:[1,1,0] neg_lo:[0,0,1] neg_hi:[0,0,1]
v_pk_fma_f32 v[130:131], v[130:131], s[86:87], v[16:17] op_sel_hi:[1,1,0] neg_lo:[0,0,1] neg_hi:[0,0,1]
v_pk_fma_f32 v[132:133], v[132:133], s[86:87], v[16:17] op_sel_hi:[1,1,0] neg_lo:[0,0,1] neg_hi:[0,0,1]
v_pk_fma_f32 v[134:135], v[134:135], s[86:87], v[16:17] op_sel_hi:[1,1,0] neg_lo:[0,0,1] neg_hi:[0,0,1]
v_pk_fma_f32 v[136:137], v[136:137], s[86:87], v[16:17] op_sel_hi:[1,1,0] neg_lo:[0,0,1] neg_hi:[0,0,1]
v_pk_fma_f32 v[138:139], v[138:139], s[86:87], v[16:17] op_sel_hi:[1,1,0] neg_lo:[0,0,1] neg_hi:[0,0,1]
v_pk_fma_f32 v[140:141], v[140:141], s[86:87], v[16:17] op_sel_hi:[1,1,0] neg_lo:[0,0,1] neg_hi:[0,0,1]
v_pk_fma_f32 v[142:143], v[142:143], s[86:87], v[16:17] op_sel_hi:[1,1,0] neg_lo:[0,0,1] neg_hi:[0,0,1]
v_exp_f32 v128, v128
v_exp_f32 v129, v129
v_exp_f32 v130, v130
v_exp_f32 v131, v131
v_exp_f32 v132, v132
v_exp_f32 v133, v133
v_exp_f32 v134, v134
v_exp_f32 v135, v135
v_exp_f32 v136, v136
v_exp_f32 v137, v137
v_exp_f32 v138, v138
v_exp_f32 v139, v139
v_exp_f32 v140, v140
v_exp_f32 v141, v141
v_exp_f32 v142, v142
v_exp_f32 v143, v143
v_cvt_pk_bf16_f32 v128, v128, v129
v_cvt_pk_bf16_f32 v129, v130, v131
v_cvt_pk_bf16_f32 v130, v132, v133
v_cvt_pk_bf16_f32 v131, v134, v135
v_cvt_pk_bf16_f32 v136, v136, v137
v_cvt_pk_bf16_f32 v137, v138, v139
v_cvt_pk_bf16_f32 v138, v140, v141
v_cvt_pk_bf16_f32 v139, v142, v143
v_mfma_f32_16x16x32_bf16 a[172:175], a[160:163], v[128:131], 0
ds_write_b128 v0, v[128:131] offset:2048
v_mfma_f32_16x16x32_bf16 a[172:175], a[160:163], v[136:139], a[172:175]
ds_write_b128 v0, v[136:139] offset:7168
s_add_u32 s80, s89, 98304
s_waitcnt lgkmcnt(1)
buffer_load_dwordx2 v[128:129], v5, s[40:43], s80 offen nt
buffer_load_dwordx2 v[130:131], v5, s[40:43], s80 offen offset:512 nt
buffer_load_dwordx2 v[132:133], v5, s[40:43], s80 offen offset:1024 nt
v_pk_fma_f32 v[208:209], v[208:209], s[86:87], v[16:17] op_sel_hi:[1,1,0] neg_lo:[0,0,1] neg_hi:[0,0,1]
v_pk_fma_f32 v[210:211], v[210:211], s[86:87], v[16:17] op_sel_hi:[1,1,0] neg_lo:[0,0,1] neg_hi:[0,0,1]
v_pk_fma_f32 v[212:213], v[212:213], s[86:87], v[16:17] op_sel_hi:[1,1,0] neg_lo:[0,0,1] neg_hi:[0,0,1]
v_pk_fma_f32 v[214:215], v[214:215], s[86:87], v[16:17] op_sel_hi:[1,1,0] neg_lo:[0,0,1] neg_hi:[0,0,1]
v_pk_fma_f32 v[216:217], v[216:217], s[86:87], v[16:17] op_sel_hi:[1,1,0] neg_lo:[0,0,1] neg_hi:[0,0,1]
v_pk_fma_f32 v[218:219], v[218:219], s[86:87], v[16:17] op_sel_hi:[1,1,0] neg_lo:[0,0,1] neg_hi:[0,0,1]
v_pk_fma_f32 v[220:221], v[220:221], s[86:87], v[16:17] op_sel_hi:[1,1,0] neg_lo:[0,0,1] neg_hi:[0,0,1]
v_pk_fma_f32 v[222:223], v[222:223], s[86:87], v[16:17] op_sel_hi:[1,1,0] neg_lo:[0,0,1] neg_hi:[0,0,1]
v_exp_f32 v208, v208
v_exp_f32 v209, v209
v_exp_f32 v210, v210
v_exp_f32 v211, v211
v_exp_f32 v212, v212
v_exp_f32 v213, v213
v_exp_f32 v214, v214
v_exp_f32 v215, v215
v_exp_f32 v216, v216
buffer_load_dwordx2 v[134:135], v5, s[40:43], s80 offen offset:1536 nt
s_waitcnt lgkmcnt(0)
buffer_load_dwordx2 v[136:137], v5, s[40:43], s80 offen offset:2048 nt
buffer_load_dwordx2 v[138:139], v5, s[40:43], s80 offen offset:2560 nt
v_exp_f32 v217, v217
v_exp_f32 v218, v218
v_exp_f32 v219, v219
v_exp_f32 v220, v220
v_exp_f32 v221, v221
v_exp_f32 v222, v222
v_exp_f32 v223, v223
v_cvt_pk_bf16_f32 v208, v208, v209
v_cvt_pk_bf16_f32 v209, v210, v211
v_cvt_pk_bf16_f32 v210, v212, v213
v_cvt_pk_bf16_f32 v211, v214, v215
v_cvt_pk_bf16_f32 v216, v216, v217
v_cvt_pk_bf16_f32 v217, v218, v219
)"
R"(v_cvt_pk_bf16_f32 v218, v220, v221
v_cvt_pk_bf16_f32 v219, v222, v223
v_mfma_f32_16x16x32_bf16 a[172:175], a[160:163], v[208:211], a[172:175]
ds_write_b128 v0, v[208:211] offset:12288
v_mfma_f32_16x16x32_bf16 a[172:175], a[160:163], v[216:219], a[172:175]
ds_write_b128 v0, v[216:219] offset:17408
v_mov_b32 v16, v14
v_pk_fma_f32 v[144:145], v[144:145], s[86:87], v[16:17] op_sel_hi:[1,1,0] neg_lo:[0,0,1] neg_hi:[0,0,1]
v_pk_fma_f32 v[146:147], v[146:147], s[86:87], v[16:17] op_sel_hi:[1,1,0] neg_lo:[0,0,1] neg_hi:[0,0,1]
v_pk_fma_f32 v[148:149], v[148:149], s[86:87], v[16:17] op_sel_hi:[1,1,0] neg_lo:[0,0,1] neg_hi:[0,0,1]
v_pk_fma_f32 v[150:151], v[150:151], s[86:87], v[16:17] op_sel_hi:[1,1,0] neg_lo:[0,0,1] neg_hi:[0,0,1]
v_pk_fma_f32 v[152:153], v[152:153], s[86:87], v[16:17] op_sel_hi:[1,1,0] neg_lo:[0,0,1] neg_hi:[0,0,1]
v_pk_fma_f32 v[154:155], v[154:155], s[86:87], v[16:17] op_sel_hi:[1,1,0] neg_lo:[0,0,1] neg_hi:[0,0,1]
v_pk_fma_f32 v[156:157], v[156:157], s[86:87], v[16:17] op_sel_hi:[1,1,0] neg_lo:[0,0,1] neg_hi:[0,0,1]
buffer_load_dwordx2 v[140:141], v5, s[40:43], s80 offen offset:3072 nt
buffer_load_dwordx2 v[142:143], v5, s[40:43], s80 offen offset:3584 nt
v_pk_fma_f32 v[158:159], v[158:159], s[86:87], v[16:17] op_sel_hi:[1,1,0] neg_lo:[0,0,1] neg_hi:[0,0,1]
v_exp_f32 v144, v144
v_exp_f32 v145, v145
v_exp_f32 v146, v146
v_exp_f32 v147, v147
v_exp_f32 v148, v148
v_exp_f32 v149, v149
v_exp_f32 v150, v150
v_exp_f32 v151, v151
v_exp_f32 v152, v152
v_exp_f32 v153, v153
v_exp_f32 v154, v154
v_exp_f32 v155, v155
v_exp_f32 v156, v156
v_exp_f32 v157, v157
v_exp_f32 v158, v158
v_exp_f32 v159, v159
v_cvt_pk_bf16_f32 v144, v144, v145
v_cvt_pk_bf16_f32 v145, v146, v147
v_cvt_pk_bf16_f32 v146, v148, v149
v_cvt_pk_bf16_f32 v147, v150, v151
v_cvt_pk_bf16_f32 v152, v152, v153
v_cvt_pk_bf16_f32 v153, v154, v155
v_cvt_pk_bf16_f32 v154, v156, v157
v_cvt_pk_bf16_f32 v155, v158, v159
v_mfma_f32_16x16x32_bf16 a[176:179], a[160:163], v[144:147], 0
ds_write_b128 v0, v[144:147] offset:3072
v_mfma_f32_16x16x32_bf16 a[176:179], a[160:163], v[152:155], a[176:179]
ds_write_b128 v0, v[152:155] offset:8192
s_add_u32 s80, s80, 16384
s_waitcnt lgkmcnt(1)
buffer_load_dwordx2 v[144:145], v5, s[40:43], s80 offen nt
buffer_load_dwordx2 v[146:147], v5, s[40:43], s80 offen offset:512 nt
s_waitcnt vmcnt(50)
buffer_load_dwordx2 v[148:149], v5, s[40:43], s80 offen offset:1024 nt
v_pk_fma_f32 v[224:225], v[224:225], s[86:87], v[16:17] op_sel_hi:[1,1,0] neg_lo:[0,0,1] neg_hi:[0,0,1]
v_pk_fma_f32 v[226:227], v[226:227], s[86:87], v[16:17] op_sel_hi:[1,1,0] neg_lo:[0,0,1] neg_hi:[0,0,1]
v_pk_fma_f32 v[228:229], v[228:229], s[86:87], v[16:17] op_sel_hi:[1,1,0] neg_lo:[0,0,1] neg_hi:[0,0,1]
v_pk_fma_f32 v[230:231], v[230:231], s[86:87], v[16:17] op_sel_hi:[1,1,0] neg_lo:[0,0,1] neg_hi:[0,0,1]
v_pk_fma_f32 v[232:233], v[232:233], s[86:87], v[16:17] op_sel_hi:[1,1,0] neg_lo:[0,0,1] neg_hi:[0,0,1]
v_pk_fma_f32 v[234:235], v[234:235], s[86:87], v[16:17] op_sel_hi:[1,1,0] neg_lo:[0,0,1] neg_hi:[0,0,1]
v_pk_fma_f32 v[236:237], v[236:237], s[86:87], v[16:17] op_sel_hi:[1,1,0] neg_lo:[0,0,1] neg_hi:[0,0,1]
v_pk_fma_f32 v[238:239], v[238:239], s[86:87], v[16:17] op_sel_hi:[1,1,0] neg_lo:[0,0,1] neg_hi:[0,0,1]
v_exp_f32 v224, v224
v_exp_f32 v225, v225
v_exp_f32 v226, v226
v_exp_f32 v227, v227
v_exp_f32 v228, v228
v_exp_f32 v229, v229
v_exp_f32 v230, v230
v_exp_f32 v231, v231
v_exp_f32 v232, v232
buffer_load_dwordx2 v[150:151], v5, s[40:43], s80 offen offset:1536 nt
s_waitcnt lgkmcnt(0)
buffer_load_dwordx2 v[152:153], v5, s[40:43], s80 offen offset:2048 nt
buffer_load_dwordx2 v[154:155], v5, s[40:43], s80 offen offset:2560 nt
v_exp_f32 v233, v233
v_exp_f32 v234, v234
v_exp_f32 v235, v235
v_exp_f32 v236, v236
v_exp_f32 v237, v237
v_exp_f32 v238, v238
v_exp_f32 v239, v239
v_cvt_pk_bf16_f32 v224, v224, v225
v_cvt_pk_bf16_f32 v225, v226, v227
v_cvt_pk_bf16_f32 v226, v228, v229
v_cvt_pk_bf16_f32 v227, v230, v231
v_cvt_pk_bf16_f32 v232, v232, v233
v_cvt_pk_bf16_f32 v233, v234, v235
v_cvt_pk_bf16_f32 v234, v236, v237
v_cvt_pk_bf16_f32 v235, v238, v239
v_mfma_f32_16x16x32_bf16 a[176:179], a[160:163], v[224:227], a[176:179]
ds_write_b128 v0, v[224:227] offset:13312
v_mfma_f32_16x16x32_bf16 a[176:179], a[160:163], v[232:235], a[176:179]
ds_write_b128 v0, v[232:235] offset:18432
v_mov_b32 v16, v15
v_pk_fma_f32 v[160:161], v[160:161], s[86:87], v[16:17] op_sel_hi:[1,1,0] neg_lo:[0,0,1] neg_hi:[0,0,1]
v_pk_fma_f32 v[162:163], v[162:163], s[86:87], v[16:17] op_sel_hi:[1,1,0] neg_lo:[0,0,1] neg_hi:[0,0,1]
v_pk_fma_f32 v[164:165], v[164:165], s[86:87], v[16:17] op_sel_hi:[1,1,0] neg_lo:[0,0,1] neg_hi:[0,0,1]
v_pk_fma_f32 v[166:167], v[166:167], s[86:87], v[16:17] op_sel_hi:[1,1,0] neg_lo:[0,0,1] neg_hi:[0,0,1]
v_pk_fma_f32 v[168:169], v[168:169], s[86:87], v[16:17] op_sel_hi:[1,1,0] neg_lo:[0,0,1] neg_hi:[0,0,1]
v_pk_fma_f32 v[170:171], v[170:171], s[86:87], v[16:17] op_sel_hi:[1,1,0] neg_lo:[0,0,1] neg_hi:[0,0,1]
v_pk_fma_f32 v[172:173], v[172:173], s[86:87], v[16:17] op_sel_hi:[1,1,0] neg_lo:[0,0,1] neg_hi:[0,0,1]
buffer_load_dwordx2 v[156:157], v5, s[40:43], s80 offen offset:3072 nt
buffer_load_dwordx2 v[158:159], v5, s[40:43], s80 offen offset:3584 nt
v_pk_fma_f32 v[174:175], v[174:175], s[86:87], v[16:17] op_sel_hi:[1,1,0] neg_lo:[0,0,1] neg_hi:[0,0,1]
v_exp_f32 v160, v160
v_exp_f32 v161, v161
v_exp_f32 v162, v162
v_exp_f32 v163, v163
v_exp_f32 v164, v164
v_exp_f32 v165, v165
v_exp_f32 v166, v166
v_exp_f32 v167, v167
v_exp_f32 v168, v168
v_exp_f32 v169, v169
v_exp_f32 v170, v170
v_exp_f32 v171, v171
v_exp_f32 v172, v172
v_exp_f32 v173, v173
v_exp_f32 v174, v174
v_exp_f32 v175, v175
v_cvt_pk_bf16_f32 v160, v160, v161
v_cvt_pk_bf16_f32 v161, v162, v163
v_cvt_pk_bf16_f32 v162, v164, v165
v_cvt_pk_bf16_f32 v163, v166, v167
v_cvt_pk_bf16_f32 v168, v168, v169
v_cvt_pk_bf16_f32 v169, v170, v171
v_cvt_pk_bf16_f32 v170, v172, v173
v_cvt_pk_bf16_f32 v171, v174, v175
v_mfma_f32_16x16x32_bf16 a[180:183], a[160:163], v[160:163], 0
ds_write_b128 v0, v[160:163] offset:4096
v_mfma_f32_16x16x32_bf16 a[180:183], a[160:163], v[168:171], a[180:183]
ds_write_b128 v0, v[168:171] offset:9216
s_add_u32 s80, s89, 131072
s_waitcnt lgkmcnt(1)
buffer_load_dwordx2 v[160:161], v5, s[40:43], s80 offen nt
buffer_load_dwordx2 v[162:163], v5, s[40:43], s80 offen offset:512 nt
s_waitcnt vmcnt(50)
buffer_load_dwordx2 v[164:165], v5, s[40:43], s80 offen offset:1024 nt
v_pk_fma_f32 v[240:241], v[240:241], s[86:87], v[16:17] op_sel_hi:[1,1,0] neg_lo:[0,0,1] neg_hi:[0,0,1]
v_pk_fma_f32 v[242:243], v[242:243], s[86:87], v[16:17] op_sel_hi:[1,1,0] neg_lo:[0,0,1] neg_hi:[0,0,1]
v_pk_fma_f32 v[244:245], v[244:245], s[86:87], v[16:17] op_sel_hi:[1,1,0] neg_lo:[0,0,1] neg_hi:[0,0,1]
v_pk_fma_f32 v[246:247], v[246:247], s[86:87], v[16:17] op_sel_hi:[1,1,0] neg_lo:[0,0,1] neg_hi:[0,0,1]
v_pk_fma_f32 v[248:249], v[248:249], s[86:87], v[16:17] op_sel_hi:[1,1,0] neg_lo:[0,0,1] neg_hi:[0,0,1]
v_pk_fma_f32 v[250:251], v[250:251], s[86:87], v[16:17] op_sel_hi:[1,1,0] neg_lo:[0,0,1] neg_hi:[0,0,1]
v_pk_fma_f32 v[252:253], v[252:253], s[86:87], v[16:17] op_sel_hi:[1,1,0] neg_lo:[0,0,1] neg_hi:[0,0,1]
v_pk_fma_f32 v[254:255], v[254:255], s[86:87], v[16:17] op_sel_hi:[1,1,0] neg_lo:[0,0,1] neg_hi:[0,0,1]
v_exp_f32 v240, v240
v_exp_f32 v241, v241
v_exp_f32 v242, v242
v_exp_f32 v243, v243
v_exp_f32 v244, v244
v_exp_f32 v245, v245
v_exp_f32 v246, v246
v_exp_f32 v247, v247
v_exp_f32 v248, v248
buffer_load_dwordx2 v[166:167], v5, s[40:43], s80 offen offset:1536 nt
s_waitcnt lgkmcnt(0)
buffer_load_dwordx2 v[168:169], v5, s[40:43], s80 offen offset:2048 nt
buffer_load_dwordx2 v[170:171], v5, s[40:43], s80 offen offset:2560 nt
v_exp_f32 v249, v249
v_exp_f32 v250, v250
v_exp_f32 v251, v251
v_exp_f32 v252, v252
v_exp_f32 v253, v253
v_exp_f32 v254, v254
v_exp_f32 v255, v255
v_cvt_pk_bf16_f32 v240, v240, v241
v_cvt_pk_bf16_f32 v241, v242, v243
v_cvt_pk_bf16_f32 v242, v244, v245
v_cvt_pk_bf16_f32 v243, v246, v247
v_cvt_pk_bf16_f32 v248, v248, v249
v_cvt_pk_bf16_f32 v249, v250, v251
v_cvt_pk_bf16_f32 v250, v252, v253
v_cvt_pk_bf16_f32 v251, v254, v255
v_mfma_f32_16x16x32_bf16 a[180:183], a[160:163], v[240:243], a[180:183]
ds_write_b128 v0, v[240:243] offset:14336
v_mfma_f32_16x16x32_bf16 a[180:183], a[160:163], v[248:251], a[180:183]
ds_write_b128 v0, v[248:251] offset:19456
ds_write_b32 v9, a164 offset:42240
ds_write_b32 v9, a168 offset:42496
buffer_load_dwordx2 v[172:173], v5, s[40:43], s80 offen offset:3072 nt
buffer_load_dwordx2 v[174:175], v5, s[40:43], s80 offen offset:3584 nt
ds_write_b32 v9, a172 offset:42752
ds_write_b32 v9, a176 offset:43008
s_nop 0
ds_write_b32 v9, a180 offset:43264
s_mov_b32 vcc_lo, 0x00ff00ff
s_mov_b32 vcc_hi, 0x00ff00ff
v_perm_b32 v16, v34, v32, s84
v_perm_b32 v17, v38, v36, s84
v_perm_b32 v18, v42, v40, s84
v_perm_b32 v19, v46, v44, s84
v_perm_b32 v20, v50, v48, s84
v_perm_b32 v21, v54, v52, s84
v_perm_b32 v22, v58, v56, s84
v_perm_b32 v23, v62, v60, s84
s_waitcnt lgkmcnt(0)
s_barrier
ds_read_b128 a[160:163], v2 offset:16384
ds_read_b128 a[164:167], v2 offset:17408
ds_read_b128 a[168:171], v2 offset:18432
ds_read_b128 a[172:175], v2 offset:19456
)"
R"(ds_read_b128 a[176:179], v2 offset:20480
ds_read_b128 a[180:183], v2 offset:21504
ds_read_b128 a[184:187], v2 offset:22528
ds_read_b128 a[188:191], v2 offset:23552
ds_read_b128 a[192:195], v2 offset:24576
ds_read_b128 a[196:199], v2 offset:25600
s_waitcnt lgkmcnt(5)
v_mfma_f32_16x16x32_bf16 v[176:179], v[16:19], a[160:163], 0
s_waitcnt lgkmcnt(0)
v_perm_b32 v24, v34, v32, s85
v_perm_b32 v25, v38, v36, s85
v_mfma_f32_16x16x32_bf16 v[176:179], v[20:23], a[180:183], v[176:179]
v_perm_b32 v26, v42, v40, s85
v_perm_b32 v27, v46, v44, s85
v_mfma_f32_16x16x32_bf16 v[192:195], v[16:19], a[164:167], 0
v_perm_b32 v28, v50, v48, s85
v_perm_b32 v29, v54, v52, s85
v_mfma_f32_16x16x32_bf16 v[192:195], v[20:23], a[184:187], v[192:195]
v_perm_b32 v30, v58, v56, s85
v_perm_b32 v31, v62, v60, s85
v_mfma_f32_16x16x32_bf16 v[208:211], v[16:19], a[168:171], 0
ds_read_b128 a[200:203], v2 offset:26624
v_mfma_f32_16x16x32_bf16 v[208:211], v[20:23], a[188:191], v[208:211]
ds_read_b128 a[220:223], v2 offset:31744
v_mfma_f32_16x16x32_bf16 v[224:227], v[16:19], a[172:175], 0
v_mfma_f32_16x16x32_bf16 v[224:227], v[20:23], a[192:195], v[224:227]
v_mfma_f32_16x16x32_bf16 v[240:243], v[16:19], a[176:179], 0
v_mfma_f32_16x16x32_bf16 v[240:243], v[20:23], a[196:199], v[240:243]
v_mfma_f32_16x16x32_bf16 v[180:183], v[24:27], a[160:163], 0
v_perm_b32 v16, v35, v33, s84
v_perm_b32 v17, v39, v37, s84
v_mfma_f32_16x16x32_bf16 v[180:183], v[28:31], a[180:183], v[180:183]
v_perm_b32 v18, v43, v41, s84
v_perm_b32 v19, v47, v45, s84
v_mfma_f32_16x16x32_bf16 v[196:199], v[24:27], a[164:167], 0
ds_read_b128 a[204:207], v2 offset:27648
v_perm_b32 v20, v51, v49, s84
v_mfma_f32_16x16x32_bf16 v[196:199], v[28:31], a[184:187], v[196:199]
ds_read_b128 a[224:227], v2 offset:32768
v_perm_b32 v21, v55, v53, s84
v_mfma_f32_16x16x32_bf16 v[212:215], v[24:27], a[168:171], 0
v_perm_b32 v22, v59, v57, s84
v_perm_b32 v23, v63, v61, s84
v_mfma_f32_16x16x32_bf16 v[212:215], v[28:31], a[188:191], v[212:215]
v_mfma_f32_16x16x32_bf16 v[228:231], v[24:27], a[172:175], 0
ds_read_b128 a[208:211], v2 offset:28672
v_mfma_f32_16x16x32_bf16 v[228:231], v[28:31], a[192:195], v[228:231]
ds_read_b128 a[228:231], v2 offset:33792
v_mfma_f32_16x16x32_bf16 v[244:247], v[24:27], a[176:179], 0
v_mfma_f32_16x16x32_bf16 v[244:247], v[28:31], a[196:199], v[244:247]
v_mfma_f32_16x16x32_bf16 v[184:187], v[16:19], a[160:163], 0
v_perm_b32 v24, v35, v33, s85
v_perm_b32 v25, v39, v37, s85
v_mfma_f32_16x16x32_bf16 v[184:187], v[20:23], a[180:183], v[184:187]
v_perm_b32 v26, v43, v41, s85
v_perm_b32 v27, v47, v45, s85
v_mfma_f32_16x16x32_bf16 v[200:203], v[16:19], a[164:167], 0
s_add_u32 s80, s80, 16384
v_perm_b32 v28, v51, v49, s85
v_perm_b32 v29, v55, v53, s85
v_mfma_f32_16x16x32_bf16 v[200:203], v[20:23], a[184:187], v[200:203]
v_perm_b32 v30, v59, v57, s85
v_perm_b32 v31, v63, v61, s85
v_mfma_f32_16x16x32_bf16 v[216:219], v[16:19], a[168:171], 0
ds_read_b128 a[212:215], v2 offset:29696
v_mfma_f32_16x16x32_bf16 v[216:219], v[20:23], a[188:191], v[216:219]
ds_read_b128 a[232:235], v2 offset:34816
v_mfma_f32_16x16x32_bf16 v[232:235], v[16:19], a[172:175], 0
buffer_load_dwordx2 v[32:33], v5, s[40:43], s80 offen nt
v_mfma_f32_16x16x32_bf16 v[232:235], v[20:23], a[192:195], v[232:235]
buffer_load_dwordx2 v[34:35], v5, s[40:43], s80 offen offset:512 nt
v_mfma_f32_16x16x32_bf16 v[248:251], v[16:19], a[176:179], 0
ds_read_b128 a[216:219], v2 offset:30720
v_mfma_f32_16x16x32_bf16 v[248:251], v[20:23], a[196:199], v[248:251]
ds_read_b128 a[236:239], v2 offset:35840
v_mfma_f32_16x16x32_bf16 v[188:191], v[24:27], a[160:163], 0
s_waitcnt vmcnt(42)
v_perm_b32 v16, v66, v64, s84
v_perm_b32 v17, v70, v68, s84
v_mfma_f32_16x16x32_bf16 v[188:191], v[28:31], a[180:183], v[188:191]
v_perm_b32 v18, v74, v72, s84
v_perm_b32 v19, v78, v76, s84
v_mfma_f32_16x16x32_bf16 v[204:207], v[24:27], a[164:167], 0
v_perm_b32 v20, v82, v80, s84
v_perm_b32 v21, v86, v84, s84
v_mfma_f32_16x16x32_bf16 v[204:207], v[28:31], a[184:187], v[204:207]
v_perm_b32 v22, v90, v88, s84
v_perm_b32 v23, v94, v92, s84
v_mfma_f32_16x16x32_bf16 v[220:223], v[24:27], a[168:171], 0
buffer_load_dwordx2 v[36:37], v5, s[40:43], s80 offen offset:1024 nt
v_mfma_f32_16x16x32_bf16 v[220:223], v[28:31], a[188:191], v[220:223]
buffer_load_dwordx2 v[38:39], v5, s[40:43], s80 offen offset:1536 nt
v_mfma_f32_16x16x32_bf16 v[236:239], v[24:27], a[172:175], 0
buffer_load_dwordx2 v[40:41], v5, s[40:43], s80 offen offset:2048 nt
v_mfma_f32_16x16x32_bf16 v[236:239], v[28:31], a[192:195], v[236:239]
buffer_load_dwordx2 v[42:43], v5, s[40:43], s80 offen offset:2560 nt
v_mfma_f32_16x16x32_bf16 v[252:255], v[24:27], a[176:179], 0
buffer_load_dwordx2 v[44:45], v5, s[40:43], s80 offen offset:3072 nt
v_mfma_f32_16x16x32_bf16 v[252:255], v[28:31], a[196:199], v[252:255]
buffer_load_dwordx2 v[46:47], v5, s[40:43], s80 offen offset:3584 nt
s_waitcnt lgkmcnt(1)
v_mfma_f32_16x16x32_bf16 v[176:179], v[16:19], a[200:203], v[176:179]
s_add_u32 s80, s89, 163840
v_perm_b32 v24, v66, v64, s85
v_perm_b32 v25, v70, v68, s85
v_mfma_f32_16x16x32_bf16 v[176:179], v[20:23], a[220:223], v[176:179]
v_perm_b32 v26, v74, v72, s85
v_perm_b32 v27, v78, v76, s85
v_mfma_f32_16x16x32_bf16 v[192:195], v[16:19], a[204:207], v[192:195]
v_perm_b32 v28, v82, v80, s85
v_perm_b32 v29, v86, v84, s85
v_mfma_f32_16x16x32_bf16 v[192:195], v[20:23], a[224:227], v[192:195]
v_perm_b32 v30, v90, v88, s85
v_perm_b32 v31, v94, v92, s85
v_mfma_f32_16x16x32_bf16 v[208:211], v[16:19], a[208:211], v[208:211]
ds_read_b128 a[160:163], v2 offset:36864
v_mfma_f32_16x16x32_bf16 v[208:211], v[20:23], a[228:231], v[208:211]
ds_read_b128 a[180:183], v2 offset:41984
v_mfma_f32_16x16x32_bf16 v[224:227], v[16:19], a[212:215], v[224:227]
buffer_load_dwordx2 v[48:49], v5, s[40:43], s80 offen nt
v_mfma_f32_16x16x32_bf16 v[224:227], v[20:23], a[232:235], v[224:227]
buffer_load_dwordx2 v[50:51], v5, s[40:43], s80 offen offset:512 nt
v_mfma_f32_16x16x32_bf16 v[240:243], v[16:19], a[216:219], v[240:243]
ds_read_b128 a[164:167], v2 offset:37888
s_waitcnt lgkmcnt(3)
v_mfma_f32_16x16x32_bf16 v[240:243], v[20:23], a[236:239], v[240:243]
ds_read_b128 a[184:187], v2 offset:43008
v_mfma_f32_16x16x32_bf16 v[180:183], v[24:27], a[200:203], v[180:183]
v_perm_b32 v16, v67, v65, s84
v_perm_b32 v17, v71, v69, s84
v_mfma_f32_16x16x32_bf16 v[180:183], v[28:31], a[220:223], v[180:183]
v_perm_b32 v18, v75, v73, s84
v_perm_b32 v19, v79, v77, s84
v_mfma_f32_16x16x32_bf16 v[196:199], v[24:27], a[204:207], v[196:199]
v_perm_b32 v20, v83, v81, s84
v_perm_b32 v21, v87, v85, s84
v_mfma_f32_16x16x32_bf16 v[196:199], v[28:31], a[224:227], v[196:199]
v_perm_b32 v22, v91, v89, s84
v_perm_b32 v23, v95, v93, s84
v_mfma_f32_16x16x32_bf16 v[212:215], v[24:27], a[208:211], v[212:215]
buffer_load_dwordx2 v[52:53], v5, s[40:43], s80 offen offset:1024 nt
v_mfma_f32_16x16x32_bf16 v[212:215], v[28:31], a[228:231], v[212:215]
ds_read_b128 a[168:171], v2 offset:38912
v_mfma_f32_16x16x32_bf16 v[228:231], v[24:27], a[212:215], v[228:231]
ds_read_b128 a[188:191], v2 offset:44032
v_mfma_f32_16x16x32_bf16 v[228:231], v[28:31], a[232:235], v[228:231]
buffer_load_dwordx2 v[54:55], v5, s[40:43], s80 offen offset:1536 nt
v_mfma_f32_16x16x32_bf16 v[244:247], v[24:27], a[216:219], v[244:247]
buffer_load_dwordx2 v[56:57], v5, s[40:43], s80 offen offset:2048 nt
v_mfma_f32_16x16x32_bf16 v[244:247], v[28:31], a[236:239], v[244:247]
buffer_load_dwordx2 v[58:59], v5, s[40:43], s80 offen offset:2560 nt
v_mfma_f32_16x16x32_bf16 v[184:187], v[16:19], a[200:203], v[184:187]
v_perm_b32 v24, v67, v65, s85
v_perm_b32 v25, v71, v69, s85
v_mfma_f32_16x16x32_bf16 v[184:187], v[20:23], a[220:223], v[184:187]
v_perm_b32 v26, v75, v73, s85
v_perm_b32 v27, v79, v77, s85
v_mfma_f32_16x16x32_bf16 v[200:203], v[16:19], a[204:207], v[200:203]
v_perm_b32 v28, v83, v81, s85
v_perm_b32 v29, v87, v85, s85
v_mfma_f32_16x16x32_bf16 v[200:203], v[20:23], a[224:227], v[200:203]
v_perm_b32 v30, v91, v89, s85
v_perm_b32 v31, v95, v93, s85
v_mfma_f32_16x16x32_bf16 v[216:219], v[16:19], a[208:211], v[216:219]
ds_read_b128 a[172:175], v2 offset:39936
v_mfma_f32_16x16x32_bf16 v[216:219], v[20:23], a[228:231], v[216:219]
ds_read_b128 a[192:195], v2 offset:45056
v_mfma_f32_16x16x32_bf16 v[232:235], v[16:19], a[212:215], v[232:235]
buffer_load_dwordx2 v[60:61], v5, s[40:43], s80 offen offset:3072 nt
v_mfma_f32_16x16x32_bf16 v[232:235], v[20:23], a[232:235], v[232:235]
buffer_load_dwordx2 v[62:63], v5, s[40:43], s80 offen offset:3584 nt
v_mfma_f32_16x16x32_bf16 v[248:251], v[16:19], a[216:219], v[248:251]
s_add_u32 s80, s80, 16384
ds_read_b128 a[176:179], v2 offset:40960
v_mfma_f32_16x16x32_bf16 v[248:251], v[20:23], a[236:239], v[248:251]
ds_read_b128 a[196:199], v2 offset:46080
v_mfma_f32_16x16x32_bf16 v[188:191], v[24:27], a[200:203], v[188:191]
s_waitcnt vmcnt(40)
v_perm_b32 v16, v98, v96, s84
v_perm_b32 v17, v102, v100, s84
v_mfma_f32_16x16x32_bf16 v[188:191], v[28:31], a[220:223], v[188:191]
v_perm_b32 v18, v106, v104, s84
v_perm_b32 v19, v110, v108, s84
v_mfma_f32_16x16x32_bf16 v[204:207], v[24:27], a[204:207], v[204:207]
v_perm_b32 v20, v114, v112, s84
v_perm_b32 v21, v118, v116, s84
v_mfma_f32_16x16x32_bf16 v[204:207], v[28:31], a[224:227], v[204:207]
v_perm_b32 v22, v122, v120, s84
v_perm_b32 v23, v126, v124, s84
v_mfma_f32_16x16x32_bf16 v[220:223], v[24:27], a[208:211], v[220:223]
buffer_load_dwordx2 v[64:65], v5, s[40:43], s80 offen nt
v_mfma_f32_16x16x32_bf16 v[220:223], v[28:31], a[228:231], v[220:223]
buffer_load_dwordx2 v[66:67], v5, s[40:43], s80 offen offset:512 nt
v_mfma_f32_16x16x32_bf16 v[236:239], v[24:27], a[212:215], v[236:239]
buffer_load_dwordx2 v[68:69], v5, s[40:43], s80 offen offset:1024 nt
v_mfma_f32_16x16x32_bf16 v[236:239], v[28:31], a[232:235], v[236:239]
buffer_load_dwordx2 v[70:71], v5, s[40:43], s80 offen offset:1536 nt
v_mfma_f32_16x16x32_bf16 v[252:255], v[24:27], a[216:219], v[252:255]
v_mfma_f32_16x16x32_bf16 v[252:255], v[28:31], a[236:239], v[252:255]
s_waitcnt lgkmcnt(1)
)"
R"(v_mfma_f32_16x16x32_bf16 v[176:179], v[16:19], a[160:163], v[176:179]
v_perm_b32 v24, v98, v96, s85
v_perm_b32 v25, v102, v100, s85
v_mfma_f32_16x16x32_bf16 v[176:179], v[20:23], a[180:183], v[176:179]
v_perm_b32 v26, v106, v104, s85
v_perm_b32 v27, v110, v108, s85
v_mfma_f32_16x16x32_bf16 v[192:195], v[16:19], a[164:167], v[192:195]
v_perm_b32 v28, v114, v112, s85
v_perm_b32 v29, v118, v116, s85
v_mfma_f32_16x16x32_bf16 v[192:195], v[20:23], a[184:187], v[192:195]
v_perm_b32 v30, v122, v120, s85
v_perm_b32 v31, v126, v124, s85
v_mfma_f32_16x16x32_bf16 v[208:211], v[16:19], a[168:171], v[208:211]
ds_read_b128 a[200:203], v2 offset:47104
v_mfma_f32_16x16x32_bf16 v[208:211], v[20:23], a[188:191], v[208:211]
ds_read_b128 a[220:223], v2 offset:52224
v_mfma_f32_16x16x32_bf16 v[224:227], v[16:19], a[172:175], v[224:227]
v_mfma_f32_16x16x32_bf16 v[224:227], v[20:23], a[192:195], v[224:227]
v_mfma_f32_16x16x32_bf16 v[240:243], v[16:19], a[176:179], v[240:243]
ds_read_b128 a[204:207], v2 offset:48128
s_waitcnt lgkmcnt(3)
v_mfma_f32_16x16x32_bf16 v[240:243], v[20:23], a[196:199], v[240:243]
ds_read_b128 a[224:227], v2 offset:53248
v_mfma_f32_16x16x32_bf16 v[180:183], v[24:27], a[160:163], v[180:183]
v_perm_b32 v16, v99, v97, s84
v_perm_b32 v17, v103, v101, s84
v_mfma_f32_16x16x32_bf16 v[180:183], v[28:31], a[180:183], v[180:183]
v_perm_b32 v18, v107, v105, s84
v_perm_b32 v19, v111, v109, s84
v_mfma_f32_16x16x32_bf16 v[196:199], v[24:27], a[164:167], v[196:199]
v_perm_b32 v20, v115, v113, s84
v_perm_b32 v21, v119, v117, s84
v_mfma_f32_16x16x32_bf16 v[196:199], v[28:31], a[184:187], v[196:199]
v_perm_b32 v22, v123, v121, s84
v_perm_b32 v23, v127, v125, s84
v_mfma_f32_16x16x32_bf16 v[212:215], v[24:27], a[168:171], v[212:215]
v_mfma_f32_16x16x32_bf16 v[212:215], v[28:31], a[188:191], v[212:215]
ds_read_b128 a[208:211], v2 offset:49152
v_mfma_f32_16x16x32_bf16 v[228:231], v[24:27], a[172:175], v[228:231]
ds_read_b128 a[228:231], v2 offset:54272
v_mfma_f32_16x16x32_bf16 v[228:231], v[28:31], a[192:195], v[228:231]
v_mfma_f32_16x16x32_bf16 v[244:247], v[24:27], a[176:179], v[244:247]
v_mfma_f32_16x16x32_bf16 v[244:247], v[28:31], a[196:199], v[244:247]
v_mfma_f32_16x16x32_bf16 v[184:187], v[16:19], a[160:163], v[184:187]
v_perm_b32 v24, v99, v97, s85
v_perm_b32 v25, v103, v101, s85
v_mfma_f32_16x16x32_bf16 v[184:187], v[20:23], a[180:183], v[184:187]
v_perm_b32 v26, v107, v105, s85
v_perm_b32 v27, v111, v109, s85
v_mfma_f32_16x16x32_bf16 v[200:203], v[16:19], a[164:167], v[200:203]
v_perm_b32 v28, v115, v113, s85
v_perm_b32 v29, v119, v117, s85
v_mfma_f32_16x16x32_bf16 v[200:203], v[20:23], a[184:187], v[200:203]
v_perm_b32 v30, v123, v121, s85
v_perm_b32 v31, v127, v125, s85
v_mfma_f32_16x16x32_bf16 v[216:219], v[16:19], a[168:171], v[216:219]
ds_read_b128 a[212:215], v2 offset:50176
v_mfma_f32_16x16x32_bf16 v[216:219], v[20:23], a[188:191], v[216:219]
ds_read_b128 a[232:235], v2 offset:55296
v_mfma_f32_16x16x32_bf16 v[232:235], v[16:19], a[172:175], v[232:235]
v_mfma_f32_16x16x32_bf16 v[232:235], v[20:23], a[192:195], v[232:235]
v_mfma_f32_16x16x32_bf16 v[248:251], v[16:19], a[176:179], v[248:251]
ds_read_b128 a[216:219], v2 offset:51200
v_mfma_f32_16x16x32_bf16 v[248:251], v[20:23], a[196:199], v[248:251]
ds_read_b128 a[236:239], v2 offset:56320
v_mfma_f32_16x16x32_bf16 v[188:191], v[24:27], a[160:163], v[188:191]
s_waitcnt vmcnt(28)
v_perm_b32 v16, v130, v128, s84
v_perm_b32 v17, v134, v132, s84
v_mfma_f32_16x16x32_bf16 v[188:191], v[28:31], a[180:183], v[188:191]
v_perm_b32 v18, v138, v136, s84
v_perm_b32 v19, v142, v140, s84
v_mfma_f32_16x16x32_bf16 v[204:207], v[24:27], a[164:167], v[204:207]
v_perm_b32 v20, v146, v144, s84
v_perm_b32 v21, v150, v148, s84
v_mfma_f32_16x16x32_bf16 v[204:207], v[28:31], a[184:187], v[204:207]
v_perm_b32 v22, v154, v152, s84
v_perm_b32 v23, v158, v156, s84
v_mfma_f32_16x16x32_bf16 v[220:223], v[24:27], a[168:171], v[220:223]
buffer_load_dwordx2 v[72:73], v5, s[40:43], s80 offen offset:2048 nt
v_mfma_f32_16x16x32_bf16 v[220:223], v[28:31], a[188:191], v[220:223]
buffer_load_dwordx2 v[74:75], v5, s[40:43], s80 offen offset:2560 nt
v_mfma_f32_16x16x32_bf16 v[236:239], v[24:27], a[172:175], v[236:239]
v_mfma_f32_16x16x32_bf16 v[236:239], v[28:31], a[192:195], v[236:239]
v_mfma_f32_16x16x32_bf16 v[252:255], v[24:27], a[176:179], v[252:255]
buffer_load_dwordx2 v[76:77], v5, s[40:43], s80 offen offset:3072 nt
v_mfma_f32_16x16x32_bf16 v[252:255], v[28:31], a[196:199], v[252:255]
s_waitcnt lgkmcnt(1)
v_mfma_f32_16x16x32_bf16 v[176:179], v[16:19], a[200:203], v[176:179]
v_perm_b32 v24, v130, v128, s85
v_perm_b32 v25, v134, v132, s85
v_mfma_f32_16x16x32_bf16 v[176:179], v[20:23], a[220:223], v[176:179]
v_perm_b32 v26, v138, v136, s85
v_perm_b32 v27, v142, v140, s85
v_mfma_f32_16x16x32_bf16 v[192:195], v[16:19], a[204:207], v[192:195]
v_perm_b32 v28, v146, v144, s85
v_perm_b32 v29, v150, v148, s85
v_mfma_f32_16x16x32_bf16 v[192:195], v[20:23], a[224:227], v[192:195]
v_perm_b32 v30, v154, v152, s85
v_perm_b32 v31, v158, v156, s85
v_mfma_f32_16x16x32_bf16 v[208:211], v[16:19], a[208:211], v[208:211]
ds_read_b128 a[160:163], v2 offset:57344
v_mfma_f32_16x16x32_bf16 v[208:211], v[20:23], a[228:231], v[208:211]
ds_read_b128 a[180:183], v2 offset:62464
v_mfma_f32_16x16x32_bf16 v[224:227], v[16:19], a[212:215], v[224:227]
buffer_load_dwordx2 v[78:79], v5, s[40:43], s80 offen offset:3584 nt
v_mfma_f32_16x16x32_bf16 v[224:227], v[20:23], a[232:235], v[224:227]
s_add_u32 s80, s89, 196608
v_mfma_f32_16x16x32_bf16 v[240:243], v[16:19], a[216:219], v[240:243]
ds_read_b128 a[164:167], v2 offset:58368
s_waitcnt lgkmcnt(3)
v_mfma_f32_16x16x32_bf16 v[240:243], v[20:23], a[236:239], v[240:243]
ds_read_b128 a[184:187], v2 offset:63488
v_mfma_f32_16x16x32_bf16 v[180:183], v[24:27], a[200:203], v[180:183]
v_perm_b32 v16, v131, v129, s84
v_perm_b32 v17, v135, v133, s84
v_mfma_f32_16x16x32_bf16 v[180:183], v[28:31], a[220:223], v[180:183]
v_perm_b32 v18, v139, v137, s84
v_perm_b32 v19, v143, v141, s84
v_mfma_f32_16x16x32_bf16 v[196:199], v[24:27], a[204:207], v[196:199]
v_perm_b32 v20, v147, v145, s84
v_perm_b32 v21, v151, v149, s84
v_mfma_f32_16x16x32_bf16 v[196:199], v[28:31], a[224:227], v[196:199]
v_perm_b32 v22, v155, v153, s84
v_perm_b32 v23, v159, v157, s84
v_mfma_f32_16x16x32_bf16 v[212:215], v[24:27], a[208:211], v[212:215]
buffer_load_dwordx2 v[96:97], v5, s[40:43], s80 offen nt
v_mfma_f32_16x16x32_bf16 v[212:215], v[28:31], a[228:231], v[212:215]
ds_read_b128 a[168:171], v2 offset:59392
v_mfma_f32_16x16x32_bf16 v[228:231], v[24:27], a[212:215], v[228:231]
ds_read_b128 a[188:191], v2 offset:64512
v_mfma_f32_16x16x32_bf16 v[228:231], v[28:31], a[232:235], v[228:231]
buffer_load_dwordx2 v[98:99], v5, s[40:43], s80 offen offset:512 nt
v_mfma_f32_16x16x32_bf16 v[244:247], v[24:27], a[216:219], v[244:247]
buffer_load_dwordx2 v[100:101], v5, s[40:43], s80 offen offset:1024 nt
v_mfma_f32_16x16x32_bf16 v[244:247], v[28:31], a[236:239], v[244:247]
v_mfma_f32_16x16x32_bf16 v[184:187], v[16:19], a[200:203], v[184:187]
v_perm_b32 v24, v131, v129, s85
v_perm_b32 v25, v135, v133, s85
v_mfma_f32_16x16x32_bf16 v[184:187], v[20:23], a[220:223], v[184:187]
v_perm_b32 v26, v139, v137, s85
v_perm_b32 v27, v143, v141, s85
v_mfma_f32_16x16x32_bf16 v[200:203], v[16:19], a[204:207], v[200:203]
v_perm_b32 v28, v147, v145, s85
v_perm_b32 v29, v151, v149, s85
v_mfma_f32_16x16x32_bf16 v[200:203], v[20:23], a[224:227], v[200:203]
v_perm_b32 v30, v155, v153, s85
v_perm_b32 v31, v159, v157, s85
v_mfma_f32_16x16x32_bf16 v[216:219], v[16:19], a[208:211], v[216:219]
ds_read_b128 a[172:175], v2 offset:60416
v_mfma_f32_16x16x32_bf16 v[216:219], v[20:23], a[228:231], v[216:219]
ds_read_b128 a[192:195], v3 offset:0
v_mfma_f32_16x16x32_bf16 v[232:235], v[16:19], a[212:215], v[232:235]
buffer_load_dwordx2 v[102:103], v5, s[40:43], s80 offen offset:1536 nt
v_mfma_f32_16x16x32_bf16 v[232:235], v[20:23], a[232:235], v[232:235]
buffer_load_dwordx2 v[104:105], v5, s[40:43], s80 offen offset:2048 nt
v_mfma_f32_16x16x32_bf16 v[248:251], v[16:19], a[216:219], v[248:251]
ds_read_b128 a[176:179], v2 offset:61440
v_mfma_f32_16x16x32_bf16 v[248:251], v[20:23], a[236:239], v[248:251]
ds_read_b128 a[196:199], v3 offset:1024
v_mfma_f32_16x16x32_bf16 v[188:191], v[24:27], a[200:203], v[188:191]
s_waitcnt vmcnt(21)
v_perm_b32 v16, v162, v160, s84
v_perm_b32 v17, v166, v164, s84
v_mfma_f32_16x16x32_bf16 v[188:191], v[28:31], a[220:223], v[188:191]
v_perm_b32 v18, v170, v168, s84
v_perm_b32 v19, v174, v172, s84
v_mfma_f32_16x16x32_bf16 v[204:207], v[24:27], a[204:207], v[204:207]
v_perm_b32 v20, v34, v32, s84
v_perm_b32 v21, v38, v36, s84
v_mfma_f32_16x16x32_bf16 v[204:207], v[28:31], a[224:227], v[204:207]
v_perm_b32 v22, v42, v40, s84
v_perm_b32 v23, v46, v44, s84
v_mfma_f32_16x16x32_bf16 v[220:223], v[24:27], a[208:211], v[220:223]
buffer_load_dwordx2 v[106:107], v5, s[40:43], s80 offen offset:2560 nt
v_mfma_f32_16x16x32_bf16 v[220:223], v[28:31], a[228:231], v[220:223]
buffer_load_dwordx2 v[108:109], v5, s[40:43], s80 offen offset:3072 nt
v_mfma_f32_16x16x32_bf16 v[236:239], v[24:27], a[212:215], v[236:239]
v_mfma_f32_16x16x32_bf16 v[236:239], v[28:31], a[232:235], v[236:239]
v_mfma_f32_16x16x32_bf16 v[252:255], v[24:27], a[216:219], v[252:255]
v_mfma_f32_16x16x32_bf16 v[252:255], v[28:31], a[236:239], v[252:255]
s_waitcnt lgkmcnt(1)
v_mfma_f32_16x16x32_bf16 v[176:179], v[16:19], a[160:163], v[176:179]
v_perm_b32 v24, v162, v160, s85
v_perm_b32 v25, v166, v164, s85
v_mfma_f32_16x16x32_bf16 v[176:179], v[20:23], a[180:183], v[176:179]
v_perm_b32 v26, v170, v168, s85
v_perm_b32 v27, v174, v172, s85
v_mfma_f32_16x16x32_bf16 v[192:195], v[16:19], a[164:167], v[192:195]
v_perm_b32 v28, v34, v32, s85
v_perm_b32 v29, v38, v36, s85
v_mfma_f32_16x16x32_bf16 v[192:195], v[20:23], a[184:187], v[192:195]
v_perm_b32 v30, v42, v40, s85
v_perm_b32 v31, v46, v44, s85
v_mfma_f32_16x16x32_bf16 v[208:211], v[16:19], a[168:171], v[208:211]
ds_read_b128 a[200:203], v3 offset:2048
v_mfma_f32_16x16x32_bf16 v[208:211], v[20:23], a[188:191], v[208:211]
ds_read_b128 a[220:223], v3 offset:7168
v_mfma_f32_16x16x32_bf16 v[224:227], v[16:19], a[172:175], v[224:227]
buffer_load_dwordx2 v[110:111], v5, s[40:43], s80 offen offset:3584 nt
)"
R"(v_mfma_f32_16x16x32_bf16 v[224:227], v[20:23], a[192:195], v[224:227]
s_add_u32 s80, s80, 16384
v_mfma_f32_16x16x32_bf16 v[240:243], v[16:19], a[176:179], v[240:243]
ds_read_b128 a[204:207], v3 offset:3072
s_waitcnt lgkmcnt(3)
v_mfma_f32_16x16x32_bf16 v[240:243], v[20:23], a[196:199], v[240:243]
ds_read_b128 a[224:227], v3 offset:8192
v_mfma_f32_16x16x32_bf16 v[180:183], v[24:27], a[160:163], v[180:183]
v_perm_b32 v16, v163, v161, s84
v_perm_b32 v17, v167, v165, s84
v_mfma_f32_16x16x32_bf16 v[180:183], v[28:31], a[180:183], v[180:183]
v_perm_b32 v18, v171, v169, s84
v_perm_b32 v19, v175, v173, s84
v_mfma_f32_16x16x32_bf16 v[196:199], v[24:27], a[164:167], v[196:199]
v_perm_b32 v20, v35, v33, s84
v_perm_b32 v21, v39, v37, s84
v_mfma_f32_16x16x32_bf16 v[196:199], v[28:31], a[184:187], v[196:199]
v_perm_b32 v22, v43, v41, s84
v_perm_b32 v23, v47, v45, s84
v_mfma_f32_16x16x32_bf16 v[212:215], v[24:27], a[168:171], v[212:215]
buffer_load_dwordx2 v[112:113], v5, s[40:43], s80 offen nt
v_mfma_f32_16x16x32_bf16 v[212:215], v[28:31], a[188:191], v[212:215]
ds_read_b128 a[208:211], v3 offset:4096
v_mfma_f32_16x16x32_bf16 v[228:231], v[24:27], a[172:175], v[228:231]
ds_read_b128 a[228:231], v3 offset:9216
v_mfma_f32_16x16x32_bf16 v[228:231], v[28:31], a[192:195], v[228:231]
buffer_load_dwordx2 v[114:115], v5, s[40:43], s80 offen offset:512 nt
v_mfma_f32_16x16x32_bf16 v[244:247], v[24:27], a[176:179], v[244:247]
buffer_load_dwordx2 v[116:117], v5, s[40:43], s80 offen offset:1024 nt
v_mfma_f32_16x16x32_bf16 v[244:247], v[28:31], a[196:199], v[244:247]
buffer_load_dwordx2 v[118:119], v5, s[40:43], s80 offen offset:1536 nt
v_mfma_f32_16x16x32_bf16 v[184:187], v[16:19], a[160:163], v[184:187]
v_perm_b32 v24, v163, v161, s85
v_perm_b32 v25, v167, v165, s85
v_mfma_f32_16x16x32_bf16 v[184:187], v[20:23], a[180:183], v[184:187]
v_perm_b32 v26, v171, v169, s85
v_perm_b32 v27, v175, v173, s85
v_mfma_f32_16x16x32_bf16 v[200:203], v[16:19], a[164:167], v[200:203]
v_perm_b32 v28, v35, v33, s85
v_perm_b32 v29, v39, v37, s85
v_mfma_f32_16x16x32_bf16 v[200:203], v[20:23], a[184:187], v[200:203]
v_perm_b32 v30, v43, v41, s85
v_perm_b32 v31, v47, v45, s85
v_mfma_f32_16x16x32_bf16 v[216:219], v[16:19], a[168:171], v[216:219]
ds_read_b128 a[212:215], v3 offset:5120
v_mfma_f32_16x16x32_bf16 v[216:219], v[20:23], a[188:191], v[216:219]
ds_read_b128 a[232:235], v3 offset:10240
v_mfma_f32_16x16x32_bf16 v[232:235], v[16:19], a[172:175], v[232:235]
buffer_load_dwordx2 v[120:121], v5, s[40:43], s80 offen offset:2048 nt
v_mfma_f32_16x16x32_bf16 v[232:235], v[20:23], a[192:195], v[232:235]
v_mfma_f32_16x16x32_bf16 v[248:251], v[16:19], a[176:179], v[248:251]
ds_read_b128 a[216:219], v3 offset:6144
v_mfma_f32_16x16x32_bf16 v[248:251], v[20:23], a[196:199], v[248:251]
ds_read_b128 a[236:239], v3 offset:11264
v_mfma_f32_16x16x32_bf16 v[188:191], v[24:27], a[160:163], v[188:191]
buffer_load_dwordx2 v[122:123], v5, s[40:43], s80 offen offset:2560 nt
buffer_load_dwordx2 v[124:125], v5, s[40:43], s80 offen offset:3072 nt
buffer_load_dwordx2 v[126:127], v5, s[40:43], s80 offen offset:3584 nt
s_add_u32 s80, s89, 229376
buffer_load_dwordx2 v[32:33], v5, s[40:43], s80 offen nt
buffer_load_dwordx2 v[34:35], v5, s[40:43], s80 offen offset:512 nt
buffer_load_dwordx2 v[36:37], v5, s[40:43], s80 offen offset:1024 nt
s_waitcnt vmcnt(19)
v_perm_b32 v16, v50, v48, s84
v_perm_b32 v17, v54, v52, s84
v_mfma_f32_16x16x32_bf16 v[188:191], v[28:31], a[180:183], v[188:191]
v_perm_b32 v18, v58, v56, s84
v_perm_b32 v19, v62, v60, s84
v_mfma_f32_16x16x32_bf16 v[204:207], v[24:27], a[164:167], v[204:207]
v_perm_b32 v20, v66, v64, s84
v_perm_b32 v21, v70, v68, s84
v_mfma_f32_16x16x32_bf16 v[204:207], v[28:31], a[184:187], v[204:207]
v_perm_b32 v22, v74, v72, s84
v_perm_b32 v23, v78, v76, s84
v_mfma_f32_16x16x32_bf16 v[220:223], v[24:27], a[168:171], v[220:223]
buffer_load_dwordx2 v[38:39], v5, s[40:43], s80 offen offset:1536 nt
v_mfma_f32_16x16x32_bf16 v[220:223], v[28:31], a[188:191], v[220:223]
buffer_load_dwordx2 v[40:41], v5, s[40:43], s80 offen offset:2048 nt
v_mfma_f32_16x16x32_bf16 v[236:239], v[24:27], a[172:175], v[236:239]
v_mfma_f32_16x16x32_bf16 v[236:239], v[28:31], a[192:195], v[236:239]
v_mfma_f32_16x16x32_bf16 v[252:255], v[24:27], a[176:179], v[252:255]
v_mfma_f32_16x16x32_bf16 v[252:255], v[28:31], a[196:199], v[252:255]
s_waitcnt lgkmcnt(1)
v_mfma_f32_16x16x32_bf16 v[176:179], v[16:19], a[200:203], v[176:179]
v_perm_b32 v24, v50, v48, s85
v_perm_b32 v25, v54, v52, s85
v_mfma_f32_16x16x32_bf16 v[176:179], v[20:23], a[220:223], v[176:179]
v_perm_b32 v26, v58, v56, s85
v_perm_b32 v27, v62, v60, s85
v_mfma_f32_16x16x32_bf16 v[192:195], v[16:19], a[204:207], v[192:195]
v_perm_b32 v28, v66, v64, s85
v_perm_b32 v29, v70, v68, s85
v_mfma_f32_16x16x32_bf16 v[192:195], v[20:23], a[224:227], v[192:195]
v_perm_b32 v30, v74, v72, s85
v_perm_b32 v31, v78, v76, s85
v_mfma_f32_16x16x32_bf16 v[208:211], v[16:19], a[208:211], v[208:211]
ds_read_b128 a[160:163], v3 offset:12288
v_mfma_f32_16x16x32_bf16 v[208:211], v[20:23], a[228:231], v[208:211]
ds_read_b128 a[180:183], v3 offset:17408
v_mfma_f32_16x16x32_bf16 v[224:227], v[16:19], a[212:215], v[224:227]
buffer_load_dwordx2 v[42:43], v5, s[40:43], s80 offen offset:2560 nt
v_mfma_f32_16x16x32_bf16 v[224:227], v[20:23], a[232:235], v[224:227]
buffer_load_dwordx2 v[44:45], v5, s[40:43], s80 offen offset:3072 nt
v_mfma_f32_16x16x32_bf16 v[240:243], v[16:19], a[216:219], v[240:243]
ds_read_b128 a[164:167], v3 offset:13312
s_waitcnt lgkmcnt(3)
v_mfma_f32_16x16x32_bf16 v[240:243], v[20:23], a[236:239], v[240:243]
ds_read_b128 a[184:187], v3 offset:18432
v_mfma_f32_16x16x32_bf16 v[180:183], v[24:27], a[200:203], v[180:183]
v_perm_b32 v16, v51, v49, s84
v_perm_b32 v17, v55, v53, s84
v_mfma_f32_16x16x32_bf16 v[180:183], v[28:31], a[220:223], v[180:183]
v_perm_b32 v18, v59, v57, s84
v_perm_b32 v19, v63, v61, s84
v_mfma_f32_16x16x32_bf16 v[196:199], v[24:27], a[204:207], v[196:199]
v_perm_b32 v20, v67, v65, s84
v_perm_b32 v21, v71, v69, s84
v_mfma_f32_16x16x32_bf16 v[196:199], v[28:31], a[224:227], v[196:199]
v_perm_b32 v22, v75, v73, s84
v_perm_b32 v23, v79, v77, s84
v_mfma_f32_16x16x32_bf16 v[212:215], v[24:27], a[208:211], v[212:215]
buffer_load_dwordx2 v[46:47], v5, s[40:43], s80 offen offset:3584 nt
v_mfma_f32_16x16x32_bf16 v[212:215], v[28:31], a[228:231], v[212:215]
s_add_u32 s80, s80, 16384
ds_read_b128 a[168:171], v3 offset:14336
v_mfma_f32_16x16x32_bf16 v[228:231], v[24:27], a[212:215], v[228:231]
ds_read_b128 a[188:191], v3 offset:19456
v_mfma_f32_16x16x32_bf16 v[228:231], v[28:31], a[232:235], v[228:231]
buffer_load_dwordx2 v[80:81], v5, s[40:43], s80 offen nt
v_mfma_f32_16x16x32_bf16 v[244:247], v[24:27], a[216:219], v[244:247]
v_mfma_f32_16x16x32_bf16 v[244:247], v[28:31], a[236:239], v[244:247]
buffer_load_dwordx2 v[82:83], v5, s[40:43], s80 offen offset:512 nt
v_mfma_f32_16x16x32_bf16 v[184:187], v[16:19], a[200:203], v[184:187]
v_perm_b32 v24, v51, v49, s85
v_perm_b32 v25, v55, v53, s85
v_mfma_f32_16x16x32_bf16 v[184:187], v[20:23], a[220:223], v[184:187]
v_perm_b32 v26, v59, v57, s85
v_perm_b32 v27, v63, v61, s85
v_mfma_f32_16x16x32_bf16 v[200:203], v[16:19], a[204:207], v[200:203]
v_perm_b32 v28, v67, v65, s85
v_perm_b32 v29, v71, v69, s85
v_mfma_f32_16x16x32_bf16 v[200:203], v[20:23], a[224:227], v[200:203]
v_perm_b32 v30, v75, v73, s85
v_perm_b32 v31, v79, v77, s85
v_mfma_f32_16x16x32_bf16 v[216:219], v[16:19], a[208:211], v[216:219]
ds_read_b128 a[172:175], v3 offset:15360
v_mfma_f32_16x16x32_bf16 v[216:219], v[20:23], a[228:231], v[216:219]
ds_read_b128 a[192:195], v3 offset:20480
v_mfma_f32_16x16x32_bf16 v[232:235], v[16:19], a[212:215], v[232:235]
buffer_load_dwordx2 v[84:85], v5, s[40:43], s80 offen offset:1024 nt
v_mfma_f32_16x16x32_bf16 v[232:235], v[20:23], a[232:235], v[232:235]
v_mfma_f32_16x16x32_bf16 v[248:251], v[16:19], a[216:219], v[248:251]
ds_read_b128 a[176:179], v3 offset:16384
v_mfma_f32_16x16x32_bf16 v[248:251], v[20:23], a[236:239], v[248:251]
ds_read_b128 a[196:199], v3 offset:21504
v_mfma_f32_16x16x32_bf16 v[188:191], v[24:27], a[200:203], v[188:191]
buffer_load_dwordx2 v[86:87], v5, s[40:43], s80 offen offset:1536 nt
buffer_load_dwordx2 v[88:89], v5, s[40:43], s80 offen offset:2048 nt
buffer_load_dwordx2 v[90:91], v5, s[40:43], s80 offen offset:2560 nt
buffer_load_dwordx2 v[92:93], v5, s[40:43], s80 offen offset:3072 nt
buffer_load_dwordx2 v[94:95], v5, s[40:43], s80 offen offset:3584 nt
s_cmp_eq_u32 s90, 0
s_cselect_b32 s54, 71680, 0
s_cselect_b32 s46, 3662848, 0
s_mov_b64 s[44:45], %[wq1]
s_and_b32 s45, s45, 0xffff
s_mov_b64 s[52:53], %[x1]
s_and_b32 s53, s53, 0xffff
v_accvgpr_read_b32 v6, a241
s_mov_b32 s94, s93
buffer_load_dwordx4 v[48:51], v6, s[52:55], s94 offen
s_add_u32 s94, s94, 128
s_cmp_eq_u32 s94, 896
s_cselect_b32 s94, 0, s94
buffer_load_dwordx4 v[52:55], v6, s[52:55], s94 offen
s_add_u32 s94, s94, 128
s_cmp_eq_u32 s94, 896
s_cselect_b32 s94, 0, s94
s_waitcnt vmcnt(18)
v_perm_b32 v16, v98, v96, s84
v_perm_b32 v17, v102, v100, s84
v_mfma_f32_16x16x32_bf16 v[188:191], v[28:31], a[220:223], v[188:191]
v_perm_b32 v18, v106, v104, s84
v_perm_b32 v19, v110, v108, s84
v_mfma_f32_16x16x32_bf16 v[204:207], v[24:27], a[204:207], v[204:207]
v_perm_b32 v20, v114, v112, s84
v_perm_b32 v21, v118, v116, s84
v_mfma_f32_16x16x32_bf16 v[204:207], v[28:31], a[224:227], v[204:207]
v_perm_b32 v22, v122, v120, s84
v_perm_b32 v23, v126, v124, s84
v_mfma_f32_16x16x32_bf16 v[220:223], v[24:27], a[208:211], v[220:223]
v_mfma_f32_16x16x32_bf16 v[220:223], v[28:31], a[228:231], v[220:223]
buffer_load_dwordx4 v[56:59], v6, s[52:55], s94 offen
v_mfma_f32_16x16x32_bf16 v[236:239], v[24:27], a[212:215], v[236:239]
s_add_u32 s94, s94, 128
s_cmp_eq_u32 s94, 896
s_cselect_b32 s94, 0, s94
v_mfma_f32_16x16x32_bf16 v[236:239], v[28:31], a[232:235], v[236:239]
v_mfma_f32_16x16x32_bf16 v[252:255], v[24:27], a[216:219], v[252:255]
v_mfma_f32_16x16x32_bf16 v[252:255], v[28:31], a[236:239], v[252:255]
)"
R"(s_waitcnt lgkmcnt(1)
v_mfma_f32_16x16x32_bf16 v[176:179], v[16:19], a[160:163], v[176:179]
v_perm_b32 v24, v98, v96, s85
v_perm_b32 v25, v102, v100, s85
v_mfma_f32_16x16x32_bf16 v[176:179], v[20:23], a[180:183], v[176:179]
v_perm_b32 v26, v106, v104, s85
v_perm_b32 v27, v110, v108, s85
v_mfma_f32_16x16x32_bf16 v[192:195], v[16:19], a[164:167], v[192:195]
v_perm_b32 v28, v114, v112, s85
v_perm_b32 v29, v118, v116, s85
v_mfma_f32_16x16x32_bf16 v[192:195], v[20:23], a[184:187], v[192:195]
v_perm_b32 v30, v122, v120, s85
v_perm_b32 v31, v126, v124, s85
v_mfma_f32_16x16x32_bf16 v[208:211], v[16:19], a[168:171], v[208:211]
ds_read_b128 a[200:203], v3 offset:22528
v_mfma_f32_16x16x32_bf16 v[208:211], v[20:23], a[188:191], v[208:211]
ds_read_b128 a[220:223], v3 offset:27648
v_mfma_f32_16x16x32_bf16 v[224:227], v[16:19], a[172:175], v[224:227]
buffer_load_dwordx4 v[60:63], v6, s[52:55], s94 offen
v_mfma_f32_16x16x32_bf16 v[224:227], v[20:23], a[192:195], v[224:227]
s_add_u32 s94, s94, 128
s_cmp_eq_u32 s94, 896
s_cselect_b32 s94, 0, s94
v_mfma_f32_16x16x32_bf16 v[240:243], v[16:19], a[176:179], v[240:243]
ds_read_b128 a[204:207], v3 offset:23552
s_waitcnt lgkmcnt(3)
v_mfma_f32_16x16x32_bf16 v[240:243], v[20:23], a[196:199], v[240:243]
ds_read_b128 a[224:227], v3 offset:28672
v_mfma_f32_16x16x32_bf16 v[180:183], v[24:27], a[160:163], v[180:183]
v_perm_b32 v16, v99, v97, s84
v_perm_b32 v17, v103, v101, s84
v_mfma_f32_16x16x32_bf16 v[180:183], v[28:31], a[180:183], v[180:183]
v_perm_b32 v18, v107, v105, s84
v_perm_b32 v19, v111, v109, s84
v_mfma_f32_16x16x32_bf16 v[196:199], v[24:27], a[164:167], v[196:199]
v_perm_b32 v20, v115, v113, s84
v_perm_b32 v21, v119, v117, s84
v_mfma_f32_16x16x32_bf16 v[196:199], v[28:31], a[184:187], v[196:199]
v_perm_b32 v22, v123, v121, s84
v_perm_b32 v23, v127, v125, s84
v_mfma_f32_16x16x32_bf16 v[212:215], v[24:27], a[168:171], v[212:215]
buffer_load_dwordx4 v[64:67], v6, s[52:55], s94 offen
v_mfma_f32_16x16x32_bf16 v[212:215], v[28:31], a[188:191], v[212:215]
s_add_u32 s94, s94, 128
s_cmp_eq_u32 s94, 896
s_cselect_b32 s94, 0, s94
ds_read_b128 a[208:211], v3 offset:24576
v_mfma_f32_16x16x32_bf16 v[228:231], v[24:27], a[172:175], v[228:231]
ds_read_b128 a[228:231], v3 offset:29696
v_mfma_f32_16x16x32_bf16 v[228:231], v[28:31], a[192:195], v[228:231]
v_mfma_f32_16x16x32_bf16 v[244:247], v[24:27], a[176:179], v[244:247]
v_mfma_f32_16x16x32_bf16 v[244:247], v[28:31], a[196:199], v[244:247]
buffer_load_dwordx4 v[68:71], v6, s[52:55], s94 offen
v_mfma_f32_16x16x32_bf16 v[184:187], v[16:19], a[160:163], v[184:187]
s_add_u32 s94, s94, 128
s_cmp_eq_u32 s94, 896
s_cselect_b32 s94, 0, s94
v_perm_b32 v24, v99, v97, s85
v_perm_b32 v25, v103, v101, s85
v_mfma_f32_16x16x32_bf16 v[184:187], v[20:23], a[180:183], v[184:187]
v_perm_b32 v26, v107, v105, s85
v_perm_b32 v27, v111, v109, s85
v_mfma_f32_16x16x32_bf16 v[200:203], v[16:19], a[164:167], v[200:203]
v_perm_b32 v28, v115, v113, s85
v_perm_b32 v29, v119, v117, s85
v_mfma_f32_16x16x32_bf16 v[200:203], v[20:23], a[184:187], v[200:203]
v_perm_b32 v30, v123, v121, s85
v_perm_b32 v31, v127, v125, s85
v_mfma_f32_16x16x32_bf16 v[216:219], v[16:19], a[168:171], v[216:219]
ds_read_b128 a[212:215], v3 offset:25600
v_mfma_f32_16x16x32_bf16 v[216:219], v[20:23], a[188:191], v[216:219]
ds_read_b128 a[232:235], v3 offset:30720
v_mfma_f32_16x16x32_bf16 v[232:235], v[16:19], a[172:175], v[232:235]
v_mfma_f32_16x16x32_bf16 v[232:235], v[20:23], a[192:195], v[232:235]
v_mfma_f32_16x16x32_bf16 v[248:251], v[16:19], a[176:179], v[248:251]
ds_read_b128 a[216:219], v3 offset:26624
v_mfma_f32_16x16x32_bf16 v[248:251], v[20:23], a[196:199], v[248:251]
ds_read_b128 a[236:239], v3 offset:31744
v_mfma_f32_16x16x32_bf16 v[188:191], v[24:27], a[160:163], v[188:191]
buffer_load_dwordx4 v[72:75], v6, s[52:55], s94 offen
s_add_u32 s94, s94, 128
s_cmp_eq_u32 s94, 896
s_cselect_b32 s94, 0, s94
v_add_u32 v6, 0x7000, v6
buffer_load_dwordx4 v[96:99], v6, s[52:55], s94 offen
s_add_u32 s94, s94, 128
s_cmp_eq_u32 s94, 896
s_cselect_b32 s94, 0, s94
buffer_load_dwordx4 v[100:103], v6, s[52:55], s94 offen
s_add_u32 s94, s94, 128
s_cmp_eq_u32 s94, 896
s_cselect_b32 s94, 0, s94
buffer_load_dwordx4 v[104:107], v6, s[52:55], s94 offen
s_add_u32 s94, s94, 128
s_cmp_eq_u32 s94, 896
s_cselect_b32 s94, 0, s94
buffer_load_dwordx4 v[108:111], v6, s[52:55], s94 offen
s_add_u32 s94, s94, 128
s_cmp_eq_u32 s94, 896
s_cselect_b32 s94, 0, s94
s_waitcnt vmcnt(11)
v_perm_b32 v16, v34, v32, s84
v_perm_b32 v17, v38, v36, s84
v_mfma_f32_16x16x32_bf16 v[188:191], v[28:31], a[180:183], v[188:191]
v_perm_b32 v18, v42, v40, s84
v_perm_b32 v19, v46, v44, s84
v_mfma_f32_16x16x32_bf16 v[204:207], v[24:27], a[164:167], v[204:207]
v_perm_b32 v20, v82, v80, s84
v_perm_b32 v21, v86, v84, s84
v_mfma_f32_16x16x32_bf16 v[204:207], v[28:31], a[184:187], v[204:207]
v_perm_b32 v22, v90, v88, s84
v_perm_b32 v23, v94, v92, s84
v_mfma_f32_16x16x32_bf16 v[220:223], v[24:27], a[168:171], v[220:223]
buffer_load_dwordx4 v[112:115], v6, s[52:55], s94 offen
v_mfma_f32_16x16x32_bf16 v[220:223], v[28:31], a[188:191], v[220:223]
s_add_u32 s94, s94, 128
s_cmp_eq_u32 s94, 896
s_cselect_b32 s94, 0, s94
v_mfma_f32_16x16x32_bf16 v[236:239], v[24:27], a[172:175], v[236:239]
v_mfma_f32_16x16x32_bf16 v[236:239], v[28:31], a[192:195], v[236:239]
v_mfma_f32_16x16x32_bf16 v[252:255], v[24:27], a[176:179], v[252:255]
buffer_load_dwordx4 v[116:119], v6, s[52:55], s94 offen
v_mfma_f32_16x16x32_bf16 v[252:255], v[28:31], a[196:199], v[252:255]
s_add_u32 s94, s94, 128
s_cmp_eq_u32 s94, 896
s_cselect_b32 s94, 0, s94
s_waitcnt lgkmcnt(1)
v_mfma_f32_16x16x32_bf16 v[176:179], v[16:19], a[200:203], v[176:179]
v_perm_b32 v24, v34, v32, s85
v_perm_b32 v25, v38, v36, s85
v_mfma_f32_16x16x32_bf16 v[176:179], v[20:23], a[220:223], v[176:179]
v_perm_b32 v26, v42, v40, s85
v_perm_b32 v27, v46, v44, s85
v_mfma_f32_16x16x32_bf16 v[192:195], v[16:19], a[204:207], v[192:195]
v_perm_b32 v28, v82, v80, s85
v_perm_b32 v29, v86, v84, s85
v_mfma_f32_16x16x32_bf16 v[192:195], v[20:23], a[224:227], v[192:195]
v_perm_b32 v30, v90, v88, s85
v_perm_b32 v31, v94, v92, s85
v_mfma_f32_16x16x32_bf16 v[208:211], v[16:19], a[208:211], v[208:211]
v_mfma_f32_16x16x32_bf16 v[208:211], v[20:23], a[228:231], v[208:211]
v_mfma_f32_16x16x32_bf16 v[224:227], v[16:19], a[212:215], v[224:227]
buffer_load_dwordx4 v[120:123], v6, s[52:55], s94 offen
v_mfma_f32_16x16x32_bf16 v[224:227], v[20:23], a[232:235], v[224:227]
s_add_u32 s94, s94, 128
s_cmp_eq_u32 s94, 896
s_cselect_b32 s94, 0, s94
v_mfma_f32_16x16x32_bf16 v[240:243], v[16:19], a[216:219], v[240:243]
v_add_u32 v6, 0x7000, v6
s_waitcnt lgkmcnt(0)
v_mfma_f32_16x16x32_bf16 v[240:243], v[20:23], a[236:239], v[240:243]
v_mfma_f32_16x16x32_bf16 v[180:183], v[24:27], a[200:203], v[180:183]
v_perm_b32 v16, v35, v33, s84
v_perm_b32 v17, v39, v37, s84
v_mfma_f32_16x16x32_bf16 v[180:183], v[28:31], a[220:223], v[180:183]
v_perm_b32 v18, v43, v41, s84
v_perm_b32 v19, v47, v45, s84
v_mfma_f32_16x16x32_bf16 v[196:199], v[24:27], a[204:207], v[196:199]
v_perm_b32 v20, v83, v81, s84
v_perm_b32 v21, v87, v85, s84
v_mfma_f32_16x16x32_bf16 v[196:199], v[28:31], a[224:227], v[196:199]
v_perm_b32 v22, v91, v89, s84
v_perm_b32 v23, v95, v93, s84
v_mfma_f32_16x16x32_bf16 v[212:215], v[24:27], a[208:211], v[212:215]
buffer_load_dwordx4 v[128:131], v6, s[52:55], s94 offen
v_mfma_f32_16x16x32_bf16 v[212:215], v[28:31], a[228:231], v[212:215]
s_add_u32 s94, s94, 128
s_cmp_eq_u32 s94, 896
s_cselect_b32 s94, 0, s94
v_mfma_f32_16x16x32_bf16 v[228:231], v[24:27], a[212:215], v[228:231]
v_mfma_f32_16x16x32_bf16 v[228:231], v[28:31], a[232:235], v[228:231]
v_mfma_f32_16x16x32_bf16 v[244:247], v[24:27], a[216:219], v[244:247]
v_mfma_f32_16x16x32_bf16 v[244:247], v[28:31], a[236:239], v[244:247]
v_mfma_f32_16x16x32_bf16 v[184:187], v[16:19], a[200:203], v[184:187]
v_perm_b32 v24, v35, v33, s85
v_perm_b32 v25, v39, v37, s85
v_mfma_f32_16x16x32_bf16 v[184:187], v[20:23], a[220:223], v[184:187]
v_perm_b32 v26, v43, v41, s85
v_perm_b32 v27, v47, v45, s85
v_mfma_f32_16x16x32_bf16 v[200:203], v[16:19], a[204:207], v[200:203]
v_perm_b32 v28, v83, v81, s85
v_perm_b32 v29, v87, v85, s85
v_mfma_f32_16x16x32_bf16 v[200:203], v[20:23], a[224:227], v[200:203]
v_perm_b32 v30, v91, v89, s85
v_perm_b32 v31, v95, v93, s85
v_mfma_f32_16x16x32_bf16 v[216:219], v[16:19], a[208:211], v[216:219]
buffer_load_dwordx4 v[132:135], v6, s[52:55], s94 offen
v_mfma_f32_16x16x32_bf16 v[216:219], v[20:23], a[228:231], v[216:219]
s_add_u32 s94, s94, 128
s_cmp_eq_u32 s94, 896
s_cselect_b32 s94, 0, s94
v_mfma_f32_16x16x32_bf16 v[232:235], v[16:19], a[212:215], v[232:235]
v_mfma_f32_16x16x32_bf16 v[232:235], v[20:23], a[232:235], v[232:235]
v_mfma_f32_16x16x32_bf16 v[248:251], v[16:19], a[216:219], v[248:251]
v_mfma_f32_16x16x32_bf16 v[248:251], v[20:23], a[236:239], v[248:251]
v_mfma_f32_16x16x32_bf16 v[188:191], v[24:27], a[200:203], v[188:191]
buffer_load_dwordx4 v[136:139], v6, s[52:55], s94 offen
v_mfma_f32_16x16x32_bf16 v[188:191], v[28:31], a[220:223], v[188:191]
s_add_u32 s94, s94, 128
s_cmp_eq_u32 s94, 896
)"
R"(s_cselect_b32 s94, 0, s94
v_mfma_f32_16x16x32_bf16 v[204:207], v[24:27], a[204:207], v[204:207]
v_mfma_f32_16x16x32_bf16 v[204:207], v[28:31], a[224:227], v[204:207]
v_mfma_f32_16x16x32_bf16 v[220:223], v[24:27], a[208:211], v[220:223]
v_mfma_f32_16x16x32_bf16 v[220:223], v[28:31], a[228:231], v[220:223]
v_mfma_f32_16x16x32_bf16 v[236:239], v[24:27], a[212:215], v[236:239]
v_mfma_f32_16x16x32_bf16 v[236:239], v[28:31], a[232:235], v[236:239]
v_mfma_f32_16x16x32_bf16 v[252:255], v[24:27], a[216:219], v[252:255]
buffer_load_dwordx4 v[140:143], v6, s[52:55], s94 offen
v_mfma_f32_16x16x32_bf16 v[252:255], v[28:31], a[236:239], v[252:255]
s_add_u32 s94, s94, 128
s_cmp_eq_u32 s94, 896
s_cselect_b32 s94, 0, s94
s_mov_b64 vcc, s[76:77]
ds_read_b128 v[16:19], v10 offset:42240
ds_read_b128 v[20:23], v10 offset:42496
ds_read_b128 v[24:27], v10 offset:42752
ds_read_b128 v[28:31], v10 offset:43008
s_waitcnt lgkmcnt(3)
v_add_f32 v16, v16, v17
v_add_f32 v18, v18, v19
v_add_f32 v16, v16, v18
v_rcp_f32 v11, v16
ds_read_b128 v[16:19], v10 offset:43264
s_waitcnt lgkmcnt(3)
v_add_f32 v20, v20, v21
v_add_f32 v22, v22, v23
v_add_f32 v20, v20, v22
v_rcp_f32 v12, v20
s_waitcnt lgkmcnt(2)
v_add_f32 v24, v24, v25
v_add_f32 v26, v26, v27
v_add_f32 v24, v24, v26
v_rcp_f32 v13, v24
s_waitcnt lgkmcnt(1)
v_add_f32 v28, v28, v29
v_add_f32 v30, v30, v31
v_add_f32 v28, v28, v30
v_rcp_f32 v14, v28
s_waitcnt lgkmcnt(0)
v_add_f32 v16, v16, v17
v_add_f32 v18, v18, v19
v_add_f32 v16, v16, v18
v_rcp_f32 v15, v16
v_accvgpr_read_b32 v9, a246
s_barrier
v_mov_b32 v24, v11
v_pk_mul_f32 v[176:177], v[176:177], v[24:25] op_sel_hi:[1,0]
v_pk_mul_f32 v[178:179], v[178:179], v[24:25] op_sel_hi:[1,0]
v_pk_mul_f32 v[180:181], v[180:181], v[24:25] op_sel_hi:[1,0]
v_pk_mul_f32 v[182:183], v[182:183], v[24:25] op_sel_hi:[1,0]
v_pk_mul_f32 v[184:185], v[184:185], v[24:25] op_sel_hi:[1,0]
v_pk_mul_f32 v[186:187], v[186:187], v[24:25] op_sel_hi:[1,0]
v_pk_mul_f32 v[188:189], v[188:189], v[24:25] op_sel_hi:[1,0]
v_pk_mul_f32 v[190:191], v[190:191], v[24:25] op_sel_hi:[1,0]
v_cvt_pk_bf16_f32 v16, v176, v180
v_cvt_pk_bf16_f32 v17, v184, v188
v_cvt_pk_bf16_f32 v18, v177, v181
buffer_load_dwordx4 v[32:35], v6, s[52:55], s94 offen
s_add_u32 s94, s94, 128
s_cmp_eq_u32 s94, 896
s_cselect_b32 s94, 0, s94
buffer_load_dwordx4 v[36:39], v6, s[52:55], s94 offen
s_add_u32 s94, s94, 128
s_cmp_eq_u32 s94, 896
s_cselect_b32 s94, 0, s94
v_cvt_pk_bf16_f32 v19, v185, v189
v_cvt_pk_bf16_f32 v20, v178, v182
v_cvt_pk_bf16_f32 v21, v186, v190
v_cvt_pk_bf16_f32 v22, v179, v183
v_cvt_pk_bf16_f32 v23, v187, v191
ds_write_b128 v9, v[16:19] offset:40960
ds_write_b128 v9, v[20:23] offset:41216
v_mov_b32 v24, v12
v_pk_mul_f32 v[192:193], v[192:193], v[24:25] op_sel_hi:[1,0]
v_pk_mul_f32 v[194:195], v[194:195], v[24:25] op_sel_hi:[1,0]
v_pk_mul_f32 v[196:197], v[196:197], v[24:25] op_sel_hi:[1,0]
v_pk_mul_f32 v[198:199], v[198:199], v[24:25] op_sel_hi:[1,0]
v_pk_mul_f32 v[200:201], v[200:201], v[24:25] op_sel_hi:[1,0]
v_pk_mul_f32 v[202:203], v[202:203], v[24:25] op_sel_hi:[1,0]
v_pk_mul_f32 v[204:205], v[204:205], v[24:25] op_sel_hi:[1,0]
v_pk_mul_f32 v[206:207], v[206:207], v[24:25] op_sel_hi:[1,0]
v_cvt_pk_bf16_f32 v16, v192, v196
v_cvt_pk_bf16_f32 v17, v200, v204
v_cvt_pk_bf16_f32 v18, v193, v197
v_cvt_pk_bf16_f32 v19, v201, v205
v_cvt_pk_bf16_f32 v20, v194, v198
v_cvt_pk_bf16_f32 v21, v202, v206
v_cvt_pk_bf16_f32 v22, v195, v199
v_cvt_pk_bf16_f32 v23, v203, v207
ds_write_b128 v9, v[16:19] offset:41984
ds_write_b128 v9, v[20:23] offset:42240
v_mov_b32 v24, v13
v_pk_mul_f32 v[208:209], v[208:209], v[24:25] op_sel_hi:[1,0]
v_pk_mul_f32 v[210:211], v[210:211], v[24:25] op_sel_hi:[1,0]
v_pk_mul_f32 v[212:213], v[212:213], v[24:25] op_sel_hi:[1,0]
v_pk_mul_f32 v[214:215], v[214:215], v[24:25] op_sel_hi:[1,0]
v_pk_mul_f32 v[216:217], v[216:217], v[24:25] op_sel_hi:[1,0]
v_pk_mul_f32 v[218:219], v[218:219], v[24:25] op_sel_hi:[1,0]
v_pk_mul_f32 v[220:221], v[220:221], v[24:25] op_sel_hi:[1,0]
buffer_load_dwordx4 v[40:43], v6, s[52:55], s94 offen
s_add_u32 s94, s94, 128
s_cmp_eq_u32 s94, 896
s_cselect_b32 s94, 0, s94
s_add_u32 s82, s95, 0
s_and_b32 s82, s82, 7
s_mul_i32 s82, s82, 28672
s_add_u32 s82, s82, 229376
buffer_load_dwordx4 v[80:83], v7, s[48:51], s82 offen
v_pk_mul_f32 v[222:223], v[222:223], v[24:25] op_sel_hi:[1,0]
v_cvt_pk_bf16_f32 v16, v208, v212
v_cvt_pk_bf16_f32 v17, v216, v220
v_cvt_pk_bf16_f32 v18, v209, v213
v_cvt_pk_bf16_f32 v19, v217, v221
v_cvt_pk_bf16_f32 v20, v210, v214
v_cvt_pk_bf16_f32 v21, v218, v222
v_cvt_pk_bf16_f32 v22, v211, v215
v_cvt_pk_bf16_f32 v23, v219, v223
ds_write_b128 v9, v[16:19] offset:43008
ds_write_b128 v9, v[20:23] offset:43264
v_mov_b32 v24, v14
v_pk_mul_f32 v[224:225], v[224:225], v[24:25] op_sel_hi:[1,0]
v_pk_mul_f32 v[226:227], v[226:227], v[24:25] op_sel_hi:[1,0]
v_pk_mul_f32 v[228:229], v[228:229], v[24:25] op_sel_hi:[1,0]
v_pk_mul_f32 v[230:231], v[230:231], v[24:25] op_sel_hi:[1,0]
v_pk_mul_f32 v[232:233], v[232:233], v[24:25] op_sel_hi:[1,0]
v_pk_mul_f32 v[234:235], v[234:235], v[24:25] op_sel_hi:[1,0]
v_pk_mul_f32 v[236:237], v[236:237], v[24:25] op_sel_hi:[1,0]
v_pk_mul_f32 v[238:239], v[238:239], v[24:25] op_sel_hi:[1,0]
v_cvt_pk_bf16_f32 v16, v224, v228
v_cvt_pk_bf16_f32 v17, v232, v236
v_cvt_pk_bf16_f32 v18, v225, v229
v_cvt_pk_bf16_f32 v19, v233, v237
v_cvt_pk_bf16_f32 v20, v226, v230
v_cvt_pk_bf16_f32 v21, v234, v238
v_cvt_pk_bf16_f32 v22, v227, v231
v_cvt_pk_bf16_f32 v23, v235, v239
ds_write_b128 v9, v[16:19] offset:44032
ds_write_b128 v9, v[20:23] offset:44288
v_mov_b32 v24, v15
v_pk_mul_f32 v[240:241], v[240:241], v[24:25] op_sel_hi:[1,0]
v_pk_mul_f32 v[242:243], v[242:243], v[24:25] op_sel_hi:[1,0]
v_pk_mul_f32 v[244:245], v[244:245], v[24:25] op_sel_hi:[1,0]
buffer_load_dwordx4 v[84:87], v7, s[48:51], s82 offen offset:896
buffer_load_dwordx4 v[88:91], v7, s[48:51], s82 offen offset:1792
v_pk_mul_f32 v[246:247], v[246:247], v[24:25] op_sel_hi:[1,0]
v_pk_mul_f32 v[248:249], v[248:249], v[24:25] op_sel_hi:[1,0]
v_pk_mul_f32 v[250:251], v[250:251], v[24:25] op_sel_hi:[1,0]
v_pk_mul_f32 v[252:253], v[252:253], v[24:25] op_sel_hi:[1,0]
v_pk_mul_f32 v[254:255], v[254:255], v[24:25] op_sel_hi:[1,0]
v_cvt_pk_bf16_f32 v16, v240, v244
v_cvt_pk_bf16_f32 v17, v248, v252
v_cvt_pk_bf16_f32 v18, v241, v245
v_cvt_pk_bf16_f32 v19, v249, v253
v_cvt_pk_bf16_f32 v20, v242, v246
v_cvt_pk_bf16_f32 v21, v250, v254
v_cvt_pk_bf16_f32 v22, v243, v247
v_cvt_pk_bf16_f32 v23, v251, v255
ds_write_b128 v9, v[16:19] offset:45056
ds_write_b128 v9, v[20:23] offset:45312
buffer_load_dwordx4 v[92:95], v7, s[48:51], s82 offen offset:2688
s_add_u32 s83, s82, 3584
buffer_load_dwordx4 v[144:147], v7, s[48:51], s83 offen
buffer_load_dwordx4 v[148:151], v7, s[48:51], s83 offen offset:896
buffer_load_dwordx4 v[152:155], v7, s[48:51], s83 offen offset:1792
buffer_load_dwordx4 v[156:159], v7, s[48:51], s83 offen offset:2688
s_add_u32 s82, s95, 1
s_and_b32 s82, s82, 7
s_mul_i32 s82, s82, 28672
s_add_u32 s82, s82, 229376
buffer_load_dwordx4 v[160:163], v7, s[48:51], s82 offen
buffer_load_dwordx4 v[164:167], v7, s[48:51], s82 offen offset:896
buffer_load_dwordx4 v[168:171], v7, s[48:51], s82 offen offset:1792
buffer_load_dwordx4 v[172:175], v7, s[48:51], s82 offen offset:2688
s_add_u32 s83, s82, 3584
buffer_load_dwordx4 v[176:179], v7, s[48:51], s83 offen
buffer_load_dwordx4 v[180:183], v7, s[48:51], s83 offen offset:896
buffer_load_dwordx4 v[184:187], v7, s[48:51], s83 offen offset:1792
buffer_load_dwordx4 v[188:191], v7, s[48:51], s83 offen offset:2688
s_add_u32 s82, s95, 2
s_and_b32 s82, s82, 7
s_mul_i32 s82, s82, 28672
s_add_u32 s82, s82, 229376
buffer_load_dwordx4 v[192:195], v7, s[48:51], s82 offen
buffer_load_dwordx4 v[196:199], v7, s[48:51], s82 offen offset:896
buffer_load_dwordx4 v[200:203], v7, s[48:51], s82 offen offset:1792
s_waitcnt vmcnt(11)
v_perm_b32 v16, v84, v80, s84
v_perm_b32 v17, v92, v88, s84
v_perm_b32 v18, v148, v144, s84
v_perm_b32 v19, v156, v152, s84
buffer_load_dwordx4 v[204:207], v7, s[48:51], s82 offen offset:2688
s_add_u32 s83, s82, 3584
buffer_load_dwordx4 v[208:211], v7, s[48:51], s83 offen
buffer_load_dwordx4 v[212:215], v7, s[48:51], s83 offen offset:896
buffer_load_dwordx4 v[216:219], v7, s[48:51], s83 offen offset:1792
buffer_load_dwordx4 v[220:223], v7, s[48:51], s83 offen offset:2688
s_add_u32 s82, s95, 3
s_and_b32 s82, s82, 7
s_mul_i32 s82, s82, 28672
)"
R"(s_add_u32 s82, s82, 229376
buffer_load_dwordx4 v[224:227], v7, s[48:51], s82 offen
buffer_load_dwordx4 v[228:231], v7, s[48:51], s82 offen offset:896
buffer_load_dwordx4 v[232:235], v7, s[48:51], s82 offen offset:1792
s_waitcnt vmcnt(11)
v_perm_b32 v20, v164, v160, s84
v_perm_b32 v21, v172, v168, s84
v_perm_b32 v22, v180, v176, s84
v_perm_b32 v23, v188, v184, s84
s_waitcnt lgkmcnt(0)
s_barrier
s_cmp_eq_u32 s90, 0
s_cbranch_scc1 LpkB_%=
s_and_b32 s91, s61, 0xfc00
v_and_b32 v9, 0x70, v1
v_lshlrev_b32 v9, 3, v9
v_add_u32 v9, s91, v9
buffer_load_dwordx2 v[14:15], v9, s[56:59], 0 offen sc1
LpkB_%=:
buffer_load_dwordx4 v[236:239], v7, s[48:51], s82 offen offset:2688
s_add_u32 s83, s82, 3584
buffer_load_dwordx4 v[240:243], v7, s[48:51], s83 offen
buffer_load_dwordx4 v[244:247], v7, s[48:51], s83 offen offset:896
ds_read_b128 a[160:163], v1 offset:40960
ds_read_b128 a[164:167], v1 offset:41984
ds_read_b128 a[168:171], v1 offset:43008
ds_read_b128 a[172:175], v1 offset:44032
ds_read_b128 a[176:179], v1 offset:45056
ds_read_b128 a[180:183], v1 offset:46080
ds_read_b128 a[184:187], v1 offset:47104
ds_read_b128 a[188:191], v1 offset:48128
buffer_load_dwordx4 v[248:251], v7, s[48:51], s83 offen offset:1792
buffer_load_dwordx4 v[252:255], v7, s[48:51], s83 offen offset:2688
ds_read_b128 a[192:195], v1 offset:49152
ds_read_b128 a[196:199], v1 offset:50176
s_waitcnt lgkmcnt(5)
v_mfma_f32_16x16x32_bf16 a[0:3], v[16:19], a[160:163], a[0:3]
s_waitcnt lgkmcnt(0)
v_perm_b32 v24, v84, v80, s85
v_perm_b32 v25, v92, v88, s85
v_mfma_f32_16x16x32_bf16 a[0:3], v[20:23], a[180:183], a[0:3]
v_perm_b32 v26, v148, v144, s85
v_perm_b32 v27, v156, v152, s85
v_mfma_f32_16x16x32_bf16 a[4:7], v[16:19], a[164:167], a[4:7]
v_perm_b32 v28, v164, v160, s85
v_perm_b32 v29, v172, v168, s85
v_mfma_f32_16x16x32_bf16 a[4:7], v[20:23], a[184:187], a[4:7]
v_perm_b32 v30, v180, v176, s85
v_perm_b32 v31, v188, v184, s85
v_mfma_f32_16x16x32_bf16 a[8:11], v[16:19], a[168:171], a[8:11]
v_accvgpr_read_b32 v10, a242
v_add_u32 v11, 0xe000, v10
v_mfma_f32_16x16x32_bf16 a[8:11], v[20:23], a[188:191], a[8:11]
v_mfma_f32_16x16x32_bf16 a[12:15], v[16:19], a[172:175], a[12:15]
v_mfma_f32_16x16x32_bf16 a[12:15], v[20:23], a[192:195], a[12:15]
ds_write_b128 v10, v[48:51] offset:0
v_mfma_f32_16x16x32_bf16 a[16:19], v[16:19], a[176:179], a[16:19]
ds_read_b128 a[200:203], v1 offset:51200
v_mfma_f32_16x16x32_bf16 a[16:19], v[20:23], a[196:199], a[16:19]
ds_read_b128 a[220:223], v1 offset:56320
v_mfma_f32_16x16x32_bf16 a[20:23], v[24:27], a[160:163], a[20:23]
v_perm_b32 v16, v85, v81, s84
v_perm_b32 v17, v93, v89, s84
v_mfma_f32_16x16x32_bf16 a[20:23], v[28:31], a[180:183], a[20:23]
v_perm_b32 v18, v149, v145, s84
v_perm_b32 v19, v157, v153, s84
v_mfma_f32_16x16x32_bf16 a[24:27], v[24:27], a[164:167], a[24:27]
v_perm_b32 v20, v165, v161, s84
v_perm_b32 v21, v173, v169, s84
v_mfma_f32_16x16x32_bf16 a[24:27], v[28:31], a[184:187], a[24:27]
v_perm_b32 v22, v181, v177, s84
v_perm_b32 v23, v189, v185, s84
v_mfma_f32_16x16x32_bf16 a[28:31], v[24:27], a[168:171], a[28:31]
ds_write_b128 v10, v[52:55] offset:2048
v_mfma_f32_16x16x32_bf16 a[28:31], v[28:31], a[188:191], a[28:31]
v_mfma_f32_16x16x32_bf16 a[32:35], v[24:27], a[172:175], a[32:35]
v_mfma_f32_16x16x32_bf16 a[32:35], v[28:31], a[192:195], a[32:35]
ds_write_b128 v10, v[56:59] offset:4096
v_mfma_f32_16x16x32_bf16 a[36:39], v[24:27], a[176:179], a[36:39]
ds_write_b128 v10, v[60:63] offset:6144
v_mfma_f32_16x16x32_bf16 a[36:39], v[28:31], a[196:199], a[36:39]
s_add_u32 s82, s95, 4
s_and_b32 s82, s82, 7
s_mul_i32 s82, s82, 28672
s_add_u32 s82, s82, 229376
ds_write_b128 v10, v[64:67] offset:8192
v_mfma_f32_16x16x32_bf16 a[40:43], v[16:19], a[160:163], a[40:43]
v_perm_b32 v24, v85, v81, s85
v_perm_b32 v25, v93, v89, s85
v_mfma_f32_16x16x32_bf16 a[40:43], v[20:23], a[180:183], a[40:43]
v_perm_b32 v26, v149, v145, s85
v_perm_b32 v27, v157, v153, s85
v_mfma_f32_16x16x32_bf16 a[44:47], v[16:19], a[164:167], a[44:47]
v_perm_b32 v28, v165, v161, s85
v_perm_b32 v29, v173, v169, s85
v_mfma_f32_16x16x32_bf16 a[44:47], v[20:23], a[184:187], a[44:47]
v_perm_b32 v30, v181, v177, s85
v_perm_b32 v31, v189, v185, s85
v_mfma_f32_16x16x32_bf16 a[48:51], v[16:19], a[168:171], a[48:51]
s_waitcnt lgkmcnt(6)
buffer_load_dwordx4 v[48:51], v7, s[48:51], s82 offen
ds_read_b128 a[204:207], v1 offset:52224
v_mfma_f32_16x16x32_bf16 a[48:51], v[20:23], a[188:191], a[48:51]
s_waitcnt lgkmcnt(4)
buffer_load_dwordx4 v[52:55], v7, s[48:51], s82 offen offset:896
ds_read_b128 a[224:227], v1 offset:57344
v_mfma_f32_16x16x32_bf16 a[52:55], v[16:19], a[172:175], a[52:55]
s_waitcnt lgkmcnt(4)
buffer_load_dwordx4 v[56:59], v7, s[48:51], s82 offen offset:1792
v_mfma_f32_16x16x32_bf16 a[52:55], v[20:23], a[192:195], a[52:55]
ds_write_b128 v10, v[68:71] offset:10240
v_mfma_f32_16x16x32_bf16 a[56:59], v[16:19], a[176:179], a[56:59]
ds_write_b128 v10, v[72:75] offset:12288
v_mfma_f32_16x16x32_bf16 a[56:59], v[20:23], a[196:199], a[56:59]
ds_write_b128 v10, v[96:99] offset:28672
v_mfma_f32_16x16x32_bf16 a[60:63], v[24:27], a[160:163], a[60:63]
v_perm_b32 v16, v86, v82, s84
v_perm_b32 v17, v94, v90, s84
v_mfma_f32_16x16x32_bf16 a[60:63], v[28:31], a[180:183], a[60:63]
v_perm_b32 v18, v150, v146, s84
v_perm_b32 v19, v158, v154, s84
v_mfma_f32_16x16x32_bf16 a[64:67], v[24:27], a[164:167], a[64:67]
v_perm_b32 v20, v166, v162, s84
v_perm_b32 v21, v174, v170, s84
v_mfma_f32_16x16x32_bf16 a[64:67], v[28:31], a[184:187], a[64:67]
v_perm_b32 v22, v182, v178, s84
v_perm_b32 v23, v190, v186, s84
v_mfma_f32_16x16x32_bf16 a[68:71], v[24:27], a[168:171], a[68:71]
ds_write_b128 v10, v[100:103] offset:30720
v_mfma_f32_16x16x32_bf16 a[68:71], v[28:31], a[188:191], a[68:71]
s_waitcnt lgkmcnt(7)
buffer_load_dwordx4 v[60:63], v7, s[48:51], s82 offen offset:2688
v_mfma_f32_16x16x32_bf16 a[72:75], v[24:27], a[172:175], a[72:75]
s_add_u32 s83, s82, 3584
s_waitcnt lgkmcnt(6)
buffer_load_dwordx4 v[64:67], v7, s[48:51], s83 offen
ds_read_b128 a[208:211], v1 offset:53248
v_mfma_f32_16x16x32_bf16 a[72:75], v[28:31], a[192:195], a[72:75]
s_waitcnt lgkmcnt(4)
buffer_load_dwordx4 v[68:71], v7, s[48:51], s83 offen offset:896
ds_read_b128 a[228:231], v1 offset:58368
v_mfma_f32_16x16x32_bf16 a[76:79], v[24:27], a[176:179], a[76:79]
ds_write_b128 v10, v[104:107] offset:32768
v_mfma_f32_16x16x32_bf16 a[76:79], v[28:31], a[196:199], a[76:79]
ds_write_b128 v10, v[108:111] offset:34816
v_mfma_f32_16x16x32_bf16 a[80:83], v[16:19], a[160:163], a[80:83]
v_perm_b32 v24, v86, v82, s85
v_perm_b32 v25, v94, v90, s85
v_mfma_f32_16x16x32_bf16 a[80:83], v[20:23], a[180:183], a[80:83]
v_perm_b32 v26, v150, v146, s85
v_perm_b32 v27, v158, v154, s85
v_mfma_f32_16x16x32_bf16 a[84:87], v[16:19], a[164:167], a[84:87]
v_perm_b32 v28, v166, v162, s85
v_perm_b32 v29, v174, v170, s85
v_mfma_f32_16x16x32_bf16 a[84:87], v[20:23], a[184:187], a[84:87]
v_perm_b32 v30, v182, v178, s85
v_perm_b32 v31, v190, v186, s85
v_mfma_f32_16x16x32_bf16 a[88:91], v[16:19], a[168:171], a[88:91]
ds_write_b128 v10, v[112:115] offset:36864
v_mfma_f32_16x16x32_bf16 a[88:91], v[20:23], a[188:191], a[88:91]
s_waitcnt lgkmcnt(7)
buffer_load_dwordx4 v[72:75], v7, s[48:51], s83 offen offset:1792
v_mfma_f32_16x16x32_bf16 a[92:95], v[16:19], a[172:175], a[92:95]
buffer_load_dwordx4 v[76:79], v7, s[48:51], s83 offen offset:2688
v_mfma_f32_16x16x32_bf16 a[92:95], v[20:23], a[192:195], a[92:95]
s_add_u32 s82, s95, 5
s_and_b32 s82, s82, 7
s_mul_i32 s82, s82, 28672
s_add_u32 s82, s82, 229376
ds_write_b128 v10, v[116:119] offset:38912
v_mfma_f32_16x16x32_bf16 a[96:99], v[16:19], a[176:179], a[96:99]
ds_write_b128 v10, v[120:123] offset:40960
v_mfma_f32_16x16x32_bf16 a[96:99], v[20:23], a[196:199], a[96:99]
ds_write_b128 v11, v[128:131] offset:0
v_mfma_f32_16x16x32_bf16 a[100:103], v[24:27], a[160:163], a[100:103]
v_perm_b32 v16, v87, v83, s84
v_perm_b32 v17, v95, v91, s84
v_mfma_f32_16x16x32_bf16 a[100:103], v[28:31], a[180:183], a[100:103]
v_perm_b32 v18, v151, v147, s84
v_perm_b32 v19, v159, v155, s84
v_mfma_f32_16x16x32_bf16 a[104:107], v[24:27], a[164:167], a[104:107]
ds_read_b128 a[212:215], v1 offset:54272
v_perm_b32 v20, v167, v163, s84
v_mfma_f32_16x16x32_bf16 a[104:107], v[28:31], a[184:187], a[104:107]
ds_read_b128 a[232:235], v1 offset:59392
v_perm_b32 v21, v175, v171, s84
v_mfma_f32_16x16x32_bf16 a[108:111], v[24:27], a[168:171], a[108:111]
v_perm_b32 v22, v183, v179, s84
v_perm_b32 v23, v191, v187, s84
v_mfma_f32_16x16x32_bf16 a[108:111], v[28:31], a[188:191], a[108:111]
s_waitcnt lgkmcnt(11)
buffer_load_dwordx4 v[96:99], v7, s[48:51], s82 offen
v_mfma_f32_16x16x32_bf16 a[112:115], v[24:27], a[172:175], a[112:115]
s_waitcnt lgkmcnt(10)
buffer_load_dwordx4 v[100:103], v7, s[48:51], s82 offen offset:896
v_mfma_f32_16x16x32_bf16 a[112:115], v[28:31], a[192:195], a[112:115]
ds_write_b128 v11, v[132:135] offset:2048
v_mfma_f32_16x16x32_bf16 a[116:119], v[24:27], a[176:179], a[116:119]
ds_write_b128 v11, v[136:139] offset:4096
v_mfma_f32_16x16x32_bf16 a[116:119], v[28:31], a[196:199], a[116:119]
)"
R"(ds_write_b128 v11, v[140:143] offset:6144
v_mfma_f32_16x16x32_bf16 a[120:123], v[16:19], a[160:163], a[120:123]
v_perm_b32 v24, v87, v83, s85
v_perm_b32 v25, v95, v91, s85
v_mfma_f32_16x16x32_bf16 a[120:123], v[20:23], a[180:183], a[120:123]
v_perm_b32 v26, v151, v147, s85
v_perm_b32 v27, v159, v155, s85
v_mfma_f32_16x16x32_bf16 a[124:127], v[16:19], a[164:167], a[124:127]
v_perm_b32 v28, v167, v163, s85
v_perm_b32 v29, v175, v171, s85
v_mfma_f32_16x16x32_bf16 a[124:127], v[20:23], a[184:187], a[124:127]
v_perm_b32 v30, v183, v179, s85
v_perm_b32 v31, v191, v187, s85
v_mfma_f32_16x16x32_bf16 a[128:131], v[16:19], a[168:171], a[128:131]
s_waitcnt lgkmcnt(10)
buffer_load_dwordx4 v[104:107], v7, s[48:51], s82 offen offset:1792
ds_read_b128 a[216:219], v1 offset:55296
v_mfma_f32_16x16x32_bf16 a[128:131], v[20:23], a[188:191], a[128:131]
s_waitcnt lgkmcnt(10)
buffer_load_dwordx4 v[108:111], v7, s[48:51], s82 offen offset:2688
ds_read_b128 a[236:239], v1 offset:60416
v_mfma_f32_16x16x32_bf16 a[132:135], v[16:19], a[172:175], a[132:135]
s_add_u32 s83, s82, 3584
v_mfma_f32_16x16x32_bf16 a[132:135], v[20:23], a[192:195], a[132:135]
ds_write_b128 v11, v[32:35] offset:8192
v_mfma_f32_16x16x32_bf16 a[136:139], v[16:19], a[176:179], a[136:139]
ds_write_b128 v11, v[36:39] offset:10240
v_mfma_f32_16x16x32_bf16 a[136:139], v[20:23], a[196:199], a[136:139]
ds_write_b128 v11, v[40:43] offset:12288
v_mfma_f32_16x16x32_bf16 a[140:143], v[24:27], a[160:163], a[140:143]
s_waitcnt vmcnt(20)
v_perm_b32 v16, v196, v192, s84
v_perm_b32 v17, v204, v200, s84
v_mfma_f32_16x16x32_bf16 a[140:143], v[28:31], a[180:183], a[140:143]
v_perm_b32 v18, v212, v208, s84
v_perm_b32 v19, v220, v216, s84
v_mfma_f32_16x16x32_bf16 a[144:147], v[24:27], a[164:167], a[144:147]
s_waitcnt vmcnt(12)
v_perm_b32 v20, v228, v224, s84
v_perm_b32 v21, v236, v232, s84
v_mfma_f32_16x16x32_bf16 a[144:147], v[28:31], a[184:187], a[144:147]
v_perm_b32 v22, v244, v240, s84
v_perm_b32 v23, v252, v248, s84
v_mfma_f32_16x16x32_bf16 a[148:151], v[24:27], a[168:171], a[148:151]
s_waitcnt lgkmcnt(13)
buffer_load_dwordx4 v[112:115], v7, s[48:51], s83 offen
v_mfma_f32_16x16x32_bf16 a[148:151], v[28:31], a[188:191], a[148:151]
s_waitcnt lgkmcnt(12)
buffer_load_dwordx4 v[116:119], v7, s[48:51], s83 offen offset:896
v_mfma_f32_16x16x32_bf16 a[152:155], v[24:27], a[172:175], a[152:155]
s_waitcnt lgkmcnt(11)
buffer_load_dwordx4 v[120:123], v7, s[48:51], s83 offen offset:1792
v_mfma_f32_16x16x32_bf16 a[152:155], v[28:31], a[192:195], a[152:155]
buffer_load_dwordx4 v[124:127], v7, s[48:51], s83 offen offset:2688
v_mfma_f32_16x16x32_bf16 a[156:159], v[24:27], a[176:179], a[156:159]
s_add_u32 s82, s95, 6
s_and_b32 s82, s82, 7
s_mul_i32 s82, s82, 28672
s_add_u32 s82, s82, 229376
v_mfma_f32_16x16x32_bf16 a[156:159], v[28:31], a[196:199], a[156:159]
s_waitcnt lgkmcnt(2)
buffer_load_dwordx4 v[32:35], v7, s[48:51], s82 offen
v_mfma_f32_16x16x32_bf16 a[0:3], v[16:19], a[200:203], a[0:3]
v_perm_b32 v24, v196, v192, s85
v_perm_b32 v25, v204, v200, s85
v_mfma_f32_16x16x32_bf16 a[0:3], v[20:23], a[220:223], a[0:3]
v_perm_b32 v26, v212, v208, s85
v_perm_b32 v27, v220, v216, s85
v_mfma_f32_16x16x32_bf16 a[4:7], v[16:19], a[204:207], a[4:7]
v_perm_b32 v28, v228, v224, s85
v_perm_b32 v29, v236, v232, s85
v_mfma_f32_16x16x32_bf16 a[4:7], v[20:23], a[224:227], a[4:7]
v_perm_b32 v30, v244, v240, s85
v_perm_b32 v31, v252, v248, s85
v_mfma_f32_16x16x32_bf16 a[8:11], v[16:19], a[208:211], a[8:11]
s_waitcnt lgkmcnt(1)
buffer_load_dwordx4 v[36:39], v7, s[48:51], s82 offen offset:896
v_mfma_f32_16x16x32_bf16 a[8:11], v[20:23], a[228:231], a[8:11]
s_waitcnt lgkmcnt(0)
buffer_load_dwordx4 v[40:43], v7, s[48:51], s82 offen offset:1792
v_mfma_f32_16x16x32_bf16 a[12:15], v[16:19], a[212:215], a[12:15]
ds_read_b128 a[160:163], v1 offset:61440
v_mfma_f32_16x16x32_bf16 a[12:15], v[20:23], a[232:235], a[12:15]
ds_read_b128 a[180:183], v2 offset:1024
v_mfma_f32_16x16x32_bf16 a[16:19], v[16:19], a[216:219], a[16:19]
buffer_load_dwordx4 v[44:47], v7, s[48:51], s82 offen offset:2688
v_mfma_f32_16x16x32_bf16 a[16:19], v[20:23], a[236:239], a[16:19]
s_add_u32 s83, s82, 3584
v_mfma_f32_16x16x32_bf16 a[20:23], v[24:27], a[200:203], a[20:23]
v_perm_b32 v16, v197, v193, s84
v_perm_b32 v17, v205, v201, s84
v_mfma_f32_16x16x32_bf16 a[20:23], v[28:31], a[220:223], a[20:23]
v_perm_b32 v18, v213, v209, s84
v_perm_b32 v19, v221, v217, s84
v_mfma_f32_16x16x32_bf16 a[24:27], v[24:27], a[204:207], a[24:27]
v_perm_b32 v20, v229, v225, s84
v_perm_b32 v21, v237, v233, s84
v_mfma_f32_16x16x32_bf16 a[24:27], v[28:31], a[224:227], a[24:27]
v_perm_b32 v22, v245, v241, s84
v_perm_b32 v23, v253, v249, s84
v_mfma_f32_16x16x32_bf16 a[28:31], v[24:27], a[208:211], a[28:31]
buffer_load_dwordx4 v[80:83], v7, s[48:51], s83 offen
v_mfma_f32_16x16x32_bf16 a[28:31], v[28:31], a[228:231], a[28:31]
buffer_load_dwordx4 v[84:87], v7, s[48:51], s83 offen offset:896
v_mfma_f32_16x16x32_bf16 a[32:35], v[24:27], a[212:215], a[32:35]
buffer_load_dwordx4 v[88:91], v7, s[48:51], s83 offen offset:1792
v_mfma_f32_16x16x32_bf16 a[32:35], v[28:31], a[232:235], a[32:35]
buffer_load_dwordx4 v[92:95], v7, s[48:51], s83 offen offset:2688
v_mfma_f32_16x16x32_bf16 a[36:39], v[24:27], a[216:219], a[36:39]
s_add_u32 s82, s95, 7
s_and_b32 s82, s82, 7
s_mul_i32 s82, s82, 28672
s_add_u32 s82, s82, 229376
v_mfma_f32_16x16x32_bf16 a[36:39], v[28:31], a[236:239], a[36:39]
v_mfma_f32_16x16x32_bf16 a[40:43], v[16:19], a[200:203], a[40:43]
v_perm_b32 v24, v197, v193, s85
v_perm_b32 v25, v205, v201, s85
v_mfma_f32_16x16x32_bf16 a[40:43], v[20:23], a[220:223], a[40:43]
v_perm_b32 v26, v213, v209, s85
v_perm_b32 v27, v221, v217, s85
v_mfma_f32_16x16x32_bf16 a[44:47], v[16:19], a[204:207], a[44:47]
ds_read_b128 a[164:167], v1 offset:62464
v_perm_b32 v28, v229, v225, s85
v_mfma_f32_16x16x32_bf16 a[44:47], v[20:23], a[224:227], a[44:47]
ds_read_b128 a[184:187], v2 offset:2048
v_perm_b32 v29, v237, v233, s85
v_mfma_f32_16x16x32_bf16 a[48:51], v[16:19], a[208:211], a[48:51]
v_perm_b32 v30, v245, v241, s85
v_perm_b32 v31, v253, v249, s85
v_mfma_f32_16x16x32_bf16 a[48:51], v[20:23], a[228:231], a[48:51]
buffer_load_dwordx4 v[128:131], v7, s[48:51], s82 offen
v_mfma_f32_16x16x32_bf16 a[52:55], v[16:19], a[212:215], a[52:55]
buffer_load_dwordx4 v[132:135], v7, s[48:51], s82 offen offset:896
v_mfma_f32_16x16x32_bf16 a[52:55], v[20:23], a[232:235], a[52:55]
v_mfma_f32_16x16x32_bf16 a[56:59], v[16:19], a[216:219], a[56:59]
v_mfma_f32_16x16x32_bf16 a[56:59], v[20:23], a[236:239], a[56:59]
buffer_load_dwordx4 v[136:139], v7, s[48:51], s82 offen offset:1792
v_mfma_f32_16x16x32_bf16 a[60:63], v[24:27], a[200:203], a[60:63]
v_perm_b32 v16, v198, v194, s84
v_perm_b32 v17, v206, v202, s84
v_mfma_f32_16x16x32_bf16 a[60:63], v[28:31], a[220:223], a[60:63]
v_perm_b32 v18, v214, v210, s84
v_perm_b32 v19, v222, v218, s84
v_mfma_f32_16x16x32_bf16 a[64:67], v[24:27], a[204:207], a[64:67]
v_perm_b32 v20, v230, v226, s84
v_perm_b32 v21, v238, v234, s84
v_mfma_f32_16x16x32_bf16 a[64:67], v[28:31], a[224:227], a[64:67]
v_perm_b32 v22, v246, v242, s84
v_perm_b32 v23, v254, v250, s84
v_mfma_f32_16x16x32_bf16 a[68:71], v[24:27], a[208:211], a[68:71]
buffer_load_dwordx4 v[140:143], v7, s[48:51], s82 offen offset:2688
v_mfma_f32_16x16x32_bf16 a[68:71], v[28:31], a[228:231], a[68:71]
s_add_u32 s83, s82, 3584
ds_read_b128 a[168:171], v1 offset:63488
v_mfma_f32_16x16x32_bf16 a[72:75], v[24:27], a[212:215], a[72:75]
ds_read_b128 a[188:191], v2 offset:3072
v_mfma_f32_16x16x32_bf16 a[72:75], v[28:31], a[232:235], a[72:75]
buffer_load_dwordx4 v[144:147], v7, s[48:51], s83 offen
v_mfma_f32_16x16x32_bf16 a[76:79], v[24:27], a[216:219], a[76:79]
v_mfma_f32_16x16x32_bf16 a[76:79], v[28:31], a[236:239], a[76:79]
buffer_load_dwordx4 v[148:151], v7, s[48:51], s83 offen offset:896
v_mfma_f32_16x16x32_bf16 a[80:83], v[16:19], a[200:203], a[80:83]
v_perm_b32 v24, v198, v194, s85
v_perm_b32 v25, v206, v202, s85
v_mfma_f32_16x16x32_bf16 a[80:83], v[20:23], a[220:223], a[80:83]
v_perm_b32 v26, v214, v210, s85
v_perm_b32 v27, v222, v218, s85
v_mfma_f32_16x16x32_bf16 a[84:87], v[16:19], a[204:207], a[84:87]
v_perm_b32 v28, v230, v226, s85
v_perm_b32 v29, v238, v234, s85
v_mfma_f32_16x16x32_bf16 a[84:87], v[20:23], a[224:227], a[84:87]
v_perm_b32 v30, v246, v242, s85
v_perm_b32 v31, v254, v250, s85
v_mfma_f32_16x16x32_bf16 a[88:91], v[16:19], a[208:211], a[88:91]
buffer_load_dwordx4 v[152:155], v7, s[48:51], s83 offen offset:1792
v_mfma_f32_16x16x32_bf16 a[88:91], v[20:23], a[228:231], a[88:91]
buffer_load_dwordx4 v[156:159], v7, s[48:51], s83 offen offset:2688
v_mfma_f32_16x16x32_bf16 a[92:95], v[16:19], a[212:215], a[92:95]
v_mfma_f32_16x16x32_bf16 a[92:95], v[20:23], a[232:235], a[92:95]
v_mfma_f32_16x16x32_bf16 a[96:99], v[16:19], a[216:219], a[96:99]
v_mfma_f32_16x16x32_bf16 a[96:99], v[20:23], a[236:239], a[96:99]
v_mfma_f32_16x16x32_bf16 a[100:103], v[24:27], a[200:203], a[100:103]
v_perm_b32 v16, v199, v195, s84
v_perm_b32 v17, v207, v203, s84
v_mfma_f32_16x16x32_bf16 a[100:103], v[28:31], a[220:223], a[100:103]
v_perm_b32 v18, v215, v211, s84
v_perm_b32 v19, v223, v219, s84
v_mfma_f32_16x16x32_bf16 a[104:107], v[24:27], a[204:207], a[104:107]
ds_read_b128 a[172:175], v1 offset:64512
v_perm_b32 v20, v231, v227, s84
v_mfma_f32_16x16x32_bf16 a[104:107], v[28:31], a[224:227], a[104:107]
ds_read_b128 a[192:195], v2 offset:4096
v_perm_b32 v21, v239, v235, s84
v_mfma_f32_16x16x32_bf16 a[108:111], v[24:27], a[208:211], a[108:111]
v_perm_b32 v22, v247, v243, s84
v_perm_b32 v23, v255, v251, s84
v_mfma_f32_16x16x32_bf16 a[108:111], v[28:31], a[228:231], a[108:111]
v_mfma_f32_16x16x32_bf16 a[112:115], v[24:27], a[212:215], a[112:115]
v_mfma_f32_16x16x32_bf16 a[112:115], v[28:31], a[232:235], a[112:115]
v_mfma_f32_16x16x32_bf16 a[116:119], v[24:27], a[216:219], a[116:119]
)"
R"(v_mfma_f32_16x16x32_bf16 a[116:119], v[28:31], a[236:239], a[116:119]
v_mfma_f32_16x16x32_bf16 a[120:123], v[16:19], a[200:203], a[120:123]
v_perm_b32 v24, v199, v195, s85
v_perm_b32 v25, v207, v203, s85
v_mfma_f32_16x16x32_bf16 a[120:123], v[20:23], a[220:223], a[120:123]
v_perm_b32 v26, v215, v211, s85
v_perm_b32 v27, v223, v219, s85
v_mfma_f32_16x16x32_bf16 a[124:127], v[16:19], a[204:207], a[124:127]
v_perm_b32 v28, v231, v227, s85
v_perm_b32 v29, v239, v235, s85
v_mfma_f32_16x16x32_bf16 a[124:127], v[20:23], a[224:227], a[124:127]
v_perm_b32 v30, v247, v243, s85
v_perm_b32 v31, v255, v251, s85
v_mfma_f32_16x16x32_bf16 a[128:131], v[16:19], a[208:211], a[128:131]
ds_read_b128 a[176:179], v2 offset:0
v_mfma_f32_16x16x32_bf16 a[128:131], v[20:23], a[228:231], a[128:131]
ds_read_b128 a[196:199], v2 offset:5120
v_mfma_f32_16x16x32_bf16 a[132:135], v[16:19], a[212:215], a[132:135]
v_mfma_f32_16x16x32_bf16 a[132:135], v[20:23], a[232:235], a[132:135]
v_mfma_f32_16x16x32_bf16 a[136:139], v[16:19], a[216:219], a[136:139]
v_mfma_f32_16x16x32_bf16 a[136:139], v[20:23], a[236:239], a[136:139]
v_mfma_f32_16x16x32_bf16 a[140:143], v[24:27], a[200:203], a[140:143]
s_waitcnt vmcnt(24)
v_perm_b32 v16, v52, v48, s84
v_perm_b32 v17, v60, v56, s84
v_mfma_f32_16x16x32_bf16 a[140:143], v[28:31], a[220:223], a[140:143]
v_perm_b32 v18, v68, v64, s84
v_perm_b32 v19, v76, v72, s84
v_mfma_f32_16x16x32_bf16 a[144:147], v[24:27], a[204:207], a[144:147]
s_waitcnt vmcnt(16)
v_perm_b32 v20, v100, v96, s84
v_perm_b32 v21, v108, v104, s84
v_mfma_f32_16x16x32_bf16 a[144:147], v[28:31], a[224:227], a[144:147]
v_perm_b32 v22, v116, v112, s84
v_perm_b32 v23, v124, v120, s84
v_mfma_f32_16x16x32_bf16 a[148:151], v[24:27], a[208:211], a[148:151]
v_mfma_f32_16x16x32_bf16 a[148:151], v[28:31], a[228:231], a[148:151]
v_mfma_f32_16x16x32_bf16 a[152:155], v[24:27], a[212:215], a[152:155]
v_mfma_f32_16x16x32_bf16 a[152:155], v[28:31], a[232:235], a[152:155]
v_mfma_f32_16x16x32_bf16 a[156:159], v[24:27], a[216:219], a[156:159]
v_mfma_f32_16x16x32_bf16 a[156:159], v[28:31], a[236:239], a[156:159]
s_waitcnt lgkmcnt(1)
v_mfma_f32_16x16x32_bf16 a[0:3], v[16:19], a[160:163], a[0:3]
v_perm_b32 v24, v52, v48, s85
v_perm_b32 v25, v60, v56, s85
v_mfma_f32_16x16x32_bf16 a[0:3], v[20:23], a[180:183], a[0:3]
v_perm_b32 v26, v68, v64, s85
v_perm_b32 v27, v76, v72, s85
v_mfma_f32_16x16x32_bf16 a[4:7], v[16:19], a[164:167], a[4:7]
v_perm_b32 v28, v100, v96, s85
v_perm_b32 v29, v108, v104, s85
v_mfma_f32_16x16x32_bf16 a[4:7], v[20:23], a[184:187], a[4:7]
v_perm_b32 v30, v116, v112, s85
v_perm_b32 v31, v124, v120, s85
v_mfma_f32_16x16x32_bf16 a[8:11], v[16:19], a[168:171], a[8:11]
v_mfma_f32_16x16x32_bf16 a[8:11], v[20:23], a[188:191], a[8:11]
v_mfma_f32_16x16x32_bf16 a[12:15], v[16:19], a[172:175], a[12:15]
ds_read_b128 a[200:203], v2 offset:6144
v_mfma_f32_16x16x32_bf16 a[12:15], v[20:23], a[192:195], a[12:15]
ds_read_b128 a[220:223], v2 offset:11264
v_mfma_f32_16x16x32_bf16 a[16:19], v[16:19], a[176:179], a[16:19]
s_waitcnt lgkmcnt(2)
v_mfma_f32_16x16x32_bf16 a[16:19], v[20:23], a[196:199], a[16:19]
v_mfma_f32_16x16x32_bf16 a[20:23], v[24:27], a[160:163], a[20:23]
v_perm_b32 v16, v53, v49, s84
v_perm_b32 v17, v61, v57, s84
v_mfma_f32_16x16x32_bf16 a[20:23], v[28:31], a[180:183], a[20:23]
v_perm_b32 v18, v69, v65, s84
v_perm_b32 v19, v77, v73, s84
v_mfma_f32_16x16x32_bf16 a[24:27], v[24:27], a[164:167], a[24:27]
v_perm_b32 v20, v101, v97, s84
v_perm_b32 v21, v109, v105, s84
v_mfma_f32_16x16x32_bf16 a[24:27], v[28:31], a[184:187], a[24:27]
v_perm_b32 v22, v117, v113, s84
v_perm_b32 v23, v125, v121, s84
v_mfma_f32_16x16x32_bf16 a[28:31], v[24:27], a[168:171], a[28:31]
v_mfma_f32_16x16x32_bf16 a[28:31], v[28:31], a[188:191], a[28:31]
v_mfma_f32_16x16x32_bf16 a[32:35], v[24:27], a[172:175], a[32:35]
v_mfma_f32_16x16x32_bf16 a[32:35], v[28:31], a[192:195], a[32:35]
v_mfma_f32_16x16x32_bf16 a[36:39], v[24:27], a[176:179], a[36:39]
v_mfma_f32_16x16x32_bf16 a[36:39], v[28:31], a[196:199], a[36:39]
v_mfma_f32_16x16x32_bf16 a[40:43], v[16:19], a[160:163], a[40:43]
v_perm_b32 v24, v53, v49, s85
v_perm_b32 v25, v61, v57, s85
v_mfma_f32_16x16x32_bf16 a[40:43], v[20:23], a[180:183], a[40:43]
v_perm_b32 v26, v69, v65, s85
v_perm_b32 v27, v77, v73, s85
v_mfma_f32_16x16x32_bf16 a[44:47], v[16:19], a[164:167], a[44:47]
ds_read_b128 a[204:207], v2 offset:7168
v_perm_b32 v28, v101, v97, s85
v_mfma_f32_16x16x32_bf16 a[44:47], v[20:23], a[184:187], a[44:47]
ds_read_b128 a[224:227], v2 offset:12288
v_perm_b32 v29, v109, v105, s85
v_mfma_f32_16x16x32_bf16 a[48:51], v[16:19], a[168:171], a[48:51]
v_perm_b32 v30, v117, v113, s85
v_perm_b32 v31, v125, v121, s85
v_mfma_f32_16x16x32_bf16 a[48:51], v[20:23], a[188:191], a[48:51]
v_mfma_f32_16x16x32_bf16 a[52:55], v[16:19], a[172:175], a[52:55]
v_mfma_f32_16x16x32_bf16 a[52:55], v[20:23], a[192:195], a[52:55]
v_mfma_f32_16x16x32_bf16 a[56:59], v[16:19], a[176:179], a[56:59]
v_mfma_f32_16x16x32_bf16 a[56:59], v[20:23], a[196:199], a[56:59]
v_mfma_f32_16x16x32_bf16 a[60:63], v[24:27], a[160:163], a[60:63]
v_perm_b32 v16, v54, v50, s84
v_perm_b32 v17, v62, v58, s84
v_mfma_f32_16x16x32_bf16 a[60:63], v[28:31], a[180:183], a[60:63]
v_perm_b32 v18, v70, v66, s84
v_perm_b32 v19, v78, v74, s84
v_mfma_f32_16x16x32_bf16 a[64:67], v[24:27], a[164:167], a[64:67]
v_perm_b32 v20, v102, v98, s84
v_perm_b32 v21, v110, v106, s84
v_mfma_f32_16x16x32_bf16 a[64:67], v[28:31], a[184:187], a[64:67]
v_perm_b32 v22, v118, v114, s84
v_perm_b32 v23, v126, v122, s84
v_mfma_f32_16x16x32_bf16 a[68:71], v[24:27], a[168:171], a[68:71]
v_mfma_f32_16x16x32_bf16 a[68:71], v[28:31], a[188:191], a[68:71]
ds_read_b128 a[208:211], v2 offset:8192
v_mfma_f32_16x16x32_bf16 a[72:75], v[24:27], a[172:175], a[72:75]
ds_read_b128 a[228:231], v2 offset:13312
v_mfma_f32_16x16x32_bf16 a[72:75], v[28:31], a[192:195], a[72:75]
v_mfma_f32_16x16x32_bf16 a[76:79], v[24:27], a[176:179], a[76:79]
v_mfma_f32_16x16x32_bf16 a[76:79], v[28:31], a[196:199], a[76:79]
v_mfma_f32_16x16x32_bf16 a[80:83], v[16:19], a[160:163], a[80:83]
v_perm_b32 v24, v54, v50, s85
v_perm_b32 v25, v62, v58, s85
v_mfma_f32_16x16x32_bf16 a[80:83], v[20:23], a[180:183], a[80:83]
v_perm_b32 v26, v70, v66, s85
v_perm_b32 v27, v78, v74, s85
v_mfma_f32_16x16x32_bf16 a[84:87], v[16:19], a[164:167], a[84:87]
v_perm_b32 v28, v102, v98, s85
v_perm_b32 v29, v110, v106, s85
v_mfma_f32_16x16x32_bf16 a[84:87], v[20:23], a[184:187], a[84:87]
v_perm_b32 v30, v118, v114, s85
v_perm_b32 v31, v126, v122, s85
v_mfma_f32_16x16x32_bf16 a[88:91], v[16:19], a[168:171], a[88:91]
v_mfma_f32_16x16x32_bf16 a[88:91], v[20:23], a[188:191], a[88:91]
v_mfma_f32_16x16x32_bf16 a[92:95], v[16:19], a[172:175], a[92:95]
v_mfma_f32_16x16x32_bf16 a[92:95], v[20:23], a[192:195], a[92:95]
v_mfma_f32_16x16x32_bf16 a[96:99], v[16:19], a[176:179], a[96:99]
v_mfma_f32_16x16x32_bf16 a[96:99], v[20:23], a[196:199], a[96:99]
v_mfma_f32_16x16x32_bf16 a[100:103], v[24:27], a[160:163], a[100:103]
v_perm_b32 v16, v55, v51, s84
v_perm_b32 v17, v63, v59, s84
v_mfma_f32_16x16x32_bf16 a[100:103], v[28:31], a[180:183], a[100:103]
v_perm_b32 v18, v71, v67, s84
v_perm_b32 v19, v79, v75, s84
v_mfma_f32_16x16x32_bf16 a[104:107], v[24:27], a[164:167], a[104:107]
ds_read_b128 a[212:215], v2 offset:9216
v_perm_b32 v20, v103, v99, s84
v_mfma_f32_16x16x32_bf16 a[104:107], v[28:31], a[184:187], a[104:107]
ds_read_b128 a[232:235], v2 offset:14336
v_perm_b32 v21, v111, v107, s84
v_mfma_f32_16x16x32_bf16 a[108:111], v[24:27], a[168:171], a[108:111]
v_perm_b32 v22, v119, v115, s84
v_perm_b32 v23, v127, v123, s84
v_mfma_f32_16x16x32_bf16 a[108:111], v[28:31], a[188:191], a[108:111]
v_mfma_f32_16x16x32_bf16 a[112:115], v[24:27], a[172:175], a[112:115]
v_mfma_f32_16x16x32_bf16 a[112:115], v[28:31], a[192:195], a[112:115]
v_mfma_f32_16x16x32_bf16 a[116:119], v[24:27], a[176:179], a[116:119]
v_mfma_f32_16x16x32_bf16 a[116:119], v[28:31], a[196:199], a[116:119]
v_mfma_f32_16x16x32_bf16 a[120:123], v[16:19], a[160:163], a[120:123]
v_perm_b32 v24, v55, v51, s85
v_perm_b32 v25, v63, v59, s85
v_mfma_f32_16x16x32_bf16 a[120:123], v[20:23], a[180:183], a[120:123]
v_perm_b32 v26, v71, v67, s85
v_perm_b32 v27, v79, v75, s85
v_mfma_f32_16x16x32_bf16 a[124:127], v[16:19], a[164:167], a[124:127]
v_perm_b32 v28, v103, v99, s85
v_perm_b32 v29, v111, v107, s85
v_mfma_f32_16x16x32_bf16 a[124:127], v[20:23], a[184:187], a[124:127]
v_perm_b32 v30, v119, v115, s85
v_perm_b32 v31, v127, v123, s85
v_mfma_f32_16x16x32_bf16 a[128:131], v[16:19], a[168:171], a[128:131]
ds_read_b128 a[216:219], v2 offset:10240
v_mfma_f32_16x16x32_bf16 a[128:131], v[20:23], a[188:191], a[128:131]
ds_read_b128 a[236:239], v2 offset:15360
v_mfma_f32_16x16x32_bf16 a[132:135], v[16:19], a[172:175], a[132:135]
v_mfma_f32_16x16x32_bf16 a[132:135], v[20:23], a[192:195], a[132:135]
v_mfma_f32_16x16x32_bf16 a[136:139], v[16:19], a[176:179], a[136:139]
v_mfma_f32_16x16x32_bf16 a[136:139], v[20:23], a[196:199], a[136:139]
v_mfma_f32_16x16x32_bf16 a[140:143], v[24:27], a[160:163], a[140:143]
s_waitcnt vmcnt(8)
v_perm_b32 v16, v36, v32, s84
v_perm_b32 v17, v44, v40, s84
v_mfma_f32_16x16x32_bf16 a[140:143], v[28:31], a[180:183], a[140:143]
v_perm_b32 v18, v84, v80, s84
v_perm_b32 v19, v92, v88, s84
v_mfma_f32_16x16x32_bf16 a[144:147], v[24:27], a[164:167], a[144:147]
s_waitcnt vmcnt(0)
v_perm_b32 v20, v132, v128, s84
v_perm_b32 v21, v140, v136, s84
v_mfma_f32_16x16x32_bf16 a[144:147], v[28:31], a[184:187], a[144:147]
v_perm_b32 v22, v148, v144, s84
v_perm_b32 v23, v156, v152, s84
v_mfma_f32_16x16x32_bf16 a[148:151], v[24:27], a[168:171], a[148:151]
v_mfma_f32_16x16x32_bf16 a[148:151], v[28:31], a[188:191], a[148:151]
v_mfma_f32_16x16x32_bf16 a[152:155], v[24:27], a[172:175], a[152:155]
v_mfma_f32_16x16x32_bf16 a[152:155], v[28:31], a[192:195], a[152:155]
v_mfma_f32_16x16x32_bf16 a[156:159], v[24:27], a[176:179], a[156:159]
v_mfma_f32_16x16x32_bf16 a[156:159], v[28:31], a[196:199], a[156:159]
s_waitcnt lgkmcnt(1)
)"
R"(v_mfma_f32_16x16x32_bf16 a[0:3], v[16:19], a[200:203], a[0:3]
v_perm_b32 v24, v36, v32, s85
v_perm_b32 v25, v44, v40, s85
v_mfma_f32_16x16x32_bf16 a[0:3], v[20:23], a[220:223], a[0:3]
v_perm_b32 v26, v84, v80, s85
v_perm_b32 v27, v92, v88, s85
v_mfma_f32_16x16x32_bf16 a[4:7], v[16:19], a[204:207], a[4:7]
v_perm_b32 v28, v132, v128, s85
v_perm_b32 v29, v140, v136, s85
v_mfma_f32_16x16x32_bf16 a[4:7], v[20:23], a[224:227], a[4:7]
v_perm_b32 v30, v148, v144, s85
v_perm_b32 v31, v156, v152, s85
v_mfma_f32_16x16x32_bf16 a[8:11], v[16:19], a[208:211], a[8:11]
v_mfma_f32_16x16x32_bf16 a[8:11], v[20:23], a[228:231], a[8:11]
v_mfma_f32_16x16x32_bf16 a[12:15], v[16:19], a[212:215], a[12:15]
v_mfma_f32_16x16x32_bf16 a[12:15], v[20:23], a[232:235], a[12:15]
v_mfma_f32_16x16x32_bf16 a[16:19], v[16:19], a[216:219], a[16:19]
s_waitcnt lgkmcnt(0)
v_mfma_f32_16x16x32_bf16 a[16:19], v[20:23], a[236:239], a[16:19]
v_mfma_f32_16x16x32_bf16 a[20:23], v[24:27], a[200:203], a[20:23]
v_perm_b32 v16, v37, v33, s84
v_perm_b32 v17, v45, v41, s84
v_mfma_f32_16x16x32_bf16 a[20:23], v[28:31], a[220:223], a[20:23]
v_perm_b32 v18, v85, v81, s84
v_perm_b32 v19, v93, v89, s84
v_mfma_f32_16x16x32_bf16 a[24:27], v[24:27], a[204:207], a[24:27]
v_perm_b32 v20, v133, v129, s84
v_perm_b32 v21, v141, v137, s84
v_mfma_f32_16x16x32_bf16 a[24:27], v[28:31], a[224:227], a[24:27]
v_perm_b32 v22, v149, v145, s84
v_perm_b32 v23, v157, v153, s84
v_mfma_f32_16x16x32_bf16 a[28:31], v[24:27], a[208:211], a[28:31]
v_mfma_f32_16x16x32_bf16 a[28:31], v[28:31], a[228:231], a[28:31]
v_mfma_f32_16x16x32_bf16 a[32:35], v[24:27], a[212:215], a[32:35]
v_mfma_f32_16x16x32_bf16 a[32:35], v[28:31], a[232:235], a[32:35]
v_mfma_f32_16x16x32_bf16 a[36:39], v[24:27], a[216:219], a[36:39]
v_mfma_f32_16x16x32_bf16 a[36:39], v[28:31], a[236:239], a[36:39]
v_mfma_f32_16x16x32_bf16 a[40:43], v[16:19], a[200:203], a[40:43]
v_perm_b32 v24, v37, v33, s85
v_perm_b32 v25, v45, v41, s85
v_mfma_f32_16x16x32_bf16 a[40:43], v[20:23], a[220:223], a[40:43]
v_perm_b32 v26, v85, v81, s85
v_perm_b32 v27, v93, v89, s85
v_mfma_f32_16x16x32_bf16 a[44:47], v[16:19], a[204:207], a[44:47]
v_perm_b32 v28, v133, v129, s85
v_perm_b32 v29, v141, v137, s85
v_mfma_f32_16x16x32_bf16 a[44:47], v[20:23], a[224:227], a[44:47]
v_perm_b32 v30, v149, v145, s85
v_perm_b32 v31, v157, v153, s85
v_mfma_f32_16x16x32_bf16 a[48:51], v[16:19], a[208:211], a[48:51]
v_mfma_f32_16x16x32_bf16 a[48:51], v[20:23], a[228:231], a[48:51]
v_mfma_f32_16x16x32_bf16 a[52:55], v[16:19], a[212:215], a[52:55]
v_mfma_f32_16x16x32_bf16 a[52:55], v[20:23], a[232:235], a[52:55]
v_mfma_f32_16x16x32_bf16 a[56:59], v[16:19], a[216:219], a[56:59]
v_mfma_f32_16x16x32_bf16 a[56:59], v[20:23], a[236:239], a[56:59]
v_mfma_f32_16x16x32_bf16 a[60:63], v[24:27], a[200:203], a[60:63]
v_perm_b32 v16, v38, v34, s84
v_perm_b32 v17, v46, v42, s84
v_mfma_f32_16x16x32_bf16 a[60:63], v[28:31], a[220:223], a[60:63]
v_perm_b32 v18, v86, v82, s84
v_perm_b32 v19, v94, v90, s84
v_mfma_f32_16x16x32_bf16 a[64:67], v[24:27], a[204:207], a[64:67]
v_perm_b32 v20, v134, v130, s84
v_perm_b32 v21, v142, v138, s84
v_mfma_f32_16x16x32_bf16 a[64:67], v[28:31], a[224:227], a[64:67]
v_perm_b32 v22, v150, v146, s84
v_perm_b32 v23, v158, v154, s84
v_mfma_f32_16x16x32_bf16 a[68:71], v[24:27], a[208:211], a[68:71]
v_mfma_f32_16x16x32_bf16 a[68:71], v[28:31], a[228:231], a[68:71]
v_mfma_f32_16x16x32_bf16 a[72:75], v[24:27], a[212:215], a[72:75]
v_mfma_f32_16x16x32_bf16 a[72:75], v[28:31], a[232:235], a[72:75]
v_mfma_f32_16x16x32_bf16 a[76:79], v[24:27], a[216:219], a[76:79]
v_mfma_f32_16x16x32_bf16 a[76:79], v[28:31], a[236:239], a[76:79]
v_mfma_f32_16x16x32_bf16 a[80:83], v[16:19], a[200:203], a[80:83]
v_perm_b32 v24, v38, v34, s85
v_perm_b32 v25, v46, v42, s85
v_mfma_f32_16x16x32_bf16 a[80:83], v[20:23], a[220:223], a[80:83]
v_perm_b32 v26, v86, v82, s85
v_perm_b32 v27, v94, v90, s85
v_mfma_f32_16x16x32_bf16 a[84:87], v[16:19], a[204:207], a[84:87]
v_perm_b32 v28, v134, v130, s85
v_perm_b32 v29, v142, v138, s85
v_mfma_f32_16x16x32_bf16 a[84:87], v[20:23], a[224:227], a[84:87]
v_perm_b32 v30, v150, v146, s85
v_perm_b32 v31, v158, v154, s85
v_mfma_f32_16x16x32_bf16 a[88:91], v[16:19], a[208:211], a[88:91]
v_mfma_f32_16x16x32_bf16 a[88:91], v[20:23], a[228:231], a[88:91]
v_mfma_f32_16x16x32_bf16 a[92:95], v[16:19], a[212:215], a[92:95]
v_mfma_f32_16x16x32_bf16 a[92:95], v[20:23], a[232:235], a[92:95]
v_mfma_f32_16x16x32_bf16 a[96:99], v[16:19], a[216:219], a[96:99]
v_mfma_f32_16x16x32_bf16 a[96:99], v[20:23], a[236:239], a[96:99]
v_mfma_f32_16x16x32_bf16 a[100:103], v[24:27], a[200:203], a[100:103]
v_perm_b32 v16, v39, v35, s84
v_perm_b32 v17, v47, v43, s84
v_mfma_f32_16x16x32_bf16 a[100:103], v[28:31], a[220:223], a[100:103]
v_perm_b32 v18, v87, v83, s84
v_perm_b32 v19, v95, v91, s84
v_mfma_f32_16x16x32_bf16 a[104:107], v[24:27], a[204:207], a[104:107]
v_perm_b32 v20, v135, v131, s84
v_perm_b32 v21, v143, v139, s84
v_mfma_f32_16x16x32_bf16 a[104:107], v[28:31], a[224:227], a[104:107]
v_perm_b32 v22, v151, v147, s84
v_perm_b32 v23, v159, v155, s84
v_mfma_f32_16x16x32_bf16 a[108:111], v[24:27], a[208:211], a[108:111]
v_mfma_f32_16x16x32_bf16 a[108:111], v[28:31], a[228:231], a[108:111]
v_mfma_f32_16x16x32_bf16 a[112:115], v[24:27], a[212:215], a[112:115]
v_mfma_f32_16x16x32_bf16 a[112:115], v[28:31], a[232:235], a[112:115]
v_mfma_f32_16x16x32_bf16 a[116:119], v[24:27], a[216:219], a[116:119]
v_mfma_f32_16x16x32_bf16 a[116:119], v[28:31], a[236:239], a[116:119]
v_mfma_f32_16x16x32_bf16 a[120:123], v[16:19], a[200:203], a[120:123]
v_perm_b32 v24, v39, v35, s85
v_perm_b32 v25, v47, v43, s85
v_mfma_f32_16x16x32_bf16 a[120:123], v[20:23], a[220:223], a[120:123]
v_perm_b32 v26, v87, v83, s85
v_perm_b32 v27, v95, v91, s85
v_mfma_f32_16x16x32_bf16 a[124:127], v[16:19], a[204:207], a[124:127]
v_perm_b32 v28, v135, v131, s85
v_perm_b32 v29, v143, v139, s85
v_mfma_f32_16x16x32_bf16 a[124:127], v[20:23], a[224:227], a[124:127]
v_perm_b32 v30, v151, v147, s85
v_perm_b32 v31, v159, v155, s85
v_mfma_f32_16x16x32_bf16 a[128:131], v[16:19], a[208:211], a[128:131]
v_accvgpr_read_b32 v6, a240
buffer_load_dwordx4 v[32:35], v6, s[44:47], s64 offen
v_mfma_f32_16x16x32_bf16 a[128:131], v[20:23], a[228:231], a[128:131]
buffer_load_dwordx4 v[36:39], v6, s[44:47], s65 offen
v_mfma_f32_16x16x32_bf16 a[132:135], v[16:19], a[212:215], a[132:135]
buffer_load_dwordx4 v[40:43], v6, s[44:47], s66 offen
v_mfma_f32_16x16x32_bf16 a[132:135], v[20:23], a[232:235], a[132:135]
buffer_load_dwordx4 v[44:47], v6, s[44:47], s67 offen
v_mfma_f32_16x16x32_bf16 a[136:139], v[16:19], a[216:219], a[136:139]
buffer_load_dwordx4 v[48:51], v6, s[44:47], s68 offen
v_mfma_f32_16x16x32_bf16 a[136:139], v[20:23], a[236:239], a[136:139]
buffer_load_dwordx4 v[52:55], v6, s[44:47], s69 offen
v_mfma_f32_16x16x32_bf16 a[140:143], v[24:27], a[200:203], a[140:143]
buffer_load_dwordx4 v[56:59], v6, s[44:47], s70 offen
v_mfma_f32_16x16x32_bf16 a[140:143], v[28:31], a[220:223], a[140:143]
buffer_load_dwordx4 v[60:63], v6, s[44:47], s71 offen
v_mfma_f32_16x16x32_bf16 a[144:147], v[24:27], a[204:207], a[144:147]
v_add_u32 v6, 0x40000, v6
v_mfma_f32_16x16x32_bf16 a[144:147], v[28:31], a[224:227], a[144:147]
buffer_load_dwordx4 v[64:67], v6, s[44:47], s64 offen
v_mfma_f32_16x16x32_bf16 a[148:151], v[24:27], a[208:211], a[148:151]
buffer_load_dwordx4 v[68:71], v6, s[44:47], s65 offen
v_mfma_f32_16x16x32_bf16 a[148:151], v[28:31], a[228:231], a[148:151]
buffer_load_dwordx4 v[72:75], v6, s[44:47], s66 offen
v_mfma_f32_16x16x32_bf16 a[152:155], v[24:27], a[212:215], a[152:155]
buffer_load_dwordx4 v[76:79], v6, s[44:47], s67 offen
v_mfma_f32_16x16x32_bf16 a[152:155], v[28:31], a[232:235], a[152:155]
v_mfma_f32_16x16x32_bf16 a[156:159], v[24:27], a[216:219], a[156:159]
v_mfma_f32_16x16x32_bf16 a[156:159], v[28:31], a[236:239], a[156:159]
buffer_load_dwordx4 v[80:83], v6, s[44:47], s68 offen
buffer_load_dwordx4 v[84:87], v6, s[44:47], s69 offen
buffer_load_dwordx4 v[88:91], v6, s[44:47], s70 offen
buffer_load_dwordx4 v[92:95], v6, s[44:47], s71 offen
s_mov_b64 s[36:37], %[k1]
s_and_b32 s37, s37, 0xffff
s_lshr_b32 s38, s61, 15
s_xor_b32 s38, s38, 1
s_lshl_b32 s38, s38, 18
s_add_u32 s38, s38, 262144
s_cmp_eq_u32 s90, 0
s_cselect_b32 s38, s38, 0
Ltail_%=:
s_nop 7
s_cmp_lg_u32 s90, 0
s_cbranch_scc1 Lfin_%=
s_lshl_b32 s91, s90, 15
s_xor_b32 s91, s91, s61
s_lshr_b32 s60, s91, 10
s_lshl_b32 s60, s60, 6
s_bfe_u32 s91, s91, 0x30007
s_add_u32 s60, s60, s91
s_mul_i32 s60, s60, 10240
s_add_u32 s60, s60, 69632
s_mov_b32 s58, 42012672
v_add_u32 v13, 0x3ffffd00, v1
v_and_b32 v13, 0x40000000, v13
v_or_b32 v14, v8, v13
v_accvgpr_read_b32 v16, a0
v_accvgpr_read_b32 v17, a20
v_accvgpr_read_b32 v18, a40
v_accvgpr_read_b32 v19, a60
v_accvgpr_read_b32 v20, a80
v_accvgpr_read_b32 v21, a100
v_accvgpr_read_b32 v22, a120
v_accvgpr_read_b32 v23, a140
s_add_u32 s91, s60, 0
v_cvt_pk_bf16_f32 v96, v16, v17
v_cvt_pk_bf16_f32 v97, v18, v19
v_cvt_pk_bf16_f32 v98, v20, v21
v_cvt_pk_bf16_f32 v99, v22, v23
buffer_store_dwordx4 v[96:99], v8, s[56:59], s91 offen sc1
v_accvgpr_read_b32 v24, a1
v_accvgpr_read_b32 v25, a21
v_accvgpr_read_b32 v26, a41
v_accvgpr_read_b32 v27, a61
v_accvgpr_read_b32 v28, a81
v_accvgpr_read_b32 v29, a101
v_accvgpr_read_b32 v30, a121
)"
R"(v_accvgpr_read_b32 v31, a141
v_cvt_pk_bf16_f32 v100, v24, v25
v_cvt_pk_bf16_f32 v101, v26, v27
v_cvt_pk_bf16_f32 v102, v28, v29
v_cvt_pk_bf16_f32 v103, v30, v31
buffer_store_dwordx4 v[100:103], v8, s[56:59], s91 offen offset:1024 sc1
v_accvgpr_read_b32 v16, a2
v_accvgpr_read_b32 v17, a22
v_accvgpr_read_b32 v18, a42
v_accvgpr_read_b32 v19, a62
v_accvgpr_read_b32 v20, a82
v_accvgpr_read_b32 v21, a102
v_accvgpr_read_b32 v22, a122
v_accvgpr_read_b32 v23, a142
s_add_u32 s91, s60, 81920
v_cvt_pk_bf16_f32 v104, v16, v17
v_cvt_pk_bf16_f32 v105, v18, v19
v_cvt_pk_bf16_f32 v106, v20, v21
v_cvt_pk_bf16_f32 v107, v22, v23
buffer_store_dwordx4 v[104:107], v14, s[56:59], s91 offen sc1
v_accvgpr_read_b32 v24, a3
v_accvgpr_read_b32 v25, a23
v_accvgpr_read_b32 v26, a43
v_accvgpr_read_b32 v27, a63
v_accvgpr_read_b32 v28, a83
v_accvgpr_read_b32 v29, a103
v_accvgpr_read_b32 v30, a123
v_accvgpr_read_b32 v31, a143
v_cvt_pk_bf16_f32 v108, v24, v25
v_cvt_pk_bf16_f32 v109, v26, v27
v_cvt_pk_bf16_f32 v110, v28, v29
v_cvt_pk_bf16_f32 v111, v30, v31
buffer_store_dwordx4 v[108:111], v14, s[56:59], s91 offen offset:1024 sc1
v_accvgpr_read_b32 v16, a4
v_accvgpr_read_b32 v17, a24
v_accvgpr_read_b32 v18, a44
v_accvgpr_read_b32 v19, a64
v_accvgpr_read_b32 v20, a84
v_accvgpr_read_b32 v21, a104
v_accvgpr_read_b32 v22, a124
v_accvgpr_read_b32 v23, a144
s_add_u32 s91, s60, 2048
v_cvt_pk_bf16_f32 v112, v16, v17
v_cvt_pk_bf16_f32 v113, v18, v19
v_cvt_pk_bf16_f32 v114, v20, v21
v_cvt_pk_bf16_f32 v115, v22, v23
buffer_store_dwordx4 v[112:115], v8, s[56:59], s91 offen sc1
v_accvgpr_read_b32 v24, a5
v_accvgpr_read_b32 v25, a25
v_accvgpr_read_b32 v26, a45
v_accvgpr_read_b32 v27, a65
v_accvgpr_read_b32 v28, a85
v_accvgpr_read_b32 v29, a105
v_accvgpr_read_b32 v30, a125
v_accvgpr_read_b32 v31, a145
v_cvt_pk_bf16_f32 v116, v24, v25
v_cvt_pk_bf16_f32 v117, v26, v27
v_cvt_pk_bf16_f32 v118, v28, v29
v_cvt_pk_bf16_f32 v119, v30, v31
buffer_store_dwordx4 v[116:119], v8, s[56:59], s91 offen offset:1024 sc1
v_accvgpr_read_b32 v16, a6
v_accvgpr_read_b32 v17, a26
v_accvgpr_read_b32 v18, a46
v_accvgpr_read_b32 v19, a66
v_accvgpr_read_b32 v20, a86
v_accvgpr_read_b32 v21, a106
v_accvgpr_read_b32 v22, a126
v_accvgpr_read_b32 v23, a146
s_add_u32 s91, s60, 83968
v_cvt_pk_bf16_f32 v120, v16, v17
v_cvt_pk_bf16_f32 v121, v18, v19
v_cvt_pk_bf16_f32 v122, v20, v21
v_cvt_pk_bf16_f32 v123, v22, v23
buffer_store_dwordx4 v[120:123], v14, s[56:59], s91 offen sc1
v_accvgpr_read_b32 v24, a7
v_accvgpr_read_b32 v25, a27
v_accvgpr_read_b32 v26, a47
v_accvgpr_read_b32 v27, a67
v_accvgpr_read_b32 v28, a87
v_accvgpr_read_b32 v29, a107
v_accvgpr_read_b32 v30, a127
v_accvgpr_read_b32 v31, a147
v_cvt_pk_bf16_f32 v124, v24, v25
v_cvt_pk_bf16_f32 v125, v26, v27
v_cvt_pk_bf16_f32 v126, v28, v29
v_cvt_pk_bf16_f32 v127, v30, v31
buffer_store_dwordx4 v[124:127], v14, s[56:59], s91 offen offset:1024 sc1
v_accvgpr_read_b32 v16, a8
v_accvgpr_read_b32 v17, a28
v_accvgpr_read_b32 v18, a48
v_accvgpr_read_b32 v19, a68
v_accvgpr_read_b32 v20, a88
v_accvgpr_read_b32 v21, a108
v_accvgpr_read_b32 v22, a128
v_accvgpr_read_b32 v23, a148
s_add_u32 s91, s60, 4096
v_cvt_pk_bf16_f32 v96, v16, v17
v_cvt_pk_bf16_f32 v97, v18, v19
v_cvt_pk_bf16_f32 v98, v20, v21
v_cvt_pk_bf16_f32 v99, v22, v23
buffer_store_dwordx4 v[96:99], v8, s[56:59], s91 offen sc1
v_accvgpr_read_b32 v24, a9
v_accvgpr_read_b32 v25, a29
v_accvgpr_read_b32 v26, a49
v_accvgpr_read_b32 v27, a69
v_accvgpr_read_b32 v28, a89
v_accvgpr_read_b32 v29, a109
v_accvgpr_read_b32 v30, a129
v_accvgpr_read_b32 v31, a149
v_cvt_pk_bf16_f32 v100, v24, v25
v_cvt_pk_bf16_f32 v101, v26, v27
v_cvt_pk_bf16_f32 v102, v28, v29
v_cvt_pk_bf16_f32 v103, v30, v31
buffer_store_dwordx4 v[100:103], v8, s[56:59], s91 offen offset:1024 sc1
v_accvgpr_read_b32 v16, a10
v_accvgpr_read_b32 v17, a30
v_accvgpr_read_b32 v18, a50
v_accvgpr_read_b32 v19, a70
v_accvgpr_read_b32 v20, a90
v_accvgpr_read_b32 v21, a110
v_accvgpr_read_b32 v22, a130
v_accvgpr_read_b32 v23, a150
s_add_u32 s91, s60, 86016
v_cvt_pk_bf16_f32 v104, v16, v17
v_cvt_pk_bf16_f32 v105, v18, v19
v_cvt_pk_bf16_f32 v106, v20, v21
v_cvt_pk_bf16_f32 v107, v22, v23
buffer_store_dwordx4 v[104:107], v14, s[56:59], s91 offen sc1
v_accvgpr_read_b32 v24, a11
v_accvgpr_read_b32 v25, a31
v_accvgpr_read_b32 v26, a51
v_accvgpr_read_b32 v27, a71
v_accvgpr_read_b32 v28, a91
v_accvgpr_read_b32 v29, a111
v_accvgpr_read_b32 v30, a131
v_accvgpr_read_b32 v31, a151
v_cvt_pk_bf16_f32 v108, v24, v25
v_cvt_pk_bf16_f32 v109, v26, v27
v_cvt_pk_bf16_f32 v110, v28, v29
v_cvt_pk_bf16_f32 v111, v30, v31
buffer_store_dwordx4 v[108:111], v14, s[56:59], s91 offen offset:1024 sc1
v_accvgpr_read_b32 v16, a12
v_accvgpr_read_b32 v17, a32
v_accvgpr_read_b32 v18, a52
v_accvgpr_read_b32 v19, a72
v_accvgpr_read_b32 v20, a92
v_accvgpr_read_b32 v21, a112
v_accvgpr_read_b32 v22, a132
v_accvgpr_read_b32 v23, a152
s_add_u32 s91, s60, 6144
v_cvt_pk_bf16_f32 v112, v16, v17
v_cvt_pk_bf16_f32 v113, v18, v19
v_cvt_pk_bf16_f32 v114, v20, v21
v_cvt_pk_bf16_f32 v115, v22, v23
buffer_store_dwordx4 v[112:115], v8, s[56:59], s91 offen sc1
v_accvgpr_read_b32 v24, a13
v_accvgpr_read_b32 v25, a33
v_accvgpr_read_b32 v26, a53
v_accvgpr_read_b32 v27, a73
v_accvgpr_read_b32 v28, a93
v_accvgpr_read_b32 v29, a113
v_accvgpr_read_b32 v30, a133
v_accvgpr_read_b32 v31, a153
v_cvt_pk_bf16_f32 v116, v24, v25
v_cvt_pk_bf16_f32 v117, v26, v27
v_cvt_pk_bf16_f32 v118, v28, v29
v_cvt_pk_bf16_f32 v119, v30, v31
buffer_store_dwordx4 v[116:119], v8, s[56:59], s91 offen offset:1024 sc1
v_accvgpr_read_b32 v16, a14
v_accvgpr_read_b32 v17, a34
v_accvgpr_read_b32 v18, a54
v_accvgpr_read_b32 v19, a74
v_accvgpr_read_b32 v20, a94
v_accvgpr_read_b32 v21, a114
v_accvgpr_read_b32 v22, a134
v_accvgpr_read_b32 v23, a154
s_add_u32 s91, s60, 88064
v_cvt_pk_bf16_f32 v120, v16, v17
v_cvt_pk_bf16_f32 v121, v18, v19
v_cvt_pk_bf16_f32 v122, v20, v21
v_cvt_pk_bf16_f32 v123, v22, v23
buffer_store_dwordx4 v[120:123], v14, s[56:59], s91 offen sc1
v_accvgpr_read_b32 v24, a15
v_accvgpr_read_b32 v25, a35
v_accvgpr_read_b32 v26, a55
v_accvgpr_read_b32 v27, a75
v_accvgpr_read_b32 v28, a95
v_accvgpr_read_b32 v29, a115
v_accvgpr_read_b32 v30, a135
v_accvgpr_read_b32 v31, a155
v_cvt_pk_bf16_f32 v124, v24, v25
v_cvt_pk_bf16_f32 v125, v26, v27
v_cvt_pk_bf16_f32 v126, v28, v29
v_cvt_pk_bf16_f32 v127, v30, v31
buffer_store_dwordx4 v[124:127], v14, s[56:59], s91 offen offset:1024 sc1
v_accvgpr_read_b32 v16, a16
v_accvgpr_read_b32 v17, a36
v_accvgpr_read_b32 v18, a56
v_accvgpr_read_b32 v19, a76
v_accvgpr_read_b32 v20, a96
)"
R"(v_accvgpr_read_b32 v21, a116
v_accvgpr_read_b32 v22, a136
v_accvgpr_read_b32 v23, a156
s_add_u32 s91, s60, 8192
v_cvt_pk_bf16_f32 v96, v16, v17
v_cvt_pk_bf16_f32 v97, v18, v19
v_cvt_pk_bf16_f32 v98, v20, v21
v_cvt_pk_bf16_f32 v99, v22, v23
buffer_store_dwordx4 v[96:99], v8, s[56:59], s91 offen sc1
v_accvgpr_read_b32 v24, a17
v_accvgpr_read_b32 v25, a37
v_accvgpr_read_b32 v26, a57
v_accvgpr_read_b32 v27, a77
v_accvgpr_read_b32 v28, a97
v_accvgpr_read_b32 v29, a117
v_accvgpr_read_b32 v30, a137
v_accvgpr_read_b32 v31, a157
v_cvt_pk_bf16_f32 v100, v24, v25
v_cvt_pk_bf16_f32 v101, v26, v27
v_cvt_pk_bf16_f32 v102, v28, v29
v_cvt_pk_bf16_f32 v103, v30, v31
buffer_store_dwordx4 v[100:103], v8, s[56:59], s91 offen offset:1024 sc1
v_accvgpr_read_b32 v16, a18
v_accvgpr_read_b32 v17, a38
v_accvgpr_read_b32 v18, a58
v_accvgpr_read_b32 v19, a78
v_accvgpr_read_b32 v20, a98
v_accvgpr_read_b32 v21, a118
v_accvgpr_read_b32 v22, a138
v_accvgpr_read_b32 v23, a158
s_add_u32 s91, s60, 90112
v_cvt_pk_bf16_f32 v104, v16, v17
v_cvt_pk_bf16_f32 v105, v18, v19
v_cvt_pk_bf16_f32 v106, v20, v21
v_cvt_pk_bf16_f32 v107, v22, v23
buffer_store_dwordx4 v[104:107], v14, s[56:59], s91 offen sc1
v_accvgpr_read_b32 v24, a19
v_accvgpr_read_b32 v25, a39
v_accvgpr_read_b32 v26, a59
v_accvgpr_read_b32 v27, a79
v_accvgpr_read_b32 v28, a99
v_accvgpr_read_b32 v29, a119
v_accvgpr_read_b32 v30, a139
v_accvgpr_read_b32 v31, a159
v_cvt_pk_bf16_f32 v108, v24, v25
v_cvt_pk_bf16_f32 v109, v26, v27
v_cvt_pk_bf16_f32 v110, v28, v29
v_cvt_pk_bf16_f32 v111, v30, v31
buffer_store_dwordx4 v[108:111], v14, s[56:59], s91 offen offset:1024 sc1
s_mov_b32 s90, 1
s_branch Lblock_%=
Lfin_%=:
v_accvgpr_read_b32 v28, a252
v_accvgpr_read_b32 v29, a253
v_add_u32 v28, 1, v28
v_add_u32 v29, 1, v29
s_waitcnt vmcnt(0)
v_cmp_eq_u64 vcc, v[14:15], v[28:29]
s_nop 4
s_cmp_eq_u64 vcc, -1
s_cbranch_scc1 Lok0_%=
s_mov_b32 s81, 65536
Lpoll0_%=:
buffer_load_dwordx2 v[30:31], v9, s[56:59], 0 offen sc1
s_waitcnt vmcnt(0)
v_cmp_eq_u64 vcc, v[30:31], v[28:29]
s_sub_u32 s81, s81, 1
s_nop 3
s_cselect_b64 s[62:63], -1, vcc
s_cmp_eq_u64 s[62:63], -1
s_cbranch_scc1 Lpoll0x_%=
s_sleep 2
s_branch Lpoll0_%=
Lpoll0x_%=:
Lok0_%=:
v_add_u32 v13, 0x3ffffd00, v1
v_and_b32 v13, 0x40000000, v13
s_bfe_u32 s91, s61, 0x10007
s_lshl_b32 s91, s91, 30
v_and_b32 v13, s91, v13
s_lshl_b32 s81, s92, 10
v_add3_u32 v10, s81, v1, v13
s_xor_b32 s91, s61, 0
s_lshr_b32 s91, s91, 7
s_mul_i32 s91, s91, 81920
s_add_u32 s91, s91, 69632
s_add_u32 s81, s91, 0
buffer_load_dwordx4 v[176:179], v10, s[56:59], s81 offen sc1
s_add_u32 s81, s91, 10240
buffer_load_dwordx4 v[180:183], v10, s[56:59], s81 offen sc1
s_add_u32 s81, s91, 20480
buffer_load_dwordx4 v[184:187], v10, s[56:59], s81 offen sc1
s_add_u32 s81, s91, 30720
buffer_load_dwordx4 v[188:191], v10, s[56:59], s81 offen sc1
s_add_u32 s81, s91, 40960
buffer_load_dwordx4 v[192:195], v10, s[56:59], s81 offen sc1
s_add_u32 s81, s91, 51200
buffer_load_dwordx4 v[196:199], v10, s[56:59], s81 offen sc1
s_add_u32 s81, s91, 61440
buffer_load_dwordx4 v[200:203], v10, s[56:59], s81 offen sc1
s_add_u32 s81, s91, 71680
buffer_load_dwordx4 v[204:207], v10, s[56:59], s81 offen sc1
s_add_u32 s81, s91, 4096
buffer_load_dwordx4 v[208:211], v10, s[56:59], s81 offen sc1
s_add_u32 s81, s91, 14336
buffer_load_dwordx4 v[212:215], v10, s[56:59], s81 offen sc1
s_add_u32 s81, s91, 24576
buffer_load_dwordx4 v[216:219], v10, s[56:59], s81 offen sc1
s_add_u32 s81, s91, 34816
buffer_load_dwordx4 v[220:223], v10, s[56:59], s81 offen sc1
s_add_u32 s81, s91, 45056
buffer_load_dwordx4 v[224:227], v10, s[56:59], s81 offen sc1
s_add_u32 s81, s91, 55296
buffer_load_dwordx4 v[228:231], v10, s[56:59], s81 offen sc1
s_add_u32 s81, s91, 65536
buffer_load_dwordx4 v[232:235], v10, s[56:59], s81 offen sc1
s_add_u32 s81, s91, 75776
buffer_load_dwordx4 v[236:239], v10, s[56:59], s81 offen sc1
s_lshr_b32 s81, s92, 1
s_lshl_b32 s81, s81, 30
s_or_b32 s91, s91, s81
s_add_u32 s81, s91, 8192
buffer_load_dwordx4 v[32:35], v10, s[56:59], s81 offen sc1
s_add_u32 s81, s91, 18432
buffer_load_dwordx4 v[36:39], v10, s[56:59], s81 offen sc1
s_add_u32 s81, s91, 28672
buffer_load_dwordx4 v[40:43], v10, s[56:59], s81 offen sc1
s_add_u32 s81, s91, 38912
buffer_load_dwordx4 v[44:47], v10, s[56:59], s81 offen sc1
s_add_u32 s81, s91, 49152
buffer_load_dwordx4 v[48:51], v10, s[56:59], s81 offen sc1
s_add_u32 s81, s91, 59392
buffer_load_dwordx4 v[52:55], v10, s[56:59], s81 offen sc1
s_add_u32 s81, s91, 69632
buffer_load_dwordx4 v[56:59], v10, s[56:59], s81 offen sc1
s_add_u32 s81, s91, 79872
buffer_load_dwordx4 v[60:63], v10, s[56:59], s81 offen sc1
v_mov_b32 v0, v28
v_mov_b32 v15, v29
s_lshl_b32 s91, s90, 15
s_xor_b32 s91, s91, s61
s_lshr_b32 s60, s91, 10
s_lshl_b32 s60, s60, 6
s_bfe_u32 s91, s91, 0x30007
s_add_u32 s60, s60, s91
s_mul_i32 s60, s60, 10240
s_add_u32 s60, s60, 69632
s_mov_b32 s58, 42012672
v_add_u32 v13, 0x3ffffd00, v1
v_and_b32 v13, 0x40000000, v13
v_or_b32 v14, v8, v13
v_accvgpr_read_b32 v16, a0
v_accvgpr_read_b32 v17, a20
v_accvgpr_read_b32 v18, a40
v_accvgpr_read_b32 v19, a60
v_accvgpr_read_b32 v20, a80
v_accvgpr_read_b32 v21, a100
v_accvgpr_read_b32 v22, a120
v_accvgpr_read_b32 v23, a140
s_add_u32 s91, s60, 0
v_cvt_pk_bf16_f32 v96, v16, v17
v_cvt_pk_bf16_f32 v97, v18, v19
v_cvt_pk_bf16_f32 v98, v20, v21
v_cvt_pk_bf16_f32 v99, v22, v23
buffer_store_dwordx4 v[96:99], v8, s[56:59], s91 offen sc1
v_accvgpr_read_b32 v24, a1
v_accvgpr_read_b32 v25, a21
v_accvgpr_read_b32 v26, a41
v_accvgpr_read_b32 v27, a61
v_accvgpr_read_b32 v28, a81
v_accvgpr_read_b32 v29, a101
v_accvgpr_read_b32 v30, a121
v_accvgpr_read_b32 v31, a141
v_cvt_pk_bf16_f32 v100, v24, v25
v_cvt_pk_bf16_f32 v101, v26, v27
v_cvt_pk_bf16_f32 v102, v28, v29
v_cvt_pk_bf16_f32 v103, v30, v31
buffer_store_dwordx4 v[100:103], v8, s[56:59], s91 offen offset:1024 sc1
v_accvgpr_read_b32 v16, a2
v_accvgpr_read_b32 v17, a22
v_accvgpr_read_b32 v18, a42
v_accvgpr_read_b32 v19, a62
v_accvgpr_read_b32 v20, a82
v_accvgpr_read_b32 v21, a102
v_accvgpr_read_b32 v22, a122
v_accvgpr_read_b32 v23, a142
s_add_u32 s91, s60, 81920
v_cvt_pk_bf16_f32 v104, v16, v17
v_cvt_pk_bf16_f32 v105, v18, v19
v_cvt_pk_bf16_f32 v106, v20, v21
v_cvt_pk_bf16_f32 v107, v22, v23
buffer_store_dwordx4 v[104:107], v14, s[56:59], s91 offen sc1
v_accvgpr_read_b32 v24, a3
v_accvgpr_read_b32 v25, a23
v_accvgpr_read_b32 v26, a43
v_accvgpr_read_b32 v27, a63
v_accvgpr_read_b32 v28, a83
v_accvgpr_read_b32 v29, a103
v_accvgpr_read_b32 v30, a123
v_accvgpr_read_b32 v31, a143
)"
R"(v_cvt_pk_bf16_f32 v108, v24, v25
v_cvt_pk_bf16_f32 v109, v26, v27
v_cvt_pk_bf16_f32 v110, v28, v29
v_cvt_pk_bf16_f32 v111, v30, v31
buffer_store_dwordx4 v[108:111], v14, s[56:59], s91 offen offset:1024 sc1
v_accvgpr_read_b32 v16, a4
v_accvgpr_read_b32 v17, a24
v_accvgpr_read_b32 v18, a44
v_accvgpr_read_b32 v19, a64
v_accvgpr_read_b32 v20, a84
v_accvgpr_read_b32 v21, a104
v_accvgpr_read_b32 v22, a124
v_accvgpr_read_b32 v23, a144
s_add_u32 s91, s60, 2048
v_cvt_pk_bf16_f32 v112, v16, v17
v_cvt_pk_bf16_f32 v113, v18, v19
v_cvt_pk_bf16_f32 v114, v20, v21
v_cvt_pk_bf16_f32 v115, v22, v23
buffer_store_dwordx4 v[112:115], v8, s[56:59], s91 offen sc1
v_accvgpr_read_b32 v24, a5
v_accvgpr_read_b32 v25, a25
v_accvgpr_read_b32 v26, a45
v_accvgpr_read_b32 v27, a65
v_accvgpr_read_b32 v28, a85
v_accvgpr_read_b32 v29, a105
v_accvgpr_read_b32 v30, a125
v_accvgpr_read_b32 v31, a145
v_cvt_pk_bf16_f32 v116, v24, v25
v_cvt_pk_bf16_f32 v117, v26, v27
v_cvt_pk_bf16_f32 v118, v28, v29
v_cvt_pk_bf16_f32 v119, v30, v31
buffer_store_dwordx4 v[116:119], v8, s[56:59], s91 offen offset:1024 sc1
v_accvgpr_read_b32 v16, a6
v_accvgpr_read_b32 v17, a26
v_accvgpr_read_b32 v18, a46
v_accvgpr_read_b32 v19, a66
v_accvgpr_read_b32 v20, a86
v_accvgpr_read_b32 v21, a106
v_accvgpr_read_b32 v22, a126
v_accvgpr_read_b32 v23, a146
s_add_u32 s91, s60, 83968
v_cvt_pk_bf16_f32 v120, v16, v17
v_cvt_pk_bf16_f32 v121, v18, v19
v_cvt_pk_bf16_f32 v122, v20, v21
v_cvt_pk_bf16_f32 v123, v22, v23
buffer_store_dwordx4 v[120:123], v14, s[56:59], s91 offen sc1
v_accvgpr_read_b32 v24, a7
v_accvgpr_read_b32 v25, a27
v_accvgpr_read_b32 v26, a47
v_accvgpr_read_b32 v27, a67
v_accvgpr_read_b32 v28, a87
v_accvgpr_read_b32 v29, a107
v_accvgpr_read_b32 v30, a127
v_accvgpr_read_b32 v31, a147
v_cvt_pk_bf16_f32 v124, v24, v25
v_cvt_pk_bf16_f32 v125, v26, v27
v_cvt_pk_bf16_f32 v126, v28, v29
v_cvt_pk_bf16_f32 v127, v30, v31
buffer_store_dwordx4 v[124:127], v14, s[56:59], s91 offen offset:1024 sc1
v_accvgpr_read_b32 v16, a8
v_accvgpr_read_b32 v17, a28
v_accvgpr_read_b32 v18, a48
v_accvgpr_read_b32 v19, a68
v_accvgpr_read_b32 v20, a88
v_accvgpr_read_b32 v21, a108
v_accvgpr_read_b32 v22, a128
v_accvgpr_read_b32 v23, a148
s_add_u32 s91, s60, 4096
v_cvt_pk_bf16_f32 v96, v16, v17
v_cvt_pk_bf16_f32 v97, v18, v19
v_cvt_pk_bf16_f32 v98, v20, v21
v_cvt_pk_bf16_f32 v99, v22, v23
buffer_store_dwordx4 v[96:99], v8, s[56:59], s91 offen sc1
v_accvgpr_read_b32 v24, a9
v_accvgpr_read_b32 v25, a29
v_accvgpr_read_b32 v26, a49
v_accvgpr_read_b32 v27, a69
v_accvgpr_read_b32 v28, a89
v_accvgpr_read_b32 v29, a109
v_accvgpr_read_b32 v30, a129
v_accvgpr_read_b32 v31, a149
v_cvt_pk_bf16_f32 v100, v24, v25
v_cvt_pk_bf16_f32 v101, v26, v27
v_cvt_pk_bf16_f32 v102, v28, v29
v_cvt_pk_bf16_f32 v103, v30, v31
buffer_store_dwordx4 v[100:103], v8, s[56:59], s91 offen offset:1024 sc1
v_accvgpr_read_b32 v16, a10
v_accvgpr_read_b32 v17, a30
v_accvgpr_read_b32 v18, a50
v_accvgpr_read_b32 v19, a70
v_accvgpr_read_b32 v20, a90
v_accvgpr_read_b32 v21, a110
v_accvgpr_read_b32 v22, a130
v_accvgpr_read_b32 v23, a150
s_add_u32 s91, s60, 86016
v_cvt_pk_bf16_f32 v104, v16, v17
v_cvt_pk_bf16_f32 v105, v18, v19
v_cvt_pk_bf16_f32 v106, v20, v21
v_cvt_pk_bf16_f32 v107, v22, v23
buffer_store_dwordx4 v[104:107], v14, s[56:59], s91 offen sc1
v_accvgpr_read_b32 v24, a11
v_accvgpr_read_b32 v25, a31
v_accvgpr_read_b32 v26, a51
v_accvgpr_read_b32 v27, a71
v_accvgpr_read_b32 v28, a91
v_accvgpr_read_b32 v29, a111
v_accvgpr_read_b32 v30, a131
v_accvgpr_read_b32 v31, a151
v_cvt_pk_bf16_f32 v108, v24, v25
v_cvt_pk_bf16_f32 v109, v26, v27
v_cvt_pk_bf16_f32 v110, v28, v29
v_cvt_pk_bf16_f32 v111, v30, v31
buffer_store_dwordx4 v[108:111], v14, s[56:59], s91 offen offset:1024 sc1
v_accvgpr_read_b32 v16, a12
v_accvgpr_read_b32 v17, a32
v_accvgpr_read_b32 v18, a52
v_accvgpr_read_b32 v19, a72
v_accvgpr_read_b32 v20, a92
v_accvgpr_read_b32 v21, a112
v_accvgpr_read_b32 v22, a132
v_accvgpr_read_b32 v23, a152
s_add_u32 s91, s60, 6144
v_cvt_pk_bf16_f32 v112, v16, v17
v_cvt_pk_bf16_f32 v113, v18, v19
v_cvt_pk_bf16_f32 v114, v20, v21
v_cvt_pk_bf16_f32 v115, v22, v23
buffer_store_dwordx4 v[112:115], v8, s[56:59], s91 offen sc1
v_accvgpr_read_b32 v24, a13
v_accvgpr_read_b32 v25, a33
v_accvgpr_read_b32 v26, a53
v_accvgpr_read_b32 v27, a73
v_accvgpr_read_b32 v28, a93
v_accvgpr_read_b32 v29, a113
v_accvgpr_read_b32 v30, a133
v_accvgpr_read_b32 v31, a153
v_cvt_pk_bf16_f32 v116, v24, v25
v_cvt_pk_bf16_f32 v117, v26, v27
v_cvt_pk_bf16_f32 v118, v28, v29
v_cvt_pk_bf16_f32 v119, v30, v31
buffer_store_dwordx4 v[116:119], v8, s[56:59], s91 offen offset:1024 sc1
v_accvgpr_read_b32 v16, a14
v_accvgpr_read_b32 v17, a34
v_accvgpr_read_b32 v18, a54
v_accvgpr_read_b32 v19, a74
v_accvgpr_read_b32 v20, a94
v_accvgpr_read_b32 v21, a114
v_accvgpr_read_b32 v22, a134
v_accvgpr_read_b32 v23, a154
s_add_u32 s91, s60, 88064
v_cvt_pk_bf16_f32 v120, v16, v17
v_cvt_pk_bf16_f32 v121, v18, v19
v_cvt_pk_bf16_f32 v122, v20, v21
v_cvt_pk_bf16_f32 v123, v22, v23
buffer_store_dwordx4 v[120:123], v14, s[56:59], s91 offen sc1
v_accvgpr_read_b32 v24, a15
v_accvgpr_read_b32 v25, a35
v_accvgpr_read_b32 v26, a55
v_accvgpr_read_b32 v27, a75
v_accvgpr_read_b32 v28, a95
v_accvgpr_read_b32 v29, a115
v_accvgpr_read_b32 v30, a135
v_accvgpr_read_b32 v31, a155
v_cvt_pk_bf16_f32 v124, v24, v25
v_cvt_pk_bf16_f32 v125, v26, v27
v_cvt_pk_bf16_f32 v126, v28, v29
v_cvt_pk_bf16_f32 v127, v30, v31
buffer_store_dwordx4 v[124:127], v14, s[56:59], s91 offen offset:1024 sc1
v_accvgpr_read_b32 v16, a16
v_accvgpr_read_b32 v17, a36
v_accvgpr_read_b32 v18, a56
v_accvgpr_read_b32 v19, a76
v_accvgpr_read_b32 v20, a96
v_accvgpr_read_b32 v21, a116
v_accvgpr_read_b32 v22, a136
v_accvgpr_read_b32 v23, a156
s_add_u32 s91, s60, 8192
v_cvt_pk_bf16_f32 v96, v16, v17
v_cvt_pk_bf16_f32 v97, v18, v19
v_cvt_pk_bf16_f32 v98, v20, v21
v_cvt_pk_bf16_f32 v99, v22, v23
buffer_store_dwordx4 v[96:99], v8, s[56:59], s91 offen sc1
v_accvgpr_read_b32 v24, a17
v_accvgpr_read_b32 v25, a37
v_accvgpr_read_b32 v26, a57
v_accvgpr_read_b32 v27, a77
v_accvgpr_read_b32 v28, a97
v_accvgpr_read_b32 v29, a117
v_accvgpr_read_b32 v30, a137
v_accvgpr_read_b32 v31, a157
v_cvt_pk_bf16_f32 v100, v24, v25
v_cvt_pk_bf16_f32 v101, v26, v27
v_cvt_pk_bf16_f32 v102, v28, v29
v_cvt_pk_bf16_f32 v103, v30, v31
buffer_store_dwordx4 v[100:103], v8, s[56:59], s91 offen offset:1024 sc1
v_accvgpr_read_b32 v16, a18
v_accvgpr_read_b32 v17, a38
v_accvgpr_read_b32 v18, a58
v_accvgpr_read_b32 v19, a78
v_accvgpr_read_b32 v20, a98
v_accvgpr_read_b32 v21, a118
)"
R"(v_accvgpr_read_b32 v22, a138
v_accvgpr_read_b32 v23, a158
s_add_u32 s91, s60, 90112
v_cvt_pk_bf16_f32 v104, v16, v17
v_cvt_pk_bf16_f32 v105, v18, v19
v_cvt_pk_bf16_f32 v106, v20, v21
v_cvt_pk_bf16_f32 v107, v22, v23
buffer_store_dwordx4 v[104:107], v14, s[56:59], s91 offen sc1
v_accvgpr_read_b32 v24, a19
v_accvgpr_read_b32 v25, a39
v_accvgpr_read_b32 v26, a59
v_accvgpr_read_b32 v27, a79
v_accvgpr_read_b32 v28, a99
v_accvgpr_read_b32 v29, a119
v_accvgpr_read_b32 v30, a139
v_accvgpr_read_b32 v31, a159
v_cvt_pk_bf16_f32 v108, v24, v25
v_cvt_pk_bf16_f32 v109, v26, v27
v_cvt_pk_bf16_f32 v110, v28, v29
v_cvt_pk_bf16_f32 v111, v30, v31
buffer_store_dwordx4 v[108:111], v14, s[56:59], s91 offen offset:1024 sc1
v_mov_b32 v28, v0
v_mov_b32 v29, v15
s_bfe_u32 s81, s61, 0x5000a
s_mul_i32 s81, s81, 143360
s_mov_b64 s[40:41], %[out]
s_add_u32 s40, s40, s81
s_addc_u32 s41, s41, 0
s_and_b32 s41, s41, 0xffff
s_mov_b32 s42, 143360
s_bfe_u32 s81, s61, 0x10007
s_lshl_b32 s81, s81, 5
v_lshrrev_b32 v16, 8, v1
v_lshl_add_u32 v16, v16, 6, s81
v_cmp_gt_u32 vcc, 0xe0, v16
s_bfe_u32 s81, s61, 0x20008
s_mul_i32 s81, s81, 224
s_and_b32 s91, s92, 1
s_lshl_b32 s91, s91, 4
s_add_u32 s81, s81, s91
s_lshr_b32 s91, s92, 1
s_mul_i32 s91, s91, 28672
s_add_u32 s81, s81, s91
v_add_u32 v16, s81, v16
v_bfe_u32 v17, v1, 4, 4
s_mov_b32 s81, 1792
v_mad_u32_u24 v16, v17, s81, v16
v_mov_b32 v17, 0x1000000
v_cndmask_b32 v11, v17, v16, vcc
s_waitcnt vmcnt(0)
s_barrier
s_cmp_lg_u32 s92, 0
s_cbranch_scc1 LpubL_%=
s_mov_b64 exec, 1
v_accvgpr_read_b32 v16, a252
v_accvgpr_read_b32 v17, a253
s_xor_b32 s91, s61, 32768
v_add_u32 v16, 1, v16
v_add_u32 v17, 1, v17
v_mov_b32 v18, s91
buffer_store_dwordx2 v[16:17], v18, s[56:59], 0 offen sc1
s_mov_b64 exec, -1
LpubL_%=:
s_nop 1
v_xor_b32 v9, 0x8000, v9
s_xor_b32 s83, s61, 32768
s_lshr_b32 s83, s83, 7
s_mul_i32 s83, s83, 81920
s_add_u32 s83, s83, 69632
s_lshr_b32 s80, s92, 1
s_lshl_b32 s80, s80, 30
s_mov_b32 s82, 0
s_mov_b32 s94, 65536
buffer_load_dwordx2 v[30:31], v9, s[56:59], 0 offen sc1
s_waitcnt vmcnt(0)
Linc_%=:
v_cmp_eq_u64 vcc, v[30:31], v[28:29]
s_sub_u32 s94, s94, 1
s_nop 3
s_cselect_b32 s62, -1, vcc_lo
s_andn2_b32 s62, s62, s82
s_and_b32 s62, s62, 0xff
s_or_b32 s82, s82, s62
s_bitcmp1_b32 s62, 0
s_cbranch_scc0 Linc0_%=
s_add_u32 s81, s83, 0
buffer_load_dwordx4 v[64:67], v10, s[56:59], s81 offen sc1
s_add_u32 s81, s83, 4096
buffer_load_dwordx4 v[96:99], v10, s[56:59], s81 offen sc1
s_add_u32 s81, s83, 8192
s_or_b32 s81, s81, s80
buffer_load_dwordx4 v[128:131], v10, s[56:59], s81 offen sc1
Linc0_%=:
s_bitcmp1_b32 s62, 1
s_cbranch_scc0 Linc1_%=
s_add_u32 s81, s83, 10240
buffer_load_dwordx4 v[68:71], v10, s[56:59], s81 offen sc1
s_add_u32 s81, s83, 14336
buffer_load_dwordx4 v[100:103], v10, s[56:59], s81 offen sc1
s_add_u32 s81, s83, 18432
s_or_b32 s81, s81, s80
buffer_load_dwordx4 v[132:135], v10, s[56:59], s81 offen sc1
Linc1_%=:
s_bitcmp1_b32 s62, 2
s_cbranch_scc0 Linc2_%=
s_add_u32 s81, s83, 20480
buffer_load_dwordx4 v[72:75], v10, s[56:59], s81 offen sc1
s_add_u32 s81, s83, 24576
buffer_load_dwordx4 v[104:107], v10, s[56:59], s81 offen sc1
s_add_u32 s81, s83, 28672
s_or_b32 s81, s81, s80
buffer_load_dwordx4 v[136:139], v10, s[56:59], s81 offen sc1
Linc2_%=:
s_bitcmp1_b32 s62, 3
s_cbranch_scc0 Linc3_%=
s_add_u32 s81, s83, 30720
buffer_load_dwordx4 v[76:79], v10, s[56:59], s81 offen sc1
s_add_u32 s81, s83, 34816
buffer_load_dwordx4 v[108:111], v10, s[56:59], s81 offen sc1
s_add_u32 s81, s83, 38912
s_or_b32 s81, s81, s80
buffer_load_dwordx4 v[140:143], v10, s[56:59], s81 offen sc1
Linc3_%=:
s_bitcmp1_b32 s62, 4
s_cbranch_scc0 Linc4_%=
s_add_u32 s81, s83, 40960
buffer_load_dwordx4 v[80:83], v10, s[56:59], s81 offen sc1
s_add_u32 s81, s83, 45056
buffer_load_dwordx4 v[112:115], v10, s[56:59], s81 offen sc1
s_add_u32 s81, s83, 49152
s_or_b32 s81, s81, s80
buffer_load_dwordx4 v[144:147], v10, s[56:59], s81 offen sc1
Linc4_%=:
s_bitcmp1_b32 s62, 5
s_cbranch_scc0 Linc5_%=
s_add_u32 s81, s83, 51200
buffer_load_dwordx4 v[84:87], v10, s[56:59], s81 offen sc1
s_add_u32 s81, s83, 55296
buffer_load_dwordx4 v[116:119], v10, s[56:59], s81 offen sc1
s_add_u32 s81, s83, 59392
s_or_b32 s81, s81, s80
buffer_load_dwordx4 v[148:151], v10, s[56:59], s81 offen sc1
Linc5_%=:
s_bitcmp1_b32 s62, 6
s_cbranch_scc0 Linc6_%=
s_add_u32 s81, s83, 61440
buffer_load_dwordx4 v[88:91], v10, s[56:59], s81 offen sc1
s_add_u32 s81, s83, 65536
buffer_load_dwordx4 v[120:123], v10, s[56:59], s81 offen sc1
s_add_u32 s81, s83, 69632
s_or_b32 s81, s81, s80
buffer_load_dwordx4 v[152:155], v10, s[56:59], s81 offen sc1
Linc6_%=:
s_bitcmp1_b32 s62, 7
s_cbranch_scc0 Linc7_%=
s_add_u32 s81, s83, 71680
buffer_load_dwordx4 v[92:95], v10, s[56:59], s81 offen sc1
s_add_u32 s81, s83, 75776
buffer_load_dwordx4 v[124:127], v10, s[56:59], s81 offen sc1
s_add_u32 s81, s83, 79872
s_or_b32 s81, s81, s80
buffer_load_dwordx4 v[156:159], v10, s[56:59], s81 offen sc1
Linc7_%=:
s_cmp_eq_u32 s82, 0xff
s_cbranch_scc1 Lincx_%=
s_sleep 2
buffer_load_dwordx2 v[30:31], v9, s[56:59], 0 offen sc1
s_waitcnt vmcnt(0)
s_branch Linc_%=
Lincx_%=:
s_bfe_u32 s81, s61, 0x30007
s_or_b32 s81, s81, s92
s_cmp_lg_u32 s81, 0
s_cbranch_scc1 Lnoep_%=
s_bfe_u32 s81, s61, 0x5000a
s_lshl_b32 s81, s81, 7
s_add_u32 s81, s81, 65536
s_mov_b64 exec, 1
v_mov_b32 v30, s81
buffer_store_dwordx2 v[28:29], v30, s[56:59], 0 offen sc1
s_mov_b64 exec, -1
Lnoep_%=:
s_lshr_b32 s91, s61, 15
s_mul_i32 s91, s91, 896
v_lshlrev_b32 v16, 16, v176
v_and_b32 v17, 0xffff0000, v176
v_lshlrev_b32 v18, 16, v177
v_and_b32 v19, 0xffff0000, v177
v_lshlrev_b32 v20, 16, v178
v_and_b32 v21, 0xffff0000, v178
v_lshlrev_b32 v22, 16, v179
v_and_b32 v23, 0xffff0000, v179
v_lshlrev_b32 v26, 16, v180
v_and_b32 v27, 0xffff0000, v180
v_pk_add_f32 v[16:17], v[16:17], v[26:27]
v_lshlrev_b32 v24, 16, v181
v_and_b32 v25, 0xffff0000, v181
v_pk_add_f32 v[18:19], v[18:19], v[24:25]
v_lshlrev_b32 v26, 16, v182
v_and_b32 v27, 0xffff0000, v182
)"
R"(v_pk_add_f32 v[20:21], v[20:21], v[26:27]
v_lshlrev_b32 v24, 16, v183
v_and_b32 v25, 0xffff0000, v183
v_pk_add_f32 v[22:23], v[22:23], v[24:25]
v_lshlrev_b32 v26, 16, v184
v_and_b32 v27, 0xffff0000, v184
v_pk_add_f32 v[16:17], v[16:17], v[26:27]
v_lshlrev_b32 v24, 16, v185
v_and_b32 v25, 0xffff0000, v185
v_pk_add_f32 v[18:19], v[18:19], v[24:25]
v_lshlrev_b32 v26, 16, v186
v_and_b32 v27, 0xffff0000, v186
v_pk_add_f32 v[20:21], v[20:21], v[26:27]
v_lshlrev_b32 v24, 16, v187
v_and_b32 v25, 0xffff0000, v187
v_pk_add_f32 v[22:23], v[22:23], v[24:25]
v_lshlrev_b32 v26, 16, v188
v_and_b32 v27, 0xffff0000, v188
v_pk_add_f32 v[16:17], v[16:17], v[26:27]
v_lshlrev_b32 v24, 16, v189
v_and_b32 v25, 0xffff0000, v189
v_pk_add_f32 v[18:19], v[18:19], v[24:25]
v_lshlrev_b32 v26, 16, v190
v_and_b32 v27, 0xffff0000, v190
v_pk_add_f32 v[20:21], v[20:21], v[26:27]
v_lshlrev_b32 v24, 16, v191
v_and_b32 v25, 0xffff0000, v191
v_pk_add_f32 v[22:23], v[22:23], v[24:25]
v_lshlrev_b32 v26, 16, v192
v_and_b32 v27, 0xffff0000, v192
v_pk_add_f32 v[16:17], v[16:17], v[26:27]
v_lshlrev_b32 v24, 16, v193
v_and_b32 v25, 0xffff0000, v193
v_pk_add_f32 v[18:19], v[18:19], v[24:25]
v_lshlrev_b32 v26, 16, v194
v_and_b32 v27, 0xffff0000, v194
v_pk_add_f32 v[20:21], v[20:21], v[26:27]
v_lshlrev_b32 v24, 16, v195
v_and_b32 v25, 0xffff0000, v195
v_pk_add_f32 v[22:23], v[22:23], v[24:25]
v_lshlrev_b32 v26, 16, v196
v_and_b32 v27, 0xffff0000, v196
v_pk_add_f32 v[16:17], v[16:17], v[26:27]
v_lshlrev_b32 v24, 16, v197
v_and_b32 v25, 0xffff0000, v197
v_pk_add_f32 v[18:19], v[18:19], v[24:25]
v_lshlrev_b32 v26, 16, v198
v_and_b32 v27, 0xffff0000, v198
v_pk_add_f32 v[20:21], v[20:21], v[26:27]
v_lshlrev_b32 v24, 16, v199
v_and_b32 v25, 0xffff0000, v199
v_pk_add_f32 v[22:23], v[22:23], v[24:25]
v_lshlrev_b32 v26, 16, v200
v_and_b32 v27, 0xffff0000, v200
v_pk_add_f32 v[16:17], v[16:17], v[26:27]
v_lshlrev_b32 v24, 16, v201
v_and_b32 v25, 0xffff0000, v201
v_pk_add_f32 v[18:19], v[18:19], v[24:25]
v_lshlrev_b32 v26, 16, v202
v_and_b32 v27, 0xffff0000, v202
v_pk_add_f32 v[20:21], v[20:21], v[26:27]
v_lshlrev_b32 v24, 16, v203
v_and_b32 v25, 0xffff0000, v203
v_pk_add_f32 v[22:23], v[22:23], v[24:25]
v_lshlrev_b32 v26, 16, v204
v_and_b32 v27, 0xffff0000, v204
v_pk_add_f32 v[16:17], v[16:17], v[26:27]
v_lshlrev_b32 v24, 16, v205
v_and_b32 v25, 0xffff0000, v205
v_pk_add_f32 v[18:19], v[18:19], v[24:25]
v_lshlrev_b32 v26, 16, v206
v_and_b32 v27, 0xffff0000, v206
v_pk_add_f32 v[20:21], v[20:21], v[26:27]
v_lshlrev_b32 v24, 16, v207
v_and_b32 v25, 0xffff0000, v207
v_pk_add_f32 v[22:23], v[22:23], v[24:25]
v_cvt_pk_bf16_f32 v176, v16, v17
v_cvt_pk_bf16_f32 v177, v18, v19
v_cvt_pk_bf16_f32 v178, v20, v21
v_cvt_pk_bf16_f32 v179, v22, v23
s_add_u32 s81, s91, 0
buffer_store_dwordx4 v[176:179], v11, s[40:43], s81 offen
v_lshlrev_b32 v16, 16, v208
v_and_b32 v17, 0xffff0000, v208
v_lshlrev_b32 v18, 16, v209
v_and_b32 v19, 0xffff0000, v209
v_lshlrev_b32 v20, 16, v210
v_and_b32 v21, 0xffff0000, v210
v_lshlrev_b32 v22, 16, v211
v_and_b32 v23, 0xffff0000, v211
v_lshlrev_b32 v26, 16, v212
v_and_b32 v27, 0xffff0000, v212
v_pk_add_f32 v[16:17], v[16:17], v[26:27]
v_lshlrev_b32 v24, 16, v213
v_and_b32 v25, 0xffff0000, v213
v_pk_add_f32 v[18:19], v[18:19], v[24:25]
v_lshlrev_b32 v26, 16, v214
v_and_b32 v27, 0xffff0000, v214
v_pk_add_f32 v[20:21], v[20:21], v[26:27]
v_lshlrev_b32 v24, 16, v215
v_and_b32 v25, 0xffff0000, v215
v_pk_add_f32 v[22:23], v[22:23], v[24:25]
v_lshlrev_b32 v26, 16, v216
v_and_b32 v27, 0xffff0000, v216
v_pk_add_f32 v[16:17], v[16:17], v[26:27]
v_lshlrev_b32 v24, 16, v217
v_and_b32 v25, 0xffff0000, v217
v_pk_add_f32 v[18:19], v[18:19], v[24:25]
v_lshlrev_b32 v26, 16, v218
v_and_b32 v27, 0xffff0000, v218
v_pk_add_f32 v[20:21], v[20:21], v[26:27]
v_lshlrev_b32 v24, 16, v219
v_and_b32 v25, 0xffff0000, v219
v_pk_add_f32 v[22:23], v[22:23], v[24:25]
v_lshlrev_b32 v26, 16, v220
v_and_b32 v27, 0xffff0000, v220
v_pk_add_f32 v[16:17], v[16:17], v[26:27]
v_lshlrev_b32 v24, 16, v221
v_and_b32 v25, 0xffff0000, v221
v_pk_add_f32 v[18:19], v[18:19], v[24:25]
v_lshlrev_b32 v26, 16, v222
v_and_b32 v27, 0xffff0000, v222
v_pk_add_f32 v[20:21], v[20:21], v[26:27]
v_lshlrev_b32 v24, 16, v223
v_and_b32 v25, 0xffff0000, v223
v_pk_add_f32 v[22:23], v[22:23], v[24:25]
v_lshlrev_b32 v26, 16, v224
v_and_b32 v27, 0xffff0000, v224
v_pk_add_f32 v[16:17], v[16:17], v[26:27]
v_lshlrev_b32 v24, 16, v225
v_and_b32 v25, 0xffff0000, v225
v_pk_add_f32 v[18:19], v[18:19], v[24:25]
v_lshlrev_b32 v26, 16, v226
v_and_b32 v27, 0xffff0000, v226
v_pk_add_f32 v[20:21], v[20:21], v[26:27]
v_lshlrev_b32 v24, 16, v227
v_and_b32 v25, 0xffff0000, v227
v_pk_add_f32 v[22:23], v[22:23], v[24:25]
v_lshlrev_b32 v26, 16, v228
v_and_b32 v27, 0xffff0000, v228
v_pk_add_f32 v[16:17], v[16:17], v[26:27]
v_lshlrev_b32 v24, 16, v229
v_and_b32 v25, 0xffff0000, v229
v_pk_add_f32 v[18:19], v[18:19], v[24:25]
v_lshlrev_b32 v26, 16, v230
v_and_b32 v27, 0xffff0000, v230
v_pk_add_f32 v[20:21], v[20:21], v[26:27]
v_lshlrev_b32 v24, 16, v231
v_and_b32 v25, 0xffff0000, v231
v_pk_add_f32 v[22:23], v[22:23], v[24:25]
v_lshlrev_b32 v26, 16, v232
v_and_b32 v27, 0xffff0000, v232
v_pk_add_f32 v[16:17], v[16:17], v[26:27]
v_lshlrev_b32 v24, 16, v233
v_and_b32 v25, 0xffff0000, v233
v_pk_add_f32 v[18:19], v[18:19], v[24:25]
v_lshlrev_b32 v26, 16, v234
v_and_b32 v27, 0xffff0000, v234
v_pk_add_f32 v[20:21], v[20:21], v[26:27]
v_lshlrev_b32 v24, 16, v235
v_and_b32 v25, 0xffff0000, v235
v_pk_add_f32 v[22:23], v[22:23], v[24:25]
v_lshlrev_b32 v26, 16, v236
v_and_b32 v27, 0xffff0000, v236
v_pk_add_f32 v[16:17], v[16:17], v[26:27]
v_lshlrev_b32 v24, 16, v237
v_and_b32 v25, 0xffff0000, v237
v_pk_add_f32 v[18:19], v[18:19], v[24:25]
v_lshlrev_b32 v26, 16, v238
v_and_b32 v27, 0xffff0000, v238
v_pk_add_f32 v[20:21], v[20:21], v[26:27]
v_lshlrev_b32 v24, 16, v239
v_and_b32 v25, 0xffff0000, v239
v_pk_add_f32 v[22:23], v[22:23], v[24:25]
v_cvt_pk_bf16_f32 v208, v16, v17
v_cvt_pk_bf16_f32 v209, v18, v19
v_cvt_pk_bf16_f32 v210, v20, v21
v_cvt_pk_bf16_f32 v211, v22, v23
s_add_u32 s81, s91, 57344
buffer_store_dwordx4 v[208:211], v11, s[40:43], s81 offen
v_lshlrev_b32 v16, 16, v32
v_and_b32 v17, 0xffff0000, v32
v_lshlrev_b32 v18, 16, v33
v_and_b32 v19, 0xffff0000, v33
v_lshlrev_b32 v20, 16, v34
v_and_b32 v21, 0xffff0000, v34
v_lshlrev_b32 v22, 16, v35
v_and_b32 v23, 0xffff0000, v35
v_lshlrev_b32 v26, 16, v36
v_and_b32 v27, 0xffff0000, v36
v_pk_add_f32 v[16:17], v[16:17], v[26:27]
v_lshlrev_b32 v24, 16, v37
v_and_b32 v25, 0xffff0000, v37
v_pk_add_f32 v[18:19], v[18:19], v[24:25]
v_lshlrev_b32 v26, 16, v38
v_and_b32 v27, 0xffff0000, v38
v_pk_add_f32 v[20:21], v[20:21], v[26:27]
v_lshlrev_b32 v24, 16, v39
v_and_b32 v25, 0xffff0000, v39
v_pk_add_f32 v[22:23], v[22:23], v[24:25]
)"
R"(v_lshlrev_b32 v26, 16, v40
v_and_b32 v27, 0xffff0000, v40
v_pk_add_f32 v[16:17], v[16:17], v[26:27]
v_lshlrev_b32 v24, 16, v41
v_and_b32 v25, 0xffff0000, v41
v_pk_add_f32 v[18:19], v[18:19], v[24:25]
v_lshlrev_b32 v26, 16, v42
v_and_b32 v27, 0xffff0000, v42
v_pk_add_f32 v[20:21], v[20:21], v[26:27]
v_lshlrev_b32 v24, 16, v43
v_and_b32 v25, 0xffff0000, v43
v_pk_add_f32 v[22:23], v[22:23], v[24:25]
v_lshlrev_b32 v26, 16, v44
v_and_b32 v27, 0xffff0000, v44
v_pk_add_f32 v[16:17], v[16:17], v[26:27]
v_lshlrev_b32 v24, 16, v45
v_and_b32 v25, 0xffff0000, v45
v_pk_add_f32 v[18:19], v[18:19], v[24:25]
v_lshlrev_b32 v26, 16, v46
v_and_b32 v27, 0xffff0000, v46
v_pk_add_f32 v[20:21], v[20:21], v[26:27]
v_lshlrev_b32 v24, 16, v47
v_and_b32 v25, 0xffff0000, v47
v_pk_add_f32 v[22:23], v[22:23], v[24:25]
v_lshlrev_b32 v26, 16, v48
v_and_b32 v27, 0xffff0000, v48
v_pk_add_f32 v[16:17], v[16:17], v[26:27]
v_lshlrev_b32 v24, 16, v49
v_and_b32 v25, 0xffff0000, v49
v_pk_add_f32 v[18:19], v[18:19], v[24:25]
v_lshlrev_b32 v26, 16, v50
v_and_b32 v27, 0xffff0000, v50
v_pk_add_f32 v[20:21], v[20:21], v[26:27]
v_lshlrev_b32 v24, 16, v51
v_and_b32 v25, 0xffff0000, v51
v_pk_add_f32 v[22:23], v[22:23], v[24:25]
v_lshlrev_b32 v26, 16, v52
v_and_b32 v27, 0xffff0000, v52
v_pk_add_f32 v[16:17], v[16:17], v[26:27]
v_lshlrev_b32 v24, 16, v53
v_and_b32 v25, 0xffff0000, v53
v_pk_add_f32 v[18:19], v[18:19], v[24:25]
v_lshlrev_b32 v26, 16, v54
v_and_b32 v27, 0xffff0000, v54
v_pk_add_f32 v[20:21], v[20:21], v[26:27]
v_lshlrev_b32 v24, 16, v55
v_and_b32 v25, 0xffff0000, v55
v_pk_add_f32 v[22:23], v[22:23], v[24:25]
v_lshlrev_b32 v26, 16, v56
v_and_b32 v27, 0xffff0000, v56
v_pk_add_f32 v[16:17], v[16:17], v[26:27]
v_lshlrev_b32 v24, 16, v57
v_and_b32 v25, 0xffff0000, v57
v_pk_add_f32 v[18:19], v[18:19], v[24:25]
v_lshlrev_b32 v26, 16, v58
v_and_b32 v27, 0xffff0000, v58
v_pk_add_f32 v[20:21], v[20:21], v[26:27]
v_lshlrev_b32 v24, 16, v59
v_and_b32 v25, 0xffff0000, v59
v_pk_add_f32 v[22:23], v[22:23], v[24:25]
v_lshlrev_b32 v26, 16, v60
v_and_b32 v27, 0xffff0000, v60
v_pk_add_f32 v[16:17], v[16:17], v[26:27]
v_lshlrev_b32 v24, 16, v61
v_and_b32 v25, 0xffff0000, v61
v_pk_add_f32 v[18:19], v[18:19], v[24:25]
v_lshlrev_b32 v26, 16, v62
v_and_b32 v27, 0xffff0000, v62
v_pk_add_f32 v[20:21], v[20:21], v[26:27]
v_lshlrev_b32 v24, 16, v63
v_and_b32 v25, 0xffff0000, v63
v_pk_add_f32 v[22:23], v[22:23], v[24:25]
v_cvt_pk_bf16_f32 v32, v16, v17
v_cvt_pk_bf16_f32 v33, v18, v19
v_cvt_pk_bf16_f32 v34, v20, v21
v_cvt_pk_bf16_f32 v35, v22, v23
s_add_u32 s81, s91, 114688
buffer_store_dwordx4 v[32:35], v11, s[40:43], s81 offen
s_waitcnt vmcnt(0)
s_lshr_b32 s91, s61, 15
s_xor_b32 s91, s91, 1
s_mul_i32 s91, s91, 896
v_lshlrev_b32 v16, 16, v64
v_and_b32 v17, 0xffff0000, v64
v_lshlrev_b32 v18, 16, v65
v_and_b32 v19, 0xffff0000, v65
v_lshlrev_b32 v20, 16, v66
v_and_b32 v21, 0xffff0000, v66
v_lshlrev_b32 v22, 16, v67
v_and_b32 v23, 0xffff0000, v67
v_lshlrev_b32 v26, 16, v68
v_and_b32 v27, 0xffff0000, v68
v_pk_add_f32 v[16:17], v[16:17], v[26:27]
v_lshlrev_b32 v24, 16, v69
v_and_b32 v25, 0xffff0000, v69
v_pk_add_f32 v[18:19], v[18:19], v[24:25]
v_lshlrev_b32 v26, 16, v70
v_and_b32 v27, 0xffff0000, v70
v_pk_add_f32 v[20:21], v[20:21], v[26:27]
v_lshlrev_b32 v24, 16, v71
v_and_b32 v25, 0xffff0000, v71
v_pk_add_f32 v[22:23], v[22:23], v[24:25]
v_lshlrev_b32 v26, 16, v72
v_and_b32 v27, 0xffff0000, v72
v_pk_add_f32 v[16:17], v[16:17], v[26:27]
v_lshlrev_b32 v24, 16, v73
v_and_b32 v25, 0xffff0000, v73
v_pk_add_f32 v[18:19], v[18:19], v[24:25]
v_lshlrev_b32 v26, 16, v74
v_and_b32 v27, 0xffff0000, v74
v_pk_add_f32 v[20:21], v[20:21], v[26:27]
v_lshlrev_b32 v24, 16, v75
v_and_b32 v25, 0xffff0000, v75
v_pk_add_f32 v[22:23], v[22:23], v[24:25]
v_lshlrev_b32 v26, 16, v76
v_and_b32 v27, 0xffff0000, v76
v_pk_add_f32 v[16:17], v[16:17], v[26:27]
v_lshlrev_b32 v24, 16, v77
v_and_b32 v25, 0xffff0000, v77
v_pk_add_f32 v[18:19], v[18:19], v[24:25]
v_lshlrev_b32 v26, 16, v78
v_and_b32 v27, 0xffff0000, v78
v_pk_add_f32 v[20:21], v[20:21], v[26:27]
v_lshlrev_b32 v24, 16, v79
v_and_b32 v25, 0xffff0000, v79
v_pk_add_f32 v[22:23], v[22:23], v[24:25]
v_lshlrev_b32 v26, 16, v80
v_and_b32 v27, 0xffff0000, v80
v_pk_add_f32 v[16:17], v[16:17], v[26:27]
v_lshlrev_b32 v24, 16, v81
v_and_b32 v25, 0xffff0000, v81
v_pk_add_f32 v[18:19], v[18:19], v[24:25]
v_lshlrev_b32 v26, 16, v82
v_and_b32 v27, 0xffff0000, v82
v_pk_add_f32 v[20:21], v[20:21], v[26:27]
v_lshlrev_b32 v24, 16, v83
v_and_b32 v25, 0xffff0000, v83
v_pk_add_f32 v[22:23], v[22:23], v[24:25]
v_lshlrev_b32 v26, 16, v84
v_and_b32 v27, 0xffff0000, v84
v_pk_add_f32 v[16:17], v[16:17], v[26:27]
v_lshlrev_b32 v24, 16, v85
v_and_b32 v25, 0xffff0000, v85
v_pk_add_f32 v[18:19], v[18:19], v[24:25]
v_lshlrev_b32 v26, 16, v86
v_and_b32 v27, 0xffff0000, v86
v_pk_add_f32 v[20:21], v[20:21], v[26:27]
v_lshlrev_b32 v24, 16, v87
v_and_b32 v25, 0xffff0000, v87
v_pk_add_f32 v[22:23], v[22:23], v[24:25]
v_lshlrev_b32 v26, 16, v88
v_and_b32 v27, 0xffff0000, v88
v_pk_add_f32 v[16:17], v[16:17], v[26:27]
v_lshlrev_b32 v24, 16, v89
v_and_b32 v25, 0xffff0000, v89
v_pk_add_f32 v[18:19], v[18:19], v[24:25]
v_lshlrev_b32 v26, 16, v90
v_and_b32 v27, 0xffff0000, v90
v_pk_add_f32 v[20:21], v[20:21], v[26:27]
v_lshlrev_b32 v24, 16, v91
v_and_b32 v25, 0xffff0000, v91
v_pk_add_f32 v[22:23], v[22:23], v[24:25]
v_lshlrev_b32 v26, 16, v92
v_and_b32 v27, 0xffff0000, v92
v_pk_add_f32 v[16:17], v[16:17], v[26:27]
v_lshlrev_b32 v24, 16, v93
v_and_b32 v25, 0xffff0000, v93
v_pk_add_f32 v[18:19], v[18:19], v[24:25]
v_lshlrev_b32 v26, 16, v94
v_and_b32 v27, 0xffff0000, v94
v_pk_add_f32 v[20:21], v[20:21], v[26:27]
v_lshlrev_b32 v24, 16, v95
v_and_b32 v25, 0xffff0000, v95
v_pk_add_f32 v[22:23], v[22:23], v[24:25]
v_cvt_pk_bf16_f32 v64, v16, v17
v_cvt_pk_bf16_f32 v65, v18, v19
v_cvt_pk_bf16_f32 v66, v20, v21
v_cvt_pk_bf16_f32 v67, v22, v23
s_add_u32 s81, s91, 0
buffer_store_dwordx4 v[64:67], v11, s[40:43], s81 offen
v_lshlrev_b32 v16, 16, v96
v_and_b32 v17, 0xffff0000, v96
v_lshlrev_b32 v18, 16, v97
v_and_b32 v19, 0xffff0000, v97
v_lshlrev_b32 v20, 16, v98
v_and_b32 v21, 0xffff0000, v98
v_lshlrev_b32 v22, 16, v99
v_and_b32 v23, 0xffff0000, v99
v_lshlrev_b32 v26, 16, v100
v_and_b32 v27, 0xffff0000, v100
v_pk_add_f32 v[16:17], v[16:17], v[26:27]
v_lshlrev_b32 v24, 16, v101
v_and_b32 v25, 0xffff0000, v101
v_pk_add_f32 v[18:19], v[18:19], v[24:25]
v_lshlrev_b32 v26, 16, v102
v_and_b32 v27, 0xffff0000, v102
v_pk_add_f32 v[20:21], v[20:21], v[26:27]
v_lshlrev_b32 v24, 16, v103
v_and_b32 v25, 0xffff0000, v103
v_pk_add_f32 v[22:23], v[22:23], v[24:25]
)"
R"(v_lshlrev_b32 v26, 16, v104
v_and_b32 v27, 0xffff0000, v104
v_pk_add_f32 v[16:17], v[16:17], v[26:27]
v_lshlrev_b32 v24, 16, v105
v_and_b32 v25, 0xffff0000, v105
v_pk_add_f32 v[18:19], v[18:19], v[24:25]
v_lshlrev_b32 v26, 16, v106
v_and_b32 v27, 0xffff0000, v106
v_pk_add_f32 v[20:21], v[20:21], v[26:27]
v_lshlrev_b32 v24, 16, v107
v_and_b32 v25, 0xffff0000, v107
v_pk_add_f32 v[22:23], v[22:23], v[24:25]
v_lshlrev_b32 v26, 16, v108
v_and_b32 v27, 0xffff0000, v108
v_pk_add_f32 v[16:17], v[16:17], v[26:27]
v_lshlrev_b32 v24, 16, v109
v_and_b32 v25, 0xffff0000, v109
v_pk_add_f32 v[18:19], v[18:19], v[24:25]
v_lshlrev_b32 v26, 16, v110
v_and_b32 v27, 0xffff0000, v110
v_pk_add_f32 v[20:21], v[20:21], v[26:27]
v_lshlrev_b32 v24, 16, v111
v_and_b32 v25, 0xffff0000, v111
v_pk_add_f32 v[22:23], v[22:23], v[24:25]
v_lshlrev_b32 v26, 16, v112
v_and_b32 v27, 0xffff0000, v112
v_pk_add_f32 v[16:17], v[16:17], v[26:27]
v_lshlrev_b32 v24, 16, v113
v_and_b32 v25, 0xffff0000, v113
v_pk_add_f32 v[18:19], v[18:19], v[24:25]
v_lshlrev_b32 v26, 16, v114
v_and_b32 v27, 0xffff0000, v114
v_pk_add_f32 v[20:21], v[20:21], v[26:27]
v_lshlrev_b32 v24, 16, v115
v_and_b32 v25, 0xffff0000, v115
v_pk_add_f32 v[22:23], v[22:23], v[24:25]
v_lshlrev_b32 v26, 16, v116
v_and_b32 v27, 0xffff0000, v116
v_pk_add_f32 v[16:17], v[16:17], v[26:27]
v_lshlrev_b32 v24, 16, v117
v_and_b32 v25, 0xffff0000, v117
v_pk_add_f32 v[18:19], v[18:19], v[24:25]
v_lshlrev_b32 v26, 16, v118
v_and_b32 v27, 0xffff0000, v118
v_pk_add_f32 v[20:21], v[20:21], v[26:27]
v_lshlrev_b32 v24, 16, v119
v_and_b32 v25, 0xffff0000, v119
v_pk_add_f32 v[22:23], v[22:23], v[24:25]
v_lshlrev_b32 v26, 16, v120
v_and_b32 v27, 0xffff0000, v120
v_pk_add_f32 v[16:17], v[16:17], v[26:27]
v_lshlrev_b32 v24, 16, v121
v_and_b32 v25, 0xffff0000, v121
v_pk_add_f32 v[18:19], v[18:19], v[24:25]
v_lshlrev_b32 v26, 16, v122
v_and_b32 v27, 0xffff0000, v122
v_pk_add_f32 v[20:21], v[20:21], v[26:27]
v_lshlrev_b32 v24, 16, v123
v_and_b32 v25, 0xffff0000, v123
v_pk_add_f32 v[22:23], v[22:23], v[24:25]
v_lshlrev_b32 v26, 16, v124
v_and_b32 v27, 0xffff0000, v124
v_pk_add_f32 v[16:17], v[16:17], v[26:27]
v_lshlrev_b32 v24, 16, v125
v_and_b32 v25, 0xffff0000, v125
v_pk_add_f32 v[18:19], v[18:19], v[24:25]
v_lshlrev_b32 v26, 16, v126
v_and_b32 v27, 0xffff0000, v126
v_pk_add_f32 v[20:21], v[20:21], v[26:27]
v_lshlrev_b32 v24, 16, v127
v_and_b32 v25, 0xffff0000, v127
v_pk_add_f32 v[22:23], v[22:23], v[24:25]
v_cvt_pk_bf16_f32 v96, v16, v17
v_cvt_pk_bf16_f32 v97, v18, v19
v_cvt_pk_bf16_f32 v98, v20, v21
v_cvt_pk_bf16_f32 v99, v22, v23
s_add_u32 s81, s91, 57344
buffer_store_dwordx4 v[96:99], v11, s[40:43], s81 offen
v_lshlrev_b32 v16, 16, v128
v_and_b32 v17, 0xffff0000, v128
v_lshlrev_b32 v18, 16, v129
v_and_b32 v19, 0xffff0000, v129
v_lshlrev_b32 v20, 16, v130
v_and_b32 v21, 0xffff0000, v130
v_lshlrev_b32 v22, 16, v131
v_and_b32 v23, 0xffff0000, v131
v_lshlrev_b32 v26, 16, v132
v_and_b32 v27, 0xffff0000, v132
v_pk_add_f32 v[16:17], v[16:17], v[26:27]
v_lshlrev_b32 v24, 16, v133
v_and_b32 v25, 0xffff0000, v133
v_pk_add_f32 v[18:19], v[18:19], v[24:25]
v_lshlrev_b32 v26, 16, v134
v_and_b32 v27, 0xffff0000, v134
v_pk_add_f32 v[20:21], v[20:21], v[26:27]
v_lshlrev_b32 v24, 16, v135
v_and_b32 v25, 0xffff0000, v135
v_pk_add_f32 v[22:23], v[22:23], v[24:25]
v_lshlrev_b32 v26, 16, v136
v_and_b32 v27, 0xffff0000, v136
v_pk_add_f32 v[16:17], v[16:17], v[26:27]
v_lshlrev_b32 v24, 16, v137
v_and_b32 v25, 0xffff0000, v137
v_pk_add_f32 v[18:19], v[18:19], v[24:25]
v_lshlrev_b32 v26, 16, v138
v_and_b32 v27, 0xffff0000, v138
v_pk_add_f32 v[20:21], v[20:21], v[26:27]
v_lshlrev_b32 v24, 16, v139
v_and_b32 v25, 0xffff0000, v139
v_pk_add_f32 v[22:23], v[22:23], v[24:25]
v_lshlrev_b32 v26, 16, v140
v_and_b32 v27, 0xffff0000, v140
v_pk_add_f32 v[16:17], v[16:17], v[26:27]
v_lshlrev_b32 v24, 16, v141
v_and_b32 v25, 0xffff0000, v141
v_pk_add_f32 v[18:19], v[18:19], v[24:25]
v_lshlrev_b32 v26, 16, v142
v_and_b32 v27, 0xffff0000, v142
v_pk_add_f32 v[20:21], v[20:21], v[26:27]
v_lshlrev_b32 v24, 16, v143
v_and_b32 v25, 0xffff0000, v143
v_pk_add_f32 v[22:23], v[22:23], v[24:25]
v_lshlrev_b32 v26, 16, v144
v_and_b32 v27, 0xffff0000, v144
v_pk_add_f32 v[16:17], v[16:17], v[26:27]
v_lshlrev_b32 v24, 16, v145
v_and_b32 v25, 0xffff0000, v145
v_pk_add_f32 v[18:19], v[18:19], v[24:25]
v_lshlrev_b32 v26, 16, v146
v_and_b32 v27, 0xffff0000, v146
v_pk_add_f32 v[20:21], v[20:21], v[26:27]
v_lshlrev_b32 v24, 16, v147
v_and_b32 v25, 0xffff0000, v147
v_pk_add_f32 v[22:23], v[22:23], v[24:25]
v_lshlrev_b32 v26, 16, v148
v_and_b32 v27, 0xffff0000, v148
v_pk_add_f32 v[16:17], v[16:17], v[26:27]
v_lshlrev_b32 v24, 16, v149
v_and_b32 v25, 0xffff0000, v149
v_pk_add_f32 v[18:19], v[18:19], v[24:25]
v_lshlrev_b32 v26, 16, v150
v_and_b32 v27, 0xffff0000, v150
v_pk_add_f32 v[20:21], v[20:21], v[26:27]
v_lshlrev_b32 v24, 16, v151
v_and_b32 v25, 0xffff0000, v151
v_pk_add_f32 v[22:23], v[22:23], v[24:25]
v_lshlrev_b32 v26, 16, v152
v_and_b32 v27, 0xffff0000, v152
v_pk_add_f32 v[16:17], v[16:17], v[26:27]
v_lshlrev_b32 v24, 16, v153
v_and_b32 v25, 0xffff0000, v153
v_pk_add_f32 v[18:19], v[18:19], v[24:25]
v_lshlrev_b32 v26, 16, v154
v_and_b32 v27, 0xffff0000, v154
v_pk_add_f32 v[20:21], v[20:21], v[26:27]
v_lshlrev_b32 v24, 16, v155
v_and_b32 v25, 0xffff0000, v155
v_pk_add_f32 v[22:23], v[22:23], v[24:25]
v_lshlrev_b32 v26, 16, v156
v_and_b32 v27, 0xffff0000, v156
v_pk_add_f32 v[16:17], v[16:17], v[26:27]
v_lshlrev_b32 v24, 16, v157
v_and_b32 v25, 0xffff0000, v157
v_pk_add_f32 v[18:19], v[18:19], v[24:25]
v_lshlrev_b32 v26, 16, v158
v_and_b32 v27, 0xffff0000, v158
v_pk_add_f32 v[20:21], v[20:21], v[26:27]
v_lshlrev_b32 v24, 16, v159
v_and_b32 v25, 0xffff0000, v159
v_pk_add_f32 v[22:23], v[22:23], v[24:25]
v_cvt_pk_bf16_f32 v128, v16, v17
v_cvt_pk_bf16_f32 v129, v18, v19
v_cvt_pk_bf16_f32 v130, v20, v21
v_cvt_pk_bf16_f32 v131, v22, v23
s_add_u32 s81, s91, 114688
buffer_store_dwordx4 v[128:131], v11, s[40:43], s81 offen
Lcode1_%=:
)"

      : [tid] "+v"(tid)
      : [x0] "s"(i0[0] + batch * 71680), [k0] "s"(i0[1] + kv0), [v0] "s"(i0[2] + kv0), [wq0] "s"(i0[3] + rank * 1024), [wo0] "s"(i0[4] + rank * 458752),
        [x1] "s"(i1[0] + batch * 71680), [k1] "s"(i1[1] + kv1), [v1] "s"(i1[2] + kv1), [wq1] "s"(i1[3] + rank * 1024), [wo1] "s"(i1[4] + rank * 458752),
        [ws] "s"(p.ws), [out] "s"(p.out), [fl0] "s"(((br0 * 32 + batch) * 8 + rank) * 128)
      : "v1","v2","v3","v4","v5","v6","v7","v8","v9","v10","v11","v12","v13","v14","v15","v16","v17","v18","v19","v20","v21","v22","v23","v24","v25","v26","v27","v28","v29","v30","v31","v32","v33","v34","v35","v36","v37","v38","v39","v40","v41","v42","v43","v44","v45","v46","v47","v48","v49","v50","v51","v52","v53","v54","v55","v56","v57","v58","v59","v60","v61","v62","v63","v64","v65","v66","v67","v68","v69","v70","v71","v72","v73","v74","v75","v76","v77","v78","v79","v80","v81","v82","v83","v84","v85","v86","v87","v88","v89","v90","v91","v92","v93","v94","v95","v96","v97","v98","v99","v100","v101","v102","v103","v104","v105","v106","v107","v108","v109","v110","v111","v112","v113","v114","v115","v116","v117","v118","v119","v120","v121","v122","v123","v124","v125","v126","v127","v128","v129","v130","v131","v132","v133","v134","v135","v136","v137","v138","v139","v140","v141","v142","v143","v144","v145","v146","v147","v148","v149","v150","v151","v152","v153","v154","v155","v156","v157","v158","v159","v160","v161","v162","v163","v164","v165","v166","v167","v168","v169","v170","v171","v172","v173","v174","v175","v176","v177","v178","v179","v180","v181","v182","v183","v184","v185","v186","v187","v188","v189","v190","v191","v192","v193","v194","v195","v196","v197","v198","v199","v200","v201","v202","v203","v204","v205","v206","v207","v208","v209","v210","v211","v212","v213","v214","v215","v216","v217","v218","v219","v220","v221","v222","v223","v224","v225","v226","v227","v228","v229","v230","v231","v232","v233","v234","v235","v236","v237","v238","v239","v240","v241","v242","v243","v244","v245","v246","v247","v248","v249","v250","v251","v252","v253","v254","v255","a0","a1","a2","a3","a4","a5","a6","a7","a8","a9","a10","a11","a12","a13","a14","a15","a16","a17","a18","a19","a20","a21","a22","a23","a24","a25","a26","a27","a28","a29","a30","a31","a32","a33","a34","a35","a36","a37","a38","a39","a40","a41","a42","a43","a44","a45","a46","a47","a48","a49","a50","a51","a52","a53","a54","a55","a56","a57","a58","a59","a60","a61","a62","a63","a64","a65","a66","a67","a68","a69","a70","a71","a72","a73","a74","a75","a76","a77","a78","a79","a80","a81","a82","a83","a84","a85","a86","a87","a88","a89","a90","a91","a92","a93","a94","a95","a96","a97","a98","a99","a100","a101","a102","a103","a104","a105","a106","a107","a108","a109","a110","a111","a112","a113","a114","a115","a116","a117","a118","a119","a120","a121","a122","a123","a124","a125","a126","a127","a128","a129","a130","a131","a132","a133","a134","a135","a136","a137","a138","a139","a140","a141","a142","a143","a144","a145","a146","a147","a148","a149","a150","a151","a152","a153","a154","a155","a156","a157","a158","a159","a160","a161","a162","a163","a164","a165","a166","a167","a168","a169","a170","a171","a172","a173","a174","a175","a176","a177","a178","a179","a180","a181","a182","a183","a184","a185","a186","a187","a188","a189","a190","a191","a192","a193","a194","a195","a196","a197","a198","a199","a200","a201","a202","a203","a204","a205","a206","a207","a208","a209","a210","a211","a212","a213","a214","a215","a216","a217","a218","a219","a220","a221","a222","a223","a224","a225","a226","a227","a228","a229","a230","a231","a232","a233","a234","a235","a236","a237","a238","a239","a240","a241","a242","a243","a244","a245","a246","a247","a248","a249","a250","a251","a252","a253","a254","a255","s36","s37","s38","s39","s40","s41","s42","s43","s44","s45","s46","s47","s48","s49","s50","s51","s52","s53","s54","s55","s56","s57","s58","s59","s60","s61","s62","s63","s64","s65","s66","s67","s68","s69","s70","s71","s72","s73","s74","s75","s76","s77","s78","s79","s80","s81","s82","s83","s84","s85","s86","s87","s88","s89","s90","s91","s92","s93","s94","s95","vcc","scc","memory");
}
}

extern "C" void kernel_launch(void* const* d_in, const int* in_sizes, int n_in, void* d_out, int out_size, void* d_ws, size_t ws_size,
                              hipStream_t stream) {
  Params p;
  for (int i = 0; i < 10; ++i) p.in[i] = (const char*)d_in[i];
  p.out = (char*)d_out;
  p.ws = (char*)d_ws;
  (void)hipFuncSetAttribute((const void*)fused_attn, hipFuncAttributeMaxDynamicSharedMemorySize, 163840);
  fused_attn<<<256, 256, 163840, stream>>>(p);
}
```
